```python
import torch

def _softmax(x, axis=-1):
    return torch.softmax(x, dim=axis)
def _gelu(x):
    return torch.nn.functional.gelu(x, approximate="tanh")
def _silu(x):
    return torch.nn.functional.silu(x)
def _sigmoid(x):
    return torch.sigmoid(x)
def _layernorm(x, axis=-1, eps=1e-5):
    m = x.mean(dim=axis, keepdim=True)
    v = x.var(dim=axis, keepdim=True, unbiased=False)
    return (x - m) / torch.sqrt(v + eps)
def _scatter_add(tgt, idx, src, axis=0):
    return tgt.clone().index_add_(axis, idx, src)
def _segment_sum(src, seg, n):
    out = torch.zeros((n,) + src.shape[1:], dtype=src.dtype)
    return out.index_add_(0, seg, src)
def _pad(x, pw):
    flat = tuple(v for pair in reversed(pw) for v in pair)
    return torch.nn.functional.pad(x, flat)

kernel_name = 'interleave_layers__grouped_kv_mqa__glu_gated_bilinear__prompt'

def setup_inputs():

    _w = (lambda a: (lambda t: t.to(torch.bfloat16) if t.is_floating_point() else t)(torch.as_tensor(a)))
    def _A_setup():
        B, S, D, H = 1, 2048, 448, 16
        Dh = D // H
        return {
            "x": _w((lambda *s: torch.randn(*s, dtype=torch.bfloat16) * 0.05)(B, S, D)),
            "wq": _w((lambda *s: torch.randn(*s, dtype=torch.bfloat16) * 0.05)(D, H * Dh)),
            "wk": _w((lambda *s: torch.randn(*s, dtype=torch.bfloat16) * 0.05)(D, Dh)),
            "wv": _w((lambda *s: torch.randn(*s, dtype=torch.bfloat16) * 0.05)(D, Dh)),
            "wo": _w((lambda *s: torch.randn(*s, dtype=torch.bfloat16) * 0.05)(H * Dh, D)),
        }
    _A_in = _A_setup()

    def _B_setup():
        B, S, D, F = 1, 2048, 448, 1792
        return {
            "x": _w((lambda *s: torch.randn(*s, dtype=torch.bfloat16) * 0.05)(B, S, D)),
            "w_gate": _w((lambda *s: torch.randn(*s, dtype=torch.bfloat16) * 0.4)(D, F) * (D ** -0.5)),
            "w_up": _w((lambda *s: torch.randn(*s, dtype=torch.bfloat16) * 0.4)(D, F) * (D ** -0.5)),
            "w_down": _w((lambda *s: torch.randn(*s, dtype=torch.bfloat16) * 0.4)(F, D) * (F ** -0.5)),
        }
    _B_in = _B_setup()

    return {
        "A_x": _A_in["x"],
        "A_wq": _A_in["wq"],
        "A_wk": _A_in["wk"],
        "A_wv": _A_in["wv"],
        "A_wo": _A_in["wo"],
        "B_x": _B_in["x"],
        "B_w_gate": _B_in["w_gate"],
        "B_w_up": _B_in["w_up"],
        "B_w_down": _B_in["w_down"],
    }

def reference(A_x, A_wq, A_wk, A_wv, A_wo, B_x, B_w_gate, B_w_up, B_w_down):
    def _A_ref(x, wq, wk, wv, wo):
        B, S, D = x.shape
        H = 16
        Dh = D // H
        q = (lambda x, *d: x.permute(*d))((x @ wq).reshape(B, S, H, Dh), 0, 2, 1, 3)
        k = x @ wk
        v = x @ wv
        scores = torch.einsum("bhqd,bkd->bhqk", q, k) * (Dh ** -0.5)
        p = _softmax(scores, axis=-1)
        o = torch.einsum("bhqk,bkd->bhqd", p, v)
        return (lambda x, *d: x.permute(*d))(o, 0, 2, 1, 3).reshape(B, S, H * Dh) @ wo

    def _B_ref(x, w_gate, w_up, w_down):
        g = torch.einsum("bsd,df->bsf", x, w_gate)
        u = torch.einsum("bsd,df->bsf", x, w_up)
        h = g * u
        return torch.einsum("bsf,fd->bsd", h, w_down)

    x = A_x
    x = x + _A_ref(_layernorm(x, axis=-1), A_wq, A_wk, A_wv, A_wo)
    x = x + _B_ref(_layernorm(x, axis=-1), B_w_gate, B_w_up, B_w_down)
    x = x + _A_ref(_layernorm(x, axis=-1), A_wq, A_wk, A_wv, A_wo)
    x = x + _B_ref(_layernorm(x, axis=-1), B_w_gate, B_w_up, B_w_down)
    return x
```

```cpp
#include <hip/hip_runtime.h>
#include <stdint.h>
#include <type_traits>

typedef float    f32x16 __attribute__((ext_vector_type(16)));
typedef float    f32x4  __attribute__((ext_vector_type(4)));
typedef float    f32x2  __attribute__((ext_vector_type(2)));
typedef unsigned u32x4  __attribute__((ext_vector_type(4)));
typedef unsigned u32x2  __attribute__((ext_vector_type(2)));
typedef __bf16   bf16x2 __attribute__((ext_vector_type(2)));
typedef __amdgpu_buffer_rsrc_t rsrc_t;
#define DI __device__ __forceinline__
#define GAS __attribute__((address_space(1)))
#define SB() __builtin_amdgcn_sched_barrier(0)

constexpr int S = 2048, D = 448, F = 1792;
constexpr unsigned KB = 1024;
constexpr unsigned OFF_FLAG = 0;
constexpr unsigned OFF_CSQ  = 64 * KB;
constexpr unsigned OFF_CSGU = OFF_CSQ + 4 * KB;
constexpr unsigned OFF_KMAX = OFF_CSGU + 16 * KB;
constexpr unsigned OFF_WQ   = 128 * KB;
constexpr unsigned OFF_WO   = OFF_WQ + 18 * 28 * KB;
constexpr unsigned OFF_WGU  = OFF_WO + 14 * 32 * KB;
constexpr unsigned OFF_WD   = OFF_WGU + 16 * 196 * KB;
constexpr unsigned SZ_X = 64 * 28 * KB, SZ_ST = 14 * 2048 * 8, SZ_O = 64 * 32 * KB, SZ_H = 64 * 112 * KB, SZ_KV = 64 * 4 * KB;
constexpr unsigned OFF_X    = OFF_WD + 14 * 112 * KB;
constexpr unsigned OFF_ST   = OFF_X + 4 * SZ_X;
constexpr unsigned OFF_O    = OFF_ST + 4 * SZ_ST;
constexpr unsigned OFF_H    = OFF_O + 2 * SZ_O;
constexpr unsigned OFF_KV   = OFF_H + 2 * SZ_H;
constexpr unsigned OFF_TS   = OFF_KV + 2 * SZ_KV;
constexpr unsigned WS_USED  = OFF_TS + 512 * KB;
DI constexpr unsigned FL_X(int L, int c, int th)  { return (0 + (L * 16 + c) * 2 + th) * 256u; }
DI constexpr unsigned FL_KV(int L, int rep)       { return (64 + L * 16 + rep) * 256u; }
DI constexpr unsigned FL_O(int L, int c, int th)  { return (96 + (L * 16 + c) * 2 + th) * 256u; }
DI constexpr unsigned FL_X1(int L, int c)         { return (160 + L * 16 + c) * 256u; }
DI constexpr unsigned FL_H(int L, int c)          { return (192 + L * 16 + c) * 256u; }

constexpr float QSCALE = 0.18898223650461362f * 1.4426950408889634f;
constexpr float M_LIMIT = 48.0f;

template <int I, int N, class Fn> DI void sfor(Fn&& f) { if constexpr (I < N) { f(std::integral_constant<int, I>{}); sfor<I + 1, N>(f); } }

DI unsigned cvtpk(float lo, float hi) { f32x2 v = {lo, hi}; bf16x2 b = __builtin_convertvector(v, bf16x2); return __builtin_bit_cast(unsigned, b); }
DI float bflo(unsigned u) { return __uint_as_float(u << 16); }
DI float bfhi(unsigned u) { return __uint_as_float(u & 0xffff0000u); }
DI float dot2(unsigned a, unsigned b, float c) { return __builtin_amdgcn_fdot2_f32_bf16(__builtin_bit_cast(bf16x2, a), __builtin_bit_cast(bf16x2, b), c, false); }
DI float sum32(float v) { auto r = __builtin_amdgcn_permlane32_swap(__float_as_uint(v), __float_as_uint(v), false, false); return __uint_as_float(r[0]) + __uint_as_float(r[1]); }
DI float sum16(float v) { auto r = __builtin_amdgcn_permlane16_swap(__float_as_uint(v), __float_as_uint(v), false, false); return __uint_as_float(r[0]) + __uint_as_float(r[1]); }
DI float max32(float v) { auto r = __builtin_amdgcn_permlane32_swap(__float_as_uint(v), __float_as_uint(v), false, false); return fmaxf(__uint_as_float(r[0]), __uint_as_float(r[1])); }

DI unsigned umax16(unsigned v) {
  v = max(v, (unsigned)__builtin_amdgcn_update_dpp(0, (int)v, 0x128, 0xf, 0xf, false));
  v = max(v, (unsigned)__builtin_amdgcn_update_dpp(0, (int)v, 0x124, 0xf, 0xf, false));
  v = max(v, (unsigned)__builtin_amdgcn_update_dpp(0, (int)v, 0x122, 0xf, 0xf, false));
  v = max(v, (unsigned)__builtin_amdgcn_update_dpp(0, (int)v, 0x121, 0xf, 0xf, false));
  return v;
}
DI unsigned umax64(unsigned v) {
  v = umax16(v);
  auto a = __builtin_amdgcn_permlane16_swap(v, v, false, false); v = max(a[0], a[1]);
  auto b = __builtin_amdgcn_permlane32_swap(v, v, false, false); return max(b[0], b[1]);
}

struct PrepArgs { const uint16_t *x, *wq, *wk, *wv, *wo, *wg, *wu, *wd; char* ws; };

DI void tr8(const u32x4 (&r)[8], u32x4 (&p)[8]) {
#pragma unroll
  for (int c = 0; c < 8; ++c)
#pragma unroll
    for (int d = 0; d < 4; ++d)
      p[c][d] = __builtin_amdgcn_perm(r[2 * d + 1][c >> 1], r[2 * d][c >> 1], (c & 1) ? 0x07060302u : 0x05040100u);
}
DI float piece_sum(const u32x4& p) {
  float s = 0.f;
#pragma unroll
  for (int d = 0; d < 4; ++d) s += bflo(p[d]) + bfhi(p[d]);
  return s;
}

__global__ __launch_bounds__(256) void k_prep(PrepArgs a) {
  __shared__ float red[56 * 32 + 64];
  const int t = threadIdx.x;
  int task = blockIdx.x;
  char* ws = a.ws;
  if (task < 64) {
    const int tt = task, w = t >> 6, l = t & 63, r = l & 31, h = l >> 5;
    const uint16_t* row = a.x + (size_t)(tt * 32 + r) * D + 8 * h;
    u32x4 v[7];
#pragma unroll
    for (int i = 0; i < 7; ++i) v[i] = *(const u32x4*)(row + 16 * (7 * w + i));
    float s = 0.f, q = 0.f;
#pragma unroll
    for (int i = 0; i < 7; ++i) {
#pragma unroll
      for (int d = 0; d < 4; ++d) { s = dot2(v[i][d], 0x3f803f80u, s); q = dot2(v[i][d], v[i][d], q); }
      auto s0 = __builtin_amdgcn_permlane32_swap(v[i][0], v[i][2], false, false);
      auto s1 = __builtin_amdgcn_permlane32_swap(v[i][1], v[i][3], false, false);
      u32x4 o = {s0[0], s1[0], s0[1], s1[1]};
      *(u32x4*)(ws + OFF_X + (size_t)(tt * 28 + 7 * w + i) * KB + l * 16) = o;
    }
    s = sum32(s); q = sum32(q);
    if (h == 0) { red[(w * 32 + r) * 2] = s; red[(w * 32 + r) * 2 + 1] = q; }
    __syncthreads();
    for (int i = t; i < 14 * 32; i += 256) {
      const int slot = i >> 5, rr = i & 31;
      f32x2 o = {0.f, 0.f};
      if (slot < 4) { o[0] = red[(slot * 32 + rr) * 2]; o[1] = red[(slot * 32 + rr) * 2 + 1]; }
      *(f32x2*)(ws + OFF_ST + (size_t)(slot * 2048 + tt * 32 + rr) * 8) = o;
    }
    return;
  }
  task -= 64;
  if (task < 112) {
    const int mat = task / 56, q = task % 56, cb = ((q >> 4) << 4) + ((q & 7) << 1) + ((q >> 3) & 1);
    const int cbb = (q < 48) ? cb : (48 + ((q - 48) & 3) * 2 + ((q - 48) >> 2));
    const uint16_t* W = mat ? a.wu : a.wg;
    const int ng = t & 3, kg = t >> 2, ks = kg >> 1, h = kg & 1;
    if (t < 224) {
      u32x4 r[8], p[8];
#pragma unroll
      for (int e = 0; e < 8; ++e) r[e] = *(const u32x4*)(W + (size_t)(16 * ks + 4 * h + (e & 3) + 8 * (e >> 2)) * F + 32 * cbb + 8 * ng);
      tr8(r, p);
      const int gt = 2 * cbb + (ng >> 1), m = gt / 7, j = gt % 7, lt = (m & 1) ? j : (j == 6 ? 0 : j + 1);
      const int KS = ks >> 1, g = 2 * (ks & 1) + h;
      const int fi = lt < 3 ? KS * 6 + lt * 2 + mat : 84 + KS * 8 + (lt - 3) * 2 + mat;
      char* dst = ws + OFF_WGU + (size_t)(m * 196 + fi) * KB + (g * 16 + (ng & 1) * 8) * 16;
#pragma unroll
      for (int c = 0; c < 8; ++c) { *(u32x4*)(dst + c * 16) = p[c]; red[kg * 32 + ng * 8 + c] = piece_sum(p[c]); }
    }
    __syncthreads();
    if (t < 32) {
      float s = 0.f;
      for (int i = 0; i < 56; ++i) s += red[i * 32 + t];
      const int gt = 2 * cbb + (t >> 4), m = gt / 7, j = gt % 7, lt = (m & 1) ? j : (j == 6 ? 0 : j + 1);
      ((float*)(ws + OFF_CSGU))[((m * 7 + lt) * 2 + mat) * 16 + (t & 15)] = s;
    }
    return;
  }
  task -= 112;
  if (task < 56) {
    const int nt = task % 14, kq = task / 14;
    const int ng = t & 3, kg = 56 * kq + (t >> 2), ks = kg >> 1, h = kg & 1;
    if (t < 224) {
      u32x4 r[8], p[8];
#pragma unroll
      for (int e = 0; e < 8; ++e) r[e] = *(const u32x4*)(a.wd + (size_t)(32 * (ks >> 1) + 4 * (2 * (ks & 1) + h) + (e & 3) + 16 * (e >> 2)) * D + 32 * nt + 8 * ng);
      tr8(r, p);
      char* dst = ws + OFF_WD + (size_t)(nt * 112 + ks) * KB + (h * 32 + ng * 8) * 16;
#pragma unroll
      for (int c = 0; c < 8; ++c) *(u32x4*)(dst + c * 16) = p[c];
    }
    return;
  }
  task -= 56;
  if (task < 14) {
    const int nt = task, ng = t & 3, kg = t >> 2, ks = kg >> 1, h = kg & 1, j = ks >> 1, s = ks & 1;
    u32x4 r[8], p[8];
#pragma unroll
    for (int e = 0; e < 8; ++e) {
      const int dh = 16 * s + 4 * h + (e & 3) + 8 * (e >> 2);
      r[e] = u32x4{0, 0, 0, 0};
      if (dh < 28) r[e] = *(const u32x4*)(a.wo + (size_t)(28 * j + dh) * D + 32 * nt + 8 * ng);
    }
    tr8(r, p);
    char* dst = ws + OFF_WO + (size_t)(nt * 32 + ks) * KB + (h * 32 + ng * 8) * 16;
#pragma unroll
    for (int c = 0; c < 8; ++c) *(u32x4*)(dst + c * 16) = p[c];
    return;
  }
  task -= 14;
  if (task < 18) {
    const uint16_t* W = task < 16 ? a.wq + 28 * task : (task == 16 ? a.wk : a.wv);
    const int ld = task < 16 ? D : 28;
    char* base = ws + OFF_WQ + (size_t)task * 28 * KB;
    if (t < 196) {
      const int ng = t % 7;
      u32x2 r[2][8];
#pragma unroll
      for (int it = 0; it < 2; ++it) {
        const int kg = t / 7 + 28 * it, ks = kg >> 1, h = kg & 1;
#pragma unroll
        for (int e = 0; e < 8; ++e) r[it][e] = *(const u32x2*)(W + (size_t)(16 * ks + 4 * h + (e & 3) + 8 * (e >> 2)) * ld + 4 * ng);
      }
#pragma unroll
      for (int it = 0; it < 2; ++it) {
        const int kg = t / 7 + 28 * it, ks = kg >> 1, h = kg & 1;
#pragma unroll
        for (int c = 0; c < 4; ++c) {
          u32x4 p;
#pragma unroll
          for (int d = 0; d < 4; ++d) p[d] = __builtin_amdgcn_perm(r[it][2 * d + 1][c >> 1], r[it][2 * d][c >> 1], (c & 1) ? 0x07060302u : 0x05040100u);
          *(u32x4*)(base + ks * KB + (h * 32 + ng * 4 + c) * 16) = p;
          red[kg * 32 + ng * 4 + c] = piece_sum(p);
        }
      }
    } else if (t < 252) {
      const int kg = t - 196, ks = kg >> 1, h = kg & 1;
#pragma unroll
      for (int c = 0; c < 4; ++c) *(u32x4*)(base + ks * KB + (h * 32 + 28 + c) * 16) = u32x4{0, 0, 0, 0};
    }
    __syncthreads();
    if (t < 32) {
      float s = 0.f;
      if (t < 28) for (int i = 0; i < 56; ++i) s += red[i * 32 + t];
      ((float*)(ws + OFF_CSQ))[task * 32 + t] = s;
    }
    return;
  }
  for (int i = t; i < 64 * KB / 16; i += 256) ((u32x4*)(ws + OFF_FLAG))[i] = u32x4{0, 0, 0, 0};
  ((unsigned*)(ws + OFF_KMAX))[t] = 0xffffffffu;
}
constexpr int PREP_TASKS = 64 + 112 + 56 + 14 + 18 + 1;

#define ACLOB "a0","a1","a2","a3","a4","a5","a6","a7","a8","a9","a10","a11","a12","a13","a14","a15","a16","a17","a18","a19","a20","a21","a22","a23","a24","a25","a26","a27","a28","a29","a30","a31","a32","a33","a34","a35","a36","a37","a38","a39","a40","a41","a42","a43","a44","a45","a46","a47","a48","a49","a50","a51","a52","a53","a54","a55","a56","a57","a58","a59","a60","a61","a62","a63","a64","a65","a66","a67","a68","a69","a70","a71","a72","a73","a74","a75","a76","a77","a78","a79","a80","a81","a82","a83","a84","a85","a86","a87","a88","a89","a90","a91","a92","a93","a94","a95","a96","a97","a98","a99","a100","a101","a102","a103","a104","a105","a106","a107","a108","a109","a110","a111","a112","a113","a114","a115","a116","a117","a118","a119","a120","a121","a122","a123","a124","a125","a126","a127","a128","a129","a130","a131","a132","a133","a134","a135","a136","a137","a138","a139","a140","a141","a142","a143","a144","a145","a146","a147","a148","a149","a150","a151","a152","a153","a154","a155","a156","a157","a158","a159","a160","a161","a162","a163","a164","a165","a166","a167","a168","a169","a170","a171","a172","a173","a174","a175","a176","a177","a178","a179","a180","a181","a182","a183","a184","a185","a186","a187","a188","a189","a190","a191","a192","a193","a194","a195","a196","a197","a198","a199","a200","a201","a202","a203","a204","a205","a206","a207","a208","a209","a210","a211","a212","a213","a214","a215","a216","a217","a218","a219","a220","a221","a222","a223","a224","a225","a226","a227","a228","a229","a230","a231","a232","a233","a234","a235","a236","a237","a238","a239","a240","a241","a242","a243","a244","a245","a246","a247","a248","a249","a250","a251","a252","a253","a254","a255"

template <int IMM> DI void ld4(u32x4& d, unsigned vo, rsrc_t rs, unsigned so) {
  asm volatile("buffer_load_dwordx4 %0, %1, %2, %3 offen offset:%c4 sc1" : "=v"(d) : "v"(vo), "s"(rs), "s"(so), "i"(IMM));
}
template <int IMM> DI void ld2(f32x2& d, unsigned vo, rsrc_t rs, unsigned so) {
  asm volatile("buffer_load_dwordx2 %0, %1, %2, %3 offen offset:%c4 sc1" : "=v"(d) : "v"(vo), "s"(rs), "s"(so), "i"(IMM));
}
template <int IMM> DI void ld4f(f32x4& d, unsigned vo, rsrc_t rs, unsigned so) {
  asm volatile("buffer_load_dwordx4 %0, %1, %2, %3 offen offset:%c4" : "=v"(d) : "v"(vo), "s"(rs), "s"(so), "i"(IMM));
}
DI void ld1(float& d, unsigned vo, rsrc_t rs, unsigned so) {
  asm volatile("buffer_load_dword %0, %1, %2, %3 offen sc1" : "=v"(d) : "v"(vo), "s"(rs), "s"(so));
}
template <int AR, int IMM> DI void lda(unsigned vo, rsrc_t rs, unsigned so) {
  asm volatile("buffer_load_dwordx4 a[%c3:%c4], %0, %1, %2 offen offset:%c5" :: "v"(vo), "s"(rs), "s"(so), "i"(AR), "i"(AR + 3), "i"(IMM) : ACLOB);
}
template <int N> DI void ldl(unsigned vo, rsrc_t rs, unsigned so, unsigned lds) {
  asm volatile("s_mov_b32 m0, %3\n\ts_nop 0\n\tbuffer_load_dwordx4 %0, %1, %2 offen lds" :: "v"(vo), "s"(rs), "s"(so), "s"(lds) : "memory");
  if constexpr (N > 1) asm volatile("buffer_load_dwordx4 %0, %1, %2 offen offset:1024 lds" :: "v"(vo), "s"(rs), "s"(so) : "memory");
  if constexpr (N > 2) asm volatile("buffer_load_dwordx4 %0, %1, %2 offen offset:2048 lds" :: "v"(vo), "s"(rs), "s"(so) : "memory");
  if constexpr (N > 3) asm volatile("buffer_load_dwordx4 %0, %1, %2 offen offset:3072 lds" :: "v"(vo), "s"(rs), "s"(so) : "memory");
}
template <int IMM> DI void st4(const u32x4& d, unsigned vo, rsrc_t rs, unsigned so) {
  asm volatile("buffer_store_dwordx4 %0, %1, %2, %3 offen offset:%c4 sc1\n\ts_nop 1" :: "v"(d), "v"(vo), "s"(rs), "s"(so), "i"(IMM) : "memory");
}
template <int IMM> DI void st4p(const u32x4& d, unsigned vo, rsrc_t rs, unsigned so) {
  asm volatile("buffer_store_dwordx4 %0, %1, %2, %3 offen offset:%c4\n\ts_nop 1" :: "v"(d), "v"(vo), "s"(rs), "s"(so), "i"(IMM) : "memory");
}
template <int IMM> DI void st2(const u32x2& d, unsigned vo, rsrc_t rs, unsigned so) {
  asm volatile("buffer_store_dwordx2 %0, %1, %2, %3 offen offset:%c4 sc1" :: "v"(d), "v"(vo), "s"(rs), "s"(so), "i"(IMM) : "memory");
}
DI void st1(float d, unsigned vo, rsrc_t rs, unsigned so) {
  asm volatile("buffer_store_dword %0, %1, %2, %3 offen sc1" :: "v"(d), "v"(vo), "s"(rs), "s"(so) : "memory");
}
template <int N> DI void wv() { asm volatile("s_waitcnt vmcnt(%c0)" :: "i"(N) : "memory"); SB(); }
template <int N> DI void wl() { asm volatile("s_waitcnt lgkmcnt(%c0)" :: "i"(N) : "memory"); SB(); }
template <class T> DI void pin(T& x) { asm volatile("" : "+v"(x)); }
DI void bar() { asm volatile("s_waitcnt lgkmcnt(0)\n\ts_barrier" ::: "memory"); }
template <int N> DI void nop() { asm volatile("s_nop %c0" :: "i"(N)); SB(); }

template <int OFF> DI void dsr(u32x4& r, unsigned a) { asm volatile("ds_read_b128 %0, %1 offset:%c2" : "=v"(r) : "v"(a), "i"(OFF)); }

DI void m32(f32x16& c, const u32x4& a, const u32x4& b)  { asm volatile("v_mfma_f32_32x32x16_bf16 %0, %1, %2, %0" : "+v"(c) : "v"(a), "v"(b)); }
DI void m32z(f32x16& c, const u32x4& a, const u32x4& b) { asm volatile("v_mfma_f32_32x32x16_bf16 %0, %1, %2, 0" : "=&v"(c) : "v"(a), "v"(b)); }
template <int AR, bool Z> DI void m32a(f32x16& c, const u32x4& b) {
  if constexpr (Z) asm volatile("v_mfma_f32_32x32x16_bf16 %0, a[%c2:%c3], %1, 0" : "=&v"(c) : "v"(b), "i"(AR), "i"(AR + 3));
  else             asm volatile("v_mfma_f32_32x32x16_bf16 %0, a[%c2:%c3], %1, %0" : "+v"(c) : "v"(b), "i"(AR), "i"(AR + 3));
}
template <int AR, bool Z> DI void m32b(f32x16& c, const u32x4& a) {
  if constexpr (Z) asm volatile("v_mfma_f32_32x32x16_bf16 %0, %1, a[%c2:%c3], 0" : "=&v"(c) : "v"(a), "i"(AR), "i"(AR + 3));
  else             asm volatile("v_mfma_f32_32x32x16_bf16 %0, %1, a[%c2:%c3], %0" : "+v"(c) : "v"(a), "i"(AR), "i"(AR + 3));
}
template <bool Z> DI void m16(f32x4& c, const u32x4& a, const u32x4& b) {
  if constexpr (Z) asm volatile("v_mfma_f32_16x16x32_bf16 %0, %1, %2, 0" : "=&v"(c) : "v"(a), "v"(b));
  else             asm volatile("v_mfma_f32_16x16x32_bf16 %0, %1, %2, %0" : "+v"(c) : "v"(a), "v"(b));
}

struct Ctx {
  rsrc_t rs, ro;
  char* ws;
  unsigned vl16;
  unsigned vg16;
  int w, l, c, m;
};
DI void fresh(Ctx& x) {
  asm volatile("" : "+s"(x.w), "+s"(x.c), "+s"(x.m));
  asm volatile("" : "+v"(x.l));
  x.vl16 = x.l * 16;
  const int g = x.l >> 4;
  x.vg16 = ((g >> 1) * 64 + (g & 1) * 32 + (x.l & 15)) * 16;
}
extern __shared__ __attribute__((aligned(16))) char lds[];
constexpr unsigned SCR = 112 * KB;

DI void stamp(Ctx&) {}
DI void stamp_flush(Ctx&) {}

DI bool wave0(const Ctx& x) { int w = x.w; asm volatile("" : "+s"(w)); return w == 0; }
DI void signal(Ctx& x, unsigned fl, int n) {
  bar();
  if (wave0(x) && x.l < n) __hip_atomic_fetch_add((GAS unsigned*)(x.ws + OFF_FLAG + fl + x.l * 256), 1u, __ATOMIC_RELAXED, __HIP_MEMORY_SCOPE_AGENT);
}
DI void await(Ctx& x, unsigned fl, unsigned target) {
  if (wave0(x)) {
    for (unsigned spin = 0; spin < (1u << 18); ++spin) {
      unsigned v;
      asm volatile("buffer_load_dword %0, off, %1, %2 sc1\n\ts_waitcnt vmcnt(0)" : "=v"(v) : "s"(x.rs), "s"(OFF_FLAG + fl) : "memory");
      if ((unsigned)__builtin_amdgcn_readfirstlane(v) >= target) break;
      __builtin_amdgcn_s_sleep(1);
    }
  }
  bar();
}

DI void reduce4(Ctx& x, f32x16 (&acc)[4]) {
  sfor<1, 4>([&](auto k) {
    f32x4* p = (f32x4*)(lds + SCR + ((x.w ^ k) * 3 + k - 1) * 4096 + x.vl16);
#pragma unroll
    for (int q = 0; q < 4; ++q) p[q * 64] = f32x4{acc[k][4 * q], acc[k][4 * q + 1], acc[k][4 * q + 2], acc[k][4 * q + 3]};
  });
  bar();
  const f32x4* p = (const f32x4*)(lds + SCR + x.w * 3 * 4096 + x.vl16);
#pragma unroll
  for (int k = 0; k < 3; ++k)
#pragma unroll
    for (int q = 0; q < 4; ++q) {
      f32x4 v = p[k * 256 + q * 64];
#pragma unroll
      for (int i = 0; i < 4; ++i) acc[0][4 * q + i] += v[i];
    }
}

constexpr int A_WQ = 0, A_WO = 168, A_WD = 0;
DI void pf_wq(Ctx& x) {
  const int hp = x.m & 7, kh = x.w >> 1;
  const unsigned s0 = OFF_WQ + ((2 * hp + kh) * 28 + 14 * kh) * KB, s1 = OFF_WQ + ((2 * hp + (kh ^ 1)) * 28 + 14 * kh) * KB;
  const unsigned s2 = OFF_WQ + ((16 + (hp & 1)) * 28 + 14 * kh) * KB;
  sfor<0, 14>([&](auto i) {
    lda<A_WQ + 4 * i, (i & 3) * 1024>(x.vl16, x.rs, s0 + (i >> 2) * 4096);
    lda<A_WQ + 56 + 4 * i, (i & 3) * 1024>(x.vl16, x.rs, s1 + (i >> 2) * 4096);
    lda<A_WQ + 112 + 4 * i, (i & 3) * 1024>(x.vl16, x.rs, s2 + (i >> 2) * 4096);
  });
}
DI void pf_wo(Ctx& x) {
  const int ct = x.m < 14 ? x.m % 7 : 0;
  const unsigned s0 = OFF_WO + ((2 * ct + (x.w & 1)) * 32 + 8 * x.w) * KB, s1 = OFF_WO + ((2 * ct + ((x.w & 1) ^ 1)) * 32 + 8 * x.w) * KB;
  sfor<0, 8>([&](auto i) {
    lda<A_WO + 8 * i, (i & 3) * 1024>(x.vl16, x.rs, s0 + (i >> 2) * 4096);
    lda<A_WO + 8 * i + 4, (i & 3) * 1024>(x.vl16, x.rs, s1 + (i >> 2) * 4096);
  });
}
template <int I0, int I1> DI void pf_wd(Ctx& x) {
  const int ct = x.m < 14 ? x.m % 7 : 0;
  const unsigned s0 = OFF_WD + ((2 * ct + (x.w & 1)) * 112 + 28 * x.w) * KB, s1 = OFF_WD + ((2 * ct + ((x.w & 1) ^ 1)) * 112 + 28 * x.w) * KB;
  sfor<I0, I1>([&](auto i) {
    lda<A_WD + 8 * i, (i & 3) * 1024>(x.vl16, x.rs, s0 + (i >> 2) * 4096);
    lda<A_WD + 8 * i + 4, (i & 3) * 1024>(x.vl16, x.rs, s1 + (i >> 2) * 4096);
  });
}
DI void pf_wgu1(Ctx& x) {
  for (int t = 0; t < 14; ++t) ldl<2>(x.vl16, x.rs, OFF_WGU + (x.m * 196 + 8 * t + 2 * x.w) * KB, (8 * t + 2 * x.w) * KB);
}
DI void pf_wgu2(Ctx& x) {
  sfor<0, 3>([&](auto j) { ldl<4>(x.vl16, x.rs, OFF_WGU + (x.m * 196 + 112 + 12 * x.w + 4 * j) * KB, (112 + 12 * x.w + 4 * j) * KB); });
}
DI void pf_wgu3(Ctx& x) {
  sfor<0, 3>([&](auto j) { ldl<3>(x.vl16, x.rs, OFF_WGU + (x.m * 196 + 160 + 9 * x.w + 3 * j) * KB, (9 * x.w + 3 * j) * KB); });
}

DI void row_stats(float s, float q, float& mean, float& rstd) {
  mean = s * (1.0f / D);
  const float var = fmaxf(q * (1.0f / D) - mean * mean, 0.f);
  rstd = __builtin_amdgcn_rsqf(var + 1e-5f);
}

DI void phase_att(Ctx& x, const int L) {
  const int w = x.w, l = x.l, h = l >> 5, r = l & 31;
  const int th = x.m >> 3, hp = x.m & 7, tl = w & 1, kh = w >> 1, tt = 4 * x.c + 2 * th + tl;
  const unsigned X = OFF_X + 2 * L * SZ_X, ST = OFF_ST + 2 * L * SZ_ST, KV = OFF_KV + L * SZ_KV;
  f32x4 cq[4], ck[2]; float cv;
  {
    const unsigned so = OFF_CSQ + (2 * hp + kh) * 128;
    sfor<0, 4>([&](auto a) { ld4f<32 * a>(cq[a], h * 16, x.rs, so); });
    sfor<0, 2>([&](auto a) { ld4f<32 * a>(ck[a], h * 16, x.rs, OFF_CSQ + 16 * 128 + kh * 64); });
    ld1(cv, r * 4, x.rs, OFF_CSQ + 17 * 128);
  }
  if (L > 0) await(x, FL_X(L, x.c, th), 7);
  stamp(x);
  u32x4 xf[14]; f32x2 sp[7];
  {
    const unsigned so = X + (tt * 28 + 14 * kh) * KB;
    sfor<0, 14>([&](auto i) { ld4<(i & 3) * 1024>(xf[i], x.vl16, x.rs, so + (i >> 2) * 4096); });
    sfor<0, 7>([&](auto i) { ld2<0>(sp[i], (h * 7 * 2048 + r) * 8, x.rs, ST + (i * 2048 + tt * 32) * 8); });
  }
  f32x16 qa, qb, kv;
  if (hp == 0) {
    sfor<0, 14>([&](auto i) { wv<20 - i>(); m32a<A_WQ + 112 + 4 * i, i == 0>(kv, xf[i]); m32a<A_WQ + 4 * i, i == 0>(qa, xf[i]); m32a<A_WQ + 56 + 4 * i, i == 0>(qb, xf[i]); });
  } else if (hp == 1) {
    sfor<0, 14>([&](auto i) { wv<20 - i>(); m32b<A_WQ + 112 + 4 * i, i == 0>(kv, xf[i]); m32a<A_WQ + 4 * i, i == 0>(qa, xf[i]); m32a<A_WQ + 56 + 4 * i, i == 0>(qb, xf[i]); });
  } else {
    sfor<0, 14>([&](auto i) { wv<20 - i>(); m32a<A_WQ + 4 * i, i == 0>(qa, xf[i]); m32a<A_WQ + 56 + 4 * i, i == 0>(qb, xf[i]); });
#pragma unroll
    for (int i = 0; i < 16; ++i) kv[i] = 0.f;
  }
  wv<0>();
  sfor<0, 7>([&](auto i) { pin(sp[i]); });
  sfor<0, 4>([&](auto i) { pin(cq[i]); });
  pin(ck[0]); pin(ck[1]); pin(cv);
  float mean, rstd;
  {
    float s = 0.f, q = 0.f;
#pragma unroll
    for (int i = 0; i < 7; ++i) { s += sp[i][0]; q += sp[i][1]; }
    row_stats(sum32(s), sum32(q), mean, rstd);
  }
  nop<11>();
  pin(qa); pin(qb); pin(kv);
  {
    f32x4* p = (f32x4*)(lds + SCR + (w ^ 2) * 4096 + x.vl16);
#pragma unroll
    for (int q = 0; q < 4; ++q) p[q * 64] = f32x4{qb[4 * q], qb[4 * q + 1], qb[4 * q + 2], qb[4 * q + 3]};
    if (hp < 2) {
      f32x4* pk = (f32x4*)(lds + SCR + 16384 + (w ^ 2) * 2048 + x.vl16);
#pragma unroll
      for (int q = 0; q < 2; ++q) {
        f32x4 v;
#pragma unroll
        for (int i = 0; i < 4; ++i) v[i] = kh ? kv[4 * q + i] : kv[8 + 4 * q + i];
        pk[q * 64] = v;
      }
      if (h == 0) ((f32x2*)(lds + SCR + 32768))[w * 32 + r] = f32x2{mean, rstd};
    }
  }
  bar();
  if (hp < 2) {
    const f32x4* pk = (const f32x4*)(lds + SCR + 16384 + w * 2048 + x.vl16);
    float v[8];
#pragma unroll
    for (int q = 0; q < 2; ++q) {
      const f32x4 o = pk[q * 64];
#pragma unroll
      for (int i = 0; i < 4; ++i) v[4 * q + i] = o[i] + (kh ? kv[8 + 4 * q + i] : kv[4 * q + i]);
    }
    u32x4 pkd;
    if (hp == 0) {
#pragma unroll
      for (int i = 0; i < 8; ++i) v[i] = (v[i] - mean * ck[i >> 2][i & 3]) * rstd;
#pragma unroll
      for (int d = 0; d < 4; ++d) pkd[d] = cvtpk(v[2 * d], v[2 * d + 1]);
      if (kh && h) { pkd[2] = 0; pkd[3] = 0; }
      float n2 = 0.f;
#pragma unroll
      for (int d = 0; d < 4; ++d) n2 = dot2(pkd[d], pkd[d], n2);
      n2 = sum32(n2);
      const unsigned nb = umax64(n2 < 3.0e38f ? __float_as_uint(n2) : 0x7f800000u);
      if (kh && h) pkd[2] = 0x3f80u;
      if (l == 0) st1(__uint_as_float(nb), 0, x.rs, OFF_KMAX + (L * 128 + kh * 64 + tt) * 4);
    } else {
      const f32x4* ms = (const f32x4*)(lds + SCR + 32768 + (w * 32 + 16 * kh + 4 * h) * 8);
#pragma unroll
      for (int q = 0; q < 2; ++q) {
        const f32x4 a = ms[q * 4], b = ms[q * 4 + 1];
        v[4 * q + 0] = (v[4 * q + 0] - a[0] * cv) * a[1]; v[4 * q + 1] = (v[4 * q + 1] - a[2] * cv) * a[3];
        v[4 * q + 2] = (v[4 * q + 2] - b[0] * cv) * b[1]; v[4 * q + 3] = (v[4 * q + 3] - b[2] * cv) * b[3];
      }
#pragma unroll
      for (int d = 0; d < 4; ++d) pkd[d] = r < 28 ? cvtpk(v[2 * d], v[2 * d + 1]) : (r == 28 ? 0x3f803f80u : 0u);
    }
    st4<0>(pkd, x.vl16, x.rs, KV + (tt * 4 + hp * 2 + kh) * KB);
    wv<0>();
    signal(x, FL_KV(L, 0), 16);
  }
  u32x4 qf[2]; float qn2;
  {
    const f32x4* p = (const f32x4*)(lds + SCR + w * 4096 + x.vl16);
    const float sc = rstd * QSCALE;
#pragma unroll
    for (int q = 0; q < 4; ++q) {
      const f32x4 o = p[q * 64];
      float v[4];
#pragma unroll
      for (int i = 0; i < 4; ++i) v[i] = (qa[4 * q + i] + o[i] - mean * cq[q][i]) * sc;
      qf[q >> 1][2 * (q & 1)] = cvtpk(v[0], v[1]); qf[q >> 1][2 * (q & 1) + 1] = cvtpk(v[2], v[3]);
    }
    if (h) { qf[1][2] = 0; qf[1][3] = 0; }
    qn2 = 0.f;
#pragma unroll
    for (int d = 0; d < 8; ++d) qn2 = dot2(qf[d >> 2][d & 3], qf[d >> 2][d & 3], qn2);
    qn2 = sum32(qn2);
  }
  if (h) qf[1][2] = cvtpk(1.01f * __builtin_amdgcn_sqrtf(qn2), 0.f);
  ((u32x4*)(lds + SCR + 24576 + w * 2048 + x.vl16))[0] = qf[0];
  ((u32x4*)(lds + SCR + 24576 + w * 2048 + x.vl16))[64] = qf[1];
  stamp(x);
  if (wave0(x)) {
    unsigned k0 = 0x7f800000u, k1 = 0;
    for (unsigned spin = 0; spin < (1u << 18); ++spin) {
      unsigned v;
      asm volatile("buffer_load_dword %0, off, %3, %4 sc1\n\tbuffer_load_dword %1, %5, %3, %6 offen sc1\n\tbuffer_load_dword %2, %5, %3, %6 offen offset:256 sc1\n\ts_waitcnt vmcnt(0)"
                   : "=&v"(v), "=&v"(k0), "=&v"(k1) : "s"(x.rs), "s"(OFF_FLAG + FL_KV(L, x.c)), "v"(l * 4), "s"(OFF_KMAX + L * 512) : "memory");
      if ((unsigned)__builtin_amdgcn_readfirstlane(v) >= 64u && __all(k0 != 0xffffffffu && k1 != 0xffffffffu)) break;
      __builtin_amdgcn_s_sleep(1);
    }
    const float km = __builtin_amdgcn_sqrtf(__uint_as_float(umax64(k0)) + __uint_as_float(umax64(k1)));
    if (l == 0) *(float*)(lds + SCR + 36864) = km;
  }
  bar();
  stamp(x);
  const unsigned kvs = KV + 16 * w * 4096;
  f32x16 kf0, kf1;
  asm volatile("buffer_load_dwordx4 v[128:131], %2, %3, %4 offen sc1\n\tbuffer_load_dwordx4 v[132:135], %2, %3, %4 offen offset:1024 sc1\n\t"
               "buffer_load_dwordx4 v[136:139], %2, %3, %4 offen offset:2048 sc1\n\tbuffer_load_dwordx4 v[140:143], %2, %3, %4 offen offset:3072 sc1\n\t"
               "buffer_load_dwordx4 v[144:147], %2, %3, %5 offen sc1\n\tbuffer_load_dwordx4 v[148:151], %2, %3, %5 offen offset:1024 sc1\n\t"
               "buffer_load_dwordx4 v[152:155], %2, %3, %5 offen offset:2048 sc1\n\tbuffer_load_dwordx4 v[156:159], %2, %3, %5 offen offset:3072 sc1"
               : "={v[128:143]}"(kf0), "={v[144:159]}"(kf1) : "v"(x.vl16), "s"(x.rs), "s"(kvs), "s"(kvs + 4096));
  pf_wo(x);
  u32x4 q[4][2];
#pragma unroll
  for (int k = 0; k < 4; ++k) {
    q[k][0] = ((const u32x4*)(lds + SCR + 24576 + (w ^ k) * 2048 + x.vl16))[0];
    q[k][1] = ((const u32x4*)(lds + SCR + 24576 + (w ^ k) * 2048 + x.vl16))[64];
  }
  bool safe = true;
  {
    const float km = 1.01f * *(const float*)(lds + SCR + 36864);
#pragma unroll
    for (int k = 0; k < 4; ++k) {
      const float mrow = bflo(q[k][1][2]) * km + 0.01f;
      safe = safe && (!h || mrow <= M_LIMIT);
      if (h) q[k][1][2] = cvtpk(-mrow, 0.f);
    }
  }
  const bool fast = __all(safe);
  bar();
  stamp(x);
  f32x16 o[4];
  if (fast) {
    unsigned so_kv = kvs + 2 * 4096, so_w = OFF_WGU + (x.m * 196 + 2 * w) * KB, so_l = 2 * w * KB, cnt = 4;
    asm volatile(
      ".macro GRP s, pa, pb\n\t"
      "v_exp_f32 v200, v[\\s]\n\tv_exp_f32 v201, v[\\s+1]\n\tv_cvt_pk_bf16_f32 v[\\pa], v202, v203\n\t"
      "v_exp_f32 v202, v[\\s+2]\n\tv_exp_f32 v203, v[\\s+3]\n\tv_cvt_pk_bf16_f32 v[\\pb], v200, v201\n\t"
      ".endm\n\t"
      ".macro STEP sc, sn, k, nk, pk, tk, nt, pt\n\t"
      "v_mfma_f32_32x32x16_bf16 v[\\sn:\\sn+15], v[128+16*\\nt:128+16*\\nt+3], v[64+8*\\nk:64+8*\\nk+3], 0\n\t"
      "GRP \\sc, 199, 192\n\t"
      "v_mfma_f32_32x32x16_bf16 v[\\sn:\\sn+15], v[132+16*\\nt:132+16*\\nt+3], v[68+8*\\nk:68+8*\\nk+3], v[\\sn:\\sn+15]\n\t"
      "GRP \\sc+4, 193, 194\n\t"
      "v_mfma_f32_32x32x16_bf16 v[16*\\pk:16*\\pk+15], v[140+16*\\pt:140+16*\\pt+3], v[196:199], v[16*\\pk:16*\\pk+15]\n\t"
      "GRP \\sc+8, 195, 196\n\t"
      "v_mfma_f32_32x32x16_bf16 v[16*\\k:16*\\k+15], v[136+16*\\tk:136+16*\\tk+3], v[192:195], v[16*\\k:16*\\k+15]\n\t"
      "GRP \\sc+12, 197, 198\n\t"
      ".endm\n\t"
      ".macro TILE tk, nx, pv, ld\n\t"
      "buffer_load_dwordx4 v[128+16*\\ld:128+16*\\ld+3], %[vo], %[rs], %[skv] offen sc1\n\t"
      "buffer_load_dwordx4 v[132+16*\\ld:132+16*\\ld+3], %[vo], %[rs], %[skv] offen offset:1024 sc1\n\t"
      "buffer_load_dwordx4 v[136+16*\\ld:136+16*\\ld+3], %[vo], %[rs], %[skv] offen offset:2048 sc1\n\t"
      "buffer_load_dwordx4 v[140+16*\\ld:140+16*\\ld+3], %[vo], %[rs], %[skv] offen offset:3072 sc1\n\t"
      "s_mov_b32 m0, %[sl]\n\t"
      "s_add_u32 %[skv], %[skv], 4096\n\t"
      "s_min_u32 %[skv], %[skv], %[kvl]\n\t"
      "buffer_load_dwordx4 %[vo], %[rs], %[sw] offen lds\n\t"
      "buffer_load_dwordx4 %[vo], %[rs], %[sw] offen offset:1024 lds\n\t"
      "s_add_u32 %[sw], %[sw], 8192\n\t"
      "s_min_u32 %[sw], %[sw], %[wl]\n\t"
      "s_add_u32 %[sl], %[sl], 8192\n\t"
      "s_min_u32 %[sl], %[sl], %[ll]\n\t"
      "STEP 96, 112, 0, 1, 3, \\tk, \\tk, \\pv\n\t"
      "STEP 112, 96, 1, 2, 0, \\tk, \\tk, \\tk\n\t"
      "STEP 96, 112, 2, 3, 1, \\tk, \\tk, \\tk\n\t"
      "s_waitcnt vmcnt(8)\n\t"
      "STEP 112, 96, 3, 0, 2, \\tk, \\nx, \\tk\n\t"
      ".endm\n\t"
      "v_mov_b32 v196, 0\n\tv_mov_b32 v197, 0\n\tv_mov_b32 v198, 0\n\tv_mov_b32 v199, 0\n\tv_mov_b32 v202, 0\n\tv_mov_b32 v203, 0\n\t"
      "v_mov_b32 v188, 0\n\tv_mov_b32 v189, 0\n\tv_mov_b32 v190, 0\n\tv_mov_b32 v191, 0\n\t"
      "v_mfma_f32_32x32x16_bf16 v[0:15], v[196:199], v[196:199], 0\n\t"
      "v_mfma_f32_32x32x16_bf16 v[16:31], v[196:199], v[196:199], 0\n\t"
      "v_mfma_f32_32x32x16_bf16 v[32:47], v[196:199], v[196:199], 0\n\t"
      "v_mfma_f32_32x32x16_bf16 v[48:63], v[196:199], v[196:199], 0\n\t"
      "s_waitcnt vmcnt(20)\n\t"
      "v_mfma_f32_32x32x16_bf16 v[96:111], v[128:131], v[64:67], 0\n\t"
      "v_mfma_f32_32x32x16_bf16 v[96:111], v[132:135], v[68:71], v[96:111]\n\t"
      "s_nop 11\n\t"
      "1:\n\t"
      "TILE 0, 1, 3, 2\n\t"
      "TILE 1, 2, 0, 3\n\t"
      "TILE 2, 3, 1, 0\n\t"
      "TILE 3, 0, 2, 1\n\t"
      "s_sub_u32 %[cnt], %[cnt], 1\n\t"
      "s_cmp_lg_u32 %[cnt], 0\n\t"
      "s_cbranch_scc1 1b\n\t"
      "v_cvt_pk_bf16_f32 v199, v202, v203\n\t"
      "s_nop 1\n\t"
      "v_mfma_f32_32x32x16_bf16 v[48:63], v[188:191], v[196:199], v[48:63]\n\t"
      "s_waitcnt vmcnt(0)\n\t"
      "s_nop 11\n\t"
      ".purgem GRP\n\t.purgem STEP\n\t.purgem TILE"
      : "={v[0:15]}"(o[0]), "={v[16:31]}"(o[1]), "={v[32:47]}"(o[2]), "={v[48:63]}"(o[3]), "+{v[128:143]}"(kf0), "+{v[144:159]}"(kf1),
        [skv] "+s"(so_kv), [sw] "+s"(so_w), [sl] "+s"(so_l), [cnt] "+s"(cnt)
      : "{v[64:67]}"(q[0][0]), "{v[68:71]}"(q[0][1]), "{v[72:75]}"(q[1][0]), "{v[76:79]}"(q[1][1]),
        "{v[80:83]}"(q[2][0]), "{v[84:87]}"(q[2][1]), "{v[88:91]}"(q[3][0]), "{v[92:95]}"(q[3][1]),
        [vo] "{v204}"(x.vl16), [rs] "s"(x.rs), [kvl] "s"(kvs + 15 * 4096), [wl] "s"(so_w + 13 * 8192), [ll] "s"(so_l + 13 * 8192)
      : "memory", "scc",
        "v96","v97","v98","v99","v100","v101","v102","v103","v104","v105","v106","v107","v108","v109","v110","v111",
        "v112","v113","v114","v115","v116","v117","v118","v119","v120","v121","v122","v123","v124","v125","v126","v127",
        "v160","v161","v162","v163","v164","v165","v166","v167","v168","v169","v170","v171","v172","v173","v174","v175",
        "v176","v177","v178","v179","v180","v181","v182","v183","v184","v185","v186","v187","v188","v189","v190","v191",
        "v192","v193","v194","v195","v196","v197","v198","v199","v200","v201","v202","v203");
  } else {
    wv<0>();
    pin(kf0); pin(kf1);
    float mx[4];
#pragma unroll
    for (int k = 0; k < 4; ++k) {
      mx[k] = -3.0e38f; if (h) q[k][1][2] = 0; pin(q[k][1]);
#pragma unroll
      for (int i = 0; i < 16; ++i) o[k][i] = 0.f;
    }
    pf_wgu1(x);
    for (int tk = 0; tk < 16; ++tk) {
      u32x4 kf[4];
      sfor<0, 4>([&](auto i) { ld4<i * 1024>(kf[i], x.vl16, x.rs, kvs + tk * 4096); });
      wv<0>();
      sfor<0, 4>([&](auto k) {
        f32x16 s;
        nop<1>();
        m32z(s, kf[0], q[k][0]); m32(s, kf[1], q[k][1]);
        nop<11>(); pin(s);
        float tm = s[0];
#pragma unroll
        for (int i = 1; i < 16; ++i) tm = fmaxf(tm, s[i]);
        tm = max32(tm);
        const float mn = fmaxf(mx[k], tm), al = __builtin_amdgcn_exp2f(mx[k] - mn);
        mx[k] = mn;
#pragma unroll
        for (int i = 0; i < 16; ++i) o[k][i] *= al;
        u32x4 p0, p1;
#pragma unroll
        for (int d = 0; d < 4; ++d) {
          p0[d] = cvtpk(__builtin_amdgcn_exp2f(s[2 * d] - mn), __builtin_amdgcn_exp2f(s[2 * d + 1] - mn));
          p1[d] = cvtpk(__builtin_amdgcn_exp2f(s[8 + 2 * d] - mn), __builtin_amdgcn_exp2f(s[9 + 2 * d] - mn));
        }
        pin(p0); pin(p1); pin(o[k]);
        nop<1>();
        m32(o[k], kf[2], p0); m32(o[k], kf[3], p1);
        nop<11>(); pin(o[k]);
      });
    }
    float* mm = (float*)(lds + SCR);
#pragma unroll
    for (int k = 0; k < 4; ++k) if (h == 0) mm[((w ^ k) * 4 + w) * 32 + r] = mx[k];
    bar();
#pragma unroll
    for (int k = 0; k < 4; ++k) {
      const float* p = mm + (w ^ k) * 128 + r;
      const float g = fmaxf(fmaxf(p[0], p[32]), fmaxf(p[64], p[96])), f = __builtin_amdgcn_exp2f(mx[k] - g);
#pragma unroll
      for (int i = 0; i < 16; ++i) o[k][i] *= f;
    }
    bar();
  }
  stamp(x);
  reduce4(x, o);
  {
    auto lr = __builtin_amdgcn_permlane32_swap(__float_as_uint(o[0][12]), __float_as_uint(o[0][12]), false, false);
    const float inv = 1.0f / __uint_as_float(lr[1]);
    u32x4 a, b;
#pragma unroll
    for (int d = 0; d < 4; ++d) { a[d] = cvtpk(o[0][2 * d] * inv, o[0][2 * d + 1] * inv); b[d] = cvtpk(o[0][8 + 2 * d] * inv, o[0][9 + 2 * d] * inv); }
    if (h) { b[2] = 0; b[3] = 0; }
    const unsigned so = OFF_O + L * SZ_O + (tt * 32 + 2 * (2 * hp + kh)) * KB;
    st4<0>(a, x.vl16, x.rs, so); st4<1024>(b, x.vl16, x.rs, so);
  }
  wv<0>();
  signal(x, FL_O(L, x.c, th), 1);
}

template <bool FINAL> DI void res_epilogue(Ctx& x, const f32x16& acc, const u32x4 (&xr)[2], unsigned Xo, unsigned STo, int tt, int nt) {
  const int h = x.l >> 5, r = x.l & 31;
  u32x4 y[2];
#pragma unroll
  for (int d = 0; d < 8; ++d) y[d >> 2][d & 3] = cvtpk(acc[2 * d] + bflo(xr[d >> 2][d & 3]), acc[2 * d + 1] + bfhi(xr[d >> 2][d & 3]));
  if constexpr (FINAL) {
#pragma unroll
    for (int s = 0; s < 2; ++s) {
      auto a = __builtin_amdgcn_permlane32_swap(y[s][0], y[s][2], false, false);
      auto b = __builtin_amdgcn_permlane32_swap(y[s][1], y[s][3], false, false);
      y[s] = u32x4{a[0], b[0], a[1], b[1]};
    }
    const unsigned vo = r * (D * 2) + h * 16, so = tt * 32 * D * 2 + nt * 64;
    st4p<0>(y[0], vo, x.ro, so); st4p<32>(y[1], vo, x.ro, so);
  } else {
    const unsigned so = Xo + (tt * 28 + 2 * nt) * KB;
    st4<0>(y[0], x.vl16, x.rs, so); st4<1024>(y[1], x.vl16, x.rs, so);
    float s = 0.f, q = 0.f;
#pragma unroll
    for (int d = 0; d < 8; ++d) { s = dot2(y[d >> 2][d & 3], 0x3f803f80u, s); q = dot2(y[d >> 2][d & 3], y[d >> 2][d & 3], q); }
    s = sum32(s); q = sum32(q);
    if (h == 0) st2<0>(u32x2{__float_as_uint(s), __float_as_uint(q)}, r * 8, x.rs, STo + (nt * 2048 + tt * 32) * 8);
  }
}

DI void phase_oproj(Ctx& x, const int L) {
  const int w = x.w;
  if (x.m < 14) {
    const int th = x.m / 7, ct = x.m % 7, tt0 = 4 * x.c + 2 * th, tt = tt0 + (w >> 1), nt = 2 * ct + (w & 1);
    u32x4 xr[2];
    { const unsigned so = OFF_X + 2 * L * SZ_X + (tt * 28 + 2 * nt) * KB; ld4<0>(xr[0], x.vl16, x.rs, so); ld4<1024>(xr[1], x.vl16, x.rs, so); }
    await(x, FL_O(L, x.c, th), 8);
    stamp(x);
    u32x4 b[8][2];
    const unsigned s0 = OFF_O + L * SZ_O + (tt * 32 + 8 * w) * KB, s1 = OFF_O + L * SZ_O + ((tt0 + ((w >> 1) ^ 1)) * 32 + 8 * w) * KB;
    sfor<0, 8>([&](auto i) { ld4<(i & 3) * 1024>(b[i][0], x.vl16, x.rs, s0 + (i >> 2) * 4096); ld4<(i & 3) * 1024>(b[i][1], x.vl16, x.rs, s1 + (i >> 2) * 4096); });
    f32x16 acc[4];
    sfor<0, 8>([&](auto i) {
      wv<15 - 2 * i>();
      m32a<A_WO + 8 * i, i == 0>(acc[0], b[i][0]); m32a<A_WO + 8 * i + 4, i == 0>(acc[1], b[i][0]);
      wv<14 - 2 * i>();
      m32a<A_WO + 8 * i, i == 0>(acc[2], b[i][1]); m32a<A_WO + 8 * i + 4, i == 0>(acc[3], b[i][1]);
    });
    nop<11>();
    sfor<0, 4>([&](auto k) { pin(acc[k]); });
    pin(xr[0]); pin(xr[1]);
    stamp(x);
    reduce4(x, acc);
    res_epilogue<false>(x, acc[0], xr, OFF_X + (2 * L + 1) * SZ_X, OFF_ST + (2 * L + 1) * SZ_ST, tt, nt);
    wv<0>();
    signal(x, FL_X1(L, x.c), 1);
  } else {
    stamp(x); stamp(x);
  }
  pf_wgu2(x);
}

template <int NT> DI void gu_epilogue(Ctx& x, f32x4 (&acc)[NT][2][2], const f32x4 (&cs)[NT][2], const float (&mean)[2], const float (&r2)[2], u32x2 (&hp)[NT][2]) {
#pragma unroll
  for (int t = 0; t < NT; ++t)
#pragma unroll
    for (int u = 0; u < 2; ++u) {
      float v[4];
#pragma unroll
      for (int i = 0; i < 4; ++i) v[i] = (acc[t][0][u][i] - mean[u] * cs[t][0][i]) * r2[u] * (acc[t][1][u][i] - mean[u] * cs[t][1][i]);
      hp[t][u] = u32x2{cvtpk(v[0], v[1]), cvtpk(v[2], v[3])};
    }
}

DI void phase_gu(Ctx& x, const int L) {
  const int w = x.w, l = x.l, g = l >> 4, c16 = l & 15, m = x.m, tt = 4 * x.c + w;
  f32x4 csa[3][2], csb[4][2];
  sfor<0, 6>([&](auto i) { ld4f<64 * i>(csa[i >> 1][i & 1], g * 16, x.rs, OFF_CSGU + m * 896); });
  await(x, FL_X1(L, x.c), 14);
  stamp(x);
  u32x4 xf[14][2]; f32x2 sp[2][4];
  {
    const unsigned so = OFF_X + (2 * L + 1) * SZ_X + tt * 28 * KB;
    sfor<0, 14>([&](auto k) { ld4<(k & 1) * 2048>(xf[k][0], x.vg16, x.rs, so + (k >> 1) * 4096); ld4<(k & 1) * 2048 + 256>(xf[k][1], x.vg16, x.rs, so + (k >> 1) * 4096); });
    const unsigned st = OFF_ST + (2 * L + 1) * SZ_ST + tt * 32 * 8;
    sfor<0, 4>([&](auto i) {
      const int slot = (i < 3 || g < 2) ? g + 4 * i : g;
      ld2<0>(sp[0][i], (slot * 2048 + c16) * 8, x.rs, st); ld2<128>(sp[1][i], (slot * 2048 + c16) * 8, x.rs, st);
    });
  }
  const unsigned a0 = x.vl16, a1 = x.vl16 + 65536, a2 = x.vl16 + 131072;
  constexpr int DP = 6;
  u32x4 wf[8];
  auto rd = [&](auto ii) {
    constexpr int i = ii, slot = i >= 160 ? i - 160 : i;
    if constexpr (slot < 64) dsr<slot * 1024>(wf[i & 7], a0); else if constexpr (slot < 128) dsr<(slot - 64) * 1024>(wf[i & 7], a1); else dsr<(slot - 128) * 1024>(wf[i & 7], a2);
  };
  f32x4 aa[3][2][2];
  sfor<0, DP>(rd);
  sfor<0, 84>([&](auto i) {
    constexpr int KS = i / 6, t = (i % 6) >> 1, mat = i & 1;
    if constexpr (i % 6 == 0) wv<8 + 2 * (13 - KS)>();
    rd(std::integral_constant<int, i + DP>{}); wl<DP>();
    m16<KS == 0>(aa[t][mat][0], wf[i & 7], xf[KS][0]); m16<KS == 0>(aa[t][mat][1], wf[i & 7], xf[KS][1]); SB();
  });
  wv<0>();
  asm volatile("s_barrier" ::: "memory");
  pf_wgu3(x);
  float mean[2], r2[2];
  sfor<0, 8>([&](auto i) { pin(sp[i >> 2][i & 3]); });
  sfor<0, 6>([&](auto i) { pin(csa[i >> 1][i & 1]); });
#pragma unroll
  for (int u = 0; u < 2; ++u) {
    float s = sp[u][0][0] + sp[u][1][0] + sp[u][2][0], q = sp[u][0][1] + sp[u][1][1] + sp[u][2][1];
    if (g < 2) { s += sp[u][3][0]; q += sp[u][3][1]; }
    s = sum32(sum16(s)); q = sum32(sum16(q));
    float rs; row_stats(s, q, mean[u], rs); r2[u] = rs * rs;
  }
  stamp(x);
  const unsigned H = OFF_H + L * SZ_H + tt * 112 * KB;
  const int gl = (m & 1) ? 7 * m : 7 * m + 6, gp = (m & 1) ? 7 * m + 1 : 7 * m;
  {
    sfor<0, 12>([&](auto i) { pin(aa[i >> 2][(i >> 1) & 1][i & 1]); });
    u32x2 hp[3][2];
    gu_epilogue<3>(x, aa, csa, mean, r2, hp);
    const unsigned sl = H + (gl >> 1) * 2048 + (gl & 1) * 8;
    st2<0>(hp[0][0], x.vg16, x.rs, sl); st2<256>(hp[0][1], x.vg16, x.rs, sl);
    st4<0>(u32x4{hp[1][0][0], hp[1][0][1], hp[2][0][0], hp[2][0][1]}, x.vg16, x.rs, H + gp * 1024);
    st4<256>(u32x4{hp[1][1][0], hp[1][1][1], hp[2][1][0], hp[2][1][1]}, x.vg16, x.rs, H + gp * 1024);
  }
  SB();
  sfor<0, 8>([&](auto i) { ld4f<384 + 64 * i>(csb[i >> 1][i & 1], g * 16, x.rs, OFF_CSGU + m * 896); });
  pf_wd<0, 20>(x);
  f32x4 ab[4][2][2];
  sfor<84, 196>([&](auto i) {
    constexpr int KS = (i - 84) / 8, t = ((i - 84) % 8) >> 1, mat = i & 1;
    if constexpr (i + DP == 160) { wv<52>(); asm volatile("s_barrier" ::: "memory"); pf_wd<20, 28>(x); }
    if constexpr (i + DP < 196) { rd(std::integral_constant<int, i + DP>{}); wl<DP>(); } else wl<195 - i>();
    m16<KS == 0>(ab[t][mat][0], wf[i & 7], xf[KS][0]); m16<KS == 0>(ab[t][mat][1], wf[i & 7], xf[KS][1]); SB();
  });
  wv<56>();
  nop<7>();
  stamp(x);
  {
    sfor<0, 16>([&](auto i) { pin(ab[i >> 2][(i >> 1) & 1][i & 1]); });
    sfor<0, 8>([&](auto i) { pin(csb[i >> 1][i & 1]); });
    u32x2 hp[4][2];
    gu_epilogue<4>(x, ab, csb, mean, r2, hp);
    sfor<0, 2>([&](auto p) {
      st4<0>(u32x4{hp[2 * p][0][0], hp[2 * p][0][1], hp[2 * p + 1][0][0], hp[2 * p + 1][0][1]}, x.vg16, x.rs, H + (gp + 2 + 2 * p) * 1024);
      st4<256>(u32x4{hp[2 * p][1][0], hp[2 * p][1][1], hp[2 * p + 1][1][0], hp[2 * p + 1][1][1]}, x.vg16, x.rs, H + (gp + 2 + 2 * p) * 1024);
    });
  }
  wv<0>();
  signal(x, FL_H(L, x.c), 1);
}

DI void phase_down(Ctx& x, const int L) {
  const int w = x.w;
  if (x.m < 14) {
    const int th = x.m / 7, ct = x.m % 7, tt0 = 4 * x.c + 2 * th, tt = tt0 + (w >> 1), nt = 2 * ct + (w & 1);
    u32x4 xr[2];
    { const unsigned so = OFF_X + (2 * L + 1) * SZ_X + (tt * 28 + 2 * nt) * KB; ld4<0>(xr[0], x.vl16, x.rs, so); ld4<1024>(xr[1], x.vl16, x.rs, so); }
    await(x, FL_H(L, x.c), 16);
    stamp(x);
    constexpr int WN = 20;
    u32x4 b[WN][2];
    const unsigned s0 = OFF_H + L * SZ_H + (tt * 112 + 28 * w) * KB, s1 = OFF_H + L * SZ_H + ((tt0 + ((w >> 1) ^ 1)) * 112 + 28 * w) * KB;
    auto ld = [&](auto i) { ld4<(i & 3) * 1024>(b[i % WN][0], x.vl16, x.rs, s0 + (i >> 2) * 4096); ld4<(i & 3) * 1024>(b[i % WN][1], x.vl16, x.rs, s1 + (i >> 2) * 4096); };
    sfor<0, WN>(ld);
    f32x16 acc[4];
    sfor<0, 28>([&](auto i) {
      constexpr int last = (i + WN - 1 < 27 ? i + WN - 1 : 27);
      wv<2 * (last - i) + 1>();
      m32a<A_WD + 8 * i, i == 0>(acc[0], b[i % WN][0]); m32a<A_WD + 8 * i + 4, i == 0>(acc[1], b[i % WN][0]);
      wv<2 * (last - i)>();
      m32a<A_WD + 8 * i, i == 0>(acc[2], b[i % WN][1]); m32a<A_WD + 8 * i + 4, i == 0>(acc[3], b[i % WN][1]);
      if constexpr (i + WN < 28) ld(std::integral_constant<int, i + WN>{});
    });
    if (L == 0) pf_wq(x);
    nop<11>();
    sfor<0, 4>([&](auto k) { pin(acc[k]); });
    pin(xr[0]); pin(xr[1]);
    stamp(x);
    reduce4(x, acc);
    if (L == 0) {
      res_epilogue<false>(x, acc[0], xr, OFF_X + 2 * SZ_X, OFF_ST + 2 * SZ_ST, tt, nt);
      wv<0>();
      signal(x, FL_X(1, x.c, th), 1);
    } else {
      res_epilogue<true>(x, acc[0], xr, 0, 0, tt, nt);
    }
  } else {
    stamp(x); stamp(x);
    if (L == 0) { pf_wq(x); wv<0>(); }
  }
}

#define PHASE(f) { Ctx y = x; fresh(y); int Lo = L; asm volatile("" : "+s"(Lo)); f(y, Lo); }
__global__ __launch_bounds__(256, 1) void k_main(char* ws, char* out, int ph0, int ph1) {
  Ctx x;
  x.ws = ws;
  x.rs = __builtin_amdgcn_make_buffer_rsrc(ws, 0, WS_USED, 0x00020000);
  x.ro = __builtin_amdgcn_make_buffer_rsrc(out, 0, S * D * 2, 0x00020000);
  x.l = threadIdx.x & 63;
  x.w = __builtin_amdgcn_readfirstlane(threadIdx.x >> 6);
  asm volatile("s_nop 4" : "+s"(x.w));
  { const int b = blockIdx.x, r = b >> 3; x.c = 2 * (b & 7) + (r >> 4); x.m = r & 15; }
  fresh(x);
  stamp(x);
  switch (ph0 & 3) {
    case 0: pf_wq(x); break;
    case 1: pf_wo(x); pf_wgu1(x); wv<0>(); bar(); break;
    case 2: pf_wgu1(x); pf_wgu2(x); wv<0>(); bar(); break;
    default: pf_wd<0, 28>(x); wv<0>(); break;
  }
  for (int L = ph0 >> 2; L < 2; ++L) {
    const int p = 4 * L;
    if (p + 0 >= ph0 && p + 0 < ph1) PHASE(phase_att)
    if (p + 1 >= ph0 && p + 1 < ph1) PHASE(phase_oproj)
    if (p + 2 >= ph0 && p + 2 < ph1) PHASE(phase_gu)
    if (p + 3 >= ph0 && p + 3 < ph1) PHASE(phase_down)
  }
  stamp_flush(x);
}

extern "C" void kernel_launch(void* const* d_in, const int* in_sizes, int n_in, void* d_out, int out_size, void* d_ws, size_t ws_size,
                              hipStream_t stream) {
  PrepArgs a;
  a.x = (const uint16_t*)d_in[0]; a.wq = (const uint16_t*)d_in[1]; a.wk = (const uint16_t*)d_in[2]; a.wv = (const uint16_t*)d_in[3];
  a.wo = (const uint16_t*)d_in[4]; a.wg = (const uint16_t*)d_in[6]; a.wu = (const uint16_t*)d_in[7]; a.wd = (const uint16_t*)d_in[8];
  a.ws = (char*)d_ws;
  (void)hipFuncSetAttribute((const void*)k_main, hipFuncAttributeMaxDynamicSharedMemorySize, 160 * 1024);
  k_prep<<<PREP_TASKS, 256, 0, stream>>>(a);
  k_main<<<256, 256, 160 * 1024, stream>>>((char*)d_ws, (char*)d_out, 0, 8);
}
```

```cpp
#include <hip/hip_runtime.h>
#include <stdint.h>
#include <type_traits>

typedef float    f32x16 __attribute__((ext_vector_type(16)));
typedef float    f32x4  __attribute__((ext_vector_type(4)));
typedef float    f32x2  __attribute__((ext_vector_type(2)));
typedef unsigned u32x4  __attribute__((ext_vector_type(4)));
typedef unsigned u32x2  __attribute__((ext_vector_type(2)));
typedef __bf16   bf16x2 __attribute__((ext_vector_type(2)));
typedef __amdgpu_buffer_rsrc_t rsrc_t;
#define DI __device__ __forceinline__
#define GAS __attribute__((address_space(1)))
#define SB() __builtin_amdgcn_sched_barrier(0)

constexpr int S = 2048, D = 448, F = 1792;
constexpr unsigned KB = 1024;
constexpr unsigned OFF_FLAG = 0;
constexpr unsigned OFF_CSQ  = 64 * KB;
constexpr unsigned OFF_CSGU = OFF_CSQ + 4 * KB;
constexpr unsigned OFF_KMAX = OFF_CSGU + 16 * KB;
constexpr unsigned OFF_WQ   = 128 * KB;
constexpr unsigned OFF_WO   = OFF_WQ + 18 * 28 * KB;
constexpr unsigned OFF_WGU  = OFF_WO + 16 * 32 * KB;
constexpr unsigned OFF_WD   = OFF_WGU + 16 * 196 * KB;
constexpr unsigned SZ_X = 64 * 28 * KB, SZ_ST = 14 * 2048 * 8, SZ_O = 64 * 32 * KB, SZ_H = 64 * 112 * KB, SZ_KV = 64 * 4 * KB;
constexpr unsigned OFF_X    = OFF_WD + 16 * 112 * KB;
constexpr unsigned OFF_ST   = OFF_X + 4 * SZ_X;
constexpr unsigned OFF_O    = OFF_ST + 4 * SZ_ST;
constexpr unsigned OFF_H    = OFF_O + 2 * SZ_O;
constexpr unsigned OFF_KV   = OFF_H + 2 * SZ_H;
constexpr unsigned OFF_KVR  = OFF_KV + 2 * SZ_KV;
constexpr unsigned OFF_TS   = OFF_KVR + 512 * KB;
constexpr unsigned WS_USED  = OFF_TS + 520 * KB;
DI constexpr unsigned FL_X(int c, int th)         { return (0 + c * 2 + th) * 256u; }
DI constexpr unsigned FL_KV(int qt, int rep)      { return (32 + qt * 16 + rep) * 256u; }
DI constexpr unsigned FL_O(int L, int c, int th)  { return (96 + (L * 16 + c) * 2 + th) * 256u; }
DI constexpr unsigned FL_X1(int L, int c)         { return (160 + L * 16 + c) * 256u; }
DI constexpr unsigned FL_H(int L, int c, int p)   { return (192 + (L * 16 + c) * 2 + p) * 256u; }

DI int dn_pos(int ks) { const int q = ks / 28, kl = (ks % 28) >> 1, P = kl / 7, j = kl % 7;
  return 28 * q + 2 * ((j == 2 || j == 6) ? 10 + 2 * P + (j == 6) : 5 * P + (j > 2 ? j - 1 : j)) + (ks & 1); }
#define KV_DELAY 14
constexpr float QSCALE = 0.18898223650461362f * 1.4426950408889634f;
constexpr float M_LIMIT = 48.0f;

template <int I, int N, class Fn> DI void sfor(Fn&& f) { if constexpr (I < N) { f(std::integral_constant<int, I>{}); sfor<I + 1, N>(f); } }

DI unsigned cvtpk(float lo, float hi) { f32x2 v = {lo, hi}; bf16x2 b = __builtin_convertvector(v, bf16x2); return __builtin_bit_cast(unsigned, b); }
DI float bflo(unsigned u) { return __uint_as_float(u << 16); }
DI float bfhi(unsigned u) { return __uint_as_float(u & 0xffff0000u); }
DI float dot2(unsigned a, unsigned b, float c) { return __builtin_amdgcn_fdot2_f32_bf16(__builtin_bit_cast(bf16x2, a), __builtin_bit_cast(bf16x2, b), c, false); }
DI float sum32(float v) { auto r = __builtin_amdgcn_permlane32_swap(__float_as_uint(v), __float_as_uint(v), false, false); return __uint_as_float(r[0]) + __uint_as_float(r[1]); }
DI float sum16(float v) { auto r = __builtin_amdgcn_permlane16_swap(__float_as_uint(v), __float_as_uint(v), false, false); return __uint_as_float(r[0]) + __uint_as_float(r[1]); }
DI float max32(float v) { auto r = __builtin_amdgcn_permlane32_swap(__float_as_uint(v), __float_as_uint(v), false, false); return fmaxf(__uint_as_float(r[0]), __uint_as_float(r[1])); }

DI unsigned umax16(unsigned v) {
  v = max(v, (unsigned)__builtin_amdgcn_update_dpp(0, (int)v, 0x128, 0xf, 0xf, false));
  v = max(v, (unsigned)__builtin_amdgcn_update_dpp(0, (int)v, 0x124, 0xf, 0xf, false));
  v = max(v, (unsigned)__builtin_amdgcn_update_dpp(0, (int)v, 0x122, 0xf, 0xf, false));
  v = max(v, (unsigned)__builtin_amdgcn_update_dpp(0, (int)v, 0x121, 0xf, 0xf, false));
  return v;
}
DI unsigned umax64(unsigned v) {
  v = umax16(v);
  auto a = __builtin_amdgcn_permlane16_swap(v, v, false, false); v = max(a[0], a[1]);
  auto b = __builtin_amdgcn_permlane32_swap(v, v, false, false); return max(b[0], b[1]);
}

struct PrepArgs { const uint16_t *x, *wq, *wk, *wv, *wo, *wg, *wu, *wd; char* ws; };

DI void tr8(const u32x4 (&r)[8], u32x4 (&p)[8]) {
#pragma unroll
  for (int c = 0; c < 8; ++c)
#pragma unroll
    for (int d = 0; d < 4; ++d)
      p[c][d] = __builtin_amdgcn_perm(r[2 * d + 1][c >> 1], r[2 * d][c >> 1], (c & 1) ? 0x07060302u : 0x05040100u);
}
DI float piece_sum(const u32x4& p) {
  float s = 0.f;
#pragma unroll
  for (int d = 0; d < 4; ++d) s += bflo(p[d]) + bfhi(p[d]);
  return s;
}

typedef __bf16 bf16x8 __attribute__((ext_vector_type(8)));
typedef short s16x4 __attribute__((ext_vector_type(4)));
DI f32x16 mfma32(const u32x4& a, const u32x4& b, const f32x16& c) {
  return __builtin_amdgcn_mfma_f32_32x32x16_bf16(__builtin_bit_cast(bf16x8, a), __builtin_bit_cast(bf16x8, b), c, 0, 0, 0);
}
DI u32x2 trread(const char* p) { return __builtin_bit_cast(u32x2, __builtin_amdgcn_ds_read_tr16_b64_v4i16((__attribute__((address_space(3))) s16x4*)p)); }
DI void row_stats(float s, float q, float& mean, float& rstd) {
  mean = s * (1.0f / D);
  const float var = fmaxf(q * (1.0f / D) - mean * mean, 0.f);
  rstd = __builtin_amdgcn_rsqf(var + 1e-5f);
}

DI void prep_body(const PrepArgs& a, char* sm) {
  float* red = (float*)sm;
  const int t = threadIdx.x;
  int task = blockIdx.x;
  char* ws = a.ws;
  if (task < 136) {
    const bool isx = task < 64, iso = task >= 120;
    const int w = t >> 6, l = t & 63, r = l & 31, h = l >> 5;
    const int rr = isx ? r : (iso ? min(r, 27) : ((r & 7) | (((r >> 4) & 1) << 3) | (((r >> 3) & 1) << 4)));
    const uint16_t* row = (isx ? a.x + (size_t)task * 32 * D : (iso ? a.wo + (size_t)(task - 120) * 28 * D : a.wd + (size_t)(task - 64) * 32 * D)) + rr * D + 112 * w + 8 * h;
    u32x4 v[7], wk[7], wvv[7];
#pragma unroll
    for (int i = 0; i < 7; ++i) v[i] = *(const u32x4*)(row + 16 * i);
#pragma unroll
    for (int i = 0; i < 7; ++i) {
      const int c = min(i * 64 + l, 391);
      wk[i] = ((const u32x4*)(a.wk + 3136 * w))[c]; wvv[i] = ((const u32x4*)(a.wv + 3136 * w))[c];
    }
    char* wl = sm + w * 12672;
#pragma unroll
    for (int i = 0; i < 7; ++i) {
      const int c = min(i * 64 + l, 391);
      ((u32x4*)wl)[c] = wk[i]; ((u32x4*)(wl + 6336))[c] = wvv[i];
    }
    if (l < 8) ((u32x4*)(wl + (l >> 2) * 6336 + 6272))[l & 3] = u32x4{0, 0, 0, 0};
    float s = 0.f, q = 0.f;
    f32x16 ak, av, ck, cv, avt;
#pragma unroll
    for (int i = 0; i < 16; ++i) { ak[i] = 0.f; av[i] = 0.f; ck[i] = 0.f; cv[i] = 0.f; avt[i] = 0.f; }
    const u32x4 ones = {0x3f803f80u, 0x3f803f80u, 0x3f803f80u, 0x3f803f80u};
    const char* tb = wl + (4 * h + ((l >> 2) & 3)) * 56 + ((l >> 4) & 1) * 32 + (l & 3) * 8;
#pragma unroll
    for (int i = 0; i < 7; ++i) {
      if (isx) {
#pragma unroll
        for (int d = 0; d < 4; ++d) { s = dot2(v[i][d], 0x3f803f80u, s); q = dot2(v[i][d], v[i][d], q); }
      }
      auto s0 = __builtin_amdgcn_permlane32_swap(v[i][0], v[i][2], false, false);
      auto s1 = __builtin_amdgcn_permlane32_swap(v[i][1], v[i][3], false, false);
      const u32x4 o = {s0[0], s1[0], s0[1], s1[1]};
      const u32x2 k0 = trread(tb + i * 896), k1 = trread(tb + i * 896 + 448), v0 = trread(tb + 6336 + i * 896), v1 = trread(tb + 6336 + i * 896 + 448);
      const u32x4 fk = {k0[0], k0[1], k1[0], k1[1]}, fv = {v0[0], v0[1], v1[0], v1[1]};
      av = mfma32(o, fv, av);
      if (isx) {
        *(u32x4*)(ws + OFF_X + (size_t)(task * 28 + 7 * w + i) * KB + l * 16) = o;
        ak = mfma32(fk, o, ak); ck = mfma32(fk, ones, ck); cv = mfma32(ones, fv, cv); avt = mfma32(fv, o, avt);
      } else {
        ak = mfma32(o, fk, ak);
      }
    }
    f32x2* stt = (f32x2*)(sm + 4 * 12672);
    if (isx) { s = sum32(s); q = sum32(q); if (h == 0) stt[w * 32 + r] = f32x2{s, q}; }
    __syncthreads();
    if (isx) {
      f32x4* ex = (f32x4*)sm;
#pragma unroll
      for (int d = 0; d < 4; ++d) {
        ex[((d * 4 + w) * 2 + 0) * 64 + l] = f32x4{ak[4 * d], ak[4 * d + 1], ak[4 * d + 2], ak[4 * d + 3]};
        ex[((d * 4 + w) * 2 + 1) * 64 + l] = f32x4{avt[4 * d], avt[4 * d + 1], avt[4 * d + 2], avt[4 * d + 3]};
      }
      __syncthreads();
#pragma unroll
      for (int e = 0; e < 2; ++e)
        *(f32x4*)(ws + OFF_KVR + (size_t)((task * 2 + e) * 4 + w) * KB + l * 16) =
            (ex[((w * 4 + 0) * 2 + e) * 64 + l] + ex[((w * 4 + 1) * 2 + e) * 64 + l]) + (ex[((w * 4 + 2) * 2 + e) * 64 + l] + ex[((w * 4 + 3) * 2 + e) * 64 + l]);
      __syncthreads();
    }
    float rk = 1.f, rv[16];
#pragma unroll
    for (int i = 0; i < 16; ++i) rv[i] = 1.f;
    if (isx) {
      float mean;
      { const f32x2 p0 = stt[r], p1 = stt[32 + r], p2 = stt[64 + r], p3 = stt[96 + r]; row_stats(p0[0] + p1[0] + p2[0] + p3[0], p0[1] + p1[1] + p2[1] + p3[1], mean, rk); }
#pragma unroll
      for (int i = 0; i < 16; ++i) {
        const int tk = (i & 3) + 8 * (i >> 2) + 4 * h;
        const float mt = __shfl(mean, tk);
        rv[i] = __shfl(rk, tk);
        ak[i] -= mean * ck[i]; av[i] -= mt * cv[i];
      }
      for (int i = t; i < 14 * 32; i += 256) {
        const int slot = i >> 5, rq = i & 31;
        *(f32x2*)(ws + OFF_ST + (size_t)(slot * 2048 + task * 32 + rq) * 8) = slot < 4 ? stt[slot * 32 + rq] : f32x2{0.f, 0.f};
        *(u32x2*)(ws + OFF_ST + 2 * SZ_ST + (size_t)(slot * 2048 + task * 32 + rq) * 8) = u32x2{0xffffffffu, 0xffffffffu};
      }
    }
    f32x4* ex = (f32x4*)sm;
#pragma unroll
    for (int d = 0; d < 4; ++d)
#pragma unroll
      for (int e = 0; e < 2; ++e) {
        const int b = 8 * (d & 1) + 4 * e;
        ex[((d * 4 + w) * 2 + e) * 64 + l] = d < 2 ? f32x4{ak[b], ak[b + 1], ak[b + 2], ak[b + 3]} : f32x4{av[b], av[b + 1], av[b + 2], av[b + 3]};
      }
    __syncthreads();
    float y[8];
#pragma unroll
    for (int e = 0; e < 2; ++e) {
      const f32x4 u0 = ex[((w * 4 + 0) * 2 + e) * 64 + l], u1 = ex[((w * 4 + 1) * 2 + e) * 64 + l], u2 = ex[((w * 4 + 2) * 2 + e) * 64 + l], u3 = ex[((w * 4 + 3) * 2 + e) * 64 + l];
#pragma unroll
      for (int i = 0; i < 4; ++i) y[4 * e + i] = ((u0[i] + u1[i]) + (u2[i] + u3[i])) * (w < 2 ? rk : (w == 2 ? rv[4 * e + i] : rv[8 + 4 * e + i]));
    }
    u32x4 pk;
#pragma unroll
    for (int d = 0; d < 4; ++d) pk[d] = cvtpk(y[2 * d], y[2 * d + 1]);
    if (isx) {
      if (w < 2) {
        if (w == 1 && h) { pk[2] = 0; pk[3] = 0; }
        float n2 = 0.f;
#pragma unroll
        for (int d = 0; d < 4; ++d) n2 = dot2(pk[d], pk[d], n2);
        n2 = sum32(n2);
        const unsigned nb = umax64(n2 < 3.0e38f ? __float_as_uint(n2) : 0x7f800000u);
        if (l == 0) ((unsigned*)(ws + OFF_KMAX))[w * 64 + task] = nb;
        if (w == 1 && h) pk[2] = 0x3f80u;
      } else if (r >= 28) {
        const unsigned c = r == 28 ? 0x3f803f80u : 0u;
        pk = u32x4{c, c, c, c};
      }
      *(u32x4*)(ws + OFF_KV + (size_t)(task * 4 + w) * KB + l * 16) = pk;
    } else {
      if (r >= 28) pk = u32x4{0, 0, 0, 0};
      if (iso) {
        if ((w & 1) && h) { pk[2] = 0; pk[3] = 0; }
        *(u32x4*)(ws + OFF_WO + (size_t)((14 + (w >> 1)) * 32 + 2 * (task - 120) + (w & 1)) * KB + l * 16) = pk;
      } else {
        *(u32x4*)(ws + OFF_WD + (size_t)((14 + (w >> 1)) * 112 + dn_pos(2 * (task - 64) + (w & 1))) * KB + l * 16) = pk;
      }
    }
    return;
  }
  task -= 136;
  if (task < 112) {
    const int mat = task / 56, q = task % 56, cb = ((q >> 4) << 4) + ((q & 7) << 1) + ((q >> 3) & 1);
    const int cbb = (q < 48) ? cb : (48 + ((q - 48) & 3) * 2 + ((q - 48) >> 2));
    const uint16_t* W = mat ? a.wu : a.wg;
    const int ng = t & 3, kg = t >> 2, ks = kg >> 1, h = kg & 1;
    if (t < 224) {
      u32x4 r[8], p[8];
#pragma unroll
      for (int e = 0; e < 8; ++e) r[e] = *(const u32x4*)(W + (size_t)(16 * ks + 4 * h + (e & 3) + 8 * (e >> 2)) * F + 32 * cbb + 8 * ng);
      tr8(r, p);
      const int gt = 2 * cbb + (ng >> 1), m = gt / 7, j = gt % 7, lt = (m & 1) ? j : (j == 6 ? 0 : j + 1);
      const int KS = ks >> 1, g = 2 * (ks & 1) + h;
      const int fi = lt < 5 ? KS * 10 + lt * 2 + mat : 140 + KS * 4 + (lt - 5) * 2 + mat;
      char* dst = ws + OFF_WGU + (size_t)(m * 196 + fi) * KB + (g * 16 + (ng & 1) * 8) * 16;
#pragma unroll
      for (int c = 0; c < 8; ++c) { *(u32x4*)(dst + c * 16) = p[c]; red[kg * 32 + ng * 8 + c] = piece_sum(p[c]); }
    }
    __syncthreads();
    if (t < 32) {
      float s = 0.f;
      for (int i = 0; i < 56; ++i) s += red[i * 32 + t];
      const int gt = 2 * cbb + (t >> 4), m = gt / 7, j = gt % 7, lt = (m & 1) ? j : (j == 6 ? 0 : j + 1);
      ((float*)(ws + OFF_CSGU))[((m * 7 + lt) * 2 + mat) * 16 + (t & 15)] = s;
    }
    return;
  }
  task -= 112;
  if (task < 56) {
    const int nt = task % 14, kq = task / 14;
    const int ng = t & 3, kg = 56 * kq + (t >> 2), ks = kg >> 1, h = kg & 1;
    if (t < 224) {
      u32x4 r[8], p[8];
#pragma unroll
      for (int e = 0; e < 8; ++e) r[e] = *(const u32x4*)(a.wd + (size_t)(32 * (ks >> 1) + 4 * (2 * (ks & 1) + h) + (e & 3) + 16 * (e >> 2)) * D + 32 * nt + 8 * ng);
      tr8(r, p);
      char* dst = ws + OFF_WD + (size_t)(nt * 112 + dn_pos(ks)) * KB + (h * 32 + ng * 8) * 16;
#pragma unroll
      for (int c = 0; c < 8; ++c) *(u32x4*)(dst + c * 16) = p[c];
    }
    return;
  }
  task -= 56;
  if (task < 14) {
    const int nt = task, ng = t & 3, kg = t >> 2, ks = kg >> 1, h = kg & 1, j = ks >> 1, s = ks & 1;
    u32x4 r[8], p[8];
#pragma unroll
    for (int e = 0; e < 8; ++e) {
      const int dh = 16 * s + 4 * h + (e & 3) + 8 * (e >> 2);
      r[e] = u32x4{0, 0, 0, 0};
      if (dh < 28) r[e] = *(const u32x4*)(a.wo + (size_t)(28 * j + dh) * D + 32 * nt + 8 * ng);
    }
    tr8(r, p);
    char* dst = ws + OFF_WO + (size_t)(nt * 32 + ks) * KB + (h * 32 + ng * 8) * 16;
#pragma unroll
    for (int c = 0; c < 8; ++c) *(u32x4*)(dst + c * 16) = p[c];
    return;
  }
  task -= 14;
  if (task < 18) {
    const uint16_t* W = task < 16 ? a.wq + 28 * task : (task == 16 ? a.wk : a.wv);
    const int ld = task < 16 ? D : 28;
    char* base = ws + OFF_WQ + (size_t)task * 28 * KB;
    if (t < 196) {
      const int ng = t % 7;
      u32x2 r[2][8];
#pragma unroll
      for (int it = 0; it < 2; ++it) {
        const int kg = t / 7 + 28 * it, ks = kg >> 1, h = kg & 1;
#pragma unroll
        for (int e = 0; e < 8; ++e) r[it][e] = *(const u32x2*)(W + (size_t)(16 * ks + 4 * h + (e & 3) + 8 * (e >> 2)) * ld + 4 * ng);
      }
#pragma unroll
      for (int it = 0; it < 2; ++it) {
        const int kg = t / 7 + 28 * it, ks = kg >> 1, h = kg & 1;
#pragma unroll
        for (int c = 0; c < 4; ++c) {
          u32x4 p;
#pragma unroll
          for (int d = 0; d < 4; ++d) p[d] = __builtin_amdgcn_perm(r[it][2 * d + 1][c >> 1], r[it][2 * d][c >> 1], (c & 1) ? 0x07060302u : 0x05040100u);
          *(u32x4*)(base + ks * KB + (h * 32 + ng * 4 + c) * 16) = p;
          red[kg * 32 + ng * 4 + c] = piece_sum(p);
        }
      }
    } else if (t < 252) {
      const int kg = t - 196, ks = kg >> 1, h = kg & 1;
#pragma unroll
      for (int c = 0; c < 4; ++c) *(u32x4*)(base + ks * KB + (h * 32 + 28 + c) * 16) = u32x4{0, 0, 0, 0};
    }
    __syncthreads();
    if (t < 32) {
      float s = 0.f;
      if (t < 28) for (int i = 0; i < 56; ++i) s += red[i * 32 + t];
      ((float*)(ws + OFF_CSQ))[task * 32 + t] = s;
    }
    return;
  }
  for (int i = t; i < 64 * KB / 16; i += 256) ((u32x4*)(ws + OFF_FLAG))[i] = u32x4{0, 0, 0, 0};
  if (t < 128) ((unsigned*)(ws + OFF_KMAX))[128 + t] = t < 64 ? 0xffffffffu : 0u;
}
__global__ __launch_bounds__(256) void k_prep(PrepArgs a) {
  __shared__ __attribute__((aligned(16))) char sm[4 * 12672 + 1024];
  prep_body(a, sm);
}
constexpr int PREP_TASKS = 136 + 112 + 56 + 14 + 18 + 1;

#define ACLOB "a0","a1","a2","a3","a4","a5","a6","a7","a8","a9","a10","a11","a12","a13","a14","a15","a16","a17","a18","a19","a20","a21","a22","a23","a24","a25","a26","a27","a28","a29","a30","a31","a32","a33","a34","a35","a36","a37","a38","a39","a40","a41","a42","a43","a44","a45","a46","a47","a48","a49","a50","a51","a52","a53","a54","a55","a56","a57","a58","a59","a60","a61","a62","a63","a64","a65","a66","a67","a68","a69","a70","a71","a72","a73","a74","a75","a76","a77","a78","a79","a80","a81","a82","a83","a84","a85","a86","a87","a88","a89","a90","a91","a92","a93","a94","a95","a96","a97","a98","a99","a100","a101","a102","a103","a104","a105","a106","a107","a108","a109","a110","a111","a112","a113","a114","a115","a116","a117","a118","a119","a120","a121","a122","a123","a124","a125","a126","a127","a128","a129","a130","a131","a132","a133","a134","a135","a136","a137","a138","a139","a140","a141","a142","a143","a144","a145","a146","a147","a148","a149","a150","a151","a152","a153","a154","a155","a156","a157","a158","a159","a160","a161","a162","a163","a164","a165","a166","a167","a168","a169","a170","a171","a172","a173","a174","a175","a176","a177","a178","a179","a180","a181","a182","a183","a184","a185","a186","a187","a188","a189","a190","a191","a192","a193","a194","a195","a196","a197","a198","a199","a200","a201","a202","a203","a204","a205","a206","a207","a208","a209","a210","a211","a212","a213","a214","a215","a216","a217","a218","a219","a220","a221","a222","a223","a224","a225","a226","a227","a228","a229","a230","a231","a232","a233","a234","a235","a236","a237","a238","a239","a240","a241","a242","a243","a244","a245","a246","a247","a248","a249","a250","a251","a252","a253","a254","a255"

template <int IMM> DI void ld4(u32x4& d, unsigned vo, rsrc_t rs, unsigned so) {
  asm volatile("buffer_load_dwordx4 %0, %1, %2, %3 offen offset:%c4 sc1" : "=v"(d) : "v"(vo), "s"(rs), "s"(so), "i"(IMM));
}
template <int IMM> DI void ld2(f32x2& d, unsigned vo, rsrc_t rs, unsigned so) {
  asm volatile("buffer_load_dwordx2 %0, %1, %2, %3 offen offset:%c4 sc1" : "=v"(d) : "v"(vo), "s"(rs), "s"(so), "i"(IMM));
}
template <int IMM> DI void ld4f(f32x4& d, unsigned vo, rsrc_t rs, unsigned so) {
  asm volatile("buffer_load_dwordx4 %0, %1, %2, %3 offen offset:%c4" : "=v"(d) : "v"(vo), "s"(rs), "s"(so), "i"(IMM));
}
DI void ld1(float& d, unsigned vo, rsrc_t rs, unsigned so) {
  asm volatile("buffer_load_dword %0, %1, %2, %3 offen sc1" : "=v"(d) : "v"(vo), "s"(rs), "s"(so));
}
template <int AR, int IMM> DI void lda(unsigned vo, rsrc_t rs, unsigned so) {
  asm volatile("buffer_load_dwordx4 a[%c3:%c4], %0, %1, %2 offen offset:%c5" :: "v"(vo), "s"(rs), "s"(so), "i"(AR), "i"(AR + 3), "i"(IMM) : ACLOB);
}
template <int N> DI void ldl(unsigned vo, rsrc_t rs, unsigned so, unsigned lds) {
  asm volatile("s_mov_b32 m0, %3\n\ts_nop 0\n\tbuffer_load_dwordx4 %0, %1, %2 offen lds" :: "v"(vo), "s"(rs), "s"(so), "s"(lds) : "memory");
  if constexpr (N > 1) asm volatile("buffer_load_dwordx4 %0, %1, %2 offen offset:1024 lds" :: "v"(vo), "s"(rs), "s"(so) : "memory");
  if constexpr (N > 2) asm volatile("buffer_load_dwordx4 %0, %1, %2 offen offset:2048 lds" :: "v"(vo), "s"(rs), "s"(so) : "memory");
  if constexpr (N > 3) asm volatile("buffer_load_dwordx4 %0, %1, %2 offen offset:3072 lds" :: "v"(vo), "s"(rs), "s"(so) : "memory");
}
template <int IMM> DI void st4(const u32x4& d, unsigned vo, rsrc_t rs, unsigned so) {
  asm volatile("buffer_store_dwordx4 %0, %1, %2, %3 offen offset:%c4 sc1\n\ts_nop 1" :: "v"(d), "v"(vo), "s"(rs), "s"(so), "i"(IMM) : "memory");
}
template <int IMM> DI void st4p(const u32x4& d, unsigned vo, rsrc_t rs, unsigned so) {
  asm volatile("buffer_store_dwordx4 %0, %1, %2, %3 offen offset:%c4\n\ts_nop 1" :: "v"(d), "v"(vo), "s"(rs), "s"(so), "i"(IMM) : "memory");
}
template <int IMM> DI void st2(const u32x2& d, unsigned vo, rsrc_t rs, unsigned so) {
  asm volatile("buffer_store_dwordx2 %0, %1, %2, %3 offen offset:%c4 sc1" :: "v"(d), "v"(vo), "s"(rs), "s"(so), "i"(IMM) : "memory");
}
DI void st1(float d, unsigned vo, rsrc_t rs, unsigned so) {
  asm volatile("buffer_store_dword %0, %1, %2, %3 offen sc1" :: "v"(d), "v"(vo), "s"(rs), "s"(so) : "memory");
}
template <int N> DI void wv() { asm volatile("s_waitcnt vmcnt(%c0)" :: "i"(N) : "memory"); SB(); }
template <int N> DI void wl() { asm volatile("s_waitcnt lgkmcnt(%c0)" :: "i"(N) : "memory"); SB(); }
template <class T> DI void pin(T& x) { asm volatile("" : "+v"(x)); }
DI void bar() { asm volatile("s_waitcnt lgkmcnt(0)\n\ts_barrier" ::: "memory"); }
template <int N> DI void nop() { asm volatile("s_nop %c0" :: "i"(N)); SB(); }

template <int OFF> DI void dsr(u32x4& r, unsigned a) { asm volatile("ds_read_b128 %0, %1 offset:%c2" : "=v"(r) : "v"(a), "i"(OFF)); }

DI void m32(f32x16& c, const u32x4& a, const u32x4& b)  { asm volatile("v_mfma_f32_32x32x16_bf16 %0, %1, %2, %0" : "+v"(c) : "v"(a), "v"(b)); }
DI void m32z(f32x16& c, const u32x4& a, const u32x4& b) { asm volatile("v_mfma_f32_32x32x16_bf16 %0, %1, %2, 0" : "=&v"(c) : "v"(a), "v"(b)); }
template <int AR, bool Z> DI void m32a(f32x16& c, const u32x4& b) {
  if constexpr (Z) asm volatile("v_mfma_f32_32x32x16_bf16 %0, a[%c2:%c3], %1, 0" : "=&v"(c) : "v"(b), "i"(AR), "i"(AR + 3));
  else             asm volatile("v_mfma_f32_32x32x16_bf16 %0, a[%c2:%c3], %1, %0" : "+v"(c) : "v"(b), "i"(AR), "i"(AR + 3));
}
template <int AR, bool Z> DI void m32b(f32x16& c, const u32x4& a) {
  if constexpr (Z) asm volatile("v_mfma_f32_32x32x16_bf16 %0, %1, a[%c2:%c3], 0" : "=&v"(c) : "v"(a), "i"(AR), "i"(AR + 3));
  else             asm volatile("v_mfma_f32_32x32x16_bf16 %0, %1, a[%c2:%c3], %0" : "+v"(c) : "v"(a), "i"(AR), "i"(AR + 3));
}
template <bool Z> DI void m16(f32x4& c, const u32x4& a, const u32x4& b) {
  if constexpr (Z) asm volatile("v_mfma_f32_16x16x32_bf16 %0, %1, %2, 0" : "=&v"(c) : "v"(a), "v"(b));
  else             asm volatile("v_mfma_f32_16x16x32_bf16 %0, %1, %2, %0" : "+v"(c) : "v"(a), "v"(b));
}

template <bool Z, int AR> DI void m16a(f32x4& c, const u32x4& a) {
  if constexpr (Z) asm volatile("v_mfma_f32_16x16x32_bf16 %0, %1, a[%c2:%c3], 0" : "=&v"(c) : "v"(a), "i"(AR), "i"(AR + 3));
  else             asm volatile("v_mfma_f32_16x16x32_bf16 %0, %1, a[%c2:%c3], %0" : "+v"(c) : "v"(a), "i"(AR), "i"(AR + 3));
}
template <int AR, int IMM> DI void lda1(unsigned vo, rsrc_t rs, unsigned so) {
  asm volatile("buffer_load_dwordx4 a[%c3:%c4], %0, %1, %2 offen offset:%c5 sc1" :: "v"(vo), "s"(rs), "s"(so), "i"(AR), "i"(AR + 3), "i"(IMM) : ACLOB);
}
DI void ldflag(unsigned& v, rsrc_t rs, unsigned so) { asm volatile("buffer_load_dword %0, off, %1, %2 sc1" : "=v"(v) : "s"(rs), "s"(so)); }

struct Ctx {
  rsrc_t rs, ro;
  char* ws;
  unsigned vl16;
  unsigned vg16;
  int w, l, c, m;
  unsigned fa;
};
DI void fresh(Ctx& x) {
  asm volatile("" : "+s"(x.w), "+s"(x.c), "+s"(x.m));
  asm volatile("" : "+v"(x.l));
  x.vl16 = x.l * 16;
  const int g = x.l >> 4;
  x.vg16 = ((g >> 1) * 64 + (g & 1) * 32 + (x.l & 15)) * 16;
}
extern __shared__ __attribute__((aligned(16))) char lds[];
constexpr unsigned SCR = 112 * KB;

DI void fstamp(Ctx&) {}
DI void stamp(Ctx&) {}
DI void stamp_flush(Ctx&) {}

DI bool wave0(const Ctx& x) { int w = x.w; asm volatile("" : "+s"(w)); return w == 0; }
DI void count(Ctx& x, unsigned fl, int n) {
  if (wave0(x)) {
    const unsigned long long mask = (1ull << n) - 1;
    unsigned long long keep;
    asm volatile("s_mov_b64 %0, exec\n\ts_mov_b64 exec, %5\n\tbuffer_atomic_add %1, %2, %3, %4 offen\n\ts_mov_b64 exec, %0"
                 : "=&s"(keep) : "v"(1u), "v"(x.l * 256), "s"(x.rs), "s"(OFF_FLAG + fl), "s"(mask) : "memory");
  }
}
DI void signal(Ctx& x, unsigned fl, int n) { bar(); count(x, fl, n); }
DI void await(Ctx& x, unsigned fl, unsigned target) {
  if (wave0(x)) {
    for (unsigned spin = 0; spin < (1u << 18); ++spin) {
      unsigned v;
      asm volatile("buffer_load_dword %0, off, %1, %2 sc1\n\ts_waitcnt vmcnt(0)" : "=v"(v) : "s"(x.rs), "s"(OFF_FLAG + fl) : "memory");
      if ((unsigned)__builtin_amdgcn_readfirstlane(v) >= target) break;
      __builtin_amdgcn_s_sleep(1);
    }
  }
  bar();
}

DI void recheck(Ctx& x, unsigned v, unsigned fl, unsigned target) {
  pin(v);
  for (unsigned spin = 0; (unsigned)__builtin_amdgcn_readfirstlane(v) < target && spin < (1u << 18); ++spin) {
    __builtin_amdgcn_s_sleep(2);
    asm volatile("buffer_load_dword %0, off, %1, %2 sc1\n\ts_waitcnt vmcnt(0)" : "=v"(v) : "s"(x.rs), "s"(OFF_FLAG + fl) : "memory");
  }
  SB();
}

template <class Hook> DI void reduce4(Ctx& x, f32x16 (&acc)[4], Hook&& hook) {
  sfor<1, 4>([&](auto k) {
    f32x4* p = (f32x4*)(lds + SCR + ((x.w ^ k) * 3 + k - 1) * 4096 + x.vl16);
#pragma unroll
    for (int q = 0; q < 4; ++q) p[q * 64] = f32x4{acc[k][4 * q], acc[k][4 * q + 1], acc[k][4 * q + 2], acc[k][4 * q + 3]};
    SB(); hook(k); SB();
  });
  bar();
  const f32x4* p = (const f32x4*)(lds + SCR + x.w * 3 * 4096 + x.vl16);
#pragma unroll
  for (int k = 0; k < 3; ++k)
#pragma unroll
    for (int q = 0; q < 4; ++q) {
      f32x4 v = p[k * 256 + q * 64];
#pragma unroll
      for (int i = 0; i < 4; ++i) acc[0][4 * q + i] += v[i];
    }
}

constexpr int A_WO = 184, A_WD = 0, A_GX = 224;
constexpr int dn_ks(int o) { return o < 20 ? 2 * ((o >> 1) % 5 + ((o >> 1) % 5 >= 2) + 7 * (o / 10)) + (o & 1) : 2 * (((o - 20) >> 1) * 4 - ((o - 20) >> 2) + 2) + (o & 1); }
static_assert(dn_ks(0) == 0 && dn_ks(4) == 6 && dn_ks(9) == 11 && dn_ks(10) == 14 && dn_ks(19) == 25 && dn_ks(20) == 4 && dn_ks(22) == 12 && dn_ks(24) == 18 && dn_ks(27) == 27, "");
struct Two { unsigned s0, s1; };
constexpr int SL_WQ = 56;
template <int J> DI void pf_wq(Ctx& x) {
  ldl<1>(x.vl16, x.rs, OFF_WQ + ((x.m & 7) * 56 + 14 * x.w + J) * KB, (SL_WQ + 14 * x.w + J) * KB);
}
DI Two base_wo(const Ctx& x) { const int ct = x.m < 14 ? x.m % 7 : 7;
  return Two{OFF_WO + ((2 * ct + (x.w & 1)) * 32 + 8 * x.w) * KB, OFF_WO + ((2 * ct + ((x.w & 1) ^ 1)) * 32 + 8 * x.w) * KB}; }
DI void pf_wo(Ctx& x) {
  const Two b = base_wo(x);
  sfor<0, 8>([&](auto i) {
    lda<A_WO + 8 * i, (i & 3) * 1024>(x.vl16, x.rs, b.s0 + (i >> 2) * 4096);
    lda<A_WO + 8 * i + 4, (i & 3) * 1024>(x.vl16, x.rs, b.s1 + (i >> 2) * 4096);
  });
}
DI Two base_wd(const Ctx& x) { const int ct = x.m < 14 ? x.m % 7 : 7;
  return Two{OFF_WD + ((2 * ct + (x.w & 1)) * 112 + 28 * x.w) * KB, OFF_WD + ((2 * ct + ((x.w & 1) ^ 1)) * 112 + 28 * x.w) * KB}; }
template <int I0, int I1> DI void pf_wd(Ctx& x, const Two& b) {
  sfor<I0, I1>([&](auto f) {
    constexpr int i = f >> 1;
    lda<A_WD + 4 * f, (i & 3) * 1024>(x.vl16, x.rs, ((f & 1) ? b.s1 : b.s0) + (i >> 2) * 4096);
  });
}
DI void pf_wgu1(Ctx& x) {
  for (int t = 0; t < 14; ++t) ldl<2>(x.vl16, x.rs, OFF_WGU + (x.m * 196 + 8 * t + 2 * x.w) * KB, (8 * t + 2 * x.w) * KB);
}
template <int J> DI void pf_wgu2(Ctx& x) {
  ldl<1>(x.vl16, x.rs, OFF_WGU + (x.m * 196 + 112 + 4 * J + x.w) * KB, (112 + 4 * J + x.w) * KB);
}
template <int J> DI void pf_wgu3(Ctx& x) {
  ldl<1>(x.vl16, x.rs, OFF_WGU + (x.m * 196 + 160 + 9 * x.w + J) * KB, (9 * x.w + J) * KB);
}

DI void phase_att(Ctx& x, const int L) {
  const int w = x.w, l = x.l, h = l >> 5, r = l & 31;
  const int th = x.m >> 3, hp = x.m & 7, tl = w & 1, kh = w >> 1, tt = 4 * x.c + 2 * th + tl;
  const unsigned X = OFF_X + 2 * L * SZ_X, ST = OFF_ST + 2 * L * SZ_ST, KV = OFF_KV + L * SZ_KV;
  f32x4 cq[4];
  sfor<0, 4>([&](auto a) { ld4f<32 * a>(cq[a], h * 16, x.rs, OFF_CSQ + (2 * hp + kh) * 128); });
  if (L > 0) {
    if (x.m >= 14) wv<0>();
    await(x, FL_X(x.c, th), 7);
    if (x.m >= 14) count(x, FL_KV(x.c >> 2, 0), 16);
  }
  stamp(x);
  u32x4 xf[14]; f32x2 sp[7];
  {
    const unsigned so = X + (tt * 28 + 14 * kh) * KB;
    sfor<0, 14>([&](auto i) { ld4<(i & 3) * 1024>(xf[i], x.vl16, x.rs, so + (i >> 2) * 4096); });
    sfor<0, 7>([&](auto i) { ld2<0>(sp[i], (h * 7 * 2048 + r) * 8, x.rs, ST + (i * 2048 + tt * 32) * 8); });
  }
  f32x16 qa, qb;
  {
    if (L == 0) { wv<21>(); bar(); }
    u32x4 wa[14], wb[14];
    const unsigned pa = x.vl16 + (SL_WQ + 28 * kh + 14 * kh) * KB, pb = x.vl16 + (SL_WQ + 28 * (kh ^ 1) + 14 * kh) * KB;
    sfor<0, 7>([&](auto i) { dsr<i * 1024>(wb[i], pb); dsr<i * 1024>(wa[i], pa); });
    sfor<0, 14>([&](auto i) {
      wv<20 - i>();
      if constexpr (i < 7) wl<12>(); else wl<2 * (13 - i)>();
      if constexpr (i == 0) { m32z(qb, wb[i], xf[i]); m32z(qa, wa[i], xf[i]); } else { m32(qb, wb[i], xf[i]); m32(qa, wa[i], xf[i]); }
      if constexpr (i < 7) { dsr<(i + 7) * 1024>(wb[i + 7], pb); dsr<(i + 7) * 1024>(wa[i + 7], pa); }
    });
  }
  wv<0>();
  sfor<0, 7>([&](auto i) { pin(sp[i]); });
  sfor<0, 4>([&](auto i) { pin(cq[i]); });
  float mean, rstd;
  {
    float s = 0.f, q = 0.f;
#pragma unroll
    for (int i = 0; i < 7; ++i) { s += sp[i][0]; q += sp[i][1]; }
    row_stats(sum32(s), sum32(q), mean, rstd);
  }
  unsigned pv, k0, k1;
  asm volatile("buffer_load_dword %0, %7, %3, %4 offen sc1\n\tbuffer_load_dword %1, %5, %3, %6 offen sc1\n\tbuffer_load_dword %2, %5, %3, %6 offen offset:256 sc1"
               : "=&v"(pv), "=&v"(k0), "=&v"(k1) : "s"(x.rs), "s"(OFF_FLAG + FL_KV(0, x.c)), "v"(l * 4), "s"(OFF_KMAX + L * 512), "v"((l & 3) * 4096));
  nop<11>();
  pin(qa); pin(qb);
  {
    f32x4* p = (f32x4*)(lds + SCR + (w ^ 2) * 4096 + x.vl16);
#pragma unroll
    for (int q = 0; q < 4; ++q) p[q * 64] = f32x4{qb[4 * q], qb[4 * q + 1], qb[4 * q + 2], qb[4 * q + 3]};
  }
  bar();
  {
    u32x4 qf[2];
    const f32x4* p = (const f32x4*)(lds + SCR + w * 4096 + x.vl16);
    const float sc = rstd * QSCALE;
#pragma unroll
    for (int q = 0; q < 4; ++q) {
      const f32x4 o = p[q * 64];
      float v[4];
#pragma unroll
      for (int i = 0; i < 4; ++i) v[i] = (qa[4 * q + i] + o[i] - mean * cq[q][i]) * sc;
      qf[q >> 1][2 * (q & 1)] = cvtpk(v[0], v[1]); qf[q >> 1][2 * (q & 1) + 1] = cvtpk(v[2], v[3]);
    }
    if (h) { qf[1][2] = 0; qf[1][3] = 0; }
    float qn2 = 0.f;
#pragma unroll
    for (int d = 0; d < 8; ++d) qn2 = dot2(qf[d >> 2][d & 3], qf[d >> 2][d & 3], qn2);
    qn2 = sum32(qn2);
    if (h) qf[1][2] = cvtpk(1.01f * __builtin_amdgcn_sqrtf(qn2), 0.f);
    ((u32x4*)(lds + SCR + 24576 + w * 2048 + x.vl16))[0] = qf[0];
    ((u32x4*)(lds + SCR + 24576 + w * 2048 + x.vl16))[64] = qf[1];
  }
  stamp(x);
  wv<0>();
  pin(pv); pin(k0); pin(k1);
  {
    const unsigned need = L ? 8u : 0u;
    for (unsigned spin = 0; !__all(pv >= need && k0 != 0xffffffffu) && spin < (1u << 16); ++spin) {
      __builtin_amdgcn_s_sleep(4);
      asm volatile("buffer_load_dword %0, %3, %1, %2 offen sc1\n\ts_waitcnt vmcnt(0)" : "=v"(pv) : "s"(x.rs), "s"(OFF_FLAG + FL_KV(0, x.c)), "v"((l & 3) * 4096) : "memory");
      if (__all(pv >= need))
        asm volatile("buffer_load_dword %0, %3, %1, %2 offen sc1\n\ts_waitcnt vmcnt(0)" : "=v"(k0) : "s"(x.rs), "s"(OFF_KMAX + L * 512), "v"(l * 4) : "memory");
    }
  }
  stamp(x);
  const unsigned kvs = KV + 16 * w * 4096;
  f32x16 kf0, kf1;
  asm volatile("buffer_load_dwordx4 v[128:131], %2, %3, %4 offen sc1\n\tbuffer_load_dwordx4 v[132:135], %2, %3, %4 offen offset:1024 sc1\n\t"
               "buffer_load_dwordx4 v[136:139], %2, %3, %4 offen offset:2048 sc1\n\tbuffer_load_dwordx4 v[140:143], %2, %3, %4 offen offset:3072 sc1\n\t"
               "buffer_load_dwordx4 v[144:147], %2, %3, %5 offen sc1\n\tbuffer_load_dwordx4 v[148:151], %2, %3, %5 offen offset:1024 sc1\n\t"
               "buffer_load_dwordx4 v[152:155], %2, %3, %5 offen offset:2048 sc1\n\tbuffer_load_dwordx4 v[156:159], %2, %3, %5 offen offset:3072 sc1"
               : "={v[128:143]}"(kf0), "={v[144:159]}"(kf1) : "v"(x.vl16), "s"(x.rs), "s"(kvs), "s"(kvs + 4096));
  const float kmx = 1.01f * __builtin_amdgcn_sqrtf(__uint_as_float(umax64(k0)) + __uint_as_float(umax64(k1)));
  bar();
  u32x4 q[4][2];
#pragma unroll
  for (int k = 0; k < 4; ++k) {
    q[k][0] = ((const u32x4*)(lds + SCR + 24576 + (w ^ k) * 2048 + x.vl16))[0];
    q[k][1] = ((const u32x4*)(lds + SCR + 24576 + (w ^ k) * 2048 + x.vl16))[64];
  }
  bool safe = true;
  {
#pragma unroll
    for (int k = 0; k < 4; ++k) {
      const float mrow = bflo(q[k][1][2]) * kmx + 0.01f;
      safe = safe && (!h || mrow <= M_LIMIT);
      if (h) q[k][1][2] = cvtpk(-mrow, 0.f);
    }
  }
  const bool fast = __all(safe);
  stamp(x);
  f32x16 o[4];
  if (fast) {
    unsigned so_kv = kvs + 2 * 4096, so_w = OFF_WGU + (x.m * 196 + 2 * w) * KB, so_l = 2 * w * KB;
    Two ab = base_wo(x);
    const Two wd = base_wd(x);
    asm volatile(
      ".macro GRP s, pa, pb\n\t"
      "v_exp_f32 v200, v[\\s]\n\tv_exp_f32 v201, v[\\s+1]\n\tv_cvt_pk_bf16_f32 v[\\pa], v202, v203\n\t"
      "v_exp_f32 v202, v[\\s+2]\n\tv_exp_f32 v203, v[\\s+3]\n\tv_cvt_pk_bf16_f32 v[\\pb], v200, v201\n\t"
      ".endm\n\t"
      ".macro MEM kind, i, ld\n\t"
      ".if \\kind == 1\n\t"
      "buffer_load_dwordx4 v[128+16*\\ld+4*\\i:128+16*\\ld+4*\\i+3], %[vo], %[rs], %[skv] offen offset:1024*\\i sc1\n\t"
      ".elseif \\kind == 2\n\t"
      "buffer_load_dwordx4 %[vo], %[rs], %[sw] offen offset:1024*\\i lds\n\t"
      ".elseif \\kind == 3\n\t"
      ".if att_n < 62\n\t"
      ".if att_n < 16\n\t.set att_r, 184+4*att_n\n\t.else\n\t.set att_r, 4*(att_n-16)\n\t.endif\n\t"
      ".if att_n & 1\n\t"
      "buffer_load_dwordx4 a[att_r:att_r+3], %[vo], %[rs], %[ab1] offen offset:1024*((att_n>>1)&3)\n\t"
      ".else\n\t"
      "buffer_load_dwordx4 a[att_r:att_r+3], %[vo], %[rs], %[ab0] offen offset:1024*((att_n>>1)&3)\n\t"
      ".endif\n\t"
      ".if att_n == 15\n\t"
      "s_mov_b32 %[ab0], %[wd0]\n\ts_mov_b32 %[ab1], %[wd1]\n\t"
      ".elseif (att_n & 7) == 7\n\t"
      "s_add_u32 %[ab0], %[ab0], 4096\n\ts_add_u32 %[ab1], %[ab1], 4096\n\t"
      ".endif\n\t"
      ".set att_n, att_n+1\n\t"
      ".endif\n\t"
      ".endif\n\t"
      ".endm\n\t"
      ".macro STEP sc, sn, k, nk, pk, tk, nt, pt, ld, m0k, m0i, m1k, m2k, m2i\n\t"
      "v_mfma_f32_32x32x16_bf16 v[\\sn:\\sn+15], v[128+16*\\nt:128+16*\\nt+3], v[64+8*\\nk:64+8*\\nk+3], 0\n\t"
      "MEM \\m0k, \\m0i, \\ld\n\t"
      "GRP \\sc, 199, 192\n\t"
      "v_mfma_f32_32x32x16_bf16 v[\\sn:\\sn+15], v[132+16*\\nt:132+16*\\nt+3], v[68+8*\\nk:68+8*\\nk+3], v[\\sn:\\sn+15]\n\t"
      "MEM \\m1k, 0, 0\n\t"
      "GRP \\sc+4, 193, 194\n\t"
      "v_mfma_f32_32x32x16_bf16 v[16*\\pk:16*\\pk+15], v[140+16*\\pt:140+16*\\pt+3], v[196:199], v[16*\\pk:16*\\pk+15]\n\t"
      "MEM \\m2k, \\m2i, \\ld\n\t"
      "GRP \\sc+8, 195, 196\n\t"
      "v_mfma_f32_32x32x16_bf16 v[16*\\k:16*\\k+15], v[136+16*\\tk:136+16*\\tk+3], v[192:195], v[16*\\k:16*\\k+15]\n\t"
      "GRP \\sc+12, 197, 198\n\t"
      ".endm\n\t"
      ".macro TILE tk, nx, pv, ld, on, wait\n\t"
      "STEP 96, 112, 0, 1, 3, \\tk, \\tk, \\pv, \\ld, \\on, 0, 3, \\on, 1\n\t"
      "STEP 112, 96, 1, 2, 0, \\tk, \\tk, \\tk, \\ld, \\on, 2, 3, \\on, 3\n\t"
      ".if \\on\n\t"
      "s_mov_b32 m0, %[sl]\n\t"
      "s_add_u32 %[skv], %[skv], 4096\n\t"
      ".endif\n\t"
      "STEP 96, 112, 2, 3, 1, \\tk, \\tk, \\tk, \\ld, 2*\\on, 0, 3, 2*\\on, 1\n\t"
      "s_waitcnt vmcnt(\\wait)\n\t"
      ".if \\on\n\t"
      "s_add_u32 %[sw], %[sw], 8192\n\t"
      "s_add_u32 %[sl], %[sl], 8192\n\t"
      ".endif\n\t"
      "STEP 112, 96, 3, 0, 2, \\tk, \\nx, \\tk, \\ld, 0, 0, 3, 0, 0\n\t"
      ".endm\n\t"
      ".set att_n, 0\n\t"
      "v_mov_b32 v196, 0\n\tv_mov_b32 v197, 0\n\tv_mov_b32 v198, 0\n\tv_mov_b32 v199, 0\n\tv_mov_b32 v202, 0\n\tv_mov_b32 v203, 0\n\t"
      "v_mov_b32 v188, 0\n\tv_mov_b32 v189, 0\n\tv_mov_b32 v190, 0\n\tv_mov_b32 v191, 0\n\t"
      "v_mfma_f32_32x32x16_bf16 v[0:15], v[196:199], v[196:199], 0\n\t"
      "v_mfma_f32_32x32x16_bf16 v[16:31], v[196:199], v[196:199], 0\n\t"
      "v_mfma_f32_32x32x16_bf16 v[32:47], v[196:199], v[196:199], 0\n\t"
      "v_mfma_f32_32x32x16_bf16 v[48:63], v[196:199], v[196:199], 0\n\t"
      "s_waitcnt vmcnt(4)\n\t"
      "v_mfma_f32_32x32x16_bf16 v[96:111], v[128:131], v[64:67], 0\n\t"
      "v_mfma_f32_32x32x16_bf16 v[96:111], v[132:135], v[68:71], v[96:111]\n\t"
      "s_nop 11\n\t"
      "TILE 0, 1, 3, 2, 1, 9\n\tTILE 1, 2, 0, 3, 1, 13\n\tTILE 2, 3, 1, 0, 1, 13\n\tTILE 3, 0, 2, 1, 1, 13\n\t"
      "TILE 0, 1, 3, 2, 1, 13\n\tTILE 1, 2, 0, 3, 1, 13\n\tTILE 2, 3, 1, 0, 1, 13\n\tTILE 3, 0, 2, 1, 1, 13\n\t"
      "TILE 0, 1, 3, 2, 1, 13\n\tTILE 1, 2, 0, 3, 1, 13\n\tTILE 2, 3, 1, 0, 1, 13\n\tTILE 3, 0, 2, 1, 1, 13\n\t"
      "TILE 0, 1, 3, 2, 1, 13\n\tTILE 1, 2, 0, 3, 1, 13\n\tTILE 2, 3, 1, 0, 0, 7\n\tTILE 3, 3, 2, 1, 0, 63\n\t"
      "v_cvt_pk_bf16_f32 v199, v202, v203\n\t"
      "s_nop 1\n\t"
      "v_mfma_f32_32x32x16_bf16 v[48:63], v[188:191], v[196:199], v[48:63]\n\t"
      "s_nop 11\n\t"
      ".purgem GRP\n\t.purgem MEM\n\t.purgem STEP\n\t.purgem TILE"
      : "={v[0:15]}"(o[0]), "={v[16:31]}"(o[1]), "={v[32:47]}"(o[2]), "={v[48:63]}"(o[3]), "+{v[128:143]}"(kf0), "+{v[144:159]}"(kf1),
        [skv] "+s"(so_kv), [sw] "+s"(so_w), [sl] "+s"(so_l), [ab0] "+s"(ab.s0), [ab1] "+s"(ab.s1)
      : "{v[64:67]}"(q[0][0]), "{v[68:71]}"(q[0][1]), "{v[72:75]}"(q[1][0]), "{v[76:79]}"(q[1][1]),
        "{v[80:83]}"(q[2][0]), "{v[84:87]}"(q[2][1]), "{v[88:91]}"(q[3][0]), "{v[92:95]}"(q[3][1]),
        [vo] "{v204}"(x.vl16), [rs] "s"(x.rs), [wd0] "s"(wd.s0), [wd1] "s"(wd.s1)
      : "memory", "scc",
        "v96","v97","v98","v99","v100","v101","v102","v103","v104","v105","v106","v107","v108","v109","v110","v111",
        "v112","v113","v114","v115","v116","v117","v118","v119","v120","v121","v122","v123","v124","v125","v126","v127",
        "v160","v161","v162","v163","v164","v165","v166","v167","v168","v169","v170","v171","v172","v173","v174","v175",
        "v176","v177","v178","v179","v180","v181","v182","v183","v184","v185","v186","v187","v188","v189","v190","v191",
        "v192","v193","v194","v195","v196","v197","v198","v199","v200","v201","v202","v203", ACLOB);
    bar();
  } else {
    wv<0>();
    pin(kf0); pin(kf1);
    float mx[4];
#pragma unroll
    for (int k = 0; k < 4; ++k) {
      mx[k] = -3.0e38f; if (h) q[k][1][2] = 0; pin(q[k][1]);
#pragma unroll
      for (int i = 0; i < 16; ++i) o[k][i] = 0.f;
    }
    pf_wo(x);
    pf_wgu1(x);
    wv<0>();
    pf_wd<0, 46>(x, base_wd(x));
    for (int tk = 0; tk < 16; ++tk) {
      u32x4 kf[4];
      sfor<0, 4>([&](auto i) { ld4<i * 1024>(kf[i], x.vl16, x.rs, kvs + tk * 4096); });
      wv<0>();
      sfor<0, 4>([&](auto k) {
        f32x16 s;
        nop<1>();
        m32z(s, kf[0], q[k][0]); m32(s, kf[1], q[k][1]);
        nop<11>(); pin(s);
        float tm = s[0];
#pragma unroll
        for (int i = 1; i < 16; ++i) tm = fmaxf(tm, s[i]);
        tm = max32(tm);
        const float mn = fmaxf(mx[k], tm), al = __builtin_amdgcn_exp2f(mx[k] - mn);
        mx[k] = mn;
#pragma unroll
        for (int i = 0; i < 16; ++i) o[k][i] *= al;
        u32x4 p0, p1;
#pragma unroll
        for (int d = 0; d < 4; ++d) {
          p0[d] = cvtpk(__builtin_amdgcn_exp2f(s[2 * d] - mn), __builtin_amdgcn_exp2f(s[2 * d + 1] - mn));
          p1[d] = cvtpk(__builtin_amdgcn_exp2f(s[8 + 2 * d] - mn), __builtin_amdgcn_exp2f(s[9 + 2 * d] - mn));
        }
        pin(p0); pin(p1); pin(o[k]);
        nop<1>();
        m32(o[k], kf[2], p0); m32(o[k], kf[3], p1);
        nop<11>(); pin(o[k]);
      });
    }
    bar();
    float* mm = (float*)(lds + SCR);
#pragma unroll
    for (int k = 0; k < 4; ++k) if (h == 0) mm[((w ^ k) * 4 + w) * 32 + r] = mx[k];
    bar();
#pragma unroll
    for (int k = 0; k < 4; ++k) {
      const float* p = mm + (w ^ k) * 128 + r;
      const float g = fmaxf(fmaxf(p[0], p[32]), fmaxf(p[64], p[96])), f = __builtin_amdgcn_exp2f(mx[k] - g);
#pragma unroll
      for (int i = 0; i < 16; ++i) o[k][i] *= f;
    }
    bar();
  }
  stamp(x);
  reduce4(x, o, [](auto) {});
  pin(o[0]); fstamp(x);
  {
    auto lr = __builtin_amdgcn_permlane32_swap(__float_as_uint(o[0][12]), __float_as_uint(o[0][12]), false, false);
    const float inv = __builtin_amdgcn_rcpf(__uint_as_float(lr[1]));
    u32x4 a, b;
#pragma unroll
    for (int d = 0; d < 4; ++d) { a[d] = cvtpk(o[0][2 * d] * inv, o[0][2 * d + 1] * inv); b[d] = cvtpk(o[0][8 + 2 * d] * inv, o[0][9 + 2 * d] * inv); }
    if (h) { b[2] = 0; b[3] = 0; }
    const unsigned so = OFF_O + L * SZ_O + (tt * 32 + 2 * (2 * hp + kh)) * KB;
    st4<0>(a, x.vl16, x.rs, so); st4<1024>(b, x.vl16, x.rs, so);
  }
  fstamp(x);
  wv<0>();
  fstamp(x);
  signal(x, FL_O(L, x.c, th), 1);
}

template <bool FINAL> DI void res_epilogue(Ctx& x, const f32x16& acc, const u32x4 (&xr)[2], unsigned Xo, unsigned STo, int tt, int nt) {
  const int h = x.l >> 5, r = x.l & 31;
  u32x4 y[2];
#pragma unroll
  for (int d = 0; d < 8; ++d) y[d >> 2][d & 3] = cvtpk(acc[2 * d] + bflo(xr[d >> 2][d & 3]), acc[2 * d + 1] + bfhi(xr[d >> 2][d & 3]));
  if constexpr (FINAL) {
#pragma unroll
    for (int s = 0; s < 2; ++s) {
      auto a = __builtin_amdgcn_permlane32_swap(y[s][0], y[s][2], false, false);
      auto b = __builtin_amdgcn_permlane32_swap(y[s][1], y[s][3], false, false);
      y[s] = u32x4{a[0], b[0], a[1], b[1]};
    }
    const unsigned vo = r * (D * 2) + h * 16, so = tt * 32 * D * 2 + nt * 64;
    st4p<0>(y[0], vo, x.ro, so); st4p<32>(y[1], vo, x.ro, so);
  } else {
    const unsigned so = Xo + (tt * 28 + 2 * nt) * KB;
    st4<0>(y[0], x.vl16, x.rs, so); st4<1024>(y[1], x.vl16, x.rs, so);
    float s = 0.f, q = 0.f, s1 = 0.f, q1 = 0.f;
#pragma unroll
    for (int d = 0; d < 4; ++d) {
      s = dot2(y[0][d], 0x3f803f80u, s); q = dot2(y[0][d], y[0][d], q); s1 = dot2(y[1][d], 0x3f803f80u, s1); q1 = dot2(y[1][d], y[1][d], q1);
    }
    s = sum32(s + s1); q = sum32(q + q1);
    if (h == 0) st2<0>(u32x2{__float_as_uint(s), min(__float_as_uint(q), 0xfffffffeu)}, r * 8, x.rs, STo + (nt * 2048 + tt * 32) * 8);
  }
}

DI void phase_oproj(Ctx& x, const int L) {
  const int w = x.w;
  const bool kvw = x.m >= 14;
  if (!kvw || L == 0) {
    const int th = kvw ? x.m - 14 : x.m / 7, ct = kvw ? 7 : x.m % 7, tt0 = 4 * x.c + 2 * th, tt = tt0 + (w >> 1), nt = 2 * ct + (w & 1);
    u32x4 xr[4];
    const unsigned sr = kvw ? OFF_KVR + (tt * 2 + (w & 1)) * 4 * KB : OFF_X + 2 * L * SZ_X + (tt * 28 + 2 * nt) * KB;
    sfor<0, 4>([&](auto i) { ld4<1024 * i>(xr[i], x.vl16, x.rs, sr); });
    await(x, FL_O(L, x.c, th), 8);
    stamp(x);
    u32x4 b[8][2];
    const unsigned s0 = OFF_O + L * SZ_O + (tt * 32 + 8 * w) * KB, s1 = OFF_O + L * SZ_O + ((tt0 + ((w >> 1) ^ 1)) * 32 + 8 * w) * KB;
    auto ld = [&](auto i) { ld4<(i & 3) * 1024>(b[i][0], x.vl16, x.rs, s0 + (i >> 2) * 4096); ld4<(i & 3) * 1024>(b[i][1], x.vl16, x.rs, s1 + (i >> 2) * 4096); };
    constexpr int WN = 5;
    sfor<0, WN>(ld);
    f32x16 acc[4];
    sfor<0, 8>([&](auto i) {
      constexpr int last = (i + WN - 1 < 7 ? i + WN - 1 : 7);
      wv<2 * (last - i) + 1>();
      m32a<A_WO + 8 * i, i == 0>(acc[0], b[i][0]); m32a<A_WO + 8 * i + 4, i == 0>(acc[1], b[i][0]);
      wv<2 * (last - i)>();
      m32a<A_WO + 8 * i, i == 0>(acc[2], b[i][1]); m32a<A_WO + 8 * i + 4, i == 0>(acc[3], b[i][1]);
      if constexpr (i + WN < 8) ld(std::integral_constant<int, i + WN>{});
    });
    nop<11>();
    sfor<0, 4>([&](auto k) { pin(acc[k]); pin(xr[k]); });
    stamp(x);
    reduce4(x, acc, [](auto) {});
    pin(acc[0]); fstamp(x);
    if (!kvw) {
      const u32x4 x2[2] = {xr[0], xr[1]};
      res_epilogue<false>(x, acc[0], x2, OFF_X + (2 * L + 1) * SZ_X, OFF_ST + (2 * L + 1) * SZ_ST, tt, nt);
      fstamp(x);
      wv<0>();
      fstamp(x);
      signal(x, FL_X1(L, x.c), 1);
    } else {
      sfor<0, 4>([&](auto q) {
        u32x4 y;
#pragma unroll
        for (int i = 0; i < 4; ++i) y[i] = __float_as_uint(acc[0][4 * q + i] + __uint_as_float(xr[q][i]));
        st4<1024 * q>(y, x.vl16, x.rs, sr);
      });
      fstamp(x); fstamp(x);
      bar();
    }
    fstamp(x);
  } else {
    stamp(x); stamp(x); fstamp(x); fstamp(x); fstamp(x); fstamp(x);
  }
}

DI void gu_tile(const f32x4 (&acc)[2][2], const char* csp, const float (&mean)[2], const float (&r2)[2], u32x2 (&hp)[2]) {
  const f32x4 cg = *(const f32x4*)csp, cu = *(const f32x4*)(csp + 64);
#pragma unroll
  for (int u = 0; u < 2; ++u) {
    const f32x2 nm = {-mean[u], -mean[u]}, rr = {r2[u], r2[u]};
#pragma unroll
    for (int e = 0; e < 2; ++e) {
      const f32x2 g = __builtin_elementwise_fma(nm, f32x2{cg[2 * e], cg[2 * e + 1]}, f32x2{acc[0][u][2 * e], acc[0][u][2 * e + 1]});
      const f32x2 v = __builtin_elementwise_fma(nm, f32x2{cu[2 * e], cu[2 * e + 1]}, f32x2{acc[1][u][2 * e], acc[1][u][2 * e + 1]});
      const f32x2 y = g * rr * v;
      hp[u][e] = cvtpk(y[0], y[1]);
    }
  }
}

DI void phase_gu(Ctx& x, const int L) {
  const int w = x.w, l = x.l, g = l >> 4, c16 = l & 15, m = x.m, tt = 4 * x.c + w;
  await(x, FL_X1(L, x.c), 14);
  stamp(x);
  constexpr int NXV = 11;
  u32x4 xf[NXV][2]; f32x2 sp[2][4];
  const unsigned sx = OFF_X + (2 * L + 1) * SZ_X + tt * 28 * KB, st = OFF_ST + (2 * L + 1) * SZ_ST + tt * 32 * 8;
  auto ldx = [&](auto k, auto u) {
    if constexpr (k < NXV) ld4<(k & 1) * 2048 + 256 * u>(xf[k][u], x.vg16, x.rs, sx + (k >> 1) * 4096);
    else if constexpr (k < 14) lda1<A_GX + 8 * (k - NXV) + 4 * u, (k & 1) * 2048 + 256 * u>(x.vg16, x.rs, sx + (k >> 1) * 4096);
    else { constexpr int i = k - 14; ld2<128 * u>(sp[u][i], (((i < 3 || g < 2) ? g + 4 * i : g) * 2048 + c16) * 8, x.rs, st); }
  };
  using I0 = std::integral_constant<int, 0>; using I1 = std::integral_constant<int, 1>;
  constexpr int XA = 4;
  sfor<0, XA>([&](auto k) { ldx(k, I0{}); ldx(k, I1{}); });
  const unsigned a0 = x.vl16, a1 = x.vl16 + 65536, a2 = x.vl16 + 131072;
  constexpr int DP = 6;
  u32x4 wf[8];
  auto rd = [&](auto ii) {
    constexpr int i = ii, slot = i >= 160 ? i - 160 : i;
    if constexpr (slot < 64) dsr<slot * 1024>(wf[i & 7], a0); else if constexpr (slot < 128) dsr<(slot - 64) * 1024>(wf[i & 7], a1); else dsr<(slot - 128) * 1024>(wf[i & 7], a2);
  };
  auto mm = [&](f32x4& c, const u32x4& a, auto k, auto u) {
    if constexpr (k < NXV) m16<k == 0>(c, a, xf[k][u]); else m16a<false, A_GX + 8 * (k - NXV) + 4 * u>(c, a);
  };
  f32x4 aa[5][2][2];
  float mean[2], r2[2];
  sfor<0, DP>(rd);
  sfor<0, 140>([&](auto i) {
    constexpr int KS = i / 10, s = i % 10, t = s >> 1, mat = i & 1;
    using K = std::integral_constant<int, KS>;
    constexpr auto late = [](int k) { return k < 0 ? 0 : (k <= 10 ? 1 : 0) + (k >= 6 && k <= 9 ? 1 : 0); };
    constexpr auto all = [](int k) { return k < 0 ? 2 : (k + XA < 14 ? 2 : 0) + (k <= 10 ? 2 : 0) + (k >= 6 && k <= 9 ? 2 : 0); };
    if constexpr (s == 0) {
      wv<late(KS - 4) + all(KS - 3) + all(KS - 2) + all(KS - 1)>();
      if constexpr (KS == 6) asm volatile("s_barrier" ::: "memory");
      if constexpr (KS == 10) asm volatile("s_barrier" ::: "memory");
    }
    rd(std::integral_constant<int, i + DP>{}); wl<DP>();
    mm(aa[t][mat][0], wf[i & 7], K{}, I0{});
    if constexpr (s == 1 && KS + XA < 14) ldx(std::integral_constant<int, KS + XA>{}, I0{});
    if constexpr (s == 6 && KS + XA < 14) ldx(std::integral_constant<int, KS + XA>{}, I1{});
    if constexpr (KS <= 5 && (s == 3 || s == 8)) pf_wgu2<2 * KS + (s == 8)>(x);
    if constexpr (KS == 6 && s == 3) ldl<1>(x.vl16, x.rs, OFF_CSGU + m * 896, 40 * KB);
    if constexpr (KS >= 6 && KS <= 10 && (s == 3 || s == 8) && !(KS == 6 && s == 3)) pf_wgu3<2 * (KS - 6) + (s == 8) - 1>(x);
    if constexpr (KS >= 6 && KS <= 9 && s == 4) ldx(std::integral_constant<int, 8 + KS>{}, I0{});
    if constexpr (KS >= 6 && KS <= 9 && s == 9) ldx(std::integral_constant<int, 8 + KS>{}, I1{});
    if constexpr (KS == 13 && (s == 2 || s == 6)) {
      constexpr int u = s == 6;
      if constexpr (u == 0) wv<2>();
      sfor<0, 4>([&](auto e) { pin(sp[u][e]); });
      float sm = sp[u][0][0] + sp[u][1][0] + sp[u][2][0], q = sp[u][0][1] + sp[u][1][1] + sp[u][2][1];
      if (g < 2) { sm += sp[u][3][0]; q += sp[u][3][1]; }
      sm = sum32(sum16(sm)); q = sum32(sum16(q));
      float rs; row_stats(sm, q, mean[u], rs); r2[u] = rs * rs;
      pin(mean[u]); pin(r2[u]);
    }
    mm(aa[t][mat][1], wf[i & 7], K{}, I1{}); SB();
  });
  fstamp(x);
  wv<0>();
  asm volatile("s_barrier" ::: "memory");
  stamp(x);
  const unsigned H = OFF_H + L * SZ_H + tt * 112 * KB;
  const int gl = (m & 1) ? 7 * m : 7 * m + 6, gp = (m & 1) ? 7 * m + 1 : 7 * m;
  const char* csp = lds + 40 * KB + g * 16;
  auto pair = [&](const f32x4 (&t0)[2][2], const f32x4 (&t1)[2][2], int lt) {
    u32x2 h0[2], h1[2];
    gu_tile(t0, csp + lt * 128, mean, r2, h0); gu_tile(t1, csp + lt * 128 + 128, mean, r2, h1);
    st4<0>(u32x4{h0[0][0], h0[0][1], h1[0][0], h1[0][1]}, x.vg16, x.rs, H + (gp + lt - 1) * 1024);
    st4<256>(u32x4{h0[1][0], h0[1][1], h1[1][0], h1[1][1]}, x.vg16, x.rs, H + (gp + lt - 1) * 1024);
  };
  {
    sfor<0, 20>([&](auto i) { pin(aa[i >> 2][(i >> 1) & 1][i & 1]); });
    u32x2 hl[2];
    gu_tile(aa[0], csp, mean, r2, hl);
    const unsigned sl = H + (gl >> 1) * 2048 + (gl & 1) * 8;
    st2<0>(hl[0], x.vg16, x.rs, sl); st2<256>(hl[1], x.vg16, x.rs, sl);
    pair(aa[1], aa[2], 1);
    pair(aa[3], aa[4], 3);
  }
  asm volatile("s_waitcnt lgkmcnt(%c0)" :: "i"(0) : "memory");
  SB();
  fstamp(x);
  f32x4 ab[2][2][2];
  const Two wd = base_wd(x);
  sfor<140, 196>([&](auto i) {
    constexpr int j = i - 140, KS = j / 4, t = (j % 4) >> 1, mat = i & 1;
    using K = std::integral_constant<int, KS>;
    if constexpr (j == 24) {
      fstamp(x);
      wv<12>();
      asm volatile("s_barrier" ::: "memory");
      count(x, FL_H(L, x.c, 0), 1);
      SB();
      fstamp(x);
    }
    if constexpr (i + DP < 196) { rd(std::integral_constant<int, i + DP>{}); wl<DP>(); } else wl<195 - i>();
    mm(ab[t][mat][0], wf[i & 7], K{}, I0{});
    if constexpr ((j & 1) == 0 && j < 20) pf_wd<46 + j / 2, 47 + j / 2>(x, wd);
    if constexpr ((j & 1) == 0 && j >= 20 && j < 48) pf_wq<(j - 20) / 2>(x);
    if constexpr (j == 55) ldflag(x.fa, x.rs, OFF_FLAG + FL_H(L, x.c, 0));
    mm(ab[t][mat][1], wf[i & 7], K{}, I1{}); SB();
  });
  nop<7>();
  stamp(x);
  sfor<0, 8>([&](auto i) { pin(ab[i >> 2][(i >> 1) & 1][i & 1]); });
  pair(ab[0], ab[1], 5);
}

DI void phase_down(Ctx& x, const int L) {
  const int w = x.w;
  const bool kvw = x.m >= 14;
  if (kvw && L > 0) {
    wv<0>(); signal(x, FL_H(L, x.c, 1), 1);
    stamp(x); fstamp(x); fstamp(x); stamp(x);
    return;
  }
  const int th = kvw ? x.m - 14 : x.m / 7, ct = kvw ? 7 : x.m % 7, tt0 = 4 * x.c + 2 * th, tt = tt0 + (w >> 1), tto = tt0 + ((w >> 1) ^ 1), nt = 2 * ct + (w & 1);
  wv<2>();
  recheck(x, x.fa, FL_H(L, x.c, 0), 16);
  stamp(x);
  constexpr int WN = 7;
  u32x4 b[WN][2], xr[4];
  const unsigned s0 = OFF_H + L * SZ_H + (tt * 112 + 28 * w) * KB, s1 = OFF_H + L * SZ_H + (tto * 112 + 28 * w) * KB;
  auto ld = [&](auto o) { constexpr int i = dn_ks(o);
    ld4<(i & 3) * 1024>(b[o % WN][0], x.vl16, x.rs, s0 + (i >> 2) * 4096); ld4<(i & 3) * 1024>(b[o % WN][1], x.vl16, x.rs, s1 + (i >> 2) * 4096); };
  sfor<0, WN>(ld);
  f32x16 acc[4];
  unsigned fb;
  sfor<0, 28>([&](auto i) {
    constexpr int last = i + WN - 1 < 27 ? i + WN - 1 : 27, ex = (i >= 7 && i <= 12) ? 1 : ((i >= 21) ? 4 : 0);
    wv<2 * (last - i) + 1 + ex>();
    if constexpr (i == 0) signal(x, FL_H(L, x.c, 1), 1);
    m32a<A_WD + 8 * i, i == 0>(acc[0], b[i % WN][0]); m32a<A_WD + 8 * i + 4, i == 0>(acc[1], b[i % WN][0]);
    wv<2 * (last - i) + ex>();
    m32a<A_WD + 8 * i, i == 0>(acc[2], b[i % WN][1]); m32a<A_WD + 8 * i + 4, i == 0>(acc[3], b[i % WN][1]);
    if constexpr (i == 6) ldflag(fb, x.rs, OFF_FLAG + FL_H(L, x.c, 1));
    if constexpr (i + WN == 20) { fstamp(x); recheck(x, fb, FL_H(L, x.c, 1), 16); fstamp(x); }
    if constexpr (i + WN < 28) ld(std::integral_constant<int, i + WN>{});
    if constexpr (i + WN == 27) {
      const unsigned sr = kvw ? OFF_KVR + (tt * 2 + (w & 1)) * 4 * KB : OFF_X + (2 * L + 1) * SZ_X + (tt * 28 + 2 * nt) * KB;
      sfor<0, 4>([&](auto q) { ld4<1024 * q>(xr[q], x.vl16, x.rs, sr); });
    }
  });
  nop<11>();
  sfor<0, 4>([&](auto k) { pin(acc[k]); });
  stamp(x);
  reduce4(x, acc, [](auto) {});
  wv<0>();
  sfor<0, 4>([&](auto k) { pin(xr[k]); });
  const u32x4 x2[2] = {xr[0], xr[1]};
  if (L > 0) {
    res_epilogue<true>(x, acc[0], x2, 0, 0, tt, nt);
  } else if (!kvw) {
    res_epilogue<false>(x, acc[0], x2, OFF_X + 2 * SZ_X, OFF_ST + 2 * SZ_ST, tt, nt);
    wv<0>();
    signal(x, FL_X(x.c, th), 1);
  } else {
    const int h = x.l >> 5, r = x.l & 31;
    f32x4 cs[4]; f32x2 sp[7];
    sfor<0, 4>([&](auto a) { ld4f<32 * a>(cs[a], h * 16, x.rs, OFF_CSQ + (16 + (w & 1)) * 128); });
    __builtin_amdgcn_s_sleep(KV_DELAY);
    for (unsigned spin = 0; spin < (1u << 16); ++spin) {
      sfor<0, 7>([&](auto i) { ld2<0>(sp[i], (h * 7 * 2048 + r) * 8, x.rs, OFF_ST + 2 * SZ_ST + (i * 2048 + tt * 32) * 8); });
      wv<0>();
      bool ok = true;
      sfor<0, 7>([&](auto i) { pin(sp[i]); ok = ok && __float_as_uint(sp[i][1]) != 0xffffffffu; });
      if (__all(ok)) break;
      __builtin_amdgcn_s_sleep(4);
    }
    sfor<0, 4>([&](auto a) { pin(cs[a]); });
    float s = 0.f, q = 0.f, mean, rstd;
#pragma unroll
    for (int i = 0; i < 7; ++i) { s += sp[i][0]; q += sp[i][1]; }
    row_stats(sum32(s), sum32(q), mean, rstd);
    u32x4 y[2];
#pragma unroll
    for (int d = 0; d < 8; ++d) {
      const float v0 = acc[0][2 * d] + __uint_as_float(xr[d >> 1][2 * (d & 1)]), v1 = acc[0][2 * d + 1] + __uint_as_float(xr[d >> 1][2 * (d & 1) + 1]);
      y[d >> 2][d & 3] = cvtpk((v0 - mean * cs[d >> 1][2 * (d & 1)]) * rstd, (v1 - mean * cs[d >> 1][2 * (d & 1) + 1]) * rstd);
    }
    if (h) { y[1][2] = 0; y[1][3] = 0; }
    if ((w & 1) == 0) {
      float n2 = 0.f;
#pragma unroll
      for (int d = 0; d < 8; ++d) n2 = dot2(y[d >> 2][d & 3], y[d >> 2][d & 3], n2);
      n2 = sum32(n2);
      const unsigned nb = umax64(n2 < 3.0e38f ? __float_as_uint(n2) : 0x7f800000u);
      if (x.l == 0) st1(__uint_as_float(nb), 0, x.rs, OFF_KMAX + (128 + tt) * 4);
      if (h) y[1][2] = 0x3f80u;
    } else {
      if (h) y[1][2] = 0x3f80u;
      char* im = lds + w * 2048;
#pragma unroll
      for (int a = 0; a < 4; ++a) *(u32x2*)(im + r * 64 + 16 * a + 8 * h) = u32x2{y[a >> 1][2 * (a & 1)], y[a >> 1][2 * (a & 1) + 1]};
      asm volatile("s_waitcnt lgkmcnt(0)" ::: "memory");
      const char* tb = im + (4 * h + ((x.l >> 2) & 3)) * 64 + ((x.l >> 4) & 1) * 32 + (x.l & 3) * 8;
#pragma unroll
      for (int s2 = 0; s2 < 2; ++s2) {
        const u32x2 lo = trread(tb + s2 * 1024), hi = trread(tb + s2 * 1024 + 512);
        y[s2] = u32x4{lo[0], lo[1], hi[0], hi[1]};
      }
    }
    const unsigned so = OFF_KV + SZ_KV + (tt * 4 + 2 * (w & 1)) * KB;
    st4<0>(y[0], x.vl16, x.rs, so); st4<1024>(y[1], x.vl16, x.rs, so);
  }
}
DI void phase_ffn(Ctx& x, const int L) { phase_gu(x, L); stamp(x); fresh(x); phase_down(x, L); }

#define PHASE(f) { Ctx y = x; fresh(y); int Lo = L; asm volatile("" : "+s"(Lo)); f(y, Lo); }
__global__ __launch_bounds__(256, 1) void k_main(char* ws, char* out, int ph0, int ph1) {
  Ctx x;
  x.ws = ws;
  x.rs = __builtin_amdgcn_make_buffer_rsrc(ws, 0, WS_USED, 0x00020000);
  x.ro = __builtin_amdgcn_make_buffer_rsrc(out, 0, S * D * 2, 0x00020000);
  x.l = threadIdx.x & 63;
  x.w = __builtin_amdgcn_readfirstlane(threadIdx.x >> 6);
  asm volatile("s_nop 4" : "+s"(x.w));
  { const int b = blockIdx.x, r = b >> 3; x.c = 2 * (b & 7) + (r >> 4); x.m = r & 15; }
  fresh(x);
  stamp(x);
  switch (ph0 & 3) {
    case 0: sfor<0, 14>([&](auto j) { pf_wq<j>(x); }); if (ph0) { wv<0>(); bar(); } break;
    case 1: pf_wo(x); pf_wgu1(x); wv<0>(); pf_wd<0, 46>(x, base_wd(x)); wv<0>(); bar(); break;
    default: pf_wgu1(x); wv<0>(); pf_wd<0, 46>(x, base_wd(x)); wv<0>(); bar(); break;
  }
  for (int L = ph0 >> 2; L < 2; ++L) {
    const int p = 4 * L;
    if (p + 0 >= ph0 && p + 0 < ph1) PHASE(phase_att)
    if (p + 1 >= ph0 && p + 1 < ph1) PHASE(phase_oproj)
    if (p + 2 >= ph0 && p + 2 < ph1) PHASE(phase_ffn)
  }
  stamp_flush(x);
}

extern "C" void kernel_launch(void* const* d_in, const int* in_sizes, int n_in, void* d_out, int out_size, void* d_ws, size_t ws_size,
                              hipStream_t stream) {
  PrepArgs a;
  a.x = (const uint16_t*)d_in[0]; a.wq = (const uint16_t*)d_in[1]; a.wk = (const uint16_t*)d_in[2]; a.wv = (const uint16_t*)d_in[3];
  a.wo = (const uint16_t*)d_in[4]; a.wg = (const uint16_t*)d_in[6]; a.wu = (const uint16_t*)d_in[7]; a.wd = (const uint16_t*)d_in[8];
  a.ws = (char*)d_ws;
  (void)hipFuncSetAttribute((const void*)k_main, hipFuncAttributeMaxDynamicSharedMemorySize, 160 * 1024);
  k_prep<<<PREP_TASKS, 256, 0, stream>>>(a);
  k_main<<<256, 256, 160 * 1024, stream>>>((char*)d_ws, (char*)d_out, 0, 8);
}
```

```cpp
#include <hip/hip_runtime.h>
#include <stdint.h>
#include <type_traits>

typedef float    f32x16 __attribute__((ext_vector_type(16)));
typedef float    f32x4  __attribute__((ext_vector_type(4)));
typedef float    f32x2  __attribute__((ext_vector_type(2)));
typedef unsigned u32x4  __attribute__((ext_vector_type(4)));
typedef unsigned u32x2  __attribute__((ext_vector_type(2)));
typedef __bf16   bf16x2 __attribute__((ext_vector_type(2)));
typedef __amdgpu_buffer_rsrc_t rsrc_t;
#define DI __device__ __forceinline__
#define GAS __attribute__((address_space(1)))
#define SB() __builtin_amdgcn_sched_barrier(0)

constexpr int S = 2048, D = 448, F = 1792;
constexpr unsigned KB = 1024;
constexpr unsigned OFF_FLAG = 0;
constexpr unsigned OFF_CSQ  = 64 * KB;
constexpr unsigned OFF_CSGU = OFF_CSQ + 4 * KB;
constexpr unsigned OFF_SLOT = OFF_CSGU + 32 * KB;
constexpr unsigned OFF_TAG  = OFF_SLOT + 16 * KB;
constexpr unsigned OFF_WQ   = 128 * KB;
constexpr unsigned OFF_WO   = OFF_WQ + 18 * 28 * KB;
constexpr unsigned OFF_WGU  = OFF_WO + 14 * 32 * KB;
constexpr unsigned OFF_WD   = OFF_WGU + 16 * 196 * KB;
constexpr unsigned SZ_X = 64 * 28 * KB, SZ_ST = 14 * 2048 * 8, SZ_O = 64 * 32 * KB, SZ_H = 64 * 112 * KB, SZ_KV = 64 * 4 * KB;
constexpr unsigned OFF_X    = OFF_WD + 14 * 112 * KB;
constexpr unsigned OFF_ST   = OFF_X + 4 * SZ_X;
constexpr unsigned OFF_O    = OFF_ST + 4 * SZ_ST;
constexpr unsigned OFF_H    = OFF_O + 2 * SZ_O;
constexpr unsigned OFF_KV   = OFF_H + 2 * SZ_H;
constexpr unsigned OFF_TS   = OFF_KV + 2 * SZ_KV;
constexpr unsigned WS_USED  = OFF_TS + 520 * KB;
DI constexpr unsigned FL_X(int c, int th)         { return (c * 16 + th) * 256u; }
DI constexpr unsigned FL_O(int L, int c, int th)  { return (c * 16 + 2 + L * 2 + th) * 256u; }
DI constexpr unsigned FL_X1(int L, int c)         { return (c * 16 + 6 + L) * 256u; }
DI constexpr unsigned FL_H(int L, int c, int p)   { return (c * 16 + 8 + L * 2 + p) * 256u; }
DI constexpr unsigned FL_W(int c)                 { return (c * 16 + 12) * 256u; }

DI int dn_pos(int ks) { const int q = ks / 28, kl = (ks % 28) >> 1, P = kl / 7, j = kl % 7;
  return 28 * q + 2 * ((j == 2 || j == 6) ? 10 + 2 * P + (j == 6) : 5 * P + (j > 2 ? j - 1 : j)) + (ks & 1); }
constexpr float QSCALE = 0.18898223650461362f * 1.4426950408889634f;
constexpr float M_LIMIT = 48.0f;

template <int I, int N, class Fn> DI void sfor(Fn&& f) { if constexpr (I < N) { f(std::integral_constant<int, I>{}); sfor<I + 1, N>(f); } }

DI unsigned cvtpk(float lo, float hi) { f32x2 v = {lo, hi}; bf16x2 b = __builtin_convertvector(v, bf16x2); return __builtin_bit_cast(unsigned, b); }
DI float bflo(unsigned u) { return __uint_as_float(u << 16); }
DI float bfhi(unsigned u) { return __uint_as_float(u & 0xffff0000u); }
DI float dot2(unsigned a, unsigned b, float c) { return __builtin_amdgcn_fdot2_f32_bf16(__builtin_bit_cast(bf16x2, a), __builtin_bit_cast(bf16x2, b), c, false); }
DI float sum32(float v) { auto r = __builtin_amdgcn_permlane32_swap(__float_as_uint(v), __float_as_uint(v), false, false); return __uint_as_float(r[0]) + __uint_as_float(r[1]); }
DI float sum16(float v) { auto r = __builtin_amdgcn_permlane16_swap(__float_as_uint(v), __float_as_uint(v), false, false); return __uint_as_float(r[0]) + __uint_as_float(r[1]); }
DI float max32(float v) { auto r = __builtin_amdgcn_permlane32_swap(__float_as_uint(v), __float_as_uint(v), false, false); return fmaxf(__uint_as_float(r[0]), __uint_as_float(r[1])); }

DI unsigned umax16(unsigned v) {
  v = max(v, (unsigned)__builtin_amdgcn_update_dpp(0, (int)v, 0x128, 0xf, 0xf, false));
  v = max(v, (unsigned)__builtin_amdgcn_update_dpp(0, (int)v, 0x124, 0xf, 0xf, false));
  v = max(v, (unsigned)__builtin_amdgcn_update_dpp(0, (int)v, 0x122, 0xf, 0xf, false));
  v = max(v, (unsigned)__builtin_amdgcn_update_dpp(0, (int)v, 0x121, 0xf, 0xf, false));
  return v;
}
DI unsigned umax64(unsigned v) {
  v = umax16(v);
  auto a = __builtin_amdgcn_permlane16_swap(v, v, false, false); v = max(a[0], a[1]);
  auto b = __builtin_amdgcn_permlane32_swap(v, v, false, false); return max(b[0], b[1]);
}

typedef short s16x4 __attribute__((ext_vector_type(4)));
DI u32x2 trread(const char* p) { return __builtin_bit_cast(u32x2, __builtin_amdgcn_ds_read_tr16_b64_v4i16((__attribute__((address_space(3))) s16x4*)p)); }
DI u32x4 trfrag(const char* lo, const char* hi) { const u32x2 a = trread(lo), b = trread(hi); return u32x4{a[0], a[1], b[0], b[1]}; }
DI void row_stats(float s, float q, float& mean, float& rstd) {
  mean = s * (1.0f / D);
  const float var = fmaxf(q * (1.0f / D) - mean * mean, 0.f);
  rstd = __builtin_amdgcn_rsqf(var + 1e-5f);
}
DI float fragsum(const u32x4& f, float s) {
#pragma unroll
  for (int d = 0; d < 4; ++d) s = dot2(f[d], 0x3f803f80u, s);
  return s;
}
struct Args { const char *x, *wq, *wk, *wv, *wo, *wg, *wu, *wd; char* ws; char* out; };

#define ACLOB "a0","a1","a2","a3","a4","a5","a6","a7","a8","a9","a10","a11","a12","a13","a14","a15","a16","a17","a18","a19","a20","a21","a22","a23","a24","a25","a26","a27","a28","a29","a30","a31","a32","a33","a34","a35","a36","a37","a38","a39","a40","a41","a42","a43","a44","a45","a46","a47","a48","a49","a50","a51","a52","a53","a54","a55","a56","a57","a58","a59","a60","a61","a62","a63","a64","a65","a66","a67","a68","a69","a70","a71","a72","a73","a74","a75","a76","a77","a78","a79","a80","a81","a82","a83","a84","a85","a86","a87","a88","a89","a90","a91","a92","a93","a94","a95","a96","a97","a98","a99","a100","a101","a102","a103","a104","a105","a106","a107","a108","a109","a110","a111","a112","a113","a114","a115","a116","a117","a118","a119","a120","a121","a122","a123","a124","a125","a126","a127","a128","a129","a130","a131","a132","a133","a134","a135","a136","a137","a138","a139","a140","a141","a142","a143","a144","a145","a146","a147","a148","a149","a150","a151","a152","a153","a154","a155","a156","a157","a158","a159","a160","a161","a162","a163","a164","a165","a166","a167","a168","a169","a170","a171","a172","a173","a174","a175","a176","a177","a178","a179","a180","a181","a182","a183","a184","a185","a186","a187","a188","a189","a190","a191","a192","a193","a194","a195","a196","a197","a198","a199","a200","a201","a202","a203","a204","a205","a206","a207","a208","a209","a210","a211","a212","a213","a214","a215","a216","a217","a218","a219","a220","a221","a222","a223","a224","a225","a226","a227","a228","a229","a230","a231","a232","a233","a234","a235","a236","a237","a238","a239","a240","a241","a242","a243","a244","a245","a246","a247","a248","a249","a250","a251","a252","a253","a254","a255"

template <int IMM> DI void ld4(u32x4& d, unsigned vo, rsrc_t rs, unsigned so) {
  asm volatile("buffer_load_dwordx4 %0, %1, %2, %3 offen offset:%c4 sc1" : "=v"(d) : "v"(vo), "s"(rs), "s"(so), "i"(IMM));
}
template <int IMM> DI void ld2(f32x2& d, unsigned vo, rsrc_t rs, unsigned so) {
  asm volatile("buffer_load_dwordx2 %0, %1, %2, %3 offen offset:%c4 sc1" : "=v"(d) : "v"(vo), "s"(rs), "s"(so), "i"(IMM));
}
template <int IMM> DI void ld4i(u32x4& d, unsigned vo, rsrc_t rs, unsigned so) {
  asm volatile("buffer_load_dwordx4 %0, %1, %2, %3 offen offset:%c4" : "=v"(d) : "v"(vo), "s"(rs), "s"(so), "i"(IMM));
}
template <int IMM> DI void ld2i(u32x2& d, unsigned vo, rsrc_t rs, unsigned so) {
  asm volatile("buffer_load_dwordx2 %0, %1, %2, %3 offen offset:%c4" : "=v"(d) : "v"(vo), "s"(rs), "s"(so), "i"(IMM));
}
template <int IMM> DI void ld2u(u32x2& d, unsigned vo, rsrc_t rs, unsigned so) {
  asm volatile("buffer_load_dwordx2 %0, %1, %2, %3 offen offset:%c4 sc1" : "=v"(d) : "v"(vo), "s"(rs), "s"(so), "i"(IMM));
}
template <int IMM> DI void ld4f(f32x4& d, unsigned vo, rsrc_t rs, unsigned so) {
  asm volatile("buffer_load_dwordx4 %0, %1, %2, %3 offen offset:%c4 sc1" : "=v"(d) : "v"(vo), "s"(rs), "s"(so), "i"(IMM));
}
DI void ld1(float& d, unsigned vo, rsrc_t rs, unsigned so) {
  asm volatile("buffer_load_dword %0, %1, %2, %3 offen sc1" : "=v"(d) : "v"(vo), "s"(rs), "s"(so));
}
template <int AR, int IMM> DI void lda(unsigned vo, rsrc_t rs, unsigned so) {
  asm volatile("buffer_load_dwordx4 a[%c3:%c4], %0, %1, %2 offen offset:%c5 sc1" :: "v"(vo), "s"(rs), "s"(so), "i"(AR), "i"(AR + 3), "i"(IMM) : ACLOB);
}
template <int N> DI void ldl(unsigned vo, rsrc_t rs, unsigned so, unsigned lds) {
  asm volatile("s_mov_b32 m0, %3\n\ts_nop 0\n\tbuffer_load_dwordx4 %0, %1, %2 offen sc1 lds" :: "v"(vo), "s"(rs), "s"(so), "s"(lds) : "memory");
  if constexpr (N > 1) asm volatile("buffer_load_dwordx4 %0, %1, %2 offen offset:1024 sc1 lds" :: "v"(vo), "s"(rs), "s"(so) : "memory");
  if constexpr (N > 2) asm volatile("buffer_load_dwordx4 %0, %1, %2 offen offset:2048 sc1 lds" :: "v"(vo), "s"(rs), "s"(so) : "memory");
  if constexpr (N > 3) asm volatile("buffer_load_dwordx4 %0, %1, %2 offen offset:3072 sc1 lds" :: "v"(vo), "s"(rs), "s"(so) : "memory");
}
template <int IMM> DI void st4(const u32x4& d, unsigned vo, rsrc_t rs, unsigned so) {
  asm volatile("buffer_store_dwordx4 %0, %1, %2, %3 offen offset:%c4 sc1\n\ts_nop 1" :: "v"(d), "v"(vo), "s"(rs), "s"(so), "i"(IMM) : "memory");
}
template <int IMM> DI void st4p(const u32x4& d, unsigned vo, rsrc_t rs, unsigned so) {
  asm volatile("buffer_store_dwordx4 %0, %1, %2, %3 offen offset:%c4\n\ts_nop 1" :: "v"(d), "v"(vo), "s"(rs), "s"(so), "i"(IMM) : "memory");
}
template <int IMM> DI void st2(const u32x2& d, unsigned vo, rsrc_t rs, unsigned so) {
  asm volatile("buffer_store_dwordx2 %0, %1, %2, %3 offen offset:%c4 sc1" :: "v"(d), "v"(vo), "s"(rs), "s"(so), "i"(IMM) : "memory");
}
DI void st1(float d, unsigned vo, rsrc_t rs, unsigned so) {
  asm volatile("buffer_store_dword %0, %1, %2, %3 offen sc1" :: "v"(d), "v"(vo), "s"(rs), "s"(so) : "memory");
}
template <int N> DI void wv() { asm volatile("s_waitcnt vmcnt(%c0)" :: "i"(N) : "memory"); SB(); }
template <int N> DI void wl() { asm volatile("s_waitcnt lgkmcnt(%c0)" :: "i"(N) : "memory"); SB(); }
template <class T> DI void pin(T& x) { asm volatile("" : "+v"(x)); }
DI void bar() { asm volatile("s_waitcnt lgkmcnt(0)\n\ts_barrier" ::: "memory"); }
template <int N> DI void nop() { asm volatile("s_nop %c0" :: "i"(N)); SB(); }

template <int OFF> DI void trd(u32x2& r, unsigned a) { asm volatile("ds_read_b64_tr_b16 %0, %1 offset:%c2" : "=v"(r) : "v"(a), "i"(OFF)); }
template <int OFF> DI void dsr(u32x4& r, unsigned a) { asm volatile("ds_read_b128 %0, %1 offset:%c2" : "=v"(r) : "v"(a), "i"(OFF)); }

DI void m32(f32x16& c, const u32x4& a, const u32x4& b)  { asm volatile("v_mfma_f32_32x32x16_bf16 %0, %1, %2, %0" : "+v"(c) : "v"(a), "v"(b)); }
DI void m32z(f32x16& c, const u32x4& a, const u32x4& b) { asm volatile("v_mfma_f32_32x32x16_bf16 %0, %1, %2, 0" : "=&v"(c) : "v"(a), "v"(b)); }
template <int AR, bool Z> DI void m32a(f32x16& c, const u32x4& b) {
  if constexpr (Z) asm volatile("v_mfma_f32_32x32x16_bf16 %0, a[%c2:%c3], %1, 0" : "=&v"(c) : "v"(b), "i"(AR), "i"(AR + 3));
  else             asm volatile("v_mfma_f32_32x32x16_bf16 %0, a[%c2:%c3], %1, %0" : "+v"(c) : "v"(b), "i"(AR), "i"(AR + 3));
}
template <int AR, bool Z> DI void m32b(f32x16& c, const u32x4& a) {
  if constexpr (Z) asm volatile("v_mfma_f32_32x32x16_bf16 %0, %1, a[%c2:%c3], 0" : "=&v"(c) : "v"(a), "i"(AR), "i"(AR + 3));
  else             asm volatile("v_mfma_f32_32x32x16_bf16 %0, %1, a[%c2:%c3], %0" : "+v"(c) : "v"(a), "i"(AR), "i"(AR + 3));
}
template <bool Z> DI void m16(f32x4& c, const u32x4& a, const u32x4& b) {
  if constexpr (Z) asm volatile("v_mfma_f32_16x16x32_bf16 %0, %1, %2, 0" : "=&v"(c) : "v"(a), "v"(b));
  else             asm volatile("v_mfma_f32_16x16x32_bf16 %0, %1, %2, %0" : "+v"(c) : "v"(a), "v"(b));
}

template <bool Z, int AR> DI void m16a(f32x4& c, const u32x4& a) {
  if constexpr (Z) asm volatile("v_mfma_f32_16x16x32_bf16 %0, %1, a[%c2:%c3], 0" : "=&v"(c) : "v"(a), "i"(AR), "i"(AR + 3));
  else             asm volatile("v_mfma_f32_16x16x32_bf16 %0, %1, a[%c2:%c3], %0" : "+v"(c) : "v"(a), "i"(AR), "i"(AR + 3));
}
DI void ldflag(unsigned& v, rsrc_t rs, unsigned so) { asm volatile("buffer_load_dword %0, off, %1, %2 sc1" : "=v"(v) : "s"(rs), "s"(so)); }

struct Ctx {
  rsrc_t rs;
  unsigned tlo, thi;
  unsigned vl16;
  unsigned vg16;
  int w, l, c, m;
  unsigned fa;
};
DI void fresh(Ctx& x) {
  asm volatile("" : "+s"(x.w), "+s"(x.c), "+s"(x.m));
  asm volatile("v_mbcnt_lo_u32_b32 %0, -1, 0\n\tv_mbcnt_hi_u32_b32 %0, -1, %0" : "=v"(x.l));
  x.vl16 = x.l * 16;
  const int g = x.l >> 4;
  x.vg16 = ((g >> 1) * 64 + (g & 1) * 32 + (x.l & 15)) * 16;
}
extern __shared__ __attribute__((aligned(16))) char lds[];
constexpr unsigned SCR = 112 * KB;

DI void pstamp(Ctx&) {}
DI void fstamp(Ctx&) {}
DI void stamp(Ctx&) {}
DI void stamp_flush(Ctx&) {}

DI void get_tag(Ctx& x) {
  unsigned lo, hi;
  asm volatile("v_accvgpr_read_b32 v255, a252\n\ts_nop 1\n\tv_readfirstlane_b32 %0, v255\n\tv_accvgpr_read_b32 v255, a253\n\ts_nop 1\n\tv_readfirstlane_b32 %1, v255\n\ts_nop 4"
               : "=s"(lo), "=s"(hi) :: "v255");
  const unsigned long long e = ((unsigned long long)hi << 32) | lo;
  unsigned long long n = e * 0x9E3779B97F4A7C15ull + 0x632BE59BD9B4E019ull;
  if (((n ^ e) >> 16) == 0) n += 0x10000;
  x.tlo = (unsigned)n; x.thi = (unsigned)(n >> 32);
}
DI bool wave0(const Ctx& x) { int w = x.w; asm volatile("" : "+s"(w)); return w == 0; }
DI void count(Ctx& x, unsigned fl, int n) {
  if (wave0(x)) {
    const unsigned long long mask = (1ull << n) - 1;
    unsigned long long keep;
    asm volatile("s_mov_b64 %0, exec\n\ts_mov_b64 exec, %5\n\tbuffer_atomic_add %1, %2, %3, %4 offen\n\ts_mov_b64 exec, %0"
                 : "=&s"(keep) : "v"(1u), "v"(x.l * 256), "s"(x.rs), "s"(OFF_FLAG + fl), "s"(mask) : "memory");
  }
}
DI void signal(Ctx& x, unsigned fl, int n) { bar(); count(x, fl, n); }
DI void await(Ctx& x, unsigned fl, unsigned target) {
  if (wave0(x)) {
#pragma nounroll
    for (unsigned spin = 0; spin < (1u << 18); ++spin) {
      unsigned v;
      asm volatile("buffer_load_dword %0, off, %1, %2 sc1\n\ts_waitcnt vmcnt(0)" : "=v"(v) : "s"(x.rs), "s"(OFF_FLAG + fl) : "memory");
      if ((unsigned)__builtin_amdgcn_readfirstlane(v) >= target) break;
      __builtin_amdgcn_s_sleep(1);
    }
  }
  bar();
}

DI void recheck(Ctx& x, unsigned v, unsigned fl, unsigned target) {
  pin(v);
#pragma nounroll
  for (unsigned spin = 0; (unsigned)__builtin_amdgcn_readfirstlane(v) < target && spin < (1u << 18); ++spin) {
    __builtin_amdgcn_s_sleep(2);
    asm volatile("buffer_load_dword %0, off, %1, %2 sc1\n\ts_waitcnt vmcnt(0)" : "=v"(v) : "s"(x.rs), "s"(OFF_FLAG + fl) : "memory");
  }
  SB();
}

template <class Hook> DI void reduce4(Ctx& x, f32x16 (&acc)[4], Hook&& hook) {
  sfor<1, 4>([&](auto k) {
    f32x4* p = (f32x4*)(lds + SCR + ((x.w ^ k) * 3 + k - 1) * 4096 + x.vl16);
#pragma unroll
    for (int q = 0; q < 4; ++q) p[q * 64] = f32x4{acc[k][4 * q], acc[k][4 * q + 1], acc[k][4 * q + 2], acc[k][4 * q + 3]};
    SB(); hook(k); SB();
  });
  bar();
  const f32x4* p = (const f32x4*)(lds + SCR + x.w * 3 * 4096 + x.vl16);
#pragma unroll
  for (int k = 0; k < 3; ++k)
#pragma unroll
    for (int q = 0; q < 4; ++q) {
      f32x4 v = p[k * 256 + q * 64];
#pragma unroll
      for (int i = 0; i < 4; ++i) acc[0][4 * q + i] += v[i];
    }
}

constexpr int A_WO = 184, A_WD = 0, A_GX = 224;
constexpr int dn_ks(int o) { return o < 20 ? 2 * ((o >> 1) % 5 + ((o >> 1) % 5 >= 2) + 7 * (o / 10)) + (o & 1) : 2 * (((o - 20) >> 1) * 4 - ((o - 20) >> 2) + 2) + (o & 1); }
static_assert(dn_ks(0) == 0 && dn_ks(4) == 6 && dn_ks(9) == 11 && dn_ks(10) == 14 && dn_ks(19) == 25 && dn_ks(20) == 4 && dn_ks(22) == 12 && dn_ks(24) == 18 && dn_ks(27) == 27, "");
struct Two { unsigned s0, s1; };
constexpr int SL_WQ = 56;
template <int J> DI void pf_wq(Ctx& x) {
  ldl<1>(x.vl16, x.rs, OFF_WQ + ((x.m & 7) * 56 + 14 * x.w + J) * KB, (SL_WQ + 14 * x.w + J) * KB);
}
DI Two base_wo(const Ctx& x) { const int ct = x.m % 7;
  return Two{OFF_WO + ((2 * ct + (x.w & 1)) * 32 + 8 * x.w) * KB, OFF_WO + ((2 * ct + ((x.w & 1) ^ 1)) * 32 + 8 * x.w) * KB}; }
DI void pf_wo(Ctx& x) {
  const Two b = base_wo(x);
  sfor<0, 8>([&](auto i) {
    lda<A_WO + 8 * i, (i & 3) * 1024>(x.vl16, x.rs, b.s0 + (i >> 2) * 4096);
    lda<A_WO + 8 * i + 4, (i & 3) * 1024>(x.vl16, x.rs, b.s1 + (i >> 2) * 4096);
  });
}
DI Two base_wd(const Ctx& x) { const int ct = x.m % 7;
  return Two{OFF_WD + ((2 * ct + (x.w & 1)) * 112 + 28 * x.w) * KB, OFF_WD + ((2 * ct + ((x.w & 1) ^ 1)) * 112 + 28 * x.w) * KB}; }
template <int I0, int I1> DI void pf_wd(Ctx& x, const Two& b) {
  sfor<I0, I1>([&](auto f) {
    constexpr int i = f >> 1;
    lda<A_WD + 4 * f, (i & 3) * 1024>(x.vl16, x.rs, ((f & 1) ? b.s1 : b.s0) + (i >> 2) * 4096);
  });
}
DI void pf_wgu1(Ctx& x) {
  for (int t = 0; t < 14; ++t) ldl<2>(x.vl16, x.rs, OFF_WGU + (x.m * 196 + 8 * t + 2 * x.w) * KB, (8 * t + 2 * x.w) * KB);
}
template <int J> DI void pf_wgu3(Ctx& x) {
  ldl<1>(x.vl16, x.rs, OFF_WGU + (x.m * 196 + 160 + 9 * x.w + J) * KB, (9 * x.w + J) * KB);
}

constexpr unsigned S_QP = SCR, S_KP = SCR + 16384, S_QF = SCR + 24576, S_ST = SCR + 32768, S_CS = SCR + 33792, S_KM = SCR + 36864, S_TR = SCR + 40960;
DI void kv_finish(Ctx& x, const float (&raw)[16], const f32x4 (&cs)[4], float mean, float rstd, bool isv, unsigned so, int slot) {
  const int h = x.l >> 5, r = x.l & 31;
  u32x4 y[2];
#pragma unroll
  for (int d = 0; d < 8; ++d)
    y[d >> 2][d & 3] = cvtpk((raw[2 * d] - mean * cs[d >> 1][2 * (d & 1)]) * rstd, (raw[2 * d + 1] - mean * cs[d >> 1][2 * (d & 1) + 1]) * rstd);
  if (h) { y[1][2] = 0; y[1][3] = 0; }
  unsigned nb = 0;
  if (!isv) {
    float n2 = 0.f;
#pragma unroll
    for (int d = 0; d < 8; ++d) n2 = dot2(y[d >> 2][d & 3], y[d >> 2][d & 3], n2);
    n2 = sum32(n2);
    nb = umax64(n2 < 3.0e38f ? __float_as_uint(n2) : 0x7f800000u);
    nb = cvtpk(1.01f * __builtin_amdgcn_sqrtf(__uint_as_float(nb)), 0.f);
    if (h) y[1][2] = 0x3f80u;
  } else {
    if (h) y[1][2] = 0x3f80u;
    char* im = lds + S_TR + slot * 2048;
#pragma unroll
    for (int a = 0; a < 4; ++a) *(u32x2*)(im + r * 64 + 16 * a + 8 * h) = u32x2{y[a >> 1][2 * (a & 1)], y[a >> 1][2 * (a & 1) + 1]};
    asm volatile("s_waitcnt lgkmcnt(0)" ::: "memory");
    const char* tb = im + (4 * h + ((x.l >> 2) & 3)) * 64 + ((x.l >> 4) & 1) * 32 + (x.l & 3) * 8;
    y[0] = trfrag(tb, tb + 512); y[1] = trfrag(tb + 1024, tb + 1536);
  }
  if (x.l == 0) ((unsigned*)(lds + S_KM))[slot] = nb;
  st4<0>(y[0], x.vl16, x.rs, so); st4<1024>(y[1], x.vl16, x.rs, so);
}
DI void kv_publish(Ctx& x, int L, int p) {
  if (wave0(x)) {
    const u32x2 k = *(const u32x2*)(lds + S_KM);
    if (x.l < 16) st2<0>(u32x2{(x.tlo & 0xffff0000u) | max(k[0], k[1]), x.thi}, x.l * 512, x.rs, OFF_SLOT + (L * 1024 + p) * 8);
  }
}
DI void q_finish(Ctx& x, const f32x16& qa, const f32x4 (&cq)[4], float mean, float rstd) {
  const int h = x.l >> 5;
  u32x4 qf[2];
  const f32x4* p = (const f32x4*)(lds + S_QP + x.w * 4096 + x.vl16);
  const float sc = rstd * QSCALE;
#pragma unroll
  for (int q = 0; q < 4; ++q) {
    const f32x4 o = p[q * 64];
    float v[4];
#pragma unroll
    for (int i = 0; i < 4; ++i) v[i] = (qa[4 * q + i] + o[i] - mean * cq[q][i]) * sc;
    qf[q >> 1][2 * (q & 1)] = cvtpk(v[0], v[1]); qf[q >> 1][2 * (q & 1) + 1] = cvtpk(v[2], v[3]);
  }
  if (h) { qf[1][2] = 0; qf[1][3] = 0; }
  float qn2 = 0.f, qn3 = 0.f;
#pragma unroll
  for (int d = 0; d < 4; ++d) { qn2 = dot2(qf[0][d], qf[0][d], qn2); qn3 = dot2(qf[1][d], qf[1][d], qn3); }
  qn2 = sum32(qn2 + qn3);
  if (h) qf[1][2] = cvtpk(1.01f * __builtin_amdgcn_sqrtf(qn2), 0.f);
  ((u32x4*)(lds + S_QF + x.w * 2048 + x.vl16))[0] = qf[0];
  ((u32x4*)(lds + S_QF + x.w * 2048 + x.vl16))[64] = qf[1];
}
DI void put_tile(char* p, const f32x16& t) {
#pragma unroll
  for (int q = 0; q < 4; ++q) ((f32x4*)p)[q * 64] = f32x4{t[4 * q], t[4 * q + 1], t[4 * q + 2], t[4 * q + 3]};
}

constexpr unsigned IMG_Q = 0, IMG_T = 56 * KB;
DI rsrc_t mkrs(const char* p, unsigned n) { return __builtin_amdgcn_make_buffer_rsrc((char*)p, 0, n, 0x00020000); }

DI void phase_pre(Ctx& x, const Args& a) {
  const int w = x.w, l = x.l, h = l >> 5, r = l & 31;
  const int th = x.m >> 3, hp = x.m & 7, tl = w & 1, kh = w >> 1, tt = 4 * x.c + 2 * th + tl, wi = 2 * x.c + th;
  const bool kvp = hp < 2;
  { unsigned long long pc; unsigned len;
    asm volatile("s_getpc_b64 %0\n.Lk_begin:\n\ts_mov_b32 %1, .Lk_end-.Lk_begin" : "=s"(pc), "=s"(len));
    const rsrc_t rc = mkrs((const char*)pc, len);
    asm volatile("buffer_load_dwordx4 a[112:115], %0, %1, %2 offen\n\tbuffer_load_dwordx4 a[112:115], %0, %1, %2 offen offset:1024"
                 :: "v"(x.vl16), "s"(rc), "s"((unsigned)(hp * 8 + 2 * w) * KB) : ACLOB); }
  u32x4 xf[14];
  { const rsrc_t rx = mkrs(a.x, S * D * 2);
    sfor<0, 14>([&](auto i) { ld4i<32 * i>(xf[i], r * 896 + h * 16, rx, tt * 32 * 896 + kh * 448); }); }
  asm volatile("buffer_load_dwordx2 a[252:253], off, %0, %1 sc1" :: "s"(x.rs), "s"(OFF_TAG) : ACLOB);
  pstamp(x);
  const int task = wi * 6 + hp - 2;
  auto ask_q = [&]() {
    const rsrc_t rq = mkrs(a.wq, D * D * 2);
    const unsigned vo = (8 * w + (l >> 3)) * 896 + (l & 7) * 16;
    sfor<0, 14>([&](auto i) { ldl<1>(vo, rq, hp * 112 + i * 32 * 896, IMG_Q + (4 * i + w) * KB); });
  };
  auto ask_t = [&](const char* tp, unsigned tn, unsigned ts, unsigned tb) {
    const rsrc_t rt = mkrs(tp, tn);
    const unsigned vo = (8 * w + (l >> 3)) * ts + (l & 7) * 16;
    sfor<0, 7>([&](auto i) { ldl<1>(vo, rt, tb + i * 32 * ts, IMG_T + (4 * i + w) * KB); });
  };
  if (kvp) ask_t(hp ? a.wv : a.wk, D * 56, 128, 0);
  ask_q();
#define TT 0
  pstamp(x);
  stamp(x);
  wv<15 + TT>();
  pstamp(x);
  {
    float s = 0.f, q = 0.f, s1 = 0.f, q1 = 0.f;
    sfor<0, 14>([&](auto i) {
      pin(xf[i]);
      s = dot2(xf[i][0], 0x3f803f80u, s); q = dot2(xf[i][0], xf[i][0], q); s1 = dot2(xf[i][1], 0x3f803f80u, s1); q1 = dot2(xf[i][1], xf[i][1], q1);
      s = dot2(xf[i][2], 0x3f803f80u, s); q = dot2(xf[i][2], xf[i][2], q); s1 = dot2(xf[i][3], 0x3f803f80u, s1); q1 = dot2(xf[i][3], xf[i][3], q1);
    });
    s = sum32(s + s1); q = sum32(q + q1);
    if (h == 0) ((f32x2*)(lds + S_ST))[w * 32 + r] = f32x2{s, q};
  }
  pstamp(x);
  if (!kvp) {
    if (task < 112) ask_t(task < 56 ? a.wg : a.wu, D * F * 2, F * 2, (task & 1) * 224 * F * 2 + ((task % 56) >> 1) * 128);
    else if (task < 168) ask_t(a.wd, D * F * 2, 896, ((task - 112) / 7) * 224 * 896 + ((task - 112) % 7) * 128);
    else ask_t(a.wo, task < 182 ? D * D * 2 : 0, 896, ((task - 168) / 7) * 224 * 896 + ((task - 168) % 7) * 128);
  }
  if (kvp) wv<14 + TT>(); else wv<7 + TT>();
  bar();
  stamp(x);
  float mean, rstd;
  { const f32x2 p0 = ((const f32x2*)(lds + S_ST))[tl * 32 + r], p1 = ((const f32x2*)(lds + S_ST))[(tl + 2) * 32 + r];
    row_stats(p0[0] + p1[0], p0[1] + p1[1], mean, rstd); }
  float* csw = (float*)(lds + S_CS);
  const int tro = 8 * h + ((l >> 2) & 3), tco = ((l >> 4) & 1) * 32 + (l & 3) * 8;
  if (kvp) {
    f32x16 kv; float cs = 0.f;
    const unsigned tb = IMG_T + (224 * kh + tro) * 56 + tco;
    u32x2 f[14][2];
    auto rd = [&](auto i) { trd<i * 896>(f[i][0], tb); trd<i * 896 + 224>(f[i][1], tb); };
    sfor<0, 6>(rd);
    sfor<0, 14>([&](auto i) {
      if constexpr (i + 6 < 14) { rd(std::integral_constant<int, i + 6>{}); wl<12>(); } else wl<2 * (13 - i)>();
      pin(f[i][0]); pin(f[i][1]);
      u32x4 g = {f[i][0][0], f[i][0][1], f[i][1][0], f[i][1][1]};
      pin(g); nop<1>();
      if constexpr (i == 0) m32z(kv, g, xf[i]); else m32(kv, g, xf[i]);
      cs = fragsum(g, cs); pin(cs);
    });
    cs = sum32(cs);
    if (h == 0) csw[(w * 3 + 2) * 32 + r] = cs;
    nop<11>(); pin(kv);
    pstamp(x);
    if (kh) put_tile(lds + S_KP + tl * 4096 + x.vl16, kv);
    if (x.m == 0 && w == 3) { if (l < 16) st1(0.f, l * 256, x.rs, OFF_FLAG + x.c * 4096); }
    bar();
    if (!kh) {
      float raw[16]; f32x4 c4[4];
      sfor<0, 4>([&](auto q) {
        const f32x4 o = ((const f32x4*)(lds + S_KP + tl * 4096 + x.vl16))[q * 64];
        c4[q] = *(const f32x4*)(csw + (w * 3 + 2) * 32 + 8 * q + 4 * h) + *(const f32x4*)(csw + ((w + 2) * 3 + 2) * 32 + 8 * q + 4 * h);
#pragma unroll
        for (int i = 0; i < 4; ++i) raw[4 * q + i] = kv[4 * q + i] + o[i];
      });
      kv_finish(x, raw, c4, mean, rstd, hp == 1, OFF_KV + (tt * 4 + 2 * hp) * KB, tl);
      if (tt == 0 && r == 0) sfor<0, 4>([&](auto q) { st4<32 * q>(__builtin_bit_cast(u32x4, c4[q]), h * 16, x.rs, OFF_CSQ + (16 + hp) * 128); });
    }
    pstamp(x);
    wv<0>();
    bar();
    get_tag(x);
    kv_publish(x, 0, 2 * wi + hp);
    pstamp(x);
  } else { pstamp(x); pstamp(x); pstamp(x); }
  f32x16 qa, qb;
  {
    float ca = 0.f;
    const unsigned ta = IMG_Q + (224 * kh + tro) * 128 + tco + 56 * kh, tb = IMG_Q + (224 * kh + tro) * 128 + tco + 56 * (kh ^ 1);
    u32x2 f[14][4];
    auto rd = [&](auto i) { trd<i * 2048>(f[i][0], tb); trd<i * 2048 + 512>(f[i][1], tb); trd<i * 2048>(f[i][2], ta); trd<i * 2048 + 512>(f[i][3], ta); };
    wl<0>();
    sfor<0, 3>(rd);
    sfor<0, 14>([&](auto i) {
      if constexpr (i + 3 < 14) { wl<8>(); rd(std::integral_constant<int, i + 3>{}); } else wl<4 * (13 - i)>();
      sfor<0, 4>([&](auto e) { pin(f[i][e]); });
      u32x4 gb = {f[i][0][0], f[i][0][1], f[i][1][0], f[i][1][1]}, ga = {f[i][2][0], f[i][2][1], f[i][3][0], f[i][3][1]};
      pin(ga); pin(gb); nop<1>();
      if constexpr (i == 0) m32z(qb, gb, xf[i]); else m32(qb, gb, xf[i]);
      if constexpr (i == 0) m32z(qa, ga, xf[i]); else m32(qa, ga, xf[i]);
      if (kh == tl) ca = fragsum(ga, ca); else ca = fragsum(gb, ca);
      pin(ca);
    });
    pstamp(x);
    ca = sum32(ca);
    if (h == 0) csw[(w * 3) * 32 + r] = ca;
  }
  nop<11>(); pin(qa); pin(qb);
  put_tile(lds + S_QP + (w ^ 2) * 4096 + x.vl16, qb);
  pstamp(x);
  wv<0>();
  bar();
  stamp(x);
  const bool own = x.c == hp && th == 0;
  {
    f32x4 cq[4];
    sfor<0, 4>([&](auto q) {
      cq[q] = *(const f32x4*)(csw + (kh * 3) * 32 + 8 * q + 4 * h) + *(const f32x4*)(csw + ((kh + 2) * 3) * 32 + 8 * q + 4 * h);
      if (own && tl == 0 && r == 0) st4<32 * q>(__builtin_bit_cast(u32x4, cq[q]), h * 16, x.rs, OFF_CSQ + (2 * hp + kh) * 128);
    });
    q_finish(x, qa, cq, mean, rstd);
  }
  pstamp(x);
  const int sro = 4 * h + ((l >> 2) & 3);
  if (own) {
    const char* tb = lds + IMG_Q + (224 * (w & 1) + sro) * 128 + tco + 56 * (w >> 1);
    sfor<0, 14>([&](auto i) { st4<(i & 3) * 1024>(trfrag(tb + i * 2048, tb + i * 2048 + 1024), x.vl16, x.rs, OFF_WQ + (hp * 56 + 14 * w + (i & ~3)) * KB); });
  }
  if (kvp) {
    if (wi == 8) {
      const char* tb = lds + IMG_T + (112 * w + sro) * 56 + tco;
      sfor<0, 7>([&](auto i) { st4<(i & 3) * 1024>(trfrag(tb + i * 896, tb + i * 896 + 448), x.vl16, x.rs, OFF_WQ + ((16 + hp) * 28 + 7 * w + (i & ~3)) * KB); });
    }
  } else if (task < 112) {
    const int g = l >> 4, mat = task >= 56, gt = 4 * ((task % 56) >> 1) + w, m = gt / 7, j = gt % 7, lt = (m & 1) ? j : (j == 6 ? 0 : j + 1);
    const char* tb = lds + IMG_T + (16 * (g >> 1) + 4 * (g & 1) + ((l >> 2) & 3)) * 128 + 32 * w + (l & 3) * 8;
    const unsigned st = lt < 5 ? 10 * KB : 4 * KB, so = OFF_WGU + (m * 196 + (lt < 5 ? lt * 2 + mat : 140 + (lt - 5) * 2 + mat)) * KB + (task & 1) * 7 * st;
    u32x4 f[7];
    sfor<0, 7>([&](auto i) { f[i] = trfrag(tb + i * 4096, tb + i * 4096 + 1024); });
    float cs = 0.f;
    sfor<0, 7>([&](auto i) { st4<0>(f[i], x.vl16, x.rs, so + i * st); cs = fragsum(f[i], cs); });
    cs = sum32(sum16(cs));
    if (l < 16) st1(cs, l * 4, x.rs, OFF_CSGU + m * 2048 + (task & 1) * 1024 + (lt * 2 + mat) * 64);
  } else if (task < 168) {
    const int nt = 2 * ((task - 112) % 7) + (w & 1), k0 = 14 * ((task - 112) / 7);
    const char* tb = lds + IMG_T + sro * 128 + 64 * (w & 1) + tco;
    u32x4 f[7];
    sfor<0, 7>([&](auto i) { const int ks = 7 * (w >> 1) + i; const char* p = tb + (32 * (ks >> 1) + 8 * (ks & 1)) * 128; f[i] = trfrag(p, p + 2048); });
    sfor<0, 7>([&](auto i) { st4<0>(f[i], x.vl16, x.rs, OFF_WD + (nt * 112 + dn_pos(k0 + 7 * (w >> 1) + i)) * KB); });
  } else if (task < 182) {
    const int nt = 2 * ((task - 168) % 7) + (w & 1);
    const char* tb = lds + IMG_T + (112 * (w >> 1) + sro) * 128 + 64 * (w & 1) + tco;
    u32x4 f[8];
    sfor<0, 8>([&](auto i) { const char* p = tb + (28 * (i >> 1) + 16 * (i & 1)) * 128; f[i] = trfrag(p, p + 1024); if ((i & 1) && h) { f[i][2] = 0; f[i][3] = 0; } });
    sfor<0, 8>([&](auto i) { st4<(i & 3) * 1024>(f[i], x.vl16, x.rs, OFF_WO + (nt * 32 + 16 * ((task - 168) / 7) + 8 * (w >> 1) + (i & ~3)) * KB); });
  }
}

DI void ask_slots(Ctx& x, int L) {
  if (wave0(x)) asm volatile("buffer_load_dwordx2 a[250:251], %0, %1, %2 offen sc1" :: "v"(x.l * 8), "s"(x.rs), "s"(OFF_SLOT + (L * 16 + x.c) * 512) : ACLOB);
}
DI void phase_first(Ctx& x, const Args& a) {
  phase_pre(x, a);
  pstamp(x);
  ask_slots(x, 0);
}

DI void phase_att(Ctx& x, const int L, f32x16 (&g2)[3]) {
  const int w = x.w, l = x.l, h = l >> 5, r = l & 31;
  const int th = x.m >> 3, hp = x.m & 7, tl = w & 1, kh = w >> 1, tt = 4 * x.c + 2 * th + tl;
  const unsigned KV = OFF_KV + L * SZ_KV;
  get_tag(x);
  if (L > 0) {
    f32x4 cq[4], ck[4];
    sfor<0, 4>([&](auto q) { ld4f<32 * q>(cq[q], h * 16, x.rs, OFF_CSQ + (2 * hp + kh) * 128); ld4f<32 * q>(ck[q], h * 16, x.rs, OFF_CSQ + (16 + (hp & 1)) * 128); });
    await(x, FL_X(x.c, th), 7);
    stamp(x);
    u32x4 xf[14]; f32x2 sp[7];
    {
      const unsigned so = OFF_X + 2 * SZ_X + (tt * 28 + 14 * kh) * KB;
      sfor<0, 14>([&](auto i) { ld4<(i & 3) * 1024>(xf[i], x.vl16, x.rs, so + (i >> 2) * 4096); });
      sfor<0, 7>([&](auto i) { ld2<0>(sp[i], (h * 7 * 2048 + r) * 8, x.rs, OFF_ST + 2 * SZ_ST + (i * 2048 + tt * 32) * 8); });
    }
    float mean, rstd;
    auto stats = [&]() {
      wv<0>();
      sfor<0, 7>([&](auto i) { pin(sp[i]); });
      sfor<0, 4>([&](auto i) { pin(cq[i]); pin(ck[i]); });
      float s = 0.f, q = 0.f;
#pragma unroll
      for (int i = 0; i < 7; ++i) { s += sp[i][0]; q += sp[i][1]; }
      row_stats(sum32(s), sum32(q), mean, rstd);
    };
    f32x16 qa, qb;
    u32x4 wa[14], wb[14];
    const unsigned pa = x.vl16 + (SL_WQ + 28 * kh + 14 * kh) * KB, pb = x.vl16 + (SL_WQ + 28 * (kh ^ 1) + 14 * kh) * KB;
    if (hp < 2) {
      f32x16 kv;
      sfor<0, 14>([&](auto i) { dsr<i * 1024>(wa[i], x.vl16 + 14 * kh * KB); });
      sfor<0, 14>([&](auto i) {
        wv<20 - i>(); wl<13 - i>();
        if constexpr (i == 0) m32z(kv, wa[i], xf[i]); else m32(kv, wa[i], xf[i]);
      });
      stats();
      nop<11>(); pin(kv);
      if (kh) put_tile(lds + S_KP + tl * 4096 + x.vl16, kv);
      bar();
      if (!kh) {
        float raw[16];
        sfor<0, 4>([&](auto q) {
          const f32x4 o = ((const f32x4*)(lds + S_KP + tl * 4096 + x.vl16))[q * 64];
#pragma unroll
          for (int i = 0; i < 4; ++i) raw[4 * q + i] = kv[4 * q + i] + o[i];
        });
        kv_finish(x, raw, ck, mean, rstd, hp == 1, KV + (tt * 4 + 2 * hp) * KB, tl);
      }
      wl<0>();
      sfor<0, 7>([&](auto i) { dsr<i * 1024>(wb[i], pb); dsr<i * 1024>(wa[i], pa); });
      sfor<0, 14>([&](auto i) {
        if constexpr (i < 7) wl<12>(); else wl<2 * (13 - i)>();
        if constexpr (i == 0) { m32z(qb, wb[i], xf[i]); m32z(qa, wa[i], xf[i]); } else { m32(qb, wb[i], xf[i]); m32(qa, wa[i], xf[i]); }
        if constexpr (i < 7) { dsr<(i + 7) * 1024>(wb[i + 7], pb); dsr<(i + 7) * 1024>(wa[i + 7], pa); }
      });
      wv<0>();
    } else {
      sfor<0, 7>([&](auto i) { dsr<i * 1024>(wb[i], pb); dsr<i * 1024>(wa[i], pa); });
      sfor<0, 14>([&](auto i) {
        wv<20 - i>();
        if constexpr (i < 7) wl<12>(); else wl<2 * (13 - i)>();
        if constexpr (i == 0) { m32z(qb, wb[i], xf[i]); m32z(qa, wa[i], xf[i]); } else { m32(qb, wb[i], xf[i]); m32(qa, wa[i], xf[i]); }
        if constexpr (i < 7) { dsr<(i + 7) * 1024>(wb[i + 7], pb); dsr<(i + 7) * 1024>(wa[i + 7], pa); }
      });
      stats();
    }
    nop<11>();
    pin(qa); pin(qb);
    put_tile(lds + S_QP + (w ^ 2) * 4096 + x.vl16, qb);
    bar();
    if (hp < 2) kv_publish(x, 1, 4 * x.c + 2 * th + hp);
    stamp(x); stamp(x);
    q_finish(x, qa, cq, mean, rstd);
    ask_slots(x, 1);
  }
  stamp(x);
  wv<0>();
  if (wave0(x)) {
    unsigned s0, s1;
#pragma nounroll
    for (unsigned spin = 0; spin < (1u << 18); ++spin) {
      asm volatile("v_accvgpr_read_b32 %0, a250\n\tv_accvgpr_read_b32 %1, a251" : "=v"(s0), "=v"(s1));
      if (__all(s1 == x.thi && ((s0 ^ x.tlo) >> 16) == 0)) break;
      __builtin_amdgcn_s_sleep(2);
      ask_slots(x, L); wv<0>();
    }
    const unsigned km = umax64(s0 << 16);
    if (l == 0) ((unsigned*)(lds + S_KM))[4] = km;
  }
  bar();
  if (L == 0) count(x, FL_W(x.c), 1);
  stamp(x);
  const unsigned kvs = KV + 16 * w * 4096;
  f32x16 kf0, kf1, kf2;
  asm volatile("buffer_load_dwordx4 v[128:131], %2, %3, %4 offen sc1\n\tbuffer_load_dwordx4 v[132:135], %2, %3, %4 offen offset:1024 sc1\n\t"
               "buffer_load_dwordx4 v[136:139], %2, %3, %4 offen offset:2048 sc1\n\tbuffer_load_dwordx4 v[140:143], %2, %3, %4 offen offset:3072 sc1\n\t"
               "buffer_load_dwordx4 v[144:147], %2, %3, %5 offen sc1\n\tbuffer_load_dwordx4 v[148:151], %2, %3, %5 offen offset:1024 sc1\n\t"
               "buffer_load_dwordx4 v[152:155], %2, %3, %5 offen offset:2048 sc1\n\tbuffer_load_dwordx4 v[156:159], %2, %3, %5 offen offset:3072 sc1"
               : "={v[128:143]}"(kf0), "={v[144:159]}"(kf1) : "v"(x.vl16), "s"(x.rs), "s"(kvs), "s"(kvs + 4096));
  asm volatile("buffer_load_dwordx4 v[160:163], %1, %2, %3 offen sc1\n\tbuffer_load_dwordx4 v[164:167], %1, %2, %3 offen offset:1024 sc1\n\t"
               "buffer_load_dwordx4 v[168:171], %1, %2, %3 offen offset:2048 sc1\n\tbuffer_load_dwordx4 v[172:175], %1, %2, %3 offen offset:3072 sc1"
               : "={v[160:175]}"(kf2) : "v"(x.vl16), "s"(x.rs), "s"(kvs + 8192));
  const float kmx = ((const float*)(lds + S_KM))[4];
  u32x4 q[4][2];
#pragma unroll
  for (int k = 0; k < 4; ++k) {
    q[k][0] = ((const u32x4*)(lds + S_QF + (w ^ k) * 2048 + x.vl16))[0];
    q[k][1] = ((const u32x4*)(lds + S_QF + (w ^ k) * 2048 + x.vl16))[64];
  }
  bool safe = true;
  {
#pragma unroll
    for (int k = 0; k < 4; ++k) {
      const float mrow = bflo(q[k][1][2]) * kmx + 0.01f;
      safe = safe && (!h || mrow <= M_LIMIT);
      if (h) q[k][1][2] = cvtpk(-mrow, 0.f);
    }
  }
  const bool fast = __all(safe);
  stamp(x);
  f32x16 o[4];
  if (fast) {
    unsigned so_kv = kvs + 3 * 4096, so_w = OFF_WGU + (x.m * 196 + 2 * w) * KB, so_l = 2 * w * KB, spin = 1u << 16;
    Two ab = base_wo(x);
    const Two wd = base_wd(x);
    asm volatile(
      ".macro GRP s, pa, pb\n\t"
      "v_exp_f32 v200, v[\\s]\n\tv_exp_f32 v201, v[\\s+1]\n\tv_cvt_pk_bf16_f32 v[\\pa], v202, v203\n\t"
      "v_exp_f32 v202, v[\\s+2]\n\tv_exp_f32 v203, v[\\s+3]\n\tv_cvt_pk_bf16_f32 v[\\pb], v200, v201\n\t"
      ".endm\n\t"
      ".macro MEM kind, i, ld\n\t"
      ".if \\kind == 1\n\t"
      "buffer_load_dwordx4 v[128+16*\\ld+4*\\i:128+16*\\ld+4*\\i+3], %[vo], %[rs], %[skv] offen offset:1024*\\i sc1\n\t"
      ".elseif \\kind == 2\n\t"
      "buffer_load_dword v206, v205, %[rs], 0 offen offset:3072 sc1\n\t"
      ".elseif \\kind == 3\n\t"
      ".if att_n < 16 || att_n >= 56\n\t"
      ".if att_n < 102\n\t"
      ".if att_n < 16\n\t.set att_r, 184+4*att_n\n\t.else\n\t.set att_r, 4*(att_n-56)\n\t.endif\n\t"
      ".if att_n & 1\n\t"
      "buffer_load_dwordx4 a[att_r:att_r+3], %[vo], %[rs], %[ab1] offen offset:1024*((att_n>>1)&3) sc1\n\t"
      ".else\n\t"
      "buffer_load_dwordx4 a[att_r:att_r+3], %[vo], %[rs], %[ab0] offen offset:1024*((att_n>>1)&3) sc1\n\t"
      ".endif\n\t"
      ".if att_n == 15\n\t"
      "s_mov_b32 %[ab0], %[wd0]\n\ts_mov_b32 %[ab1], %[wd1]\n\t"
      ".elseif (att_n & 7) == 7\n\t"
      "s_add_u32 %[ab0], %[ab0], 4096\n\ts_add_u32 %[ab1], %[ab1], 4096\n\t"
      ".endif\n\t"
      ".endif\n\t"
      ".elseif att_n < 44\n\t"
      ".if att_n & 1\n\t"
      "buffer_load_dwordx4 %[vo], %[rs], %[sw] offen offset:1024 sc1 lds\n\t"
      "s_add_u32 %[sw], %[sw], 8192\n\ts_add_u32 m0, m0, 8192\n\t"
      ".else\n\t"
      "buffer_load_dwordx4 %[vo], %[rs], %[sw] offen sc1 lds\n\t"
      ".endif\n\t"
      ".else\n\t"
      "buffer_load_dwordx4 v[208+4*(att_n-44):208+4*(att_n-44)+3], %[vo], %[rs], %[sw] offen offset:1024*(att_n&1) sc1\n\t"
      ".if att_n & 1\n\ts_add_u32 %[sw], %[sw], 8192\n\t.endif\n\t"
      ".endif\n\t"
      ".set att_n, att_n+1\n\t"
      ".endif\n\t"
      ".endm\n\t"
      ".macro STEP sc, sn, k, nk, pk, tk, nt, pt, ld, m0k, m0i, m1k, m2k, m2i, m3k\n\t"
      "v_mfma_f32_32x32x16_bf16 v[\\sn:\\sn+15], v[128+16*\\nt:128+16*\\nt+3], v[64+8*\\nk:64+8*\\nk+3], 0\n\t"
      "MEM \\m0k, \\m0i, \\ld\n\t"
      "GRP \\sc, 199, 192\n\t"
      "v_mfma_f32_32x32x16_bf16 v[\\sn:\\sn+15], v[132+16*\\nt:132+16*\\nt+3], v[68+8*\\nk:68+8*\\nk+3], v[\\sn:\\sn+15]\n\t"
      "MEM \\m1k, 0, 0\n\t"
      "GRP \\sc+4, 193, 194\n\t"
      "v_mfma_f32_32x32x16_bf16 v[16*\\pk:16*\\pk+15], v[140+16*\\pt:140+16*\\pt+3], v[196:199], v[16*\\pk:16*\\pk+15]\n\t"
      "MEM \\m2k, \\m2i, \\ld\n\t"
      "GRP \\sc+8, 195, 196\n\t"
      "v_mfma_f32_32x32x16_bf16 v[16*\\k:16*\\k+15], v[136+16*\\tk:136+16*\\tk+3], v[192:195], v[16*\\k:16*\\k+15]\n\t"
      "MEM \\m3k, 0, 0\n\t"
      "GRP \\sc+12, 197, 198\n\t"
      ".endm\n\t"
      ".macro TILE tk, nx, pv, on, wait, p, fl=0\n\t"
      "STEP 96, 112, 0, 1, 3, \\tk, \\tk, \\pv, \\pv, \\p, 0, \\p, \\p, 0, \\fl\n\t"
      "STEP 112, 96, 1, 2, 0, \\tk, \\tk, \\tk, \\pv, \\on, 0, \\p, \\on, 1, 0\n\t"
      "STEP 96, 112, 2, 3, 1, \\tk, \\tk, \\tk, \\pv, \\on, 2, \\p, \\on, 3, 0\n\t"
      ".if \\on\n\ts_add_u32 %[skv], %[skv], 4096\n\t.endif\n\t"
      "s_waitcnt vmcnt(\\wait)\n\t"
      "STEP 112, 96, 3, 0, 2, \\tk, \\nx, \\tk, \\pv, \\p, 0, \\p, \\p, 0, 0\n\t"
      ".endm\n\t"
      ".set att_n, 0\n\t"
      "s_mov_b32 m0, %[sl]\n\t"
      "v_mov_b32 v196, 0\n\tv_mov_b32 v197, 0\n\tv_mov_b32 v198, 0\n\tv_mov_b32 v199, 0\n\tv_mov_b32 v202, 0\n\tv_mov_b32 v203, 0\n\t"
      "v_mov_b32 v188, 0\n\tv_mov_b32 v189, 0\n\tv_mov_b32 v190, 0\n\tv_mov_b32 v191, 0\n\t"
      "v_mfma_f32_32x32x16_bf16 v[0:15], v[196:199], v[196:199], 0\n\t"
      "v_mfma_f32_32x32x16_bf16 v[16:31], v[196:199], v[196:199], 0\n\t"
      "v_mfma_f32_32x32x16_bf16 v[32:47], v[196:199], v[196:199], 0\n\t"
      "v_mfma_f32_32x32x16_bf16 v[48:63], v[196:199], v[196:199], 0\n\t"
      "s_waitcnt vmcnt(8)\n\t"
      "v_mfma_f32_32x32x16_bf16 v[96:111], v[128:131], v[64:67], 0\n\t"
      "v_mfma_f32_32x32x16_bf16 v[96:111], v[132:135], v[68:71], v[96:111]\n\t"
      "s_nop 11\n\t"
      "TILE 0, 1, 3, 1, 8, 0\n\t"
      "TILE 1, 2, 0, 1, 8, 0\n\tTILE 2, 3, 1, 1, 9, 0, 2\n\t"
      "s_waitcnt vmcnt(4)\n\t"
      "1:\n\t"
      "v_cmp_gt_u32 vcc, 16, v206\n\t"
      "s_and_b64 vcc, exec, vcc\n\t"
      "s_cbranch_vccz 2f\n\t"
      "s_sleep 24\n\t"
      "buffer_load_dword v206, v205, %[rs], 0 offen offset:3072 sc1\n\t"
      "s_sub_u32 %[spin], %[spin], 1\n\t"
      "s_waitcnt vmcnt(0)\n\t"
      "s_cbranch_scc0 1b\n\t"
      "2:\n\t"
      "TILE 3, 0, 2, 1, 14, 3\n\t"
      "TILE 0, 1, 3, 1, 21, 3\n\tTILE 1, 2, 0, 1, 24, 3\n\tTILE 2, 3, 1, 1, 24, 3\n\tTILE 3, 0, 2, 1, 24, 3\n\t"
      "TILE 0, 1, 3, 1, 24, 3\n\tTILE 1, 2, 0, 1, 24, 3\n\tTILE 2, 3, 1, 1, 24, 3\n\tTILE 3, 0, 2, 1, 24, 3\n\t"
      "TILE 0, 1, 3, 1, 24, 3\n\tTILE 1, 2, 0, 0, 20, 3\n\tTILE 2, 3, 1, 0, 16, 3\n\tTILE 3, 3, 2, 0, 63, 3\n\t"
      "v_cvt_pk_bf16_f32 v199, v202, v203\n\t"
      "s_nop 1\n\t"
      "v_mfma_f32_32x32x16_bf16 v[48:63], v[188:191], v[196:199], v[48:63]\n\t"
      "s_nop 11\n\t"
      ".purgem GRP\n\t.purgem MEM\n\t.purgem STEP\n\t.purgem TILE"
      : "={v[0:15]}"(o[0]), "={v[16:31]}"(o[1]), "={v[32:47]}"(o[2]), "={v[48:63]}"(o[3]), "+{v[128:143]}"(kf0), "+{v[144:159]}"(kf1), "+{v[160:175]}"(kf2),
        "={v[208:223]}"(g2[0]), "={v[224:239]}"(g2[1]), "={v[240:255]}"(g2[2]),
        [skv] "+s"(so_kv), [sw] "+s"(so_w), [ab0] "+s"(ab.s0), [ab1] "+s"(ab.s1), [spin] "+s"(spin)
      : "{v[64:67]}"(q[0][0]), "{v[68:71]}"(q[0][1]), "{v[72:75]}"(q[1][0]), "{v[76:79]}"(q[1][1]),
        "{v[80:83]}"(q[2][0]), "{v[84:87]}"(q[2][1]), "{v[88:91]}"(q[3][0]), "{v[92:95]}"(q[3][1]),
        [vo] "{v204}"(x.vl16), "{v205}"((l & 15) * 4096), [rs] "s"(x.rs), [sl] "s"(so_l), [wd0] "s"(wd.s0), [wd1] "s"(wd.s1)
      : "memory", "scc", "vcc",
        "v96","v97","v98","v99","v100","v101","v102","v103","v104","v105","v106","v107","v108","v109","v110","v111",
        "v112","v113","v114","v115","v116","v117","v118","v119","v120","v121","v122","v123","v124","v125","v126","v127",
        "v176","v177","v178","v179","v180","v181","v182","v183","v184","v185","v186","v187","v188","v189","v190","v191",
        "v192","v193","v194","v195","v196","v197","v198","v199","v200","v201","v202","v203","v206", ACLOB);
    bar();
  } else {
    wv<0>();
    pin(kf0); pin(kf1); pin(kf2);
    float mx[4];
#pragma unroll
    for (int k = 0; k < 4; ++k) {
      mx[k] = -3.0e38f; if (h) q[k][1][2] = 0; pin(q[k][1]);
#pragma unroll
      for (int i = 0; i < 16; ++i) o[k][i] = 0.f;
    }
#pragma nounroll
    for (unsigned spin = 0; spin < (1u << 16); ++spin) {
      float v;
      ld1(v, (l & 15) * 4096, x.rs, OFF_FLAG + FL_W(0)); wv<0>(); pin(v);
      if (__all(__float_as_uint(v) >= 16u)) break;
      __builtin_amdgcn_s_sleep(24);
    }
    pf_wo(x);
    pf_wgu1(x);
    sfor<0, 12>([&](auto j) {
      u32x4 t;
      ld4<1024 * (j & 1)>(t, x.vl16, x.rs, OFF_WGU + (x.m * 196 + 112 + 8 * (j >> 1) + 2 * w) * KB); wv<0>(); pin(t);
#pragma unroll
      for (int i = 0; i < 4; ++i) g2[j >> 2][4 * (j & 3) + i] = __uint_as_float(t[i]);
    });
    pf_wd<0, 46>(x, base_wd(x));
    for (int tk = 0; tk < 16; ++tk) {
      u32x4 kf[4];
      sfor<0, 4>([&](auto i) { ld4<i * 1024>(kf[i], x.vl16, x.rs, kvs + tk * 4096); });
      wv<0>();
      sfor<0, 4>([&](auto k) {
        f32x16 s;
        nop<1>();
        m32z(s, kf[0], q[k][0]); m32(s, kf[1], q[k][1]);
        nop<11>(); pin(s);
        float tm = s[0];
#pragma unroll
        for (int i = 1; i < 16; ++i) tm = fmaxf(tm, s[i]);
        tm = max32(tm);
        const float mn = fmaxf(mx[k], tm), al = __builtin_amdgcn_exp2f(mx[k] - mn);
        mx[k] = mn;
#pragma unroll
        for (int i = 0; i < 16; ++i) o[k][i] *= al;
        u32x4 p0, p1;
#pragma unroll
        for (int d = 0; d < 4; ++d) {
          p0[d] = cvtpk(__builtin_amdgcn_exp2f(s[2 * d] - mn), __builtin_amdgcn_exp2f(s[2 * d + 1] - mn));
          p1[d] = cvtpk(__builtin_amdgcn_exp2f(s[8 + 2 * d] - mn), __builtin_amdgcn_exp2f(s[9 + 2 * d] - mn));
        }
        pin(p0); pin(p1); pin(o[k]);
        nop<1>();
        m32(o[k], kf[2], p0); m32(o[k], kf[3], p1);
        nop<11>(); pin(o[k]);
      });
    }
    bar();
    float* mm = (float*)(lds + SCR);
#pragma unroll
    for (int k = 0; k < 4; ++k) if (h == 0) mm[((w ^ k) * 4 + w) * 32 + r] = mx[k];
    bar();
#pragma unroll
    for (int k = 0; k < 4; ++k) {
      const float* p = mm + (w ^ k) * 128 + r;
      const float g = fmaxf(fmaxf(p[0], p[32]), fmaxf(p[64], p[96])), f = __builtin_amdgcn_exp2f(mx[k] - g);
#pragma unroll
      for (int i = 0; i < 16; ++i) o[k][i] *= f;
    }
    bar();
  }
  stamp(x);
  reduce4(x, o, [](auto) {});
  pin(o[0]); fstamp(x);
  {
    auto lr = __builtin_amdgcn_permlane32_swap(__float_as_uint(o[0][12]), __float_as_uint(o[0][12]), false, false);
    const float inv = __builtin_amdgcn_rcpf(__uint_as_float(lr[1]));
    u32x4 a, b;
#pragma unroll
    for (int d = 0; d < 4; ++d) { a[d] = cvtpk(o[0][2 * d] * inv, o[0][2 * d + 1] * inv); b[d] = cvtpk(o[0][8 + 2 * d] * inv, o[0][9 + 2 * d] * inv); }
    if (h) { b[2] = 0; b[3] = 0; }
    const unsigned so = OFF_O + L * SZ_O + (tt * 32 + 2 * (2 * hp + kh)) * KB;
    st4<0>(a, x.vl16, x.rs, so); st4<1024>(b, x.vl16, x.rs, so);
  }
  fstamp(x);
  wv<0>();
  fstamp(x);
  signal(x, FL_O(L, x.c, th), 1);
}

template <bool FINAL> DI void res_epilogue(Ctx& x, const f32x16& acc, const u32x4 (&xr)[2], unsigned Xo, unsigned STo, int tt, int nt, char* out = nullptr) {
  const int h = x.l >> 5, r = x.l & 31;
  u32x4 y[2];
#pragma unroll
  for (int d = 0; d < 8; ++d) y[d >> 2][d & 3] = cvtpk(acc[2 * d] + bflo(xr[d >> 2][d & 3]), acc[2 * d + 1] + bfhi(xr[d >> 2][d & 3]));
  if constexpr (FINAL) {
#pragma unroll
    for (int s = 0; s < 2; ++s) {
      auto a = __builtin_amdgcn_permlane32_swap(y[s][0], y[s][2], false, false);
      auto b = __builtin_amdgcn_permlane32_swap(y[s][1], y[s][3], false, false);
      y[s] = u32x4{a[0], b[0], a[1], b[1]};
    }
    const unsigned vo = r * (D * 2) + h * 16, so = tt * 32 * D * 2 + nt * 64;
    const rsrc_t ro = mkrs(out, S * D * 2);
    st4p<0>(y[0], vo, ro, so); st4p<32>(y[1], vo, ro, so);
  } else {
    const unsigned so = Xo + (tt * 28 + 2 * nt) * KB;
    st4<0>(y[0], x.vl16, x.rs, so); st4<1024>(y[1], x.vl16, x.rs, so);
    float s = 0.f, q = 0.f, s1 = 0.f, q1 = 0.f;
#pragma unroll
    for (int d = 0; d < 4; ++d) {
      s = dot2(y[0][d], 0x3f803f80u, s); q = dot2(y[0][d], y[0][d], q); s1 = dot2(y[1][d], 0x3f803f80u, s1); q1 = dot2(y[1][d], y[1][d], q1);
    }
    s = sum32(s + s1); q = sum32(q + q1);
    if (h == 0) st2<0>(u32x2{__float_as_uint(s), __float_as_uint(q)}, r * 8, x.rs, STo + (nt * 2048 + tt * 32) * 8);
  }
}

DI void phase_oproj(Ctx& x, const int L, const Args& a, const f32x16 (&g2)[3]) {
  const int w = x.w;
  if (x.m < 14) {
    const int th = x.m / 7, ct = x.m % 7, tt0 = 4 * x.c + 2 * th, tt = tt0 + (w >> 1), nt = 2 * ct + (w & 1);
    u32x2 x0[4];
    if (L == 0) {
      const rsrc_t rx = mkrs(a.x, S * D * 2);
      sfor<0, 4>([&](auto i) { ld2i<16 * i>(x0[i], (x.l & 31) * 896 + (x.l >> 5) * 8, rx, tt * 32 * 896 + nt * 64); });
    } else {
      sfor<0, 4>([&](auto i) { ld2u<(i >> 1) * 1024 + (i & 1) * 8>(x0[i], x.vl16, x.rs, OFF_X + 2 * SZ_X + (tt * 28 + 2 * nt) * KB); });
    }
    await(x, FL_O(L, x.c, th), 8);
    stamp(x);
    u32x4 b[8][2];
    const unsigned s0 = OFF_O + L * SZ_O + (tt * 32 + 8 * w) * KB, s1 = OFF_O + L * SZ_O + ((tt0 + ((w >> 1) ^ 1)) * 32 + 8 * w) * KB;
    auto ld = [&](auto i) { ld4<(i & 3) * 1024>(b[i][0], x.vl16, x.rs, s0 + (i >> 2) * 4096); ld4<(i & 3) * 1024>(b[i][1], x.vl16, x.rs, s1 + (i >> 2) * 4096); };
    constexpr int WN = 5;
    sfor<0, WN>(ld);
    f32x16 acc[4];
    sfor<0, 8>([&](auto i) {
      constexpr int last = (i + WN - 1 < 7 ? i + WN - 1 : 7);
      wv<2 * (last - i) + 1>();
      m32a<A_WO + 8 * i, i == 0>(acc[0], b[i][0]); m32a<A_WO + 8 * i + 4, i == 0>(acc[1], b[i][0]);
      wv<2 * (last - i)>();
      m32a<A_WO + 8 * i, i == 0>(acc[2], b[i][1]); m32a<A_WO + 8 * i + 4, i == 0>(acc[3], b[i][1]);
      if constexpr (i + WN < 8) ld(std::integral_constant<int, i + WN>{});
    });
    nop<11>();
    sfor<0, 4>([&](auto k) { pin(acc[k]); pin(x0[k]); });
    stamp(x);
    reduce4(x, acc, [](auto) {});
    pin(acc[0]); fstamp(x);
    const u32x4 x2[2] = {u32x4{x0[0][0], x0[0][1], x0[1][0], x0[1][1]}, u32x4{x0[2][0], x0[2][1], x0[3][0], x0[3][1]}};
    res_epilogue<false>(x, acc[0], x2, OFF_X + (2 * L + 1) * SZ_X, OFF_ST + (2 * L + 1) * SZ_ST, tt, nt);
    fstamp(x);
    wv<0>();
    fstamp(x);
    signal(x, FL_X1(L, x.c), 1);
    fstamp(x);
  } else {
    stamp(x); stamp(x); fstamp(x); fstamp(x); fstamp(x); fstamp(x);
  }
#pragma unroll
  for (int j = 0; j < 12; ++j)
    *(f32x4*)(lds + (112 + 8 * (j >> 1) + 2 * w + (j & 1)) * KB + x.vl16) = f32x4{g2[j >> 2][4 * (j & 3)], g2[j >> 2][4 * (j & 3) + 1], g2[j >> 2][4 * (j & 3) + 2], g2[j >> 2][4 * (j & 3) + 3]};
}

DI void gu_tile(const f32x4 (&acc)[2][2], const char* csp, const float (&mean)[2], const float (&r2)[2], u32x2 (&hp)[2]) {
  const f32x4 cg = *(const f32x4*)csp, cu = *(const f32x4*)(csp + 64);
#pragma unroll
  for (int u = 0; u < 2; ++u) {
    const f32x2 nm = {-mean[u], -mean[u]}, rr = {r2[u], r2[u]};
#pragma unroll
    for (int e = 0; e < 2; ++e) {
      const f32x2 g = __builtin_elementwise_fma(nm, f32x2{cg[2 * e], cg[2 * e + 1]}, f32x2{acc[0][u][2 * e], acc[0][u][2 * e + 1]});
      const f32x2 v = __builtin_elementwise_fma(nm, f32x2{cu[2 * e], cu[2 * e + 1]}, f32x2{acc[1][u][2 * e], acc[1][u][2 * e + 1]});
      const f32x2 y = g * rr * v;
      hp[u][e] = cvtpk(y[0], y[1]);
    }
  }
}

DI void phase_gu(Ctx& x, const int L) {
  const int w = x.w, l = x.l, g = l >> 4, c16 = l & 15, m = x.m, tt = 4 * x.c + w;
  await(x, FL_X1(L, x.c), 14);
  stamp(x);
  constexpr int NXV = 11;
  u32x4 xf[NXV][2]; f32x2 sp[2][4];
  const unsigned sx = OFF_X + (2 * L + 1) * SZ_X + tt * 28 * KB, st = OFF_ST + (2 * L + 1) * SZ_ST + tt * 32 * 8;
  auto ldx = [&](auto k, auto u) {
    if constexpr (k < NXV) ld4<(k & 1) * 2048 + 256 * u>(xf[k][u], x.vg16, x.rs, sx + (k >> 1) * 4096);
    else if constexpr (k < 14) lda<A_GX + 8 * (k - NXV) + 4 * u, (k & 1) * 2048 + 256 * u>(x.vg16, x.rs, sx + (k >> 1) * 4096);
    else { constexpr int i = k - 14; ld2<128 * u>(sp[u][i], (((i < 3 || g < 2) ? g + 4 * i : g) * 2048 + c16) * 8, x.rs, st); }
  };
  using I0 = std::integral_constant<int, 0>; using I1 = std::integral_constant<int, 1>;
  constexpr int XA = 3;
  sfor<0, XA>([&](auto k) { ldx(k, I0{}); ldx(k, I1{}); });
  const unsigned a0 = x.vl16, a1 = x.vl16 + 65536, a2 = x.vl16 + 131072;
  constexpr int DP = 6;
  u32x4 wf[8];
  auto rd = [&](auto ii) {
    constexpr int i = ii, slot = i >= 160 ? i - 160 : i;
    if constexpr (slot < 64) dsr<slot * 1024>(wf[i & 7], a0); else if constexpr (slot < 128) dsr<(slot - 64) * 1024>(wf[i & 7], a1); else dsr<(slot - 128) * 1024>(wf[i & 7], a2);
  };
  auto mm = [&](f32x4& c, const u32x4& a, auto k, auto u) {
    if constexpr (k < NXV) m16<k == 0>(c, a, xf[k][u]); else m16a<false, A_GX + 8 * (k - NXV) + 4 * u>(c, a);
  };
  f32x4 aa[5][2][2], ch0, ch1;
  float mean[2], r2[2];
  sfor<0, DP>(rd);
  sfor<0, 140>([&](auto i) {
    constexpr int KS = i / 10, s = i % 10, t = s >> 1, mat = i & 1;
    using K = std::integral_constant<int, KS>;
    constexpr auto side = [](int k) { return (k >= 0 && k <= 3) || (k >= 5 && k <= 9); };
    constexpr auto all = [](int k) { return (k + XA < 14 ? 2 : 0) + ((k >= 0 && k <= 3) || (k >= 5 && k <= 9) ? 2 : 0) + (k == 5 ? 1 : 0); };
    if constexpr (s == 0) {
      wv<(side(KS - 3) ? 1 : 0) + all(KS - 2) + all(KS - 1)>();
      if constexpr (KS == 5) asm volatile("s_barrier" ::: "memory");
    }
    rd(std::integral_constant<int, i + DP>{}); wl<DP>();
    mm(aa[t][mat][0], wf[i & 7], K{}, I0{});
    if constexpr (s == 1 && KS + XA < 14) ldx(std::integral_constant<int, KS + XA>{}, I0{});
    if constexpr (s == 6 && KS + XA < 14) ldx(std::integral_constant<int, KS + XA>{}, I1{});
    if constexpr (KS <= 3 && s == 3) ldx(std::integral_constant<int, 14 + KS>{}, I0{});
    if constexpr (KS <= 3 && s == 8) ldx(std::integral_constant<int, 14 + KS>{}, I1{});
    if constexpr (KS == 5 && s == 3) asm volatile("buffer_load_dwordx4 %0, %2, %3, %4 offen sc1\n\tbuffer_load_dwordx4 %1, %2, %3, %4 offen offset:1024 sc1" : "=&v"(ch0), "=&v"(ch1) : "v"(x.vl16), "s"(x.rs), "s"(OFF_CSGU + m * 2048));
    if constexpr (KS == 11 && s == 2) { pin(ch0); pin(ch1); *(f32x4*)(lds + 40 * KB + x.vl16) = ch0 + ch1; }
    if constexpr (KS >= 5 && KS <= 9 && (s == 3 || s == 8) && !(KS == 5 && s == 3)) pf_wgu3<2 * (KS - 5) + (s == 8) - 1>(x);
    if constexpr ((KS == 10 || KS == 12) && s == 2) {
      constexpr int u = KS == 12;
      sfor<0, 4>([&](auto e) { pin(sp[u][e]); });
      float sm = sp[u][0][0] + sp[u][1][0] + sp[u][2][0], q = sp[u][0][1] + sp[u][1][1] + sp[u][2][1];
      if (g < 2) { sm += sp[u][3][0]; q += sp[u][3][1]; }
      sm = sum32(sum16(sm)); q = sum32(sum16(q));
      float rs; row_stats(sm, q, mean[u], rs); r2[u] = rs * rs;
      pin(mean[u]); pin(r2[u]);
    }
    mm(aa[t][mat][1], wf[i & 7], K{}, I1{}); SB();
  });
  fstamp(x);
  wv<0>();
  asm volatile("s_barrier" ::: "memory");
  stamp(x);
  const unsigned H = OFF_H + L * SZ_H + tt * 112 * KB;
  const int gl = (m & 1) ? 7 * m : 7 * m + 6, gp = (m & 1) ? 7 * m + 1 : 7 * m;
  const char* csp = lds + 40 * KB + g * 16;
  auto pair = [&](const f32x4 (&t0)[2][2], const f32x4 (&t1)[2][2], int lt) {
    u32x2 h0[2], h1[2];
    gu_tile(t0, csp + lt * 128, mean, r2, h0); gu_tile(t1, csp + lt * 128 + 128, mean, r2, h1);
    st4<0>(u32x4{h0[0][0], h0[0][1], h1[0][0], h1[0][1]}, x.vg16, x.rs, H + (gp + lt - 1) * 1024);
    st4<256>(u32x4{h0[1][0], h0[1][1], h1[1][0], h1[1][1]}, x.vg16, x.rs, H + (gp + lt - 1) * 1024);
  };
  {
    sfor<0, 20>([&](auto i) { pin(aa[i >> 2][(i >> 1) & 1][i & 1]); });
    u32x2 hl[2];
    gu_tile(aa[0], csp, mean, r2, hl);
    const unsigned sl = H + (gl >> 1) * 2048 + (gl & 1) * 8;
    st2<0>(hl[0], x.vg16, x.rs, sl); st2<256>(hl[1], x.vg16, x.rs, sl);
    pair(aa[1], aa[2], 1);
    pair(aa[3], aa[4], 3);
  }
  asm volatile("s_waitcnt lgkmcnt(%c0)" :: "i"(0) : "memory");
  SB();
  fstamp(x);
  f32x4 ab[2][2][2];
  const Two wd = base_wd(x);
  sfor<140, 196>([&](auto i) {
    constexpr int j = i - 140, KS = j / 4, t = (j % 4) >> 1, mat = i & 1;
    using K = std::integral_constant<int, KS>;
    if constexpr (j == 24) {
      fstamp(x);
      wv<12>();
      asm volatile("s_barrier" ::: "memory");
      count(x, FL_H(L, x.c, 0), 1);
      SB();
      fstamp(x);
    }
    if constexpr (i + DP < 196) { rd(std::integral_constant<int, i + DP>{}); wl<DP>(); } else wl<195 - i>();
    mm(ab[t][mat][0], wf[i & 7], K{}, I0{});
    if constexpr ((j & 1) == 0 && j < 20) pf_wd<46 + j / 2, 47 + j / 2>(x, wd);
    if constexpr ((j & 1) == 0 && j >= 20 && j < 48) pf_wq<(j - 20) / 2>(x);
    if constexpr (j == 55) ldflag(x.fa, x.rs, OFF_FLAG + FL_H(L, x.c, 0));
    mm(ab[t][mat][1], wf[i & 7], K{}, I1{}); SB();
  });
  nop<7>();
  stamp(x);
  sfor<0, 8>([&](auto i) { pin(ab[i >> 2][(i >> 1) & 1][i & 1]); });
  pair(ab[0], ab[1], 5);
}

DI void phase_down(Ctx& x, const int L, char* out) {
  const int w = x.w;
  if (x.m >= 14) {
    wv<0>(); signal(x, FL_H(L, x.c, 1), 1);
    stamp(x); fstamp(x); fstamp(x); stamp(x);
    return;
  }
  const int th = x.m / 7, ct = x.m % 7, tt0 = 4 * x.c + 2 * th, tt = tt0 + (w >> 1), tto = tt0 + ((w >> 1) ^ 1), nt = 2 * ct + (w & 1);
  wv<2>();
  recheck(x, x.fa, FL_H(L, x.c, 0), 16);
  stamp(x);
  constexpr int WN = 7;
  u32x4 b[WN][2], xr[2];
  const unsigned s0 = OFF_H + L * SZ_H + (tt * 112 + 28 * w) * KB, s1 = OFF_H + L * SZ_H + (tto * 112 + 28 * w) * KB;
  auto ld = [&](auto o) { constexpr int i = dn_ks(o);
    ld4<(i & 3) * 1024>(b[o % WN][0], x.vl16, x.rs, s0 + (i >> 2) * 4096); ld4<(i & 3) * 1024>(b[o % WN][1], x.vl16, x.rs, s1 + (i >> 2) * 4096); };
  sfor<0, WN>(ld);
  f32x16 acc[4];
  unsigned fb;
  sfor<0, 28>([&](auto i) {
    constexpr int last = i + WN - 1 < 27 ? i + WN - 1 : 27, ex = (i >= 7 && i <= 12) ? 1 : ((i >= 21) ? 2 : 0);
    wv<2 * (last - i) + 1 + ex>();
    if constexpr (i == 0) signal(x, FL_H(L, x.c, 1), 1);
    m32a<A_WD + 8 * i, i == 0>(acc[0], b[i % WN][0]); m32a<A_WD + 8 * i + 4, i == 0>(acc[1], b[i % WN][0]);
    wv<2 * (last - i) + ex>();
    m32a<A_WD + 8 * i, i == 0>(acc[2], b[i % WN][1]); m32a<A_WD + 8 * i + 4, i == 0>(acc[3], b[i % WN][1]);
    if constexpr (i == 6) ldflag(fb, x.rs, OFF_FLAG + FL_H(L, x.c, 1));
    if constexpr (i + WN == 20) { fstamp(x); recheck(x, fb, FL_H(L, x.c, 1), 16); fstamp(x); }
    if constexpr (i + WN < 28) ld(std::integral_constant<int, i + WN>{});
    if constexpr (i + WN == 27) {
      const unsigned sr = OFF_X + (2 * L + 1) * SZ_X + (tt * 28 + 2 * nt) * KB;
      ld4<0>(xr[0], x.vl16, x.rs, sr); ld4<1024>(xr[1], x.vl16, x.rs, sr);
    }
  });
  if ((x.m & 7) < 2) {
    ldl<4>(x.vl16, x.rs, OFF_WQ + ((16 + (x.m & 1)) * 28 + 7 * w) * KB, 7 * w * KB);
    ldl<3>(x.vl16, x.rs, OFF_WQ + ((16 + (x.m & 1)) * 28 + 7 * w + 4) * KB, (7 * w + 4) * KB);
  }
  nop<11>();
  sfor<0, 4>([&](auto k) { pin(acc[k]); });
  stamp(x);
  reduce4(x, acc, [](auto) {});
  wv<0>();
  pin(xr[0]); pin(xr[1]);
  if (L > 0) {
    res_epilogue<true>(x, acc[0], xr, 0, 0, tt, nt, out);
  } else {
    res_epilogue<false>(x, acc[0], xr, OFF_X + 2 * SZ_X, OFF_ST + 2 * SZ_ST, tt, nt);
    wv<0>();
    signal(x, FL_X(x.c, th), 1);
  }
}

#define STAMP_SYNC
__global__ __launch_bounds__(256, 1) void k_main(Args a) {
  Ctx x;
  x.rs = mkrs(a.ws, WS_USED);
  x.w = __builtin_amdgcn_readfirstlane(threadIdx.x >> 6);
  asm volatile("s_nop 4" : "+s"(x.w));
  { const int b = blockIdx.x, r = b >> 3; x.c = 2 * (b & 7) + (r >> 4); x.m = r & 15; }
  { Ctx y = x; fresh(y); stamp(y); phase_first(y, a); STAMP_SYNC }
  for (int L = 0; L < 2; ++L) {
    f32x16 g2[3];
    int Lo = L; asm volatile("" : "+s"(Lo));
    { Ctx y = x; fresh(y); phase_att(y, Lo, g2); stamp(y); STAMP_SYNC }
    { Ctx y = x; fresh(y); phase_oproj(y, Lo, a, g2); stamp(y); STAMP_SYNC }
    { Ctx y = x; fresh(y); phase_gu(y, Lo); stamp(y); fresh(y); phase_down(y, Lo, a.out); stamp(y); STAMP_SYNC }
  }
  fresh(x);
  if (blockIdx.x == 0 && wave0(x)) {
    get_tag(x);
    if (x.l == 0) st2<0>(u32x2{x.tlo, x.thi}, 0, x.rs, OFF_TAG);
  }
  stamp_flush(x);
  asm volatile(".Lk_end:");
}

extern "C" void kernel_launch(void* const* d_in, const int* in_sizes, int n_in, void* d_out, int out_size, void* d_ws, size_t ws_size,
                              hipStream_t stream) {
  Args a;
  a.x = (const char*)d_in[0]; a.wq = (const char*)d_in[1]; a.wk = (const char*)d_in[2]; a.wv = (const char*)d_in[3];
  a.wo = (const char*)d_in[4]; a.wg = (const char*)d_in[6]; a.wu = (const char*)d_in[7]; a.wd = (const char*)d_in[8];
  a.ws = (char*)d_ws; a.out = (char*)d_out;
  (void)hipFuncSetAttribute((const void*)k_main, hipFuncAttributeMaxDynamicSharedMemorySize, 160 * 1024);
  k_main<<<256, 256, 160 * 1024, stream>>>(a);
}
```

```cpp
#include <hip/hip_runtime.h>
#include <stdint.h>
#include <type_traits>

typedef float    f32x16 __attribute__((ext_vector_type(16)));
typedef float    f32x4  __attribute__((ext_vector_type(4)));
typedef float    f32x2  __attribute__((ext_vector_type(2)));
typedef unsigned u32x4  __attribute__((ext_vector_type(4)));
typedef unsigned u32x2  __attribute__((ext_vector_type(2)));
typedef __bf16   bf16x2 __attribute__((ext_vector_type(2)));
typedef __amdgpu_buffer_rsrc_t rsrc_t;
#define DI __device__ __forceinline__
#define GAS __attribute__((address_space(1)))
#define SB() __builtin_amdgcn_sched_barrier(0)

constexpr int S = 2048, D = 448, F = 1792;
constexpr unsigned KB = 1024;
constexpr unsigned OFF_FLAG = 0;
constexpr unsigned OFF_CSQ  = 64 * KB;
constexpr unsigned OFF_CSGU = OFF_CSQ + 4 * KB;
constexpr unsigned OFF_SLOT = OFF_CSGU + 32 * KB;
constexpr unsigned OFF_TAG  = OFF_SLOT + 16 * KB;
constexpr unsigned OFF_WQ   = 128 * KB;
constexpr unsigned OFF_WO   = OFF_WQ + 18 * 28 * KB;
constexpr unsigned OFF_WGU  = OFF_WO + 14 * 32 * KB;
constexpr unsigned OFF_WD   = OFF_WGU + 16 * 196 * KB;
constexpr unsigned SZ_X = 64 * 28 * KB, SZ_ST = 14 * 2048 * 8, SZ_O = 64 * 32 * KB, SZ_H = 64 * 112 * KB, SZ_KV = 64 * 4 * KB;
constexpr unsigned OFF_X    = OFF_WD + 14 * 112 * KB;
constexpr unsigned OFF_ST   = OFF_X + 4 * SZ_X;
constexpr unsigned OFF_O    = OFF_ST + 4 * SZ_ST;
constexpr unsigned OFF_H    = OFF_O + 2 * SZ_O;
constexpr unsigned OFF_KV   = OFF_H + 2 * SZ_H;
constexpr unsigned OFF_TS   = OFF_KV + 2 * SZ_KV;
constexpr unsigned WS_USED  = OFF_TS + 520 * KB;
DI constexpr unsigned FL_X(int c, int th)         { return (c * 16 + th) * 256u; }
DI constexpr unsigned FL_O(int L, int c, int th)  { return (c * 16 + 2 + L * 2 + th) * 256u; }
DI constexpr unsigned FL_X1(int L, int c)         { return (c * 16 + 6 + L) * 256u; }
DI constexpr unsigned FL_H(int L, int c, int p)   { return (c * 16 + 8 + L * 2 + p) * 256u; }
DI constexpr unsigned FL_W(int c)                 { return (c * 16 + 12) * 256u; }

DI int dn_pos(int ks) { const int q = ks / 28, kl = (ks % 28) >> 1, P = kl / 7, j = kl % 7;
  return 28 * q + 2 * ((j == 2 || j == 6) ? 10 + 2 * P + (j == 6) : 5 * P + (j > 2 ? j - 1 : j)) + (ks & 1); }
constexpr float QSCALE = 0.18898223650461362f * 1.4426950408889634f;
constexpr float M_LIMIT = 48.0f;

template <int I, int N, class Fn> DI void sfor(Fn&& f) { if constexpr (I < N) { f(std::integral_constant<int, I>{}); sfor<I + 1, N>(f); } }

DI unsigned cvtpk(float lo, float hi) { f32x2 v = {lo, hi}; bf16x2 b = __builtin_convertvector(v, bf16x2); return __builtin_bit_cast(unsigned, b); }
DI float bflo(unsigned u) { return __uint_as_float(u << 16); }
DI float bfhi(unsigned u) { return __uint_as_float(u & 0xffff0000u); }
DI float dot2(unsigned a, unsigned b, float c) { return __builtin_amdgcn_fdot2_f32_bf16(__builtin_bit_cast(bf16x2, a), __builtin_bit_cast(bf16x2, b), c, false); }
DI float sum32(float v) { auto r = __builtin_amdgcn_permlane32_swap(__float_as_uint(v), __float_as_uint(v), false, false); return __uint_as_float(r[0]) + __uint_as_float(r[1]); }
DI float sum16(float v) { auto r = __builtin_amdgcn_permlane16_swap(__float_as_uint(v), __float_as_uint(v), false, false); return __uint_as_float(r[0]) + __uint_as_float(r[1]); }
DI float max32(float v) { auto r = __builtin_amdgcn_permlane32_swap(__float_as_uint(v), __float_as_uint(v), false, false); return fmaxf(__uint_as_float(r[0]), __uint_as_float(r[1])); }

DI unsigned umax16(unsigned v) {
  v = max(v, (unsigned)__builtin_amdgcn_update_dpp(0, (int)v, 0x128, 0xf, 0xf, false));
  v = max(v, (unsigned)__builtin_amdgcn_update_dpp(0, (int)v, 0x124, 0xf, 0xf, false));
  v = max(v, (unsigned)__builtin_amdgcn_update_dpp(0, (int)v, 0x122, 0xf, 0xf, false));
  v = max(v, (unsigned)__builtin_amdgcn_update_dpp(0, (int)v, 0x121, 0xf, 0xf, false));
  return v;
}
DI unsigned umax64(unsigned v) {
  v = umax16(v);
  auto a = __builtin_amdgcn_permlane16_swap(v, v, false, false); v = max(a[0], a[1]);
  auto b = __builtin_amdgcn_permlane32_swap(v, v, false, false); return max(b[0], b[1]);
}

typedef short s16x4 __attribute__((ext_vector_type(4)));
DI u32x2 trread(const char* p) { return __builtin_bit_cast(u32x2, __builtin_amdgcn_ds_read_tr16_b64_v4i16((__attribute__((address_space(3))) s16x4*)p)); }
DI u32x4 trfrag(const char* lo, const char* hi) { const u32x2 a = trread(lo), b = trread(hi); return u32x4{a[0], a[1], b[0], b[1]}; }
DI void row_stats(float s, float q, float& mean, float& rstd) {
  mean = s * (1.0f / D);
  const float var = fmaxf(q * (1.0f / D) - mean * mean, 0.f);
  rstd = __builtin_amdgcn_rsqf(var + 1e-5f);
}
DI float fragsum(const u32x4& f, float s) {
#pragma unroll
  for (int d = 0; d < 4; ++d) s = dot2(f[d], 0x3f803f80u, s);
  return s;
}
struct Args { const char *x, *wq, *wk, *wv, *wo, *wg, *wu, *wd; char* ws; char* out; };

#define ACLOB "a0","a1","a2","a3","a4","a5","a6","a7","a8","a9","a10","a11","a12","a13","a14","a15","a16","a17","a18","a19","a20","a21","a22","a23","a24","a25","a26","a27","a28","a29","a30","a31","a32","a33","a34","a35","a36","a37","a38","a39","a40","a41","a42","a43","a44","a45","a46","a47","a48","a49","a50","a51","a52","a53","a54","a55","a56","a57","a58","a59","a60","a61","a62","a63","a64","a65","a66","a67","a68","a69","a70","a71","a72","a73","a74","a75","a76","a77","a78","a79","a80","a81","a82","a83","a84","a85","a86","a87","a88","a89","a90","a91","a92","a93","a94","a95","a96","a97","a98","a99","a100","a101","a102","a103","a104","a105","a106","a107","a108","a109","a110","a111","a112","a113","a114","a115","a116","a117","a118","a119","a120","a121","a122","a123","a124","a125","a126","a127","a128","a129","a130","a131","a132","a133","a134","a135","a136","a137","a138","a139","a140","a141","a142","a143","a144","a145","a146","a147","a148","a149","a150","a151","a152","a153","a154","a155","a156","a157","a158","a159","a160","a161","a162","a163","a164","a165","a166","a167","a168","a169","a170","a171","a172","a173","a174","a175","a176","a177","a178","a179","a180","a181","a182","a183","a184","a185","a186","a187","a188","a189","a190","a191","a192","a193","a194","a195","a196","a197","a198","a199","a200","a201","a202","a203","a204","a205","a206","a207","a208","a209","a210","a211","a212","a213","a214","a215","a216","a217","a218","a219","a220","a221","a222","a223","a224","a225","a226","a227","a228","a229","a230","a231","a232","a233","a234","a235","a236","a237","a238","a239","a240","a241","a242","a243","a244","a245","a246","a247","a248","a249","a250","a251","a252","a253","a254","a255"

template <int IMM> DI void ld4(u32x4& d, unsigned vo, rsrc_t rs, unsigned so) {
  asm volatile("buffer_load_dwordx4 %0, %1, %2, %3 offen offset:%c4 sc1" : "=v"(d) : "v"(vo), "s"(rs), "s"(so), "i"(IMM));
}
template <int IMM> DI void ld2(f32x2& d, unsigned vo, rsrc_t rs, unsigned so) {
  asm volatile("buffer_load_dwordx2 %0, %1, %2, %3 offen offset:%c4 sc1" : "=v"(d) : "v"(vo), "s"(rs), "s"(so), "i"(IMM));
}
template <int IMM> DI void ld4i(u32x4& d, unsigned vo, rsrc_t rs, unsigned so) {
  asm volatile("buffer_load_dwordx4 %0, %1, %2, %3 offen offset:%c4" : "=v"(d) : "v"(vo), "s"(rs), "s"(so), "i"(IMM));
}
template <int IMM> DI void ld2i(u32x2& d, unsigned vo, rsrc_t rs, unsigned so) {
  asm volatile("buffer_load_dwordx2 %0, %1, %2, %3 offen offset:%c4" : "=v"(d) : "v"(vo), "s"(rs), "s"(so), "i"(IMM));
}
template <int IMM> DI void ld2u(u32x2& d, unsigned vo, rsrc_t rs, unsigned so) {
  asm volatile("buffer_load_dwordx2 %0, %1, %2, %3 offen offset:%c4 sc1" : "=v"(d) : "v"(vo), "s"(rs), "s"(so), "i"(IMM));
}
template <int IMM> DI void ld4f(f32x4& d, unsigned vo, rsrc_t rs, unsigned so) {
  asm volatile("buffer_load_dwordx4 %0, %1, %2, %3 offen offset:%c4 sc1" : "=v"(d) : "v"(vo), "s"(rs), "s"(so), "i"(IMM));
}
DI void ld1(float& d, unsigned vo, rsrc_t rs, unsigned so) {
  asm volatile("buffer_load_dword %0, %1, %2, %3 offen sc1" : "=v"(d) : "v"(vo), "s"(rs), "s"(so));
}
template <int AR, int IMM> DI void lda(unsigned vo, rsrc_t rs, unsigned so) {
  asm volatile("buffer_load_dwordx4 a[%c3:%c4], %0, %1, %2 offen offset:%c5 sc1" :: "v"(vo), "s"(rs), "s"(so), "i"(AR), "i"(AR + 3), "i"(IMM) : ACLOB);
}
template <int N> DI void ldl(unsigned vo, rsrc_t rs, unsigned so, unsigned lds) {
  asm volatile("s_mov_b32 m0, %3\n\ts_nop 0\n\tbuffer_load_dwordx4 %0, %1, %2 offen sc1 lds" :: "v"(vo), "s"(rs), "s"(so), "s"(lds) : "memory");
  if constexpr (N > 1) asm volatile("buffer_load_dwordx4 %0, %1, %2 offen offset:1024 sc1 lds" :: "v"(vo), "s"(rs), "s"(so) : "memory");
  if constexpr (N > 2) asm volatile("buffer_load_dwordx4 %0, %1, %2 offen offset:2048 sc1 lds" :: "v"(vo), "s"(rs), "s"(so) : "memory");
  if constexpr (N > 3) asm volatile("buffer_load_dwordx4 %0, %1, %2 offen offset:3072 sc1 lds" :: "v"(vo), "s"(rs), "s"(so) : "memory");
}
template <int IMM> DI void st4(const u32x4& d, unsigned vo, rsrc_t rs, unsigned so) {
  asm volatile("buffer_store_dwordx4 %0, %1, %2, %3 offen offset:%c4 sc1\n\ts_nop 1" :: "v"(d), "v"(vo), "s"(rs), "s"(so), "i"(IMM) : "memory");
}
template <int IMM> DI void st4p(const u32x4& d, unsigned vo, rsrc_t rs, unsigned so) {
  asm volatile("buffer_store_dwordx4 %0, %1, %2, %3 offen offset:%c4\n\ts_nop 1" :: "v"(d), "v"(vo), "s"(rs), "s"(so), "i"(IMM) : "memory");
}
template <int IMM> DI void st2(const u32x2& d, unsigned vo, rsrc_t rs, unsigned so) {
  asm volatile("buffer_store_dwordx2 %0, %1, %2, %3 offen offset:%c4 sc1" :: "v"(d), "v"(vo), "s"(rs), "s"(so), "i"(IMM) : "memory");
}
DI void st1(float d, unsigned vo, rsrc_t rs, unsigned so) {
  asm volatile("buffer_store_dword %0, %1, %2, %3 offen sc1" :: "v"(d), "v"(vo), "s"(rs), "s"(so) : "memory");
}
template <int N> DI void wv() { asm volatile("s_waitcnt vmcnt(%c0)" :: "i"(N) : "memory"); SB(); }
template <int N> DI void wl() { asm volatile("s_waitcnt lgkmcnt(%c0)" :: "i"(N) : "memory"); SB(); }
template <class T> DI void pin(T& x) { asm volatile("" : "+v"(x)); }
DI void bar() { asm volatile("s_waitcnt lgkmcnt(0)\n\ts_barrier" ::: "memory"); }
template <int N> DI void nop() { asm volatile("s_nop %c0" :: "i"(N)); SB(); }

template <int OFF> DI void trd(u32x2& r, unsigned a) { asm volatile("ds_read_b64_tr_b16 %0, %1 offset:%c2" : "=v"(r) : "v"(a), "i"(OFF)); }
template <int OFF> DI void dsr(u32x4& r, unsigned a) { asm volatile("ds_read_b128 %0, %1 offset:%c2" : "=v"(r) : "v"(a), "i"(OFF)); }

DI void m32(f32x16& c, const u32x4& a, const u32x4& b)  { asm volatile("v_mfma_f32_32x32x16_bf16 %0, %1, %2, %0" : "+v"(c) : "v"(a), "v"(b)); }
DI void m32z(f32x16& c, const u32x4& a, const u32x4& b) { asm volatile("v_mfma_f32_32x32x16_bf16 %0, %1, %2, 0" : "=&v"(c) : "v"(a), "v"(b)); }
template <int AR, bool Z> DI void m32a(f32x16& c, const u32x4& b) {
  if constexpr (Z) asm volatile("v_mfma_f32_32x32x16_bf16 %0, a[%c2:%c3], %1, 0" : "=&v"(c) : "v"(b), "i"(AR), "i"(AR + 3));
  else             asm volatile("v_mfma_f32_32x32x16_bf16 %0, a[%c2:%c3], %1, %0" : "+v"(c) : "v"(b), "i"(AR), "i"(AR + 3));
}
template <int AR, bool Z> DI void m32b(f32x16& c, const u32x4& a) {
  if constexpr (Z) asm volatile("v_mfma_f32_32x32x16_bf16 %0, %1, a[%c2:%c3], 0" : "=&v"(c) : "v"(a), "i"(AR), "i"(AR + 3));
  else             asm volatile("v_mfma_f32_32x32x16_bf16 %0, %1, a[%c2:%c3], %0" : "+v"(c) : "v"(a), "i"(AR), "i"(AR + 3));
}
template <bool Z> DI void m16(f32x4& c, const u32x4& a, const u32x4& b) {
  if constexpr (Z) asm volatile("v_mfma_f32_16x16x32_bf16 %0, %1, %2, 0" : "=&v"(c) : "v"(a), "v"(b));
  else             asm volatile("v_mfma_f32_16x16x32_bf16 %0, %1, %2, %0" : "+v"(c) : "v"(a), "v"(b));
}

template <bool Z, int AR> DI void m16a(f32x4& c, const u32x4& a) {
  if constexpr (Z) asm volatile("v_mfma_f32_16x16x32_bf16 %0, %1, a[%c2:%c3], 0" : "=&v"(c) : "v"(a), "i"(AR), "i"(AR + 3));
  else             asm volatile("v_mfma_f32_16x16x32_bf16 %0, %1, a[%c2:%c3], %0" : "+v"(c) : "v"(a), "i"(AR), "i"(AR + 3));
}
DI void ldflag(unsigned& v, rsrc_t rs, unsigned so) { asm volatile("buffer_load_dword %0, off, %1, %2 sc1" : "=v"(v) : "s"(rs), "s"(so)); }

struct Ctx {
  rsrc_t rs;
  unsigned tlo, thi;
  unsigned vl16;
  unsigned vg16;
  int w, l, c, m;
  unsigned fa;
};
DI void fresh(Ctx& x) {
  asm volatile("" : "+s"(x.w), "+s"(x.c), "+s"(x.m));
  asm volatile("v_mbcnt_lo_u32_b32 %0, -1, 0\n\tv_mbcnt_hi_u32_b32 %0, -1, %0" : "=v"(x.l));
  x.vl16 = x.l * 16;
  const int g = x.l >> 4;
  x.vg16 = ((g >> 1) * 64 + (g & 1) * 32 + (x.l & 15)) * 16;
}
extern __shared__ __attribute__((aligned(16))) char lds[];
constexpr unsigned SCR = 112 * KB;

DI void pstamp(Ctx&) {}
DI void fstamp(Ctx&) {}
DI void stamp(Ctx&) {}
DI void stamp_flush(Ctx&) {}

DI void get_tag(Ctx& x) {
  unsigned lo, hi;
  asm volatile("v_accvgpr_read_b32 v255, a252\n\ts_nop 1\n\tv_readfirstlane_b32 %0, v255\n\tv_accvgpr_read_b32 v255, a253\n\ts_nop 1\n\tv_readfirstlane_b32 %1, v255\n\ts_nop 4"
               : "=s"(lo), "=s"(hi) :: "v255");
  const unsigned long long e = ((unsigned long long)hi << 32) | lo;
  unsigned long long n = e * 0x9E3779B97F4A7C15ull + 0x632BE59BD9B4E019ull;
  if (((n ^ e) >> 16) == 0) n += 0x10000;
  x.tlo = (unsigned)n; x.thi = (unsigned)(n >> 32);
}
DI bool wave0(const Ctx& x) { int w = x.w; asm volatile("" : "+s"(w)); return w == 0; }
DI void count(Ctx& x, unsigned fl, int n) {
  if (wave0(x)) {
    const unsigned long long mask = (1ull << n) - 1;
    unsigned long long keep;
    asm volatile("s_mov_b64 %0, exec\n\ts_mov_b64 exec, %5\n\tbuffer_atomic_add %1, %2, %3, %4 offen\n\ts_mov_b64 exec, %0"
                 : "=&s"(keep) : "v"(1u), "v"(x.l * 256), "s"(x.rs), "s"(OFF_FLAG + fl), "s"(mask) : "memory");
  }
}
DI void signal(Ctx& x, unsigned fl, int n) { bar(); count(x, fl, n); }
DI void await(Ctx& x, unsigned fl, unsigned target) {
  if (wave0(x)) {
#pragma nounroll
    for (unsigned spin = 0; spin < (1u << 18); ++spin) {
      unsigned v;
      asm volatile("buffer_load_dword %0, off, %1, %2 sc1\n\ts_waitcnt vmcnt(0)" : "=v"(v) : "s"(x.rs), "s"(OFF_FLAG + fl) : "memory");
      if ((unsigned)__builtin_amdgcn_readfirstlane(v) >= target) break;
      __builtin_amdgcn_s_sleep(1);
    }
  }
  bar();
}

DI void recheck(Ctx& x, unsigned v, unsigned fl, unsigned target) {
  pin(v);
#pragma nounroll
  for (unsigned spin = 0; (unsigned)__builtin_amdgcn_readfirstlane(v) < target && spin < (1u << 18); ++spin) {
    __builtin_amdgcn_s_sleep(2);
    asm volatile("buffer_load_dword %0, off, %1, %2 sc1\n\ts_waitcnt vmcnt(0)" : "=v"(v) : "s"(x.rs), "s"(OFF_FLAG + fl) : "memory");
  }
  SB();
}

template <class Hook> DI void reduce4(Ctx& x, f32x16 (&acc)[4], Hook&& hook) {
  sfor<1, 4>([&](auto k) {
    f32x4* p = (f32x4*)(lds + SCR + ((x.w ^ k) * 3 + k - 1) * 4096 + x.vl16);
#pragma unroll
    for (int q = 0; q < 4; ++q) p[q * 64] = f32x4{acc[k][4 * q], acc[k][4 * q + 1], acc[k][4 * q + 2], acc[k][4 * q + 3]};
    SB(); hook(k); SB();
  });
  bar();
  const f32x4* p = (const f32x4*)(lds + SCR + x.w * 3 * 4096 + x.vl16);
#pragma unroll
  for (int k = 0; k < 3; ++k)
#pragma unroll
    for (int q = 0; q < 4; ++q) {
      f32x4 v = p[k * 256 + q * 64];
#pragma unroll
      for (int i = 0; i < 4; ++i) acc[0][4 * q + i] += v[i];
    }
}

constexpr int A_WO = 184, A_WD = 0, A_GX = 224;
constexpr int dn_ks(int o) { return o < 20 ? 2 * ((o >> 1) % 5 + ((o >> 1) % 5 >= 2) + 7 * (o / 10)) + (o & 1) : 2 * (((o - 20) >> 1) * 4 - ((o - 20) >> 2) + 2) + (o & 1); }
static_assert(dn_ks(0) == 0 && dn_ks(4) == 6 && dn_ks(9) == 11 && dn_ks(10) == 14 && dn_ks(19) == 25 && dn_ks(20) == 4 && dn_ks(22) == 12 && dn_ks(24) == 18 && dn_ks(27) == 27, "");
struct Two { unsigned s0, s1; };
constexpr int SL_WQ = 56;
template <int J> DI void pf_wq(Ctx& x) {
  ldl<1>(x.vl16, x.rs, OFF_WQ + ((x.m & 7) * 56 + 14 * x.w + J) * KB, (SL_WQ + 14 * x.w + J) * KB);
}
DI Two base_wo(const Ctx& x) { const int ct = x.m % 7;
  return Two{OFF_WO + ((2 * ct + (x.w & 1)) * 32 + 8 * x.w) * KB, OFF_WO + ((2 * ct + ((x.w & 1) ^ 1)) * 32 + 8 * x.w) * KB}; }
DI void pf_wo(Ctx& x) {
  const Two b = base_wo(x);
  sfor<0, 8>([&](auto i) {
    lda<A_WO + 8 * i, (i & 3) * 1024>(x.vl16, x.rs, b.s0 + (i >> 2) * 4096);
    lda<A_WO + 8 * i + 4, (i & 3) * 1024>(x.vl16, x.rs, b.s1 + (i >> 2) * 4096);
  });
}
DI Two base_wd(const Ctx& x) { const int ct = x.m % 7;
  return Two{OFF_WD + ((2 * ct + (x.w & 1)) * 112 + 28 * x.w) * KB, OFF_WD + ((2 * ct + ((x.w & 1) ^ 1)) * 112 + 28 * x.w) * KB}; }
template <int I0, int I1> DI void pf_wd(Ctx& x, const Two& b) {
  sfor<I0, I1>([&](auto f) {
    constexpr int i = f >> 1;
    lda<A_WD + 4 * f, (i & 3) * 1024>(x.vl16, x.rs, ((f & 1) ? b.s1 : b.s0) + (i >> 2) * 4096);
  });
}
DI void pf_wgu1(Ctx& x) {
  for (int t = 0; t < 14; ++t) ldl<2>(x.vl16, x.rs, OFF_WGU + (x.m * 196 + 8 * t + 2 * x.w) * KB, (8 * t + 2 * x.w) * KB);
}
template <int J> DI void pf_wgu3(Ctx& x) {
  ldl<1>(x.vl16, x.rs, OFF_WGU + (x.m * 196 + 160 + 9 * x.w + J) * KB, (9 * x.w + J) * KB);
}

constexpr unsigned S_QP = SCR, S_KP = SCR + 16384, S_QF = SCR + 24576, S_ST = SCR + 32768, S_CS = SCR + 33792, S_KM = SCR + 36864, S_TR = SCR + 40960;
DI void kv_finish(Ctx& x, const float (&raw)[16], const f32x4 (&cs)[4], float mean, float rstd, bool isv, unsigned so, int slot) {
  const int h = x.l >> 5, r = x.l & 31;
  u32x4 y[2];
#pragma unroll
  for (int d = 0; d < 8; ++d)
    y[d >> 2][d & 3] = cvtpk((raw[2 * d] - mean * cs[d >> 1][2 * (d & 1)]) * rstd, (raw[2 * d + 1] - mean * cs[d >> 1][2 * (d & 1) + 1]) * rstd);
  if (h) { y[1][2] = 0; y[1][3] = 0; }
  unsigned nb = 0;
  if (!isv) {
    float n2 = 0.f;
#pragma unroll
    for (int d = 0; d < 8; ++d) n2 = dot2(y[d >> 2][d & 3], y[d >> 2][d & 3], n2);
    n2 = sum32(n2);
    nb = umax64(n2 < 3.0e38f ? __float_as_uint(n2) : 0x7f800000u);
    nb = cvtpk(1.01f * __builtin_amdgcn_sqrtf(__uint_as_float(nb)), 0.f);
    if (h) y[1][2] = 0x3f80u;
  } else {
    if (h) y[1][2] = 0x3f80u;
    char* im = lds + S_TR + slot * 2048;
#pragma unroll
    for (int a = 0; a < 4; ++a) *(u32x2*)(im + r * 64 + 16 * a + 8 * h) = u32x2{y[a >> 1][2 * (a & 1)], y[a >> 1][2 * (a & 1) + 1]};
    asm volatile("s_waitcnt lgkmcnt(0)" ::: "memory");
    const char* tb = im + (4 * h + ((x.l >> 2) & 3)) * 64 + ((x.l >> 4) & 1) * 32 + (x.l & 3) * 8;
    y[0] = trfrag(tb, tb + 512); y[1] = trfrag(tb + 1024, tb + 1536);
  }
  if (x.l == 0) ((unsigned*)(lds + S_KM))[slot] = nb;
  st4<0>(y[0], x.vl16, x.rs, so); st4<1024>(y[1], x.vl16, x.rs, so);
}
DI void kv_publish(Ctx& x, int L, int p) {
  if (wave0(x)) {
    const u32x2 k = *(const u32x2*)(lds + S_KM);
    if (x.l < 16) st2<0>(u32x2{(x.tlo & 0xffff0000u) | max(k[0], k[1]), x.thi}, x.l * 512, x.rs, OFF_SLOT + (L * 1024 + p) * 8);
  }
}
DI void q_finish(Ctx& x, const f32x16& qa, const f32x4 (&cq)[4], float mean, float rstd) {
  const int h = x.l >> 5;
  u32x4 qf[2];
  const f32x4* p = (const f32x4*)(lds + S_QP + x.w * 4096 + x.vl16);
  const float sc = rstd * QSCALE;
#pragma unroll
  for (int q = 0; q < 4; ++q) {
    const f32x4 o = p[q * 64];
    float v[4];
#pragma unroll
    for (int i = 0; i < 4; ++i) v[i] = (qa[4 * q + i] + o[i] - mean * cq[q][i]) * sc;
    qf[q >> 1][2 * (q & 1)] = cvtpk(v[0], v[1]); qf[q >> 1][2 * (q & 1) + 1] = cvtpk(v[2], v[3]);
  }
  if (h) { qf[1][2] = 0; qf[1][3] = 0; }
  float qn2 = 0.f, qn3 = 0.f;
#pragma unroll
  for (int d = 0; d < 4; ++d) { qn2 = dot2(qf[0][d], qf[0][d], qn2); qn3 = dot2(qf[1][d], qf[1][d], qn3); }
  qn2 = sum32(qn2 + qn3);
  if (h) qf[1][2] = cvtpk(1.01f * __builtin_amdgcn_sqrtf(qn2), 0.f);
  ((u32x4*)(lds + S_QF + x.w * 2048 + x.vl16))[0] = qf[0];
  ((u32x4*)(lds + S_QF + x.w * 2048 + x.vl16))[64] = qf[1];
}
DI void put_tile(char* p, const f32x16& t) {
#pragma unroll
  for (int q = 0; q < 4; ++q) ((f32x4*)p)[q * 64] = f32x4{t[4 * q], t[4 * q + 1], t[4 * q + 2], t[4 * q + 3]};
}

constexpr unsigned IMG_Q = 0, IMG_T = 56 * KB;
DI rsrc_t mkrs(const char* p, unsigned n) { return __builtin_amdgcn_make_buffer_rsrc((char*)p, 0, n, 0x00020000); }

DI void phase_pre(Ctx& x, const Args& a) {
  const int w = x.w, l = x.l, h = l >> 5, r = l & 31;
  const int th = x.m >> 3, hp = x.m & 7, tl = w & 1, kh = w >> 1, tt = 4 * x.c + 2 * th + tl, wi = 2 * x.c + th;
  const bool kvp = hp < 2;
  { unsigned long long pc; unsigned len;
    asm volatile("s_getpc_b64 %0\n.Lk_begin:\n\ts_mov_b32 %1, .Lk_end-.Lk_begin" : "=s"(pc), "=s"(len));
    const rsrc_t rc = mkrs((const char*)pc, len);
    asm volatile("buffer_load_dwordx4 a[112:115], %0, %1, %2 offen\n\tbuffer_load_dwordx4 a[112:115], %0, %1, %2 offen offset:1024"
                 :: "v"(x.vl16), "s"(rc), "s"((unsigned)(hp * 8 + 2 * w) * KB) : ACLOB); }
  u32x4 xf[14];
  { const rsrc_t rx = mkrs(a.x, S * D * 2);
    sfor<0, 14>([&](auto i) { ld4i<32 * i>(xf[i], r * 896 + h * 16, rx, tt * 32 * 896 + kh * 448); }); }
  asm volatile("buffer_load_dwordx2 a[252:253], off, %0, %1 sc1" :: "s"(x.rs), "s"(OFF_TAG) : ACLOB);
  pstamp(x);
  const int task = wi * 6 + hp - 2;
  auto ask_q = [&]() {
    const rsrc_t rq = mkrs(a.wq, D * D * 2);
    const unsigned vo = (8 * w + (l >> 3)) * 896 + (l & 7) * 16;
    sfor<0, 14>([&](auto i) { ldl<1>(vo, rq, hp * 112 + i * 32 * 896, IMG_Q + (4 * i + w) * KB); });
  };
  auto ask_t = [&](const char* tp, unsigned tn, unsigned ts, unsigned tb) {
    const rsrc_t rt = mkrs(tp, tn);
    const unsigned vo = (8 * w + (l >> 3)) * ts + (l & 7) * 16;
    sfor<0, 7>([&](auto i) { ldl<1>(vo, rt, tb + i * 32 * ts, IMG_T + (4 * i + w) * KB); });
  };
  if (kvp) ask_t(hp ? a.wv : a.wk, D * 56, 128, 0);
  ask_q();
  pstamp(x);
  stamp(x);
  wv<15>();
  pstamp(x);
  {
    float s = 0.f, q = 0.f, s1 = 0.f, q1 = 0.f;
    sfor<0, 14>([&](auto i) {
      pin(xf[i]);
      s = dot2(xf[i][0], 0x3f803f80u, s); q = dot2(xf[i][0], xf[i][0], q); s1 = dot2(xf[i][1], 0x3f803f80u, s1); q1 = dot2(xf[i][1], xf[i][1], q1);
      s = dot2(xf[i][2], 0x3f803f80u, s); q = dot2(xf[i][2], xf[i][2], q); s1 = dot2(xf[i][3], 0x3f803f80u, s1); q1 = dot2(xf[i][3], xf[i][3], q1);
    });
    s = sum32(s + s1); q = sum32(q + q1);
    if (h == 0) ((f32x2*)(lds + S_ST))[w * 32 + r] = f32x2{s, q};
  }
  pstamp(x);
  if (!kvp) {
    if (task < 112) ask_t(task < 56 ? a.wg : a.wu, D * F * 2, F * 2, (task & 1) * 224 * F * 2 + ((task % 56) >> 1) * 128);
    else if (task < 168) ask_t(a.wd, D * F * 2, 896, ((task - 112) / 7) * 224 * 896 + ((task - 112) % 7) * 128);
    else ask_t(a.wo, task < 182 ? D * D * 2 : 0, 896, ((task - 168) / 7) * 224 * 896 + ((task - 168) % 7) * 128);
  }
  if (kvp) wv<14>(); else wv<7>();
  bar();
  stamp(x);
  float mean, rstd;
  { const f32x2 p0 = ((const f32x2*)(lds + S_ST))[tl * 32 + r], p1 = ((const f32x2*)(lds + S_ST))[(tl + 2) * 32 + r];
    row_stats(p0[0] + p1[0], p0[1] + p1[1], mean, rstd); }
  float* csw = (float*)(lds + S_CS);
  const int tro = 8 * h + ((l >> 2) & 3), tco = ((l >> 4) & 1) * 32 + (l & 3) * 8;
  if (kvp) {
    f32x16 kv; float cs = 0.f;
    const unsigned tb = IMG_T + (224 * kh + tro) * 56 + tco;
    u32x2 f[14][2];
    auto rd = [&](auto i) { trd<i * 896>(f[i][0], tb); trd<i * 896 + 224>(f[i][1], tb); };
    sfor<0, 6>(rd);
    sfor<0, 14>([&](auto i) {
      if constexpr (i + 6 < 14) { rd(std::integral_constant<int, i + 6>{}); wl<12>(); } else wl<2 * (13 - i)>();
      pin(f[i][0]); pin(f[i][1]);
      u32x4 g = {f[i][0][0], f[i][0][1], f[i][1][0], f[i][1][1]};
      pin(g); nop<1>();
      if constexpr (i == 0) m32z(kv, g, xf[i]); else m32(kv, g, xf[i]);
      cs = fragsum(g, cs); pin(cs);
    });
    cs = sum32(cs);
    if (h == 0) csw[(w * 3 + 2) * 32 + r] = cs;
    nop<11>(); pin(kv);
    pstamp(x);
    if (kh) put_tile(lds + S_KP + tl * 4096 + x.vl16, kv);
    if (x.m == 0 && w == 3) { if (l < 16) st1(0.f, l * 256, x.rs, OFF_FLAG + x.c * 4096); }
    bar();
    if (!kh) {
      float raw[16]; f32x4 c4[4];
      sfor<0, 4>([&](auto q) {
        const f32x4 o = ((const f32x4*)(lds + S_KP + tl * 4096 + x.vl16))[q * 64];
        c4[q] = *(const f32x4*)(csw + (w * 3 + 2) * 32 + 8 * q + 4 * h) + *(const f32x4*)(csw + ((w + 2) * 3 + 2) * 32 + 8 * q + 4 * h);
#pragma unroll
        for (int i = 0; i < 4; ++i) raw[4 * q + i] = kv[4 * q + i] + o[i];
      });
      kv_finish(x, raw, c4, mean, rstd, hp == 1, OFF_KV + (tt * 4 + 2 * hp) * KB, tl);
      if (tt == 0 && r == 0) sfor<0, 4>([&](auto q) { st4<32 * q>(__builtin_bit_cast(u32x4, c4[q]), h * 16, x.rs, OFF_CSQ + (16 + hp) * 128); });
    }
    pstamp(x);
    wv<0>();
    bar();
    get_tag(x);
    kv_publish(x, 0, 2 * wi + hp);
    pstamp(x);
  } else { pstamp(x); pstamp(x); pstamp(x); }
  f32x16 qa, qb;
  {
    float ca = 0.f;
    const unsigned ta = IMG_Q + (224 * kh + tro) * 128 + tco + 56 * kh, tb = IMG_Q + (224 * kh + tro) * 128 + tco + 56 * (kh ^ 1);
    u32x2 f[14][4];
    auto rd = [&](auto i) { trd<i * 2048>(f[i][0], tb); trd<i * 2048 + 512>(f[i][1], tb); trd<i * 2048>(f[i][2], ta); trd<i * 2048 + 512>(f[i][3], ta); };
    wl<0>();
    sfor<0, 3>(rd);
    sfor<0, 14>([&](auto i) {
      if constexpr (i + 3 < 14) { wl<8>(); rd(std::integral_constant<int, i + 3>{}); } else wl<4 * (13 - i)>();
      sfor<0, 4>([&](auto e) { pin(f[i][e]); });
      u32x4 gb = {f[i][0][0], f[i][0][1], f[i][1][0], f[i][1][1]}, ga = {f[i][2][0], f[i][2][1], f[i][3][0], f[i][3][1]};
      pin(ga); pin(gb); nop<1>();
      if constexpr (i == 0) m32z(qb, gb, xf[i]); else m32(qb, gb, xf[i]);
      if constexpr (i == 0) m32z(qa, ga, xf[i]); else m32(qa, ga, xf[i]);
      if (kh == tl) ca = fragsum(ga, ca); else ca = fragsum(gb, ca);
      pin(ca);
    });
    pstamp(x);
    ca = sum32(ca);
    if (h == 0) csw[(w * 3) * 32 + r] = ca;
  }
  nop<11>(); pin(qa); pin(qb);
  put_tile(lds + S_QP + (w ^ 2) * 4096 + x.vl16, qb);
  pstamp(x);
  wv<0>();
  bar();
  stamp(x);
  const bool own = x.c == hp && th == 0;
  {
    f32x4 cq[4];
    sfor<0, 4>([&](auto q) {
      cq[q] = *(const f32x4*)(csw + (kh * 3) * 32 + 8 * q + 4 * h) + *(const f32x4*)(csw + ((kh + 2) * 3) * 32 + 8 * q + 4 * h);
      if (own && tl == 0 && r == 0) st4<32 * q>(__builtin_bit_cast(u32x4, cq[q]), h * 16, x.rs, OFF_CSQ + (2 * hp + kh) * 128);
    });
    q_finish(x, qa, cq, mean, rstd);
  }
  pstamp(x);
  const int sro = 4 * h + ((l >> 2) & 3);
  if (own) {
    const char* tb = lds + IMG_Q + (224 * (w & 1) + sro) * 128 + tco + 56 * (w >> 1);
    sfor<0, 14>([&](auto i) { st4<(i & 3) * 1024>(trfrag(tb + i * 2048, tb + i * 2048 + 1024), x.vl16, x.rs, OFF_WQ + (hp * 56 + 14 * w + (i & ~3)) * KB); });
  }
  if (kvp) {
    if (wi == 8) {
      const char* tb = lds + IMG_T + (112 * w + sro) * 56 + tco;
      sfor<0, 7>([&](auto i) { st4<(i & 3) * 1024>(trfrag(tb + i * 896, tb + i * 896 + 448), x.vl16, x.rs, OFF_WQ + ((16 + hp) * 28 + 7 * w + (i & ~3)) * KB); });
    }
  } else if (task < 112) {
    const int g = l >> 4, mat = task >= 56, gt = 4 * ((task % 56) >> 1) + w, m = gt / 7, j = gt % 7, lt = (m & 1) ? j : (j == 6 ? 0 : j + 1);
    const char* tb = lds + IMG_T + (16 * (g >> 1) + 4 * (g & 1) + ((l >> 2) & 3)) * 128 + 32 * w + (l & 3) * 8;
    const unsigned st = lt < 5 ? 10 * KB : 4 * KB, so = OFF_WGU + (m * 196 + (lt < 5 ? lt * 2 + mat : 140 + (lt - 5) * 2 + mat)) * KB + (task & 1) * 7 * st;
    u32x4 f[7];
    sfor<0, 7>([&](auto i) { f[i] = trfrag(tb + i * 4096, tb + i * 4096 + 1024); });
    float cs = 0.f;
    sfor<0, 7>([&](auto i) { st4<0>(f[i], x.vl16, x.rs, so + i * st); cs = fragsum(f[i], cs); });
    cs = sum32(sum16(cs));
    if (l < 16) st1(cs, l * 4, x.rs, OFF_CSGU + m * 2048 + (task & 1) * 1024 + (lt * 2 + mat) * 64);
  } else if (task < 168) {
    const int nt = 2 * ((task - 112) % 7) + (w & 1), k0 = 14 * ((task - 112) / 7);
    const char* tb = lds + IMG_T + sro * 128 + 64 * (w & 1) + tco;
    u32x4 f[7];
    sfor<0, 7>([&](auto i) { const int ks = 7 * (w >> 1) + i; const char* p = tb + (32 * (ks >> 1) + 8 * (ks & 1)) * 128; f[i] = trfrag(p, p + 2048); });
    sfor<0, 7>([&](auto i) { st4<0>(f[i], x.vl16, x.rs, OFF_WD + (nt * 112 + dn_pos(k0 + 7 * (w >> 1) + i)) * KB); });
  } else if (task < 182) {
    const int nt = 2 * ((task - 168) % 7) + (w & 1);
    const char* tb = lds + IMG_T + (112 * (w >> 1) + sro) * 128 + 64 * (w & 1) + tco;
    u32x4 f[8];
    sfor<0, 8>([&](auto i) { const char* p = tb + (28 * (i >> 1) + 16 * (i & 1)) * 128; f[i] = trfrag(p, p + 1024); if ((i & 1) && h) { f[i][2] = 0; f[i][3] = 0; } });
    sfor<0, 8>([&](auto i) { st4<(i & 3) * 1024>(f[i], x.vl16, x.rs, OFF_WO + (nt * 32 + 16 * ((task - 168) / 7) + 8 * (w >> 1) + (i & ~3)) * KB); });
  }
}

DI void ask_slots(Ctx& x, int L) {
  if (wave0(x)) asm volatile("buffer_load_dwordx2 a[250:251], %0, %1, %2 offen sc1" :: "v"(x.l * 8), "s"(x.rs), "s"(OFF_SLOT + (L * 16 + x.c) * 512) : ACLOB);
}
DI void phase_first(Ctx& x, const Args& a) {
  phase_pre(x, a);
  pstamp(x);
  ask_slots(x, 0);
}

DI void phase_att(Ctx& x, const int L, f32x16 (&g2)[3]) {
  const int w = x.w, l = x.l, h = l >> 5, r = l & 31;
  const int th = x.m >> 3, hp = x.m & 7, tl = w & 1, kh = w >> 1, tt = 4 * x.c + 2 * th + tl;
  const unsigned KV = OFF_KV + L * SZ_KV;
  get_tag(x);
  if (L > 0) {
    f32x4 cq[4], ck[4];
    sfor<0, 4>([&](auto q) { ld4f<32 * q>(cq[q], h * 16, x.rs, OFF_CSQ + (2 * hp + kh) * 128); ld4f<32 * q>(ck[q], h * 16, x.rs, OFF_CSQ + (16 + (hp & 1)) * 128); });
    await(x, FL_X(x.c, th), 7);
    stamp(x);
    u32x4 xf[14]; f32x2 sp[7];
    {
      const unsigned so = OFF_X + 2 * SZ_X + (tt * 28 + 14 * kh) * KB;
      sfor<0, 14>([&](auto i) { ld4<(i & 3) * 1024>(xf[i], x.vl16, x.rs, so + (i >> 2) * 4096); });
      sfor<0, 7>([&](auto i) { ld2<0>(sp[i], (h * 7 * 2048 + r) * 8, x.rs, OFF_ST + 2 * SZ_ST + (i * 2048 + tt * 32) * 8); });
    }
    float mean, rstd;
    auto stats = [&]() {
      wv<0>();
      sfor<0, 7>([&](auto i) { pin(sp[i]); });
      sfor<0, 4>([&](auto i) { pin(cq[i]); pin(ck[i]); });
      float s = 0.f, q = 0.f;
#pragma unroll
      for (int i = 0; i < 7; ++i) { s += sp[i][0]; q += sp[i][1]; }
      row_stats(sum32(s), sum32(q), mean, rstd);
    };
    f32x16 qa, qb;
    u32x4 wa[14], wb[14];
    const unsigned pa = x.vl16 + (SL_WQ + 28 * kh + 14 * kh) * KB, pb = x.vl16 + (SL_WQ + 28 * (kh ^ 1) + 14 * kh) * KB;
    if (hp < 2) {
      f32x16 kv;
      sfor<0, 14>([&](auto i) { dsr<i * 1024>(wa[i], x.vl16 + 14 * kh * KB); });
      sfor<0, 14>([&](auto i) {
        wv<20 - i>(); wl<13 - i>();
        if constexpr (i == 0) m32z(kv, wa[i], xf[i]); else m32(kv, wa[i], xf[i]);
      });
      stats();
      nop<11>(); pin(kv);
      if (kh) put_tile(lds + S_KP + tl * 4096 + x.vl16, kv);
      bar();
      if (!kh) {
        float raw[16];
        sfor<0, 4>([&](auto q) {
          const f32x4 o = ((const f32x4*)(lds + S_KP + tl * 4096 + x.vl16))[q * 64];
#pragma unroll
          for (int i = 0; i < 4; ++i) raw[4 * q + i] = kv[4 * q + i] + o[i];
        });
        kv_finish(x, raw, ck, mean, rstd, hp == 1, KV + (tt * 4 + 2 * hp) * KB, tl);
      }
      wl<0>();
      sfor<0, 7>([&](auto i) { dsr<i * 1024>(wb[i], pb); dsr<i * 1024>(wa[i], pa); });
      sfor<0, 14>([&](auto i) {
        if constexpr (i < 7) wl<12>(); else wl<2 * (13 - i)>();
        if constexpr (i == 0) { m32z(qb, wb[i], xf[i]); m32z(qa, wa[i], xf[i]); } else { m32(qb, wb[i], xf[i]); m32(qa, wa[i], xf[i]); }
        if constexpr (i < 7) { dsr<(i + 7) * 1024>(wb[i + 7], pb); dsr<(i + 7) * 1024>(wa[i + 7], pa); }
      });
      wv<0>();
    } else {
      sfor<0, 7>([&](auto i) { dsr<i * 1024>(wb[i], pb); dsr<i * 1024>(wa[i], pa); });
      sfor<0, 14>([&](auto i) {
        wv<20 - i>();
        if constexpr (i < 7) wl<12>(); else wl<2 * (13 - i)>();
        if constexpr (i == 0) { m32z(qb, wb[i], xf[i]); m32z(qa, wa[i], xf[i]); } else { m32(qb, wb[i], xf[i]); m32(qa, wa[i], xf[i]); }
        if constexpr (i < 7) { dsr<(i + 7) * 1024>(wb[i + 7], pb); dsr<(i + 7) * 1024>(wa[i + 7], pa); }
      });
      stats();
    }
    nop<11>();
    pin(qa); pin(qb);
    put_tile(lds + S_QP + (w ^ 2) * 4096 + x.vl16, qb);
    bar();
    if (hp < 2) kv_publish(x, 1, 4 * x.c + 2 * th + hp);
    stamp(x); stamp(x);
    q_finish(x, qa, cq, mean, rstd);
    ask_slots(x, 1);
  }
  stamp(x);
  wv<0>();
  if (wave0(x)) {
    unsigned s0, s1;
#pragma nounroll
    for (unsigned spin = 0; spin < (1u << 18); ++spin) {
      asm volatile("v_accvgpr_read_b32 %0, a250\n\tv_accvgpr_read_b32 %1, a251" : "=v"(s0), "=v"(s1));
      if (__all(s1 == x.thi && ((s0 ^ x.tlo) >> 16) == 0)) break;
      __builtin_amdgcn_s_sleep(2);
      ask_slots(x, L); wv<0>();
    }
    const unsigned km = umax64(s0 << 16);
    if (l == 0) ((unsigned*)(lds + S_KM))[4] = km;
  }
  bar();
  if (L == 0) count(x, FL_W(x.c), 1);
  stamp(x);
  const unsigned kvs = KV + 16 * w * 4096;
  f32x16 kf0, kf1, kf2;
  asm volatile("buffer_load_dwordx4 v[128:131], %2, %3, %4 offen sc1\n\tbuffer_load_dwordx4 v[132:135], %2, %3, %4 offen offset:1024 sc1\n\t"
               "buffer_load_dwordx4 v[136:139], %2, %3, %4 offen offset:2048 sc1\n\tbuffer_load_dwordx4 v[140:143], %2, %3, %4 offen offset:3072 sc1\n\t"
               "buffer_load_dwordx4 v[144:147], %2, %3, %5 offen sc1\n\tbuffer_load_dwordx4 v[148:151], %2, %3, %5 offen offset:1024 sc1\n\t"
               "buffer_load_dwordx4 v[152:155], %2, %3, %5 offen offset:2048 sc1\n\tbuffer_load_dwordx4 v[156:159], %2, %3, %5 offen offset:3072 sc1"
               : "={v[128:143]}"(kf0), "={v[144:159]}"(kf1) : "v"(x.vl16), "s"(x.rs), "s"(kvs), "s"(kvs + 4096));
  asm volatile("buffer_load_dwordx4 v[160:163], %1, %2, %3 offen sc1\n\tbuffer_load_dwordx4 v[164:167], %1, %2, %3 offen offset:1024 sc1\n\t"
               "buffer_load_dwordx4 v[168:171], %1, %2, %3 offen offset:2048 sc1\n\tbuffer_load_dwordx4 v[172:175], %1, %2, %3 offen offset:3072 sc1"
               : "={v[160:175]}"(kf2) : "v"(x.vl16), "s"(x.rs), "s"(kvs + 8192));
  const float kmx = ((const float*)(lds + S_KM))[4];
  u32x4 q[4][2];
#pragma unroll
  for (int k = 0; k < 4; ++k) {
    q[k][0] = ((const u32x4*)(lds + S_QF + (w ^ k) * 2048 + x.vl16))[0];
    q[k][1] = ((const u32x4*)(lds + S_QF + (w ^ k) * 2048 + x.vl16))[64];
  }
  bool safe = true;
  {
#pragma unroll
    for (int k = 0; k < 4; ++k) {
      const float mrow = bflo(q[k][1][2]) * kmx + 0.01f;
      safe = safe && (!h || mrow <= M_LIMIT);
      if (h) q[k][1][2] = cvtpk(-mrow, 0.f);
    }
  }
  const bool fast = __all(safe);
  stamp(x);
  f32x16 o[4];
  if (fast) {
    unsigned so_kv = kvs + 3 * 4096, so_w = OFF_WGU + (x.m * 196 + 2 * w) * KB, so_l = 2 * w * KB, spin = 1u << 16;
    Two ab = base_wo(x);
    const Two wd = base_wd(x);
    asm volatile(
      ".macro GRP s, pa, pb\n\t"
      "v_exp_f32 v200, v[\\s]\n\tv_exp_f32 v201, v[\\s+1]\n\tv_cvt_pk_bf16_f32 v[\\pa], v202, v203\n\t"
      "v_exp_f32 v202, v[\\s+2]\n\tv_exp_f32 v203, v[\\s+3]\n\tv_cvt_pk_bf16_f32 v[\\pb], v200, v201\n\t"
      ".endm\n\t"
      ".macro MEM kind, i, ld\n\t"
      ".if \\kind == 1\n\t"
      "buffer_load_dwordx4 v[128+16*\\ld+4*\\i:128+16*\\ld+4*\\i+3], %[vo], %[rs], %[skv] offen offset:1024*\\i sc1\n\t"
      ".elseif \\kind == 2\n\t"
      "buffer_load_dword v206, v205, %[rs], 0 offen offset:3072 sc1\n\t"
      ".elseif \\kind == 3\n\t"
      ".if att_n < 16 || att_n >= 56\n\t"
      ".if att_n < 102\n\t"
      ".if att_n < 16\n\t.set att_r, 184+4*att_n\n\t.else\n\t.set att_r, 4*(att_n-56)\n\t.endif\n\t"
      ".if att_n & 1\n\t"
      "buffer_load_dwordx4 a[att_r:att_r+3], %[vo], %[rs], %[ab1] offen offset:1024*((att_n>>1)&3) sc1\n\t"
      ".else\n\t"
      "buffer_load_dwordx4 a[att_r:att_r+3], %[vo], %[rs], %[ab0] offen offset:1024*((att_n>>1)&3) sc1\n\t"
      ".endif\n\t"
      ".if att_n == 15\n\t"
      "s_mov_b32 %[ab0], %[wd0]\n\ts_mov_b32 %[ab1], %[wd1]\n\t"
      ".elseif (att_n & 7) == 7\n\t"
      "s_add_u32 %[ab0], %[ab0], 4096\n\ts_add_u32 %[ab1], %[ab1], 4096\n\t"
      ".endif\n\t"
      ".endif\n\t"
      ".elseif att_n < 44\n\t"
      ".if att_n & 1\n\t"
      "buffer_load_dwordx4 %[vo], %[rs], %[sw] offen offset:1024 sc1 lds\n\t"
      "s_add_u32 %[sw], %[sw], 8192\n\ts_add_u32 m0, m0, 8192\n\t"
      ".else\n\t"
      "buffer_load_dwordx4 %[vo], %[rs], %[sw] offen sc1 lds\n\t"
      ".endif\n\t"
      ".else\n\t"
      "buffer_load_dwordx4 v[208+4*(att_n-44):208+4*(att_n-44)+3], %[vo], %[rs], %[sw] offen offset:1024*(att_n&1) sc1\n\t"
      ".if att_n & 1\n\ts_add_u32 %[sw], %[sw], 8192\n\t.endif\n\t"
      ".endif\n\t"
      ".set att_n, att_n+1\n\t"
      ".endif\n\t"
      ".endm\n\t"
      ".macro STEP sc, sn, k, nk, pk, tk, nt, pt, ld, m0k, m0i, m1k, m2k, m2i, m3k\n\t"
      "v_mfma_f32_32x32x16_bf16 v[\\sn:\\sn+15], v[128+16*\\nt:128+16*\\nt+3], v[64+8*\\nk:64+8*\\nk+3], 0\n\t"
      "MEM \\m0k, \\m0i, \\ld\n\t"
      "GRP \\sc, 199, 192\n\t"
      "v_mfma_f32_32x32x16_bf16 v[\\sn:\\sn+15], v[132+16*\\nt:132+16*\\nt+3], v[68+8*\\nk:68+8*\\nk+3], v[\\sn:\\sn+15]\n\t"
      "MEM \\m1k, 0, 0\n\t"
      "GRP \\sc+4, 193, 194\n\t"
      "v_mfma_f32_32x32x16_bf16 v[16*\\pk:16*\\pk+15], v[140+16*\\pt:140+16*\\pt+3], v[196:199], v[16*\\pk:16*\\pk+15]\n\t"
      "MEM \\m2k, \\m2i, \\ld\n\t"
      "GRP \\sc+8, 195, 196\n\t"
      "v_mfma_f32_32x32x16_bf16 v[16*\\k:16*\\k+15], v[136+16*\\tk:136+16*\\tk+3], v[192:195], v[16*\\k:16*\\k+15]\n\t"
      "MEM \\m3k, 0, 0\n\t"
      "GRP \\sc+12, 197, 198\n\t"
      ".endm\n\t"
      ".macro TILE tk, nx, pv, on, wait, p, fl=0\n\t"
      "STEP 96, 112, 0, 1, 3, \\tk, \\tk, \\pv, \\pv, \\p, 0, \\p, \\p, 0, \\fl\n\t"
      "STEP 112, 96, 1, 2, 0, \\tk, \\tk, \\tk, \\pv, \\on, 0, \\p, \\on, 1, 0\n\t"
      "STEP 96, 112, 2, 3, 1, \\tk, \\tk, \\tk, \\pv, \\on, 2, \\p, \\on, 3, 0\n\t"
      ".if \\on\n\ts_add_u32 %[skv], %[skv], 4096\n\t.endif\n\t"
      "s_waitcnt vmcnt(\\wait)\n\t"
      "STEP 112, 96, 3, 0, 2, \\tk, \\nx, \\tk, \\pv, \\p, 0, \\p, \\p, 0, 0\n\t"
      ".endm\n\t"
      ".set att_n, 0\n\t"
      "s_mov_b32 m0, %[sl]\n\t"
      "v_mov_b32 v196, 0\n\tv_mov_b32 v197, 0\n\tv_mov_b32 v198, 0\n\tv_mov_b32 v199, 0\n\tv_mov_b32 v202, 0\n\tv_mov_b32 v203, 0\n\t"
      "v_mov_b32 v188, 0\n\tv_mov_b32 v189, 0\n\tv_mov_b32 v190, 0\n\tv_mov_b32 v191, 0\n\t"
      "v_mfma_f32_32x32x16_bf16 v[0:15], v[196:199], v[196:199], 0\n\t"
      "v_mfma_f32_32x32x16_bf16 v[16:31], v[196:199], v[196:199], 0\n\t"
      "v_mfma_f32_32x32x16_bf16 v[32:47], v[196:199], v[196:199], 0\n\t"
      "v_mfma_f32_32x32x16_bf16 v[48:63], v[196:199], v[196:199], 0\n\t"
      "s_waitcnt vmcnt(8)\n\t"
      "v_mfma_f32_32x32x16_bf16 v[96:111], v[128:131], v[64:67], 0\n\t"
      "v_mfma_f32_32x32x16_bf16 v[96:111], v[132:135], v[68:71], v[96:111]\n\t"
      "s_nop 11\n\t"
      "TILE 0, 1, 3, 1, 8, 0\n\t"
      "TILE 1, 2, 0, 1, 8, 0\n\tTILE 2, 3, 1, 1, 9, 0, 2\n\t"
      "s_waitcnt vmcnt(4)\n\t"
      "1:\n\t"
      "v_cmp_gt_u32 vcc, 16, v206\n\t"
      "s_and_b64 vcc, exec, vcc\n\t"
      "s_cbranch_vccz 2f\n\t"
      "s_sleep 24\n\t"
      "buffer_load_dword v206, v205, %[rs], 0 offen offset:3072 sc1\n\t"
      "s_sub_u32 %[spin], %[spin], 1\n\t"
      "s_waitcnt vmcnt(0)\n\t"
      "s_cbranch_scc0 1b\n\t"
      "2:\n\t"
      "TILE 3, 0, 2, 1, 14, 3\n\t"
      "TILE 0, 1, 3, 1, 21, 3\n\tTILE 1, 2, 0, 1, 24, 3\n\tTILE 2, 3, 1, 1, 24, 3\n\tTILE 3, 0, 2, 1, 24, 3\n\t"
      "TILE 0, 1, 3, 1, 24, 3\n\tTILE 1, 2, 0, 1, 24, 3\n\tTILE 2, 3, 1, 1, 24, 3\n\tTILE 3, 0, 2, 1, 24, 3\n\t"
      "TILE 0, 1, 3, 1, 24, 3\n\tTILE 1, 2, 0, 0, 20, 3\n\tTILE 2, 3, 1, 0, 16, 3\n\tTILE 3, 3, 2, 0, 63, 3\n\t"
      "v_cvt_pk_bf16_f32 v199, v202, v203\n\t"
      "s_nop 1\n\t"
      "v_mfma_f32_32x32x16_bf16 v[48:63], v[188:191], v[196:199], v[48:63]\n\t"
      "s_nop 11\n\t"
      ".purgem GRP\n\t.purgem MEM\n\t.purgem STEP\n\t.purgem TILE"
      : "={v[0:15]}"(o[0]), "={v[16:31]}"(o[1]), "={v[32:47]}"(o[2]), "={v[48:63]}"(o[3]), "+{v[128:143]}"(kf0), "+{v[144:159]}"(kf1), "+{v[160:175]}"(kf2),
        "={v[208:223]}"(g2[0]), "={v[224:239]}"(g2[1]), "={v[240:255]}"(g2[2]),
        [skv] "+s"(so_kv), [sw] "+s"(so_w), [ab0] "+s"(ab.s0), [ab1] "+s"(ab.s1), [spin] "+s"(spin)
      : "{v[64:67]}"(q[0][0]), "{v[68:71]}"(q[0][1]), "{v[72:75]}"(q[1][0]), "{v[76:79]}"(q[1][1]),
        "{v[80:83]}"(q[2][0]), "{v[84:87]}"(q[2][1]), "{v[88:91]}"(q[3][0]), "{v[92:95]}"(q[3][1]),
        [vo] "{v204}"(x.vl16), "{v205}"((l & 15) * 4096), [rs] "s"(x.rs), [sl] "s"(so_l), [wd0] "s"(wd.s0), [wd1] "s"(wd.s1)
      : "memory", "scc", "vcc",
        "v96","v97","v98","v99","v100","v101","v102","v103","v104","v105","v106","v107","v108","v109","v110","v111",
        "v112","v113","v114","v115","v116","v117","v118","v119","v120","v121","v122","v123","v124","v125","v126","v127",
        "v176","v177","v178","v179","v180","v181","v182","v183","v184","v185","v186","v187","v188","v189","v190","v191",
        "v192","v193","v194","v195","v196","v197","v198","v199","v200","v201","v202","v203","v206", ACLOB);
    bar();
  } else {
    wv<0>();
    pin(kf0); pin(kf1); pin(kf2);
    float mx[4];
#pragma unroll
    for (int k = 0; k < 4; ++k) {
      mx[k] = -3.0e38f; if (h) q[k][1][2] = 0; pin(q[k][1]);
#pragma unroll
      for (int i = 0; i < 16; ++i) o[k][i] = 0.f;
    }
#pragma nounroll
    for (unsigned spin = 0; spin < (1u << 16); ++spin) {
      float v;
      ld1(v, (l & 15) * 4096, x.rs, OFF_FLAG + FL_W(0)); wv<0>(); pin(v);
      if (__all(__float_as_uint(v) >= 16u)) break;
      __builtin_amdgcn_s_sleep(24);
    }
    pf_wo(x);
    pf_wgu1(x);
    sfor<0, 12>([&](auto j) {
      u32x4 t;
      ld4<1024 * (j & 1)>(t, x.vl16, x.rs, OFF_WGU + (x.m * 196 + 112 + 8 * (j >> 1) + 2 * w) * KB); wv<0>(); pin(t);
#pragma unroll
      for (int i = 0; i < 4; ++i) g2[j >> 2][4 * (j & 3) + i] = __uint_as_float(t[i]);
    });
    pf_wd<0, 46>(x, base_wd(x));
    for (int tk = 0; tk < 16; ++tk) {
      u32x4 kf[4];
      sfor<0, 4>([&](auto i) { ld4<i * 1024>(kf[i], x.vl16, x.rs, kvs + tk * 4096); });
      wv<0>();
      sfor<0, 4>([&](auto k) {
        f32x16 s;
        nop<1>();
        m32z(s, kf[0], q[k][0]); m32(s, kf[1], q[k][1]);
        nop<11>(); pin(s);
        float tm = s[0];
#pragma unroll
        for (int i = 1; i < 16; ++i) tm = fmaxf(tm, s[i]);
        tm = max32(tm);
        const float mn = fmaxf(mx[k], tm), al = __builtin_amdgcn_exp2f(mx[k] - mn);
        mx[k] = mn;
#pragma unroll
        for (int i = 0; i < 16; ++i) o[k][i] *= al;
        u32x4 p0, p1;
#pragma unroll
        for (int d = 0; d < 4; ++d) {
          p0[d] = cvtpk(__builtin_amdgcn_exp2f(s[2 * d] - mn), __builtin_amdgcn_exp2f(s[2 * d + 1] - mn));
          p1[d] = cvtpk(__builtin_amdgcn_exp2f(s[8 + 2 * d] - mn), __builtin_amdgcn_exp2f(s[9 + 2 * d] - mn));
        }
        pin(p0); pin(p1); pin(o[k]);
        nop<1>();
        m32(o[k], kf[2], p0); m32(o[k], kf[3], p1);
        nop<11>(); pin(o[k]);
      });
    }
    bar();
    float* mm = (float*)(lds + SCR);
#pragma unroll
    for (int k = 0; k < 4; ++k) if (h == 0) mm[((w ^ k) * 4 + w) * 32 + r] = mx[k];
    bar();
#pragma unroll
    for (int k = 0; k < 4; ++k) {
      const float* p = mm + (w ^ k) * 128 + r;
      const float g = fmaxf(fmaxf(p[0], p[32]), fmaxf(p[64], p[96])), f = __builtin_amdgcn_exp2f(mx[k] - g);
#pragma unroll
      for (int i = 0; i < 16; ++i) o[k][i] *= f;
    }
    bar();
  }
  stamp(x);
  reduce4(x, o, [](auto) {});
  pin(o[0]); fstamp(x);
  {
    auto lr = __builtin_amdgcn_permlane32_swap(__float_as_uint(o[0][12]), __float_as_uint(o[0][12]), false, false);
    const float inv = __builtin_amdgcn_rcpf(__uint_as_float(lr[1]));
    u32x4 a, b;
#pragma unroll
    for (int d = 0; d < 4; ++d) { a[d] = cvtpk(o[0][2 * d] * inv, o[0][2 * d + 1] * inv); b[d] = cvtpk(o[0][8 + 2 * d] * inv, o[0][9 + 2 * d] * inv); }
    if (h) { b[2] = 0; b[3] = 0; }
    const unsigned so = OFF_O + L * SZ_O + (tt * 32 + 2 * (2 * hp + kh)) * KB;
    st4<0>(a, x.vl16, x.rs, so); st4<1024>(b, x.vl16, x.rs, so);
  }
  fstamp(x);
  wv<0>();
  fstamp(x);
  signal(x, FL_O(L, x.c, th), 1);
}

template <bool FINAL> DI void res_epilogue(Ctx& x, const f32x16& acc, const u32x4 (&xr)[2], unsigned Xo, unsigned STo, int tt, int nt, char* out = nullptr) {
  const int h = x.l >> 5, r = x.l & 31;
  u32x4 y[2];
#pragma unroll
  for (int d = 0; d < 8; ++d) y[d >> 2][d & 3] = cvtpk(acc[2 * d] + bflo(xr[d >> 2][d & 3]), acc[2 * d + 1] + bfhi(xr[d >> 2][d & 3]));
  if constexpr (FINAL) {
#pragma unroll
    for (int s = 0; s < 2; ++s) {
      auto a = __builtin_amdgcn_permlane32_swap(y[s][0], y[s][2], false, false);
      auto b = __builtin_amdgcn_permlane32_swap(y[s][1], y[s][3], false, false);
      y[s] = u32x4{a[0], b[0], a[1], b[1]};
    }
    const unsigned vo = r * (D * 2) + h * 16, so = tt * 32 * D * 2 + nt * 64;
    const rsrc_t ro = mkrs(out, S * D * 2);
    st4p<0>(y[0], vo, ro, so); st4p<32>(y[1], vo, ro, so);
  } else {
    const unsigned so = Xo + (tt * 28 + 2 * nt) * KB;
    st4<0>(y[0], x.vl16, x.rs, so); st4<1024>(y[1], x.vl16, x.rs, so);
    float s = 0.f, q = 0.f, s1 = 0.f, q1 = 0.f;
#pragma unroll
    for (int d = 0; d < 4; ++d) {
      s = dot2(y[0][d], 0x3f803f80u, s); q = dot2(y[0][d], y[0][d], q); s1 = dot2(y[1][d], 0x3f803f80u, s1); q1 = dot2(y[1][d], y[1][d], q1);
    }
    s = sum32(s + s1); q = sum32(q + q1);
    if (h == 0) st2<0>(u32x2{__float_as_uint(s), __float_as_uint(q)}, r * 8, x.rs, STo + (nt * 2048 + tt * 32) * 8);
  }
}

DI void phase_oproj(Ctx& x, const int L, const Args& a, const f32x16 (&g2)[3]) {
  const int w = x.w;
  if (x.m < 14) {
    const int th = x.m / 7, ct = x.m % 7, tt0 = 4 * x.c + 2 * th, tt = tt0 + (w >> 1), nt = 2 * ct + (w & 1);
    u32x2 x0[4];
    if (L == 0) {
      const rsrc_t rx = mkrs(a.x, S * D * 2);
      sfor<0, 4>([&](auto i) { ld2i<16 * i>(x0[i], (x.l & 31) * 896 + (x.l >> 5) * 8, rx, tt * 32 * 896 + nt * 64); });
    } else {
      sfor<0, 4>([&](auto i) { ld2u<(i >> 1) * 1024 + (i & 1) * 8>(x0[i], x.vl16, x.rs, OFF_X + 2 * SZ_X + (tt * 28 + 2 * nt) * KB); });
    }
    await(x, FL_O(L, x.c, th), 8);
    stamp(x);
    u32x4 b[8][2];
    const unsigned s0 = OFF_O + L * SZ_O + (tt * 32 + 8 * w) * KB, s1 = OFF_O + L * SZ_O + ((tt0 + ((w >> 1) ^ 1)) * 32 + 8 * w) * KB;
    auto ld = [&](auto i) { ld4<(i & 3) * 1024>(b[i][0], x.vl16, x.rs, s0 + (i >> 2) * 4096); ld4<(i & 3) * 1024>(b[i][1], x.vl16, x.rs, s1 + (i >> 2) * 4096); };
    constexpr int WN = 5;
    sfor<0, WN>(ld);
    f32x16 acc[4];
    sfor<0, 8>([&](auto i) {
      constexpr int last = (i + WN - 1 < 7 ? i + WN - 1 : 7);
      wv<2 * (last - i) + 1>();
      m32a<A_WO + 8 * i, i == 0>(acc[0], b[i][0]); m32a<A_WO + 8 * i + 4, i == 0>(acc[1], b[i][0]);
      wv<2 * (last - i)>();
      m32a<A_WO + 8 * i, i == 0>(acc[2], b[i][1]); m32a<A_WO + 8 * i + 4, i == 0>(acc[3], b[i][1]);
      if constexpr (i + WN < 8) ld(std::integral_constant<int, i + WN>{});
    });
    nop<11>();
    sfor<0, 4>([&](auto k) { pin(acc[k]); pin(x0[k]); });
    stamp(x);
    reduce4(x, acc, [](auto) {});
    pin(acc[0]); fstamp(x);
    const u32x4 x2[2] = {u32x4{x0[0][0], x0[0][1], x0[1][0], x0[1][1]}, u32x4{x0[2][0], x0[2][1], x0[3][0], x0[3][1]}};
    res_epilogue<false>(x, acc[0], x2, OFF_X + (2 * L + 1) * SZ_X, OFF_ST + (2 * L + 1) * SZ_ST, tt, nt);
    fstamp(x);
    wv<0>();
    fstamp(x);
    signal(x, FL_X1(L, x.c), 1);
    fstamp(x);
  } else {
    stamp(x); stamp(x); fstamp(x); fstamp(x); fstamp(x); fstamp(x);
  }
#pragma unroll
  for (int j = 0; j < 12; ++j)
    *(f32x4*)(lds + (112 + 8 * (j >> 1) + 2 * w + (j & 1)) * KB + x.vl16) = f32x4{g2[j >> 2][4 * (j & 3)], g2[j >> 2][4 * (j & 3) + 1], g2[j >> 2][4 * (j & 3) + 2], g2[j >> 2][4 * (j & 3) + 3]};
}

DI void gu_tile(const f32x4 (&acc)[2][2], const char* csp, const float (&mean)[2], const float (&r2)[2], u32x2 (&hp)[2]) {
  const f32x4 cg = *(const f32x4*)csp, cu = *(const f32x4*)(csp + 64);
#pragma unroll
  for (int u = 0; u < 2; ++u) {
    const f32x2 nm = {-mean[u], -mean[u]}, rr = {r2[u], r2[u]};
#pragma unroll
    for (int e = 0; e < 2; ++e) {
      const f32x2 g = __builtin_elementwise_fma(nm, f32x2{cg[2 * e], cg[2 * e + 1]}, f32x2{acc[0][u][2 * e], acc[0][u][2 * e + 1]});
      const f32x2 v = __builtin_elementwise_fma(nm, f32x2{cu[2 * e], cu[2 * e + 1]}, f32x2{acc[1][u][2 * e], acc[1][u][2 * e + 1]});
      const f32x2 y = g * rr * v;
      hp[u][e] = cvtpk(y[0], y[1]);
    }
  }
}

DI void phase_gu(Ctx& x, const int L) {
  const int w = x.w, l = x.l, g = l >> 4, c16 = l & 15, m = x.m, tt = 4 * x.c + w;
  await(x, FL_X1(L, x.c), 14);
  stamp(x);
  constexpr int NXV = 11;
  u32x4 xf[NXV][2]; f32x2 sp[2][4];
  const unsigned sx = OFF_X + (2 * L + 1) * SZ_X + tt * 28 * KB, st = OFF_ST + (2 * L + 1) * SZ_ST + tt * 32 * 8;
  auto ldx = [&](auto k, auto u) {
    if constexpr (k < NXV) ld4<(k & 1) * 2048 + 256 * u>(xf[k][u], x.vg16, x.rs, sx + (k >> 1) * 4096);
    else if constexpr (k < 14) lda<A_GX + 8 * (k - NXV) + 4 * u, (k & 1) * 2048 + 256 * u>(x.vg16, x.rs, sx + (k >> 1) * 4096);
    else { constexpr int i = k - 14; ld2<128 * u>(sp[u][i], (((i < 3 || g < 2) ? g + 4 * i : g) * 2048 + c16) * 8, x.rs, st); }
  };
  using I0 = std::integral_constant<int, 0>; using I1 = std::integral_constant<int, 1>;
  constexpr int XA = 3;
  sfor<0, XA>([&](auto k) { ldx(k, I0{}); ldx(k, I1{}); });
  const unsigned a0 = x.vl16, a1 = x.vl16 + 65536, a2 = x.vl16 + 131072;
  constexpr int DP = 6;
  u32x4 wf[8];
  auto rd = [&](auto ii) {
    constexpr int i = ii, slot = i >= 160 ? i - 160 : i;
    if constexpr (slot < 64) dsr<slot * 1024>(wf[i & 7], a0); else if constexpr (slot < 128) dsr<(slot - 64) * 1024>(wf[i & 7], a1); else dsr<(slot - 128) * 1024>(wf[i & 7], a2);
  };
  auto mm = [&](f32x4& c, const u32x4& a, auto k, auto u) {
    if constexpr (k < NXV) m16<k == 0>(c, a, xf[k][u]); else m16a<false, A_GX + 8 * (k - NXV) + 4 * u>(c, a);
  };
  f32x4 aa[5][2][2], ch0, ch1;
  float mean[2], r2[2];
  sfor<0, DP>(rd);
  sfor<0, 140>([&](auto i) {
    constexpr int KS = i / 10, s = i % 10, t = s >> 1, mat = i & 1;
    using K = std::integral_constant<int, KS>;
    constexpr auto side = [](int k) { return (k >= 0 && k <= 3) || (k >= 5 && k <= 9); };
    constexpr auto all = [](int k) { return (k + XA < 14 ? 2 : 0) + ((k >= 0 && k <= 3) || (k >= 5 && k <= 9) ? 2 : 0) + (k == 5 ? 1 : 0); };
    if constexpr (s == 0) {
      wv<(side(KS - 3) ? 1 : 0) + all(KS - 2) + all(KS - 1)>();
      if constexpr (KS == 5) asm volatile("s_barrier" ::: "memory");
    }
    rd(std::integral_constant<int, i + DP>{}); wl<DP>();
    mm(aa[t][mat][0], wf[i & 7], K{}, I0{});
    if constexpr (s == 1 && KS + XA < 14) ldx(std::integral_constant<int, KS + XA>{}, I0{});
    if constexpr (s == 6 && KS + XA < 14) ldx(std::integral_constant<int, KS + XA>{}, I1{});
    if constexpr (KS <= 3 && s == 3) ldx(std::integral_constant<int, 14 + KS>{}, I0{});
    if constexpr (KS <= 3 && s == 8) ldx(std::integral_constant<int, 14 + KS>{}, I1{});
    if constexpr (KS == 5 && s == 3) asm volatile("buffer_load_dwordx4 %0, %2, %3, %4 offen sc1\n\tbuffer_load_dwordx4 %1, %2, %3, %4 offen offset:1024 sc1" : "=&v"(ch0), "=&v"(ch1) : "v"(x.vl16), "s"(x.rs), "s"(OFF_CSGU + m * 2048));
    if constexpr (KS == 11 && s == 2) { pin(ch0); pin(ch1); *(f32x4*)(lds + 40 * KB + x.vl16) = ch0 + ch1; }
    if constexpr (KS >= 5 && KS <= 9 && (s == 3 || s == 8) && !(KS == 5 && s == 3)) pf_wgu3<2 * (KS - 5) + (s == 8) - 1>(x);
    if constexpr ((KS == 10 || KS == 12) && s == 2) {
      constexpr int u = KS == 12;
      sfor<0, 4>([&](auto e) { pin(sp[u][e]); });
      float sm = sp[u][0][0] + sp[u][1][0] + sp[u][2][0], q = sp[u][0][1] + sp[u][1][1] + sp[u][2][1];
      if (g < 2) { sm += sp[u][3][0]; q += sp[u][3][1]; }
      sm = sum32(sum16(sm)); q = sum32(sum16(q));
      float rs; row_stats(sm, q, mean[u], rs); r2[u] = rs * rs;
      pin(mean[u]); pin(r2[u]);
    }
    mm(aa[t][mat][1], wf[i & 7], K{}, I1{}); SB();
  });
  fstamp(x);
  wv<0>();
  asm volatile("s_barrier" ::: "memory");
  stamp(x);
  const unsigned H = OFF_H + L * SZ_H + tt * 112 * KB;
  const int gl = (m & 1) ? 7 * m : 7 * m + 6, gp = (m & 1) ? 7 * m + 1 : 7 * m;
  const char* csp = lds + 40 * KB + g * 16;
  auto pair = [&](const f32x4 (&t0)[2][2], const f32x4 (&t1)[2][2], int lt) {
    u32x2 h0[2], h1[2];
    gu_tile(t0, csp + lt * 128, mean, r2, h0); gu_tile(t1, csp + lt * 128 + 128, mean, r2, h1);
    st4<0>(u32x4{h0[0][0], h0[0][1], h1[0][0], h1[0][1]}, x.vg16, x.rs, H + (gp + lt - 1) * 1024);
    st4<256>(u32x4{h0[1][0], h0[1][1], h1[1][0], h1[1][1]}, x.vg16, x.rs, H + (gp + lt - 1) * 1024);
  };
  {
    sfor<0, 20>([&](auto i) { pin(aa[i >> 2][(i >> 1) & 1][i & 1]); });
    u32x2 hl[2];
    gu_tile(aa[0], csp, mean, r2, hl);
    const unsigned sl = H + (gl >> 1) * 2048 + (gl & 1) * 8;
    st2<0>(hl[0], x.vg16, x.rs, sl); st2<256>(hl[1], x.vg16, x.rs, sl);
    pair(aa[1], aa[2], 1);
    pair(aa[3], aa[4], 3);
  }
  asm volatile("s_waitcnt lgkmcnt(%c0)" :: "i"(0) : "memory");
  SB();
  fstamp(x);
  f32x4 ab[2][2][2];
  const Two wd = base_wd(x);
  sfor<140, 196>([&](auto i) {
    constexpr int j = i - 140, KS = j / 4, t = (j % 4) >> 1, mat = i & 1;
    using K = std::integral_constant<int, KS>;
    if constexpr (j == 24) {
      fstamp(x);
      wv<12>();
      asm volatile("s_barrier" ::: "memory");
      count(x, FL_H(L, x.c, 0), 1);
      SB();
      fstamp(x);
    }
    if constexpr (i + DP < 196) { rd(std::integral_constant<int, i + DP>{}); wl<DP>(); } else wl<195 - i>();
    mm(ab[t][mat][0], wf[i & 7], K{}, I0{});
    if constexpr ((j & 1) == 0 && j < 20) pf_wd<46 + j / 2, 47 + j / 2>(x, wd);
    if constexpr ((j & 1) == 0 && j >= 20 && j < 48) pf_wq<(j - 20) / 2>(x);
    if constexpr (j == 55) ldflag(x.fa, x.rs, OFF_FLAG + FL_H(L, x.c, 0));
    mm(ab[t][mat][1], wf[i & 7], K{}, I1{}); SB();
  });
  nop<7>();
  stamp(x);
  sfor<0, 8>([&](auto i) { pin(ab[i >> 2][(i >> 1) & 1][i & 1]); });
  pair(ab[0], ab[1], 5);
}

DI void phase_down(Ctx& x, const int L, char* out) {
  const int w = x.w;
  if (x.m >= 14) {
    wv<0>(); signal(x, FL_H(L, x.c, 1), 1);
    stamp(x); fstamp(x); fstamp(x); stamp(x);
    return;
  }
  const int th = x.m / 7, ct = x.m % 7, tt0 = 4 * x.c + 2 * th, tt = tt0 + (w >> 1), tto = tt0 + ((w >> 1) ^ 1), nt = 2 * ct + (w & 1);
  wv<2>();
  recheck(x, x.fa, FL_H(L, x.c, 0), 16);
  stamp(x);
  constexpr int WN = 7;
  u32x4 b[WN][2], xr[2];
  const unsigned s0 = OFF_H + L * SZ_H + (tt * 112 + 28 * w) * KB, s1 = OFF_H + L * SZ_H + (tto * 112 + 28 * w) * KB;
  auto ld = [&](auto o) { constexpr int i = dn_ks(o);
    ld4<(i & 3) * 1024>(b[o % WN][0], x.vl16, x.rs, s0 + (i >> 2) * 4096); ld4<(i & 3) * 1024>(b[o % WN][1], x.vl16, x.rs, s1 + (i >> 2) * 4096); };
  sfor<0, WN>(ld);
  f32x16 acc[4];
  unsigned fb;
  sfor<0, 28>([&](auto i) {
    constexpr int last = i + WN - 1 < 27 ? i + WN - 1 : 27, ex = (i >= 7 && i <= 12) ? 1 : ((i >= 21) ? 2 : 0);
    wv<2 * (last - i) + 1 + ex>();
    if constexpr (i == 0) signal(x, FL_H(L, x.c, 1), 1);
    m32a<A_WD + 8 * i, i == 0>(acc[0], b[i % WN][0]); m32a<A_WD + 8 * i + 4, i == 0>(acc[1], b[i % WN][0]);
    wv<2 * (last - i) + ex>();
    m32a<A_WD + 8 * i, i == 0>(acc[2], b[i % WN][1]); m32a<A_WD + 8 * i + 4, i == 0>(acc[3], b[i % WN][1]);
    if constexpr (i == 6) ldflag(fb, x.rs, OFF_FLAG + FL_H(L, x.c, 1));
    if constexpr (i + WN == 20) { fstamp(x); recheck(x, fb, FL_H(L, x.c, 1), 16); fstamp(x); }
    if constexpr (i + WN < 28) ld(std::integral_constant<int, i + WN>{});
    if constexpr (i + WN == 27) {
      const unsigned sr = OFF_X + (2 * L + 1) * SZ_X + (tt * 28 + 2 * nt) * KB;
      ld4<0>(xr[0], x.vl16, x.rs, sr); ld4<1024>(xr[1], x.vl16, x.rs, sr);
    }
  });
  if ((x.m & 7) < 2) {
    ldl<4>(x.vl16, x.rs, OFF_WQ + ((16 + (x.m & 1)) * 28 + 7 * w) * KB, 7 * w * KB);
    ldl<3>(x.vl16, x.rs, OFF_WQ + ((16 + (x.m & 1)) * 28 + 7 * w + 4) * KB, (7 * w + 4) * KB);
  }
  nop<11>();
  sfor<0, 4>([&](auto k) { pin(acc[k]); });
  stamp(x);
  reduce4(x, acc, [](auto) {});
  wv<0>();
  pin(xr[0]); pin(xr[1]);
  if (L > 0) {
    res_epilogue<true>(x, acc[0], xr, 0, 0, tt, nt, out);
  } else {
    res_epilogue<false>(x, acc[0], xr, OFF_X + 2 * SZ_X, OFF_ST + 2 * SZ_ST, tt, nt);
    wv<0>();
    signal(x, FL_X(x.c, th), 1);
  }
}

#define STAMP_SYNC
__global__ __launch_bounds__(256, 1) void k_main(Args a) {
  Ctx x;
  x.rs = mkrs(a.ws, WS_USED);
  x.w = __builtin_amdgcn_readfirstlane(threadIdx.x >> 6);
  asm volatile("s_nop 4" : "+s"(x.w));
  { const int b = blockIdx.x, r = b >> 3; x.c = 2 * (b & 7) + (r >> 4); x.m = r & 15; }
  { Ctx y = x; fresh(y); stamp(y); phase_first(y, a); STAMP_SYNC }
  for (int L = 0; L < 2; ++L) {
    f32x16 g2[3];
    int Lo = L; asm volatile("" : "+s"(Lo));
    { Ctx y = x; fresh(y); phase_att(y, Lo, g2); stamp(y); STAMP_SYNC }
    { Ctx y = x; fresh(y); phase_oproj(y, Lo, a, g2); stamp(y); STAMP_SYNC }
    { Ctx y = x; fresh(y); phase_gu(y, Lo); stamp(y); fresh(y); phase_down(y, Lo, a.out); stamp(y); STAMP_SYNC }
  }
  fresh(x);
  if (blockIdx.x == 0 && wave0(x)) {
    get_tag(x);
    if (x.l == 0) st2<0>(u32x2{x.tlo, x.thi}, 0, x.rs, OFF_TAG);
  }
  stamp_flush(x);
  asm volatile(".Lk_end:");
}

extern "C" void kernel_launch(void* const* d_in, const int* in_sizes, int n_in, void* d_out, int out_size, void* d_ws, size_t ws_size,
                              hipStream_t stream) {
  Args a;
  a.x = (const char*)d_in[0]; a.wq = (const char*)d_in[1]; a.wk = (const char*)d_in[2]; a.wv = (const char*)d_in[3];
  a.wo = (const char*)d_in[4]; a.wg = (const char*)d_in[6]; a.wu = (const char*)d_in[7]; a.wd = (const char*)d_in[8];
  a.ws = (char*)d_ws + ((ws_size - WS_USED) & ~(size_t)0xfffff); a.out = (char*)d_out;
  (void)hipFuncSetAttribute((const void*)k_main, hipFuncAttributeMaxDynamicSharedMemorySize, 160 * 1024);
  k_main<<<256, 256, 160 * 1024, stream>>>(a);
}
```

```cpp
#include <hip/hip_runtime.h>
#include <stdint.h>
#include <type_traits>

typedef float    f32x16 __attribute__((ext_vector_type(16)));
typedef float    f32x4  __attribute__((ext_vector_type(4)));
typedef float    f32x2  __attribute__((ext_vector_type(2)));
typedef unsigned u32x4  __attribute__((ext_vector_type(4)));
typedef unsigned u32x2  __attribute__((ext_vector_type(2)));
typedef __bf16   bf16x2 __attribute__((ext_vector_type(2)));
typedef __amdgpu_buffer_rsrc_t rsrc_t;
#define DI __device__ __forceinline__
#define GAS __attribute__((address_space(1)))
#define SB() __builtin_amdgcn_sched_barrier(0)

constexpr int S = 2048, D = 448, F = 1792;
constexpr unsigned KB = 1024;
constexpr unsigned OFF_FLAG = 0;
constexpr unsigned OFF_CSQ  = 64 * KB;
constexpr unsigned OFF_CSGU = OFF_CSQ + 4 * KB;
constexpr unsigned OFF_SLOT = OFF_CSGU + 32 * KB;
constexpr unsigned OFF_TAG  = OFF_SLOT + 32 * KB;
constexpr unsigned OFF_WQ   = 160 * KB;
constexpr unsigned OFF_WO   = OFF_WQ + 18 * 28 * KB;
constexpr unsigned OFF_WGU  = OFF_WO + 14 * 32 * KB;
constexpr unsigned OFF_WD   = OFF_WGU + 16 * 196 * KB;
constexpr unsigned SZ_X = 64 * 28 * KB, SZ_ST = 14 * 2048 * 8, SZ_O = 64 * 32 * KB, SZ_H = 64 * 112 * KB, SZ_KV = 64 * 4 * KB;
constexpr unsigned OFF_X    = OFF_WD + 14 * 112 * KB;
constexpr unsigned OFF_ST   = OFF_X + 4 * SZ_X;
constexpr unsigned OFF_O    = OFF_ST + 4 * SZ_ST;
constexpr unsigned OFF_H    = OFF_O + 2 * SZ_O;
constexpr unsigned OFF_HL   = OFF_H + 2 * SZ_H;
constexpr unsigned OFF_KV   = OFF_HL + 2 * 1024 * KB;
constexpr unsigned OFF_TS   = OFF_KV + 2 * SZ_KV;
constexpr unsigned WS_USED  = OFF_TS + 520 * KB;
DI constexpr unsigned FL_X(int c, int th)         { return (c * 16 + th) * 256u; }
DI constexpr unsigned FL_O(int L, int c, int th)  { return (c * 16 + 2 + L * 2 + th) * 256u; }
DI constexpr unsigned FL_X1(int L, int c)         { return (c * 16 + 6 + L) * 256u; }
DI constexpr unsigned FL_H(int L, int c, int p)   { return (c * 16 + 8 + L * 2 + p) * 256u; }
DI constexpr unsigned FL_W(int c)                 { return (c * 16 + 12) * 256u; }
constexpr unsigned FL_W2 = 13 * 256u;

constexpr float QSCALE = 0.18898223650461362f * 1.4426950408889634f;
constexpr float M_LIMIT = 48.0f;

template <int I, int N, class Fn> DI void sfor(Fn&& f) { if constexpr (I < N) { f(std::integral_constant<int, I>{}); sfor<I + 1, N>(f); } }

DI unsigned cvtpk(float lo, float hi) { f32x2 v = {lo, hi}; bf16x2 b = __builtin_convertvector(v, bf16x2); return __builtin_bit_cast(unsigned, b); }
DI float bflo(unsigned u) { return __uint_as_float(u << 16); }
DI float bfhi(unsigned u) { return __uint_as_float(u & 0xffff0000u); }
DI float dot2(unsigned a, unsigned b, float c) { return __builtin_amdgcn_fdot2_f32_bf16(__builtin_bit_cast(bf16x2, a), __builtin_bit_cast(bf16x2, b), c, false); }
DI float sum32(float v) { auto r = __builtin_amdgcn_permlane32_swap(__float_as_uint(v), __float_as_uint(v), false, false); return __uint_as_float(r[0]) + __uint_as_float(r[1]); }
DI float sum16(float v) { auto r = __builtin_amdgcn_permlane16_swap(__float_as_uint(v), __float_as_uint(v), false, false); return __uint_as_float(r[0]) + __uint_as_float(r[1]); }
DI float max32(float v) { auto r = __builtin_amdgcn_permlane32_swap(__float_as_uint(v), __float_as_uint(v), false, false); return fmaxf(__uint_as_float(r[0]), __uint_as_float(r[1])); }

DI unsigned umax16(unsigned v) {
  v = max(v, (unsigned)__builtin_amdgcn_update_dpp(0, (int)v, 0x128, 0xf, 0xf, false));
  v = max(v, (unsigned)__builtin_amdgcn_update_dpp(0, (int)v, 0x124, 0xf, 0xf, false));
  v = max(v, (unsigned)__builtin_amdgcn_update_dpp(0, (int)v, 0x122, 0xf, 0xf, false));
  v = max(v, (unsigned)__builtin_amdgcn_update_dpp(0, (int)v, 0x121, 0xf, 0xf, false));
  return v;
}
DI unsigned umax64(unsigned v) {
  v = umax16(v);
  auto a = __builtin_amdgcn_permlane16_swap(v, v, false, false); v = max(a[0], a[1]);
  auto b = __builtin_amdgcn_permlane32_swap(v, v, false, false); return max(b[0], b[1]);
}

typedef short s16x4 __attribute__((ext_vector_type(4)));
DI u32x2 trread(const char* p) { return __builtin_bit_cast(u32x2, __builtin_amdgcn_ds_read_tr16_b64_v4i16((__attribute__((address_space(3))) s16x4*)p)); }
DI u32x4 trfrag(const char* lo, const char* hi) { const u32x2 a = trread(lo), b = trread(hi); return u32x4{a[0], a[1], b[0], b[1]}; }
DI void row_stats(float s, float q, float& mean, float& rstd) {
  mean = s * (1.0f / D);
  const float var = fmaxf(q * (1.0f / D) - mean * mean, 0.f);
  rstd = __builtin_amdgcn_rsqf(var + 1e-5f);
}
DI float fragsum(const u32x4& f, float s) {
#pragma unroll
  for (int d = 0; d < 4; ++d) s = dot2(f[d], 0x3f803f80u, s);
  return s;
}
struct Args { const char *x, *wq, *wk, *wv, *wo, *wg, *wu, *wd; char* ws; char* out; };

#define ACLOB "a0","a1","a2","a3","a4","a5","a6","a7","a8","a9","a10","a11","a12","a13","a14","a15","a16","a17","a18","a19","a20","a21","a22","a23","a24","a25","a26","a27","a28","a29","a30","a31","a32","a33","a34","a35","a36","a37","a38","a39","a40","a41","a42","a43","a44","a45","a46","a47","a48","a49","a50","a51","a52","a53","a54","a55","a56","a57","a58","a59","a60","a61","a62","a63","a64","a65","a66","a67","a68","a69","a70","a71","a72","a73","a74","a75","a76","a77","a78","a79","a80","a81","a82","a83","a84","a85","a86","a87","a88","a89","a90","a91","a92","a93","a94","a95","a96","a97","a98","a99","a100","a101","a102","a103","a104","a105","a106","a107","a108","a109","a110","a111","a112","a113","a114","a115","a116","a117","a118","a119","a120","a121","a122","a123","a124","a125","a126","a127","a128","a129","a130","a131","a132","a133","a134","a135","a136","a137","a138","a139","a140","a141","a142","a143","a144","a145","a146","a147","a148","a149","a150","a151","a152","a153","a154","a155","a156","a157","a158","a159","a160","a161","a162","a163","a164","a165","a166","a167","a168","a169","a170","a171","a172","a173","a174","a175","a176","a177","a178","a179","a180","a181","a182","a183","a184","a185","a186","a187","a188","a189","a190","a191","a192","a193","a194","a195","a196","a197","a198","a199","a200","a201","a202","a203","a204","a205","a206","a207","a208","a209","a210","a211","a212","a213","a214","a215","a216","a217","a218","a219","a220","a221","a222","a223","a224","a225","a226","a227","a228","a229","a230","a231","a232","a233","a234","a235","a236","a237","a238","a239","a240","a241","a242","a243","a244","a245","a246","a247","a248","a249","a250","a251","a252","a253","a254","a255"

template <int IMM> DI void ld4(u32x4& d, unsigned vo, rsrc_t rs, unsigned so) {
  asm volatile("buffer_load_dwordx4 %0, %1, %2, %3 offen offset:%c4 sc1" : "=v"(d) : "v"(vo), "s"(rs), "s"(so), "i"(IMM));
}
template <int IMM> DI void ld2(f32x2& d, unsigned vo, rsrc_t rs, unsigned so) {
  asm volatile("buffer_load_dwordx2 %0, %1, %2, %3 offen offset:%c4 sc1" : "=v"(d) : "v"(vo), "s"(rs), "s"(so), "i"(IMM));
}
template <int IMM> DI void ld4i(u32x4& d, unsigned vo, rsrc_t rs, unsigned so) {
  asm volatile("buffer_load_dwordx4 %0, %1, %2, %3 offen offset:%c4" : "=v"(d) : "v"(vo), "s"(rs), "s"(so), "i"(IMM));
}
template <int IMM> DI void ld2i(u32x2& d, unsigned vo, rsrc_t rs, unsigned so) {
  asm volatile("buffer_load_dwordx2 %0, %1, %2, %3 offen offset:%c4" : "=v"(d) : "v"(vo), "s"(rs), "s"(so), "i"(IMM));
}
template <int IMM> DI void ld2u(u32x2& d, unsigned vo, rsrc_t rs, unsigned so) {
  asm volatile("buffer_load_dwordx2 %0, %1, %2, %3 offen offset:%c4 sc1" : "=v"(d) : "v"(vo), "s"(rs), "s"(so), "i"(IMM));
}
template <int IMM> DI void ld4f(f32x4& d, unsigned vo, rsrc_t rs, unsigned so) {
  asm volatile("buffer_load_dwordx4 %0, %1, %2, %3 offen offset:%c4 sc1" : "=v"(d) : "v"(vo), "s"(rs), "s"(so), "i"(IMM));
}
DI void ld1(float& d, unsigned vo, rsrc_t rs, unsigned so) {
  asm volatile("buffer_load_dword %0, %1, %2, %3 offen sc1" : "=v"(d) : "v"(vo), "s"(rs), "s"(so));
}
template <int AR, int IMM> DI void lda(unsigned vo, rsrc_t rs, unsigned so) {
  asm volatile("buffer_load_dwordx4 a[%c3:%c4], %0, %1, %2 offen offset:%c5 sc1" :: "v"(vo), "s"(rs), "s"(so), "i"(AR), "i"(AR + 3), "i"(IMM) : ACLOB);
}
template <int N> DI void ldl(unsigned vo, rsrc_t rs, unsigned so, unsigned lds) {
  asm volatile("s_mov_b32 m0, %3\n\ts_nop 0\n\tbuffer_load_dwordx4 %0, %1, %2 offen sc1 lds" :: "v"(vo), "s"(rs), "s"(so), "s"(lds) : "memory");
  if constexpr (N > 1) asm volatile("buffer_load_dwordx4 %0, %1, %2 offen offset:1024 sc1 lds" :: "v"(vo), "s"(rs), "s"(so) : "memory");
  if constexpr (N > 2) asm volatile("buffer_load_dwordx4 %0, %1, %2 offen offset:2048 sc1 lds" :: "v"(vo), "s"(rs), "s"(so) : "memory");
  if constexpr (N > 3) asm volatile("buffer_load_dwordx4 %0, %1, %2 offen offset:3072 sc1 lds" :: "v"(vo), "s"(rs), "s"(so) : "memory");
}
template <int IMM> DI void st4(const u32x4& d, unsigned vo, rsrc_t rs, unsigned so) {
  asm volatile("buffer_store_dwordx4 %0, %1, %2, %3 offen offset:%c4 sc1\n\ts_nop 1" :: "v"(d), "v"(vo), "s"(rs), "s"(so), "i"(IMM) : "memory");
}
template <int IMM> DI void st4p(const u32x4& d, unsigned vo, rsrc_t rs, unsigned so) {
  asm volatile("buffer_store_dwordx4 %0, %1, %2, %3 offen offset:%c4\n\ts_nop 1" :: "v"(d), "v"(vo), "s"(rs), "s"(so), "i"(IMM) : "memory");
}
template <int IMM> DI void st2(const u32x2& d, unsigned vo, rsrc_t rs, unsigned so) {
  asm volatile("buffer_store_dwordx2 %0, %1, %2, %3 offen offset:%c4 sc1" :: "v"(d), "v"(vo), "s"(rs), "s"(so), "i"(IMM) : "memory");
}
DI void st1(float d, unsigned vo, rsrc_t rs, unsigned so) {
  asm volatile("buffer_store_dword %0, %1, %2, %3 offen sc1" :: "v"(d), "v"(vo), "s"(rs), "s"(so) : "memory");
}
template <int N> DI void wv() { asm volatile("s_waitcnt vmcnt(%c0)" :: "i"(N) : "memory"); SB(); }
template <int N> DI void wl() { asm volatile("s_waitcnt lgkmcnt(%c0)" :: "i"(N) : "memory"); SB(); }
template <class T> DI void pin(T& x) { asm volatile("" : "+v"(x)); }
DI void bar() { asm volatile("s_waitcnt lgkmcnt(0)\n\ts_barrier" ::: "memory"); }
template <int N> DI void nop() { asm volatile("s_nop %c0" :: "i"(N)); SB(); }

template <int OFF> DI void trd(u32x2& r, unsigned a) { asm volatile("ds_read_b64_tr_b16 %0, %1 offset:%c2" : "=v"(r) : "v"(a), "i"(OFF)); }
template <int OFF> DI void dsr(u32x4& r, unsigned a) { asm volatile("ds_read_b128 %0, %1 offset:%c2" : "=v"(r) : "v"(a), "i"(OFF)); }

DI void m32(f32x16& c, const u32x4& a, const u32x4& b)  { asm volatile("v_mfma_f32_32x32x16_bf16 %0, %1, %2, %0" : "+v"(c) : "v"(a), "v"(b)); }
DI void m32z(f32x16& c, const u32x4& a, const u32x4& b) { asm volatile("v_mfma_f32_32x32x16_bf16 %0, %1, %2, 0" : "=&v"(c) : "v"(a), "v"(b)); }
template <int AR, bool Z> DI void m32a(f32x16& c, const u32x4& b) {
  if constexpr (Z) asm volatile("v_mfma_f32_32x32x16_bf16 %0, a[%c2:%c3], %1, 0" : "=&v"(c) : "v"(b), "i"(AR), "i"(AR + 3));
  else             asm volatile("v_mfma_f32_32x32x16_bf16 %0, a[%c2:%c3], %1, %0" : "+v"(c) : "v"(b), "i"(AR), "i"(AR + 3));
}
template <int AR, bool Z> DI void m32b(f32x16& c, const u32x4& a) {
  if constexpr (Z) asm volatile("v_mfma_f32_32x32x16_bf16 %0, %1, a[%c2:%c3], 0" : "=&v"(c) : "v"(a), "i"(AR), "i"(AR + 3));
  else             asm volatile("v_mfma_f32_32x32x16_bf16 %0, %1, a[%c2:%c3], %0" : "+v"(c) : "v"(a), "i"(AR), "i"(AR + 3));
}
template <bool Z> DI void m16(f32x4& c, const u32x4& a, const u32x4& b) {
  if constexpr (Z) asm volatile("v_mfma_f32_16x16x32_bf16 %0, %1, %2, 0" : "=&v"(c) : "v"(a), "v"(b));
  else             asm volatile("v_mfma_f32_16x16x32_bf16 %0, %1, %2, %0" : "+v"(c) : "v"(a), "v"(b));
}

template <bool Z, int AR> DI void m16a(f32x4& c, const u32x4& a) {
  if constexpr (Z) asm volatile("v_mfma_f32_16x16x32_bf16 %0, %1, a[%c2:%c3], 0" : "=&v"(c) : "v"(a), "i"(AR), "i"(AR + 3));
  else             asm volatile("v_mfma_f32_16x16x32_bf16 %0, %1, a[%c2:%c3], %0" : "+v"(c) : "v"(a), "i"(AR), "i"(AR + 3));
}
DI void ldflag(unsigned& v, rsrc_t rs, unsigned so) { asm volatile("buffer_load_dword %0, off, %1, %2 sc1" : "=v"(v) : "s"(rs), "s"(so)); }

struct Ctx {
  rsrc_t rs;
  unsigned tlo, thi;
  unsigned vl16;
  unsigned vg16;
  int w, l, c, m;
};
DI void fresh(Ctx& x) {
  asm volatile("" : "+s"(x.w), "+s"(x.c), "+s"(x.m));
  asm volatile("v_mbcnt_lo_u32_b32 %0, -1, 0\n\tv_mbcnt_hi_u32_b32 %0, -1, %0" : "=v"(x.l));
  x.vl16 = x.l * 16;
  const int g = x.l >> 4;
  x.vg16 = ((g >> 1) * 64 + (g & 1) * 32 + (x.l & 15)) * 16;
}
extern __shared__ __attribute__((aligned(16))) char lds[];
constexpr unsigned SCR = 112 * KB;

DI void pstamp(Ctx&) {}
DI void fstamp(Ctx&) {}
DI void stamp(Ctx&) {}
DI void stamp_flush(Ctx&) {}

DI void get_tag(Ctx& x) {
  unsigned lo, hi;
  asm volatile("v_accvgpr_read_b32 v255, a252\n\ts_nop 1\n\tv_readfirstlane_b32 %0, v255\n\tv_accvgpr_read_b32 v255, a253\n\ts_nop 1\n\tv_readfirstlane_b32 %1, v255\n\ts_nop 4"
               : "=s"(lo), "=s"(hi) :: "v255");
  const unsigned long long e = ((unsigned long long)hi << 32) | lo;
  unsigned long long n = e * 0x9E3779B97F4A7C15ull + 0x632BE59BD9B4E019ull;
  if (((n ^ e) >> 16) == 0) n += 0x10000;
  x.tlo = (unsigned)n; x.thi = (unsigned)(n >> 32);
}
DI bool wave0(const Ctx& x) { int w = x.w; asm volatile("" : "+s"(w)); return w == 0; }
DI void count(Ctx& x, unsigned fl, int n) {
  if (wave0(x)) {
    const unsigned long long mask = (1ull << n) - 1;
    unsigned long long keep;
    asm volatile("s_mov_b64 %0, exec\n\ts_mov_b64 exec, %5\n\tbuffer_atomic_add %1, %2, %3, %4 offen\n\ts_mov_b64 exec, %0"
                 : "=&s"(keep) : "v"(1u), "v"(x.l * 256), "s"(x.rs), "s"(OFF_FLAG + fl), "s"(mask) : "memory");
  }
}
DI void signal(Ctx& x, unsigned fl, int n) { bar(); count(x, fl, n); }
DI void await(Ctx& x, unsigned fl, unsigned target) {
  if (wave0(x)) {
#pragma nounroll
    for (unsigned spin = 0; spin < (1u << 18); ++spin) {
      unsigned v;
      asm volatile("buffer_load_dword %0, off, %1, %2 sc1\n\ts_waitcnt vmcnt(0)" : "=v"(v) : "s"(x.rs), "s"(OFF_FLAG + fl) : "memory");
      if ((unsigned)__builtin_amdgcn_readfirstlane(v) >= target) break;
      __builtin_amdgcn_s_sleep(1);
    }
  }
  bar();
}

DI void recheck(Ctx& x, unsigned v, unsigned fl, unsigned target) {
  pin(v);
#pragma nounroll
  for (unsigned spin = 0; (unsigned)__builtin_amdgcn_readfirstlane(v) < target && spin < (1u << 18); ++spin) {
    __builtin_amdgcn_s_sleep(2);
    asm volatile("buffer_load_dword %0, off, %1, %2 sc1\n\ts_waitcnt vmcnt(0)" : "=v"(v) : "s"(x.rs), "s"(OFF_FLAG + fl) : "memory");
  }
  SB();
}

DI void reduce_put(Ctx& x, const f32x16 (&acc)[4]) {
  sfor<1, 4>([&](auto k) {
    f32x4* p = (f32x4*)(lds + SCR + ((x.w ^ k) * 3 + k - 1) * 4096 + x.vl16);
#pragma unroll
    for (int q = 0; q < 4; ++q) p[q * 64] = f32x4{acc[k][4 * q], acc[k][4 * q + 1], acc[k][4 * q + 2], acc[k][4 * q + 3]};
  });
}
template <int K, int Q> DI void red_put1(Ctx& x, const f32x16& t) {
  asm volatile("ds_write_b128 %0, %1 offset:%c2" :: "v"(SCR + ((x.w ^ K) * 3 + K - 1) * 4096 + x.vl16),
               "v"(__builtin_shufflevector(t, t, 4 * Q, 4 * Q + 1, 4 * Q + 2, 4 * Q + 3)), "i"(Q * 1024) : "memory");
}
DI void reduce_get(Ctx& x, f32x16& own) {
  bar();
  const f32x4* p = (const f32x4*)(lds + SCR + x.w * 3 * 4096 + x.vl16);
#pragma unroll
  for (int k = 0; k < 3; ++k)
#pragma unroll
    for (int q = 0; q < 4; ++q) {
      f32x4 v = p[k * 256 + q * 64];
#pragma unroll
      for (int i = 0; i < 4; ++i) own[4 * q + i] += v[i];
    }
}

constexpr int A_WO = 184, A_WD = 0, A_GX = 224;
constexpr int dn_ks(int o) { return o < 20 ? 2 * ((o >> 1) % 5 + ((o >> 1) % 5 >= 2) + 7 * (o / 10)) + (o & 1) : 2 * (((o - 20) >> 1) * 4 - ((o - 20) >> 2) + 2) + (o & 1); }
static_assert(dn_ks(0) == 0 && dn_ks(4) == 6 && dn_ks(9) == 11 && dn_ks(10) == 14 && dn_ks(19) == 25 && dn_ks(20) == 4 && dn_ks(22) == 12 && dn_ks(24) == 18 && dn_ks(27) == 27, "");
struct Two { unsigned s0, s1; };
constexpr int SL_WQ = 56;
template <int J> DI void pf_wq(Ctx& x) {
  ldl<1>(x.vl16, x.rs, OFF_WQ + ((x.m & 7) * 56 + 14 * x.w + J) * KB, (SL_WQ + 14 * x.w + J) * KB);
}
DI Two base_wo(const Ctx& x) { const int ct = x.m % 7;
  return Two{OFF_WO + ((2 * ct + (x.w & 1)) * 32 + 8 * x.w) * KB, OFF_WO + ((2 * ct + ((x.w & 1) ^ 1)) * 32 + 8 * x.w) * KB}; }
DI void pf_wo(Ctx& x) {
  const Two b = base_wo(x);
  sfor<0, 8>([&](auto i) {
    lda<A_WO + 8 * i, (i & 3) * 1024>(x.vl16, x.rs, b.s0 + (i >> 2) * 4096);
    lda<A_WO + 8 * i + 4, (i & 3) * 1024>(x.vl16, x.rs, b.s1 + (i >> 2) * 4096);
  });
}
DI Two base_wd(const Ctx& x) { const int ct = x.m % 7;
  return Two{OFF_WD + ((2 * ct + (x.w & 1)) * 112 + 28 * x.w) * KB, OFF_WD + ((2 * ct + ((x.w & 1) ^ 1)) * 112 + 28 * x.w) * KB}; }
template <int I0, int I1> DI void pf_wd(Ctx& x, const Two& b) {
  sfor<I0, I1>([&](auto f) {
    constexpr int i = f >> 1;
    lda<A_WD + 4 * f, (i & 3) * 1024>(x.vl16, x.rs, ((f & 1) ? b.s1 : b.s0) + (i >> 2) * 4096);
  });
}
DI void pf_wgu1(Ctx& x) {
  for (int t = 0; t < 14; ++t) ldl<2>(x.vl16, x.rs, OFF_WGU + (x.m * 196 + 8 * t + 2 * x.w) * KB, (8 * t + 2 * x.w) * KB);
}
template <int J> DI void pf_wgu3(Ctx& x) {
  ldl<1>(x.vl16, x.rs, OFF_WGU + (x.m * 196 + 160 + 9 * x.w + J) * KB, (9 * x.w + J) * KB);
}

constexpr unsigned S_QP = SCR, S_KP = SCR + 16384, S_QF = SCR + 24576, S_ST = SCR + 32768, S_CS = SCR + 33792, S_KM = SCR + 36864, S_TR = SCR + 40960;
DI unsigned kv_finish(Ctx& x, const float (&raw)[16], const f32x4 (&cs)[4], float mean, float rstd, bool isv, unsigned so, int slot) {
  const int h = x.l >> 5, r = x.l & 31;
  u32x4 y[2];
#pragma unroll
  for (int d = 0; d < 8; ++d)
    y[d >> 2][d & 3] = cvtpk((raw[2 * d] - mean * cs[d >> 1][2 * (d & 1)]) * rstd, (raw[2 * d + 1] - mean * cs[d >> 1][2 * (d & 1) + 1]) * rstd);
  if (h) { y[1][2] = 0; y[1][3] = 0; }
  unsigned nb = 0;
  if (!isv) {
    float n2 = 0.f;
#pragma unroll
    for (int d = 0; d < 8; ++d) n2 = dot2(y[d >> 2][d & 3], y[d >> 2][d & 3], n2);
    n2 = sum32(n2);
    nb = umax64(n2 < 3.0e38f ? __float_as_uint(n2) : 0x7f800000u);
    nb = cvtpk(1.01f * __builtin_amdgcn_sqrtf(__uint_as_float(nb)), 0.f);
    if (h) y[1][2] = 0x3f80u;
  } else {
    if (h) y[1][2] = 0x3f80u;
    char* im = lds + S_TR + slot * 2048;
#pragma unroll
    for (int a = 0; a < 4; ++a) *(u32x2*)(im + r * 64 + 16 * a + 8 * h) = u32x2{y[a >> 1][2 * (a & 1)], y[a >> 1][2 * (a & 1) + 1]};
    asm volatile("s_waitcnt lgkmcnt(0)" ::: "memory");
    const char* tb = im + (4 * h + ((x.l >> 2) & 3)) * 64 + ((x.l >> 4) & 1) * 32 + (x.l & 3) * 8;
    y[0] = trfrag(tb, tb + 512); y[1] = trfrag(tb + 1024, tb + 1536);
  }
  st4<0>(y[0], x.vl16, x.rs, so); st4<1024>(y[1], x.vl16, x.rs, so);
  return nb;
}
DI void kv_publish(Ctx& x, int L, int p, unsigned km) {
  get_tag(x);
  st2<0>(u32x2{(x.tlo & 0xffff0000u) | km, x.thi}, x.l < 16 ? x.l * 1024 : ~0u, x.rs, OFF_SLOT + (L * 2048 + p) * 8);
}
DI void q_finish(Ctx& x, const f32x16& qa, const f32x4 (&cq)[4], float mean, float rstd) {
  const int h = x.l >> 5;
  u32x4 qf[2];
  const f32x4* p = (const f32x4*)(lds + S_QP + x.w * 4096 + x.vl16);
  const float sc = rstd * QSCALE;
#pragma unroll
  for (int q = 0; q < 4; ++q) {
    const f32x4 o = p[q * 64];
    float v[4];
#pragma unroll
    for (int i = 0; i < 4; ++i) v[i] = (qa[4 * q + i] + o[i] - mean * cq[q][i]) * sc;
    qf[q >> 1][2 * (q & 1)] = cvtpk(v[0], v[1]); qf[q >> 1][2 * (q & 1) + 1] = cvtpk(v[2], v[3]);
  }
  if (h) { qf[1][2] = 0; qf[1][3] = 0; }
  float qn2 = 0.f, qn3 = 0.f;
#pragma unroll
  for (int d = 0; d < 4; ++d) { qn2 = dot2(qf[0][d], qf[0][d], qn2); qn3 = dot2(qf[1][d], qf[1][d], qn3); }
  qn2 = sum32(qn2 + qn3);
  if (h) qf[1][2] = cvtpk(1.01f * __builtin_amdgcn_sqrtf(qn2), 0.f);
  ((u32x4*)(lds + S_QF + x.w * 2048 + x.vl16))[0] = qf[0];
  ((u32x4*)(lds + S_QF + x.w * 2048 + x.vl16))[64] = qf[1];
}
DI void put_tile(char* p, const f32x16& t) {
#pragma unroll
  for (int q = 0; q < 4; ++q) ((f32x4*)p)[q * 64] = f32x4{t[4 * q], t[4 * q + 1], t[4 * q + 2], t[4 * q + 3]};
}

constexpr unsigned IMG_Q = 0, IMG_T = 56 * KB;
DI rsrc_t mkrs(const char* p, unsigned n) { return __builtin_amdgcn_make_buffer_rsrc((char*)p, 0, n, 0x00020000); }

DI void ask_slots(Ctx& x, int L) {
  if (wave0(x)) asm volatile("buffer_load_dwordx4 a[248:251], %0, %1, %2 offen sc1" :: "v"(x.vl16), "s"(x.rs), "s"(OFF_SLOT + (L * 16 + x.c) * 1024) : ACLOB);
}
DI void kv_wait(Ctx& x, int L) {
  wv<0>();
  if (wave0(x)) {
    get_tag(x);
    unsigned s0, s1, s2, s3;
#pragma nounroll
    for (unsigned spin = 0; spin < (1u << 18); ++spin) {
      asm volatile("v_accvgpr_read_b32 %0, a248\n\tv_accvgpr_read_b32 %1, a249\n\tv_accvgpr_read_b32 %2, a250\n\tv_accvgpr_read_b32 %3, a251" : "=v"(s0), "=v"(s1), "=v"(s2), "=v"(s3));
      if (__all(s1 == x.thi && s3 == x.thi && (((s0 ^ x.tlo) | (s2 ^ x.tlo)) >> 16) == 0)) break;
      __builtin_amdgcn_s_sleep(2);
      ask_slots(x, L); wv<0>();
    }
    const unsigned km = umax64(max(s0 << 16, s2 << 16));
    if (x.l == 0) ((unsigned*)(lds + S_KM))[4] = km;
  }
  bar();
}
DI void ask_kv(Ctx& x, int L, f32x16 (&kf)[3]) {
  const unsigned kvs = OFF_KV + L * SZ_KV + 16 * x.w * 4096;
  asm volatile(".irp i,0,1,2,3\n\tbuffer_load_dwordx4 v[128+4*\\i:131+4*\\i], %3, %4, %5 offen offset:1024*\\i sc1\n\t.endr\n\t"
               ".irp i,0,1,2,3\n\tbuffer_load_dwordx4 v[144+4*\\i:147+4*\\i], %3, %4, %6 offen offset:1024*\\i sc1\n\t.endr\n\t"
               ".irp i,0,1,2,3\n\tbuffer_load_dwordx4 v[160+4*\\i:163+4*\\i], %3, %4, %7 offen offset:1024*\\i sc1\n\t.endr"
               : "={v[128:143]}"(kf[0]), "={v[144:159]}"(kf[1]), "={v[160:175]}"(kf[2]) : "{v204}"(x.vl16), "s"(x.rs), "s"(kvs), "s"(kvs + 4096), "s"(kvs + 8192));
}

DI void phase_pre(Ctx& x, const Args& a, f32x16 (&kf)[3]) {
  const int w = x.w, l = x.l, h = l >> 5, r = l & 31;
  const int th = x.m >> 3, hp = x.m & 7, tl = w & 1, kh = w >> 1, tt = 4 * x.c + 2 * th + tl, wi = 2 * x.c + th;
  const bool kvp = hp < 2;
  { unsigned long long pc; unsigned len;
    asm volatile("s_getpc_b64 %0\n.Lk_begin:\n\ts_mov_b32 %1, .Lk_end-.Lk_begin" : "=s"(pc), "=s"(len));
    const rsrc_t rc = mkrs((const char*)pc, len);
    asm volatile("buffer_load_dwordx4 a[112:115], %0, %1, %2 offen\n\tbuffer_load_dwordx4 a[112:115], %0, %1, %2 offen offset:1024"
                 :: "v"(x.vl16), "s"(rc), "s"((unsigned)(hp * 8 + 2 * w) * KB) : ACLOB); }
  if (x.m == 2) asm volatile("buffer_load_dword a116, %0, %1, 0 offen sc1" :: "v"(l < 16 ? OFF_FLAG + (x.c * 16 + l) * 256 : OFF_SLOT + ((l >> 3) & 1) * 16384 + x.c * 1024 + (l & 7) * 128), "s"(x.rs) : ACLOB);
  u32x4 xf[14];
  { const rsrc_t rx = mkrs(a.x, S * D * 2);
    sfor<0, 14>([&](auto i) { ld4i<32 * i>(xf[i], r * 896 + h * 16, rx, tt * 32 * 896 + kh * 448); }); }
  asm volatile("buffer_load_dwordx2 a[252:253], off, %0, %1 sc1" :: "s"(x.rs), "s"(OFF_TAG) : ACLOB);
  pstamp(x);
  const int task = wi * 6 + hp - 2;
  const rsrc_t rq = mkrs(a.wq, D * D * 2);
  auto ask_q = [&](auto i) {
    ldl<1>((8 * w + (l >> 3)) * 896 + (l & 7) * 16, rq, hp * 112 + i * 32 * 896, IMG_Q + (4 * i + w) * KB);
  };
  auto ask_t = [&](const char* tp, unsigned tn, unsigned ts, unsigned tb) {
    const rsrc_t rt = mkrs(tp, tn);
    const unsigned vo = (8 * w + (l >> 3)) * ts + (l & 7) * 16;
    sfor<0, 7>([&](auto i) { ldl<1>(vo, rt, tb + i * 32 * ts, IMG_T + (4 * i + w) * KB); });
  };
  const int tco = ((l >> 4) & 1) * 32 + (l & 3) * 8;
  const int sro = 4 * h + ((l >> 2) & 3);
  if (kvp) ask_t(hp ? a.wv : a.wk, D * 56, 128, 0);
  else if (task < 112) ask_t(task < 56 ? a.wg : a.wu, D * F * 2, F * 2, (task & 1) * 224 * F * 2 + ((task % 56) >> 1) * 128);
  else if (task < 168) ask_t(a.wd, D * F * 2, 896, ((task - 112) / 7) * 224 * 896 + ((task - 112) % 7) * 128);
  else ask_t(a.wo, task < 182 ? D * D * 2 : 0, 896, ((task - 168) / 7) * 224 * 896 + ((task - 168) % 7) * 128);
  wv<8>();
  {
    float s = 0.f, q = 0.f, s1 = 0.f, q1 = 0.f;
    sfor<0, 14>([&](auto i) {
      if (!kvp) ask_q(i);
      pin(xf[i]);
      s = dot2(xf[i][0], 0x3f803f80u, s); q = dot2(xf[i][0], xf[i][0], q); s1 = dot2(xf[i][1], 0x3f803f80u, s1); q1 = dot2(xf[i][1], xf[i][1], q1);
      s = dot2(xf[i][2], 0x3f803f80u, s); q = dot2(xf[i][2], xf[i][2], q); s1 = dot2(xf[i][3], 0x3f803f80u, s1); q1 = dot2(xf[i][3], xf[i][3], q1);
      pin(s); pin(q); pin(s1); pin(q1);
    });
    s = sum32(s + s1); q = sum32(q + q1);
    if (h == 0) ((f32x2*)(lds + S_ST))[w * 32 + r] = f32x2{s, q};
  }
  if (kvp) wv<0>(); else wv<14>();
  bar();
  stamp(x);
  if (!kvp) {
    if (task < 112) {
      const int g = l >> 4, mat = task >= 56, gt = 4 * ((task % 56) >> 1) + w, m = gt / 7, j = gt % 7, lt = (m & 1) ? j : (j == 6 ? 0 : j + 1);
      const char* tb = lds + IMG_T + (16 * (g >> 1) + 4 * (g & 1) + ((l >> 2) & 3)) * 128 + 32 * w + (l & 3) * 8;
      const unsigned st = lt < 5 ? 10 * KB : 4 * KB, so = OFF_WGU + (m * 196 + (lt < 5 ? lt * 2 + mat : 140 + (lt - 5) * 2 + mat)) * KB + (task & 1) * 7 * st;
      u32x4 f[7];
      sfor<0, 7>([&](auto i) { f[i] = trfrag(tb + i * 4096, tb + i * 4096 + 1024); });
      float cs = 0.f;
      sfor<0, 7>([&](auto i) { st4<0>(f[i], x.vl16, x.rs, so + i * st); cs = fragsum(f[i], cs); });
      cs = sum32(sum16(cs));
      st1(cs, l < 16 ? l * 4 : ~0u, x.rs, OFF_CSGU + m * 2048 + (task & 1) * 1024 + (lt * 2 + mat) * 64);
    } else if (task < 168) {
      const int nt = 2 * ((task - 112) % 7) + (w & 1), rb = (task - 112) / 7;
      const char* tb = lds + IMG_T + sro * 128 + 64 * (w & 1) + tco;
      const unsigned so = OFF_WD + (nt * 112 + 28 * (rb >> 1)) * KB, sa = so + (rb & 1) * 10 * KB, sb = so + (20 + (rb & 1) * 4) * KB;
      u32x4 f[7];
      if (w < 2) {
        sfor<0, 7>([&](auto i) { const char* p = tb + (32 * (i >> 1) + 8 * (i & 1)) * 128; f[i] = trfrag(p, p + 2048); });
        sfor<0, 7>([&](auto i) { st4<0>(f[i], x.vl16, x.rs, i < 4 ? sa + i * KB : (i < 6 ? sb + (i - 4) * KB : sa + 4 * KB)); });
      } else {
        sfor<0, 7>([&](auto i) { const char* p = tb + (32 * ((i + 7) >> 1) + 8 * ((i + 7) & 1)) * 128; f[i] = trfrag(p, p + 2048); });
        sfor<0, 7>([&](auto i) { st4<0>(f[i], x.vl16, x.rs, i < 5 ? sa + (5 + i) * KB : sb + (i - 3) * KB); });
      }
      st1(0.f, ~0u, x.rs, 0);
    } else {
      const int nt = 2 * ((task - 168) % 7) + (w & 1);
      const char* tb = lds + IMG_T + (112 * (w >> 1) + sro) * 128 + 64 * (w & 1) + tco;
      const unsigned vo = task < 182 ? x.vl16 : ~0u - 4096;
      u32x4 f[8];
      sfor<0, 8>([&](auto i) { const char* p = tb + (28 * (i >> 1) + 16 * (i & 1)) * 128; f[i] = trfrag(p, p + 1024); if ((i & 1) && h) { f[i][2] = 0; f[i][3] = 0; } });
      sfor<0, 8>([&](auto i) { st4<(i & 3) * 1024>(f[i], vo, x.rs, OFF_WO + (nt * 32 + 16 * ((task - 168) / 7) + 8 * (w >> 1) + (i & ~3)) * KB); });
    }
  }
  pstamp(x);
  if (!kvp) { wv<8>(); bar(); }
  stamp(x);
  float mean, rstd;
  { const f32x2 p0 = ((const f32x2*)(lds + S_ST))[tl * 32 + r], p1 = ((const f32x2*)(lds + S_ST))[(tl + 2) * 32 + r];
    row_stats(p0[0] + p1[0], p0[1] + p1[1], mean, rstd); }
  float* csw = (float*)(lds + S_CS);
  const int tro = 8 * h + ((l >> 2) & 3);
  u32x4 ones = {0x3f803f80u, 0x3f803f80u, 0x3f803f80u, 0x3f803f80u};
  pin(ones);
  if (kvp) {
    f32x16 kv, cs;
    const unsigned tb = IMG_T + (224 * kh + tro) * 56 + tco;
    u32x2 f[14][2];
    auto rd = [&](auto i) { trd<i * 896>(f[i][0], tb); trd<i * 896 + 224>(f[i][1], tb); };
    sfor<0, 7>(rd);
    sfor<0, 14>([&](auto i) {
      if constexpr (i + 7 < 14) { wl<12>(); rd(std::integral_constant<int, i + 7>{}); } else wl<2 * (13 - i)>();
      pin(f[i][0]); pin(f[i][1]);
      u32x4 g = {f[i][0][0], f[i][0][1], f[i][1][0], f[i][1][1]};
      pin(g); nop<0>();
      if constexpr (i == 0) { m32z(kv, g, xf[i]); m32z(cs, g, ones); } else { m32(kv, g, xf[i]); m32(cs, g, ones); }
    });
    nop<11>(); pin(kv); pin(cs);
    if (r == 0) sfor<0, 4>([&](auto q) { *(f32x4*)(csw + (w * 3 + 2) * 32 + 8 * q + 4 * h) = f32x4{cs[4 * q], cs[4 * q + 1], cs[4 * q + 2], cs[4 * q + 3]}; });
    pstamp(x);
    if (kh) put_tile(lds + S_KP + tl * 4096 + x.vl16, kv);
    bar();
    if (!kh) {
      if (x.m == 0 && w == 0) st1(0.f, l < 16 ? l * 256 : ~0u, x.rs, OFF_FLAG + x.c * 4096);
      float raw[16]; f32x4 c4[4];
      sfor<0, 4>([&](auto q) {
        const f32x4 o = ((const f32x4*)(lds + S_KP + tl * 4096 + x.vl16))[q * 64];
        c4[q] = *(const f32x4*)(csw + (w * 3 + 2) * 32 + 8 * q + 4 * h) + *(const f32x4*)(csw + ((w + 2) * 3 + 2) * 32 + 8 * q + 4 * h);
#pragma unroll
        for (int i = 0; i < 4; ++i) raw[4 * q + i] = kv[4 * q + i] + o[i];
      });
      const unsigned km = kv_finish(x, raw, c4, mean, rstd, hp == 1, OFF_KV + (tt * 4 + 2 * hp) * KB, tl);
      pstamp(x);
      wv<0>();
      kv_publish(x, 0, 2 * tt + hp, km);
      if (tt == 0 && r == 0) sfor<0, 4>([&](auto q) { st4<32 * q>(__builtin_bit_cast(u32x4, c4[q]), h * 16, x.rs, OFF_CSQ + (16 + hp) * 128); });
    } else {
      sfor<0, 28>([&](auto i) { ldl<1>((16 * tl + 8 * (i & 1) + (l >> 3)) * 896 + (l & 7) * 16, rq, hp * 112 + (i >> 1) * 32 * 896, IMG_Q + (4 * (i >> 1) + 2 * tl + (i & 1)) * KB); });
      pstamp(x);
      wv<0>();
    }
    bar();
    pstamp(x);
  } else { pstamp(x); pstamp(x); pstamp(x); }
  f32x16 qa;
  {
    f32x16 q1, q2, cs;
    const unsigned t1 = IMG_Q + (224 * kh + tro) * 128 + tco + 56 * tl, t2 = IMG_Q + (224 * kh + tro) * 128 + tco + 56 * (tl ^ 1);
    u32x2 f[14][4];
    auto rd = [&](auto i) { trd<i * 2048>(f[i][0], t1); trd<i * 2048 + 512>(f[i][1], t1); trd<i * 2048>(f[i][2], t2); trd<i * 2048 + 512>(f[i][3], t2); };
    wl<0>();
    sfor<0, 3>(rd);
    sfor<0, 14>([&](auto i) {
      if constexpr (i + 3 < 14) { wl<8>(); rd(std::integral_constant<int, i + 3>{}); } else wl<4 * (13 - i)>();
      sfor<0, 4>([&](auto e) { pin(f[i][e]); });
      u32x4 g1 = {f[i][0][0], f[i][0][1], f[i][1][0], f[i][1][1]}, g2 = {f[i][2][0], f[i][2][1], f[i][3][0], f[i][3][1]};
      pin(g1); pin(g2); nop<0>();
      if constexpr (i == 0) { m32z(q1, g1, xf[i]); m32z(q2, g2, xf[i]); m32z(cs, g1, ones); } else { m32(q1, g1, xf[i]); m32(q2, g2, xf[i]); m32(cs, g1, ones); }
      if constexpr (i == 10) { ask_slots(x, 0); SB(); }
    });
    nop<11>(); pin(q1); pin(q2); pin(cs);
    pstamp(x);
    if (r == 0) sfor<0, 4>([&](auto q) { *(f32x4*)(csw + (w * 3) * 32 + 8 * q + 4 * h) = f32x4{cs[4 * q], cs[4 * q + 1], cs[4 * q + 2], cs[4 * q + 3]}; });
    if (kh == tl) { put_tile(lds + S_QP + (w ^ 2) * 4096 + x.vl16, q2); qa = q1; } else { put_tile(lds + S_QP + (w ^ 2) * 4096 + x.vl16, q1); qa = q2; }
  }
  pstamp(x);
  kv_wait(x, 0);
  count(x, FL_W(x.c), 1);
  ask_kv(x, 0, kf);
  stamp(x);
  const bool own = x.c == hp && th == 0;
  {
    f32x4 cq[4];
    sfor<0, 4>([&](auto q) {
      cq[q] = *(const f32x4*)(csw + (kh * 3) * 32 + 8 * q + 4 * h) + *(const f32x4*)(csw + ((kh + 2) * 3) * 32 + 8 * q + 4 * h);
      if (own && tl == 0 && r == 0) st4<32 * q>(__builtin_bit_cast(u32x4, cq[q]), h * 16, x.rs, OFF_CSQ + (2 * hp + kh) * 128);
    });
    q_finish(x, qa, cq, mean, rstd);
  }
  pstamp(x);
  if (own) {
    const char* tb = lds + IMG_Q + (224 * (w & 1) + sro) * 128 + tco + 56 * (w >> 1);
    sfor<0, 14>([&](auto i) { st4<(i & 3) * 1024>(trfrag(tb + i * 2048, tb + i * 2048 + 1024), x.vl16, x.rs, OFF_WQ + (hp * 56 + 14 * w + (i & ~3)) * KB); });
  }
  if (kvp) {
    if (wi == 8) {
      const char* tb = lds + IMG_T + (112 * w + sro) * 56 + tco;
      sfor<0, 7>([&](auto i) { st4<(i & 3) * 1024>(trfrag(tb + i * 896, tb + i * 896 + 448), x.vl16, x.rs, OFF_WQ + ((16 + hp) * 28 + 7 * w + (i & ~3)) * KB); });
    }
  }
}

DI void phase_front(Ctx& x, f32x16 (&kf)[3]) {
  const int w = x.w, l = x.l, h = l >> 5, r = l & 31;
  const int th = x.m >> 3, hp = x.m & 7, tl = w & 1, kh = w >> 1, tt = 4 * x.c + 2 * th + tl;
  const unsigned KV = OFF_KV + SZ_KV;
  {
    f32x4 cq[4], ck[4];
    sfor<0, 4>([&](auto q) { ld4f<32 * q>(cq[q], h * 16, x.rs, OFF_CSQ + (2 * hp + kh) * 128); ld4f<32 * q>(ck[q], h * 16, x.rs, OFF_CSQ + (16 + (hp & 1)) * 128); });
    wv<0>();
    await(x, FL_X(x.c, th), 7);
    stamp(x);
    u32x4 xf[14]; f32x2 sp[7];
    {
      const unsigned so = OFF_X + 2 * SZ_X + (tt * 28 + 14 * kh) * KB;
      sfor<0, 7>([&](auto i) { ld4<(i & 3) * 1024>(xf[i], x.vl16, x.rs, so + (i >> 2) * 4096); });
      sfor<0, 7>([&](auto i) { ld2<0>(sp[i], (h * 7 * 2048 + r) * 8, x.rs, OFF_ST + 2 * SZ_ST + (i * 2048 + tt * 32) * 8); });
      sfor<7, 14>([&](auto i) { ld4<(i & 3) * 1024>(xf[i], x.vl16, x.rs, so + (i >> 2) * 4096); });
    }
    float mean, rstd;
    auto stats = [&]() {
      sfor<0, 7>([&](auto i) { pin(sp[i]); });
      sfor<0, 4>([&](auto i) { pin(cq[i]); pin(ck[i]); });
      float s = 0.f, q = 0.f;
#pragma unroll
      for (int i = 0; i < 7; ++i) { s += sp[i][0]; q += sp[i][1]; }
      row_stats(sum32(s), sum32(q), mean, rstd);
      pin(mean); pin(rstd); SB();
    };
    f32x16 qa, qb;
    u32x4 wa[14], wb[14];
    const unsigned pa = x.vl16 + (SL_WQ + 28 * kh + 14 * kh) * KB, pb = x.vl16 + (SL_WQ + 28 * (kh ^ 1) + 14 * kh) * KB;
    if (hp < 2) {
      f32x16 kv;
      sfor<0, 14>([&](auto i) { dsr<i * 1024>(wa[i], x.vl16 + 14 * kh * KB); });
      sfor<0, 14>([&](auto i) {
        wv<(i < 7 ? 20 : 13) - i>(); wl<13 - i>();
        if constexpr (i == 0) m32z(kv, wa[i], xf[i]); else m32(kv, wa[i], xf[i]);
        if constexpr (i == 7) stats();
      });
      nop<11>(); pin(kv);
      if (kh) put_tile(lds + S_KP + tl * 4096 + x.vl16, kv);
      bar();
      unsigned km = 0;
      if (!kh) {
        float raw[16];
        sfor<0, 4>([&](auto q) {
          const f32x4 o = ((const f32x4*)(lds + S_KP + tl * 4096 + x.vl16))[q * 64];
#pragma unroll
          for (int i = 0; i < 4; ++i) raw[4 * q + i] = kv[4 * q + i] + o[i];
        });
        km = kv_finish(x, raw, ck, mean, rstd, hp == 1, KV + (tt * 4 + 2 * hp) * KB, tl);
      }
      wl<0>();
      sfor<0, 7>([&](auto i) { dsr<i * 1024>(wb[i], pb); dsr<i * 1024>(wa[i], pa); });
      sfor<0, 14>([&](auto i) {
        if constexpr (i < 7) wl<12>(); else wl<2 * (13 - i)>();
        if constexpr (i == 0) { m32z(qb, wb[i], xf[i]); m32z(qa, wa[i], xf[i]); } else { m32(qb, wb[i], xf[i]); m32(qa, wa[i], xf[i]); }
        if constexpr (i < 7) { dsr<(i + 7) * 1024>(wb[i + 7], pb); dsr<(i + 7) * 1024>(wa[i + 7], pa); }
        if constexpr (i == 8) { if (!kh) { wv<0>(); kv_publish(x, 1, 2 * tt + hp, km); } SB(); }
      });
    } else {
      sfor<0, 7>([&](auto i) { dsr<i * 1024>(wb[i], pb); dsr<i * 1024>(wa[i], pa); });
      sfor<0, 14>([&](auto i) {
        wv<(i < 7 ? 20 : 13) - i>();
        if constexpr (i < 7) wl<12>(); else wl<2 * (13 - i)>();
        if constexpr (i == 0) { m32z(qb, wb[i], xf[i]); m32z(qa, wa[i], xf[i]); } else { m32(qb, wb[i], xf[i]); m32(qa, wa[i], xf[i]); }
        if constexpr (i < 7) { dsr<(i + 7) * 1024>(wb[i + 7], pb); dsr<(i + 7) * 1024>(wa[i + 7], pa); }
        if constexpr (i == 7) stats();
      });
    }
    nop<11>();
    pin(qa); pin(qb);
    put_tile(lds + S_QP + (w ^ 2) * 4096 + x.vl16, qb);
    bar();
    q_finish(x, qa, cq, mean, rstd);
    stamp(x);
    ask_slots(x, 1);
    kv_wait(x, 1);
    ask_kv(x, 1, kf);
    stamp(x);
  }
}

DI void phase_att(Ctx& x, const int L, f32x16 (&kf)[3], f32x16 (&g2)[3]) {
  const int w = x.w, l = x.l, h = l >> 5, r = l & 31;
  const int th = x.m >> 3, hp = x.m & 7, tl = w & 1, kh = w >> 1, tt = 4 * x.c + 2 * th + tl;
  const unsigned kvs = OFF_KV + L * SZ_KV + 16 * w * 4096;
  stamp(x);
  if (L == 0) bar();
  stamp(x);
  unsigned kma = S_KM + 16; asm volatile("" : "+s"(kma));
  const float kmx = *(const float*)(lds + kma);
  u32x4 q[4][2];
#pragma unroll
  for (int k = 0; k < 4; ++k) {
    q[k][0] = ((const u32x4*)(lds + S_QF + (w ^ k) * 2048 + x.vl16))[0];
    q[k][1] = ((const u32x4*)(lds + S_QF + (w ^ k) * 2048 + x.vl16))[64];
  }
  bool safe = true;
  {
#pragma unroll
    for (int k = 0; k < 4; ++k) {
      const float mrow = bflo(q[k][1][2]) * kmx + 0.01f;
      safe = safe && (!h || mrow <= M_LIMIT);
      if (h) q[k][1][2] = cvtpk(-mrow, 0.f);
    }
  }
  const bool fast = __all(safe);
  stamp(x);
  f32x16 own;
  if (fast) {
    unsigned so_kv = kvs + 3 * 4096, so_w = OFF_WGU + (x.m * 196 + 2 * w) * KB, so_l = 2 * w * KB, spin = 1u << 16, fvo = (l & 15) * 4096;
    Two ab = base_wo(x);
    const Two wd = base_wd(x);
    asm volatile(
      ".macro GRP s, pa, pb\n\t"
      "v_exp_f32 v200, v[\\s]\n\tv_exp_f32 v201, v[\\s+1]\n\tv_cvt_pk_bf16_f32 v[\\pa], v202, v203\n\t"
      "v_exp_f32 v202, v[\\s+2]\n\tv_exp_f32 v203, v[\\s+3]\n\tv_cvt_pk_bf16_f32 v[\\pb], v200, v201\n\t"
      ".endm\n\t"
      ".macro MEM kind, i, ld\n\t"
      ".if \\kind == 1\n\t"
      "buffer_load_dwordx4 v[128+16*\\ld+4*\\i:128+16*\\ld+4*\\i+3], %[vo], %[rs], %[skv] offen offset:1024*\\i sc1\n\t"
      ".elseif \\kind == 5\n\t"
      "ds_write_b128 v205, v[4*\\i:4*\\i+3] offset:1024*\\i\n\t"
      ".elseif \\kind == 6\n\t"
      "ds_write_b128 v205, v[4:7] offset:1024\n\t"
      ".elseif \\kind == 7\n\t"
      "ds_write_b128 v205, v[12:15] offset:3072\n\t"
      ".elseif \\kind == 2\n\t"
      "buffer_load_dword v206, v205, %[rs], 0 offen offset:3072 sc1\n\t"
      ".elseif \\kind == 3\n\t"
      ".if att_n < 16 || att_n >= 56\n\t"
      ".if att_n < 102\n\t"
      ".if att_n < 16\n\t.set att_r, 184+4*att_n\n\t.else\n\t.set att_r, 4*(att_n-56)\n\t.endif\n\t"
      ".if att_n & 1\n\t"
      "buffer_load_dwordx4 a[att_r:att_r+3], %[vo], %[rs], %[ab1] offen offset:1024*((att_n>>1)&3) sc1\n\t"
      ".else\n\t"
      "buffer_load_dwordx4 a[att_r:att_r+3], %[vo], %[rs], %[ab0] offen offset:1024*((att_n>>1)&3) sc1\n\t"
      ".endif\n\t"
      ".if att_n == 15\n\t"
      "s_mov_b32 %[ab0], %[wd0]\n\ts_mov_b32 %[ab1], %[wd1]\n\t"
      ".elseif (att_n & 7) == 7\n\t"
      "s_add_u32 %[ab0], %[ab0], 4096\n\ts_add_u32 %[ab1], %[ab1], 4096\n\t"
      ".endif\n\t"
      ".endif\n\t"
      ".elseif att_n < 44\n\t"
      ".if att_n & 1\n\t"
      "buffer_load_dwordx4 %[vo], %[rs], %[sw] offen offset:1024 sc1 lds\n\t"
      "s_add_u32 %[sw], %[sw], 8192\n\ts_add_u32 m0, m0, 8192\n\t"
      ".else\n\t"
      "buffer_load_dwordx4 %[vo], %[rs], %[sw] offen sc1 lds\n\t"
      ".endif\n\t"
      ".else\n\t"
      "buffer_load_dwordx4 v[208+4*(att_n-44):208+4*(att_n-44)+3], %[vo], %[rs], %[sw] offen offset:1024*(att_n&1) sc1\n\t"
      ".if att_n & 1\n\ts_add_u32 %[sw], %[sw], 8192\n\t.endif\n\t"
      ".endif\n\t"
      ".set att_n, att_n+1\n\t"
      ".endif\n\t"
      ".endm\n\t"
      ".macro STEP sc, sn, k, nk, pk, tk, nt, pt, ld, m0k, m0i, m1k, m2k, m2i, m3k\n\t"
      "v_mfma_f32_32x32x16_bf16 v[\\sn:\\sn+15], v[128+16*\\nt:128+16*\\nt+3], v[64+8*\\nk:64+8*\\nk+3], 0\n\t"
      "MEM \\m0k, \\m0i, \\ld\n\t"
      "GRP \\sc, 199, 192\n\t"
      "v_mfma_f32_32x32x16_bf16 v[\\sn:\\sn+15], v[132+16*\\nt:132+16*\\nt+3], v[68+8*\\nk:68+8*\\nk+3], v[\\sn:\\sn+15]\n\t"
      "MEM \\m1k, 0, 0\n\t"
      "GRP \\sc+4, 193, 194\n\t"
      "v_mfma_f32_32x32x16_bf16 v[16*\\pk:16*\\pk+15], v[140+16*\\pt:140+16*\\pt+3], v[196:199], v[16*\\pk:16*\\pk+15]\n\t"
      "MEM \\m2k, \\m2i, \\ld\n\t"
      "GRP \\sc+8, 195, 196\n\t"
      "v_mfma_f32_32x32x16_bf16 v[16*\\k:16*\\k+15], v[136+16*\\tk:136+16*\\tk+3], v[192:195], v[16*\\k:16*\\k+15]\n\t"
      "MEM \\m3k, 0, 0\n\t"
      "GRP \\sc+12, 197, 198\n\t"
      ".endm\n\t"
      ".macro TILE tk, nx, pv, on, wait, p, fl=0\n\t"
      "STEP 96, 112, 0, 1, 3, \\tk, \\tk, \\pv, \\pv, \\p, 0, \\p, \\p, 0, \\fl\n\t"
      "STEP 112, 96, 1, 2, 0, \\tk, \\tk, \\tk, \\pv, \\on, 0, \\p, \\on, 1, 0\n\t"
      "STEP 96, 112, 2, 3, 1, \\tk, \\tk, \\tk, \\pv, \\on, 2, \\p, \\on, 3, 0\n\t"
      ".if \\on\n\ts_add_u32 %[skv], %[skv], 4096\n\t.endif\n\t"
      "s_waitcnt vmcnt(\\wait)\n\t"
      "STEP 112, 96, 3, 0, 2, \\tk, \\nx, \\tk, \\pv, \\p, 0, \\p, \\p, 0, 0\n\t"
      ".endm\n\t"
      ".set att_n, 0\n\t"
      "s_mov_b32 m0, %[sl]\n\t"
      "v_mov_b32 v196, 0\n\tv_mov_b32 v197, 0\n\tv_mov_b32 v198, 0\n\tv_mov_b32 v199, 0\n\tv_mov_b32 v202, 0\n\tv_mov_b32 v203, 0\n\t"
      "v_mov_b32 v188, 0\n\tv_mov_b32 v189, 0\n\tv_mov_b32 v190, 0\n\tv_mov_b32 v191, 0\n\t"
      "v_mfma_f32_32x32x16_bf16 v[0:15], v[196:199], v[196:199], 0\n\t"
      "v_mfma_f32_32x32x16_bf16 v[16:31], v[196:199], v[196:199], 0\n\t"
      "v_mfma_f32_32x32x16_bf16 v[32:47], v[196:199], v[196:199], 0\n\t"
      "v_mfma_f32_32x32x16_bf16 v[48:63], v[196:199], v[196:199], 0\n\t"
      "s_waitcnt vmcnt(8)\n\t"
      "v_mfma_f32_32x32x16_bf16 v[96:111], v[128:131], v[64:67], 0\n\t"
      "v_mfma_f32_32x32x16_bf16 v[96:111], v[132:135], v[68:71], v[96:111]\n\t"
      "s_nop 11\n\t"
      "TILE 0, 1, 3, 1, 8, 0\n\t"
      "TILE 1, 2, 0, 1, 8, 0\n\tTILE 2, 3, 1, 1, 9, 0, 2\n\t"
      "s_waitcnt vmcnt(4)\n\t"
      "1:\n\t"
      "v_cmp_gt_u32 vcc, 16, v206\n\t"
      "s_and_b64 vcc, exec, vcc\n\t"
      "s_cbranch_vccz 2f\n\t"
      "s_sleep 24\n\t"
      "buffer_load_dword v206, v205, %[rs], 0 offen offset:3072 sc1\n\t"
      "s_sub_u32 %[spin], %[spin], 1\n\t"
      "s_waitcnt vmcnt(0)\n\t"
      "s_cbranch_scc0 1b\n\t"
      "2:\n\t"
      "s_barrier\n\t"
      "TILE 3, 0, 2, 1, 14, 3\n\t"
      "TILE 0, 1, 3, 1, 21, 3\n\tTILE 1, 2, 0, 1, 24, 3\n\tTILE 2, 3, 1, 1, 24, 3\n\tTILE 3, 0, 2, 1, 24, 3\n\t"
      "TILE 0, 1, 3, 1, 24, 3\n\tTILE 1, 2, 0, 1, 24, 3\n\tTILE 2, 3, 1, 1, 24, 3\n\tTILE 3, 0, 2, 1, 24, 3\n\t"
      "TILE 0, 1, 3, 1, 24, 3\n\tTILE 1, 2, 0, 0, 20, 3\n\tTILE 2, 3, 1, 0, 16, 3\n\t"
      "v_add_u32 v205, %[d1], v204\n\tv_add_u32 v206, %[d2], v204\n\t"
      "STEP 96, 112, 0, 1, 3, 3, 3, 2, 0, 3, 0, 3, 3, 0, 3\n\t"
      "STEP 112, 96, 1, 2, 0, 3, 3, 3, 0, 3, 0, 3, 0, 0, 0\n\t"
      "STEP 96, 112, 2, 3, 1, 3, 3, 3, 0, 5, 0, 6, 5, 2, 7\n\t"
      "ds_write_b128 v206, v[16:19]\n\t"
      "GRP 112, 199, 192\n\t"
      "ds_write_b128 v206, v[20:23] offset:1024\n\t"
      "GRP 116, 193, 194\n\t"
      "v_mfma_f32_32x32x16_bf16 v[32:47], v[188:191], v[196:199], v[32:47]\n\t"
      "ds_write_b128 v206, v[24:27] offset:2048\n\t"
      "GRP 120, 195, 196\n\t"
      "v_mfma_f32_32x32x16_bf16 v[48:63], v[184:187], v[192:195], v[48:63]\n\t"
      "ds_write_b128 v206, v[28:31] offset:3072\n\t"
      "GRP 124, 197, 198\n\t"
      "v_add_u32 v200, %[d3], v204\n\t"
      "v_cvt_pk_bf16_f32 v199, v202, v203\n\t"
      "s_nop 1\n\t"
      "v_mfma_f32_32x32x16_bf16 v[48:63], v[188:191], v[196:199], v[48:63]\n\t"
      "ds_write_b128 v200, v[32:35]\n\tds_write_b128 v200, v[36:39] offset:1024\n\tds_write_b128 v200, v[40:43] offset:2048\n\tds_write_b128 v200, v[44:47] offset:3072\n\t"
      "s_nop 7\n\t"
      ".purgem GRP\n\t.purgem MEM\n\t.purgem STEP\n\t.purgem TILE"
      : "={v[48:63]}"(own), "+{v[128:143]}"(kf[0]), "+{v[144:159]}"(kf[1]), "+{v[160:175]}"(kf[2]),
        "={v[208:223]}"(g2[0]), "={v[224:239]}"(g2[1]), "={v[240:255]}"(g2[2]),
        [skv] "+s"(so_kv), [sw] "+s"(so_w), [ab0] "+s"(ab.s0), [ab1] "+s"(ab.s1), [spin] "+s"(spin), "+{v205}"(fvo)
      : "{v[64:67]}"(q[1][0]), "{v[68:71]}"(q[1][1]), "{v[72:75]}"(q[2][0]), "{v[76:79]}"(q[2][1]),
        "{v[80:83]}"(q[3][0]), "{v[84:87]}"(q[3][1]), "{v[88:91]}"(q[0][0]), "{v[92:95]}"(q[0][1]),
        [vo] "{v204}"(x.vl16), [d1] "s"(SCR + (w ^ 1) * 3 * 4096), [d2] "s"(SCR + ((w ^ 2) * 3 + 1) * 4096), [d3] "s"(SCR + ((w ^ 3) * 3 + 2) * 4096), [rs] "s"(x.rs), [sl] "s"(so_l), [wd0] "s"(wd.s0), [wd1] "s"(wd.s1)
      : "memory", "scc", "vcc",
        "v0","v1","v2","v3","v4","v5","v6","v7","v8","v9","v10","v11","v12","v13","v14","v15","v16","v17","v18","v19","v20","v21","v22","v23",
        "v24","v25","v26","v27","v28","v29","v30","v31","v32","v33","v34","v35","v36","v37","v38","v39","v40","v41","v42","v43","v44","v45","v46","v47",
        "v96","v97","v98","v99","v100","v101","v102","v103","v104","v105","v106","v107","v108","v109","v110","v111",
        "v112","v113","v114","v115","v116","v117","v118","v119","v120","v121","v122","v123","v124","v125","v126","v127",
        "v176","v177","v178","v179","v180","v181","v182","v183","v184","v185","v186","v187","v188","v189","v190","v191",
        "v192","v193","v194","v195","v196","v197","v198","v199","v200","v201","v202","v203","v206", ACLOB);
  } else {
    f32x16 o[4];
    wv<0>();
    pin(kf[0]); pin(kf[1]); pin(kf[2]);
    float mx[4];
#pragma unroll
    for (int k = 0; k < 4; ++k) {
      mx[k] = -3.0e38f; if (h) q[k][1][2] = 0; pin(q[k][1]);
#pragma unroll
      for (int i = 0; i < 16; ++i) o[k][i] = 0.f;
    }
#pragma nounroll
    for (unsigned spin = 0; spin < (1u << 16); ++spin) {
      float v;
      ld1(v, (l & 15) * 4096, x.rs, OFF_FLAG + FL_W(0)); wv<0>(); pin(v);
      if (__all(__float_as_uint(v) >= 16u)) break;
      __builtin_amdgcn_s_sleep(24);
    }
    pf_wo(x);
    pf_wgu1(x);
    sfor<0, 12>([&](auto j) {
      u32x4 t;
      ld4<1024 * (j & 1)>(t, x.vl16, x.rs, OFF_WGU + (x.m * 196 + 112 + 8 * (j >> 1) + 2 * w) * KB); wv<0>(); pin(t);
#pragma unroll
      for (int i = 0; i < 4; ++i) g2[j >> 2][4 * (j & 3) + i] = __uint_as_float(t[i]);
    });
    pf_wd<0, 46>(x, base_wd(x));
    for (int tk = 0; tk < 16; ++tk) {
      u32x4 kf[4];
      sfor<0, 4>([&](auto i) { ld4<i * 1024>(kf[i], x.vl16, x.rs, kvs + tk * 4096); });
      wv<0>();
      sfor<0, 4>([&](auto k) {
        f32x16 s;
        nop<1>();
        m32z(s, kf[0], q[k][0]); m32(s, kf[1], q[k][1]);
        nop<11>(); pin(s);
        float tm = s[0];
#pragma unroll
        for (int i = 1; i < 16; ++i) tm = fmaxf(tm, s[i]);
        tm = max32(tm);
        const float mn = fmaxf(mx[k], tm), al = __builtin_amdgcn_exp2f(mx[k] - mn);
        mx[k] = mn;
#pragma unroll
        for (int i = 0; i < 16; ++i) o[k][i] *= al;
        u32x4 p0, p1;
#pragma unroll
        for (int d = 0; d < 4; ++d) {
          p0[d] = cvtpk(__builtin_amdgcn_exp2f(s[2 * d] - mn), __builtin_amdgcn_exp2f(s[2 * d + 1] - mn));
          p1[d] = cvtpk(__builtin_amdgcn_exp2f(s[8 + 2 * d] - mn), __builtin_amdgcn_exp2f(s[9 + 2 * d] - mn));
        }
        pin(p0); pin(p1); pin(o[k]);
        nop<1>();
        m32(o[k], kf[2], p0); m32(o[k], kf[3], p1);
        nop<11>(); pin(o[k]);
      });
    }
    bar();
    float* mm = (float*)(lds + SCR);
#pragma unroll
    for (int k = 0; k < 4; ++k) if (h == 0) mm[((w ^ k) * 4 + w) * 32 + r] = mx[k];
    bar();
#pragma unroll
    for (int k = 0; k < 4; ++k) {
      const float* p = mm + (w ^ k) * 128 + r;
      const float g = fmaxf(fmaxf(p[0], p[32]), fmaxf(p[64], p[96])), f = __builtin_amdgcn_exp2f(mx[k] - g);
#pragma unroll
      for (int i = 0; i < 16; ++i) o[k][i] *= f;
    }
    bar();
    reduce_put(x, o);
    own = o[0];
  }
  stamp(x);
  reduce_get(x, own);
  pin(own); fstamp(x);
  {
    auto lr = __builtin_amdgcn_permlane32_swap(__float_as_uint(own[12]), __float_as_uint(own[12]), false, false);
    const float inv = __builtin_amdgcn_rcpf(__uint_as_float(lr[1]));
    u32x4 a, b;
#pragma unroll
    for (int d = 0; d < 4; ++d) { a[d] = cvtpk(own[2 * d] * inv, own[2 * d + 1] * inv); b[d] = cvtpk(own[8 + 2 * d] * inv, own[9 + 2 * d] * inv); }
    if (h) { b[2] = 0; b[3] = 0; }
    const unsigned so = OFF_O + L * SZ_O + (tt * 32 + 2 * (2 * hp + kh)) * KB;
    st4<0>(a, x.vl16, x.rs, so); st4<1024>(b, x.vl16, x.rs, so);
  }
  fstamp(x);
  wv<0>();
  fstamp(x);
  signal(x, FL_O(L, x.c, th), 1);
  if (L == 0 && th == 0) {
    if (x.c == hp) count(x, FL_W2, 1);
    if (hp < 2 && (x.c == 0 || x.c == 4)) count(x, FL_W2, 1);
  }
}

template <bool FINAL> DI void res_epilogue(Ctx& x, const f32x16& acc, const u32x4 (&xr)[2], unsigned Xo, unsigned STo, int tt, int nt, char* out = nullptr) {
  const int h = x.l >> 5, r = x.l & 31;
  u32x4 y[2];
#pragma unroll
  for (int d = 0; d < 8; ++d) y[d >> 2][d & 3] = cvtpk(acc[2 * d] + bflo(xr[d >> 2][d & 3]), acc[2 * d + 1] + bfhi(xr[d >> 2][d & 3]));
  if constexpr (FINAL) {
#pragma unroll
    for (int s = 0; s < 2; ++s) {
      auto a = __builtin_amdgcn_permlane32_swap(y[s][0], y[s][2], false, false);
      auto b = __builtin_amdgcn_permlane32_swap(y[s][1], y[s][3], false, false);
      y[s] = u32x4{a[0], b[0], a[1], b[1]};
    }
    const unsigned vo = r * (D * 2) + h * 16, so = tt * 32 * D * 2 + nt * 64;
    const rsrc_t ro = mkrs(out, S * D * 2);
    st4p<0>(y[0], vo, ro, so); st4p<32>(y[1], vo, ro, so);
  } else {
    const unsigned so = Xo + (tt * 28 + 2 * nt) * KB;
    st4<0>(y[0], x.vl16, x.rs, so); st4<1024>(y[1], x.vl16, x.rs, so);
    float s = 0.f, q = 0.f, s1 = 0.f, q1 = 0.f;
#pragma unroll
    for (int d = 0; d < 4; ++d) {
      s = dot2(y[0][d], 0x3f803f80u, s); q = dot2(y[0][d], y[0][d], q); s1 = dot2(y[1][d], 0x3f803f80u, s1); q1 = dot2(y[1][d], y[1][d], q1);
    }
    s = sum32(s + s1); q = sum32(q + q1);
    if (h == 0) st2<0>(u32x2{__float_as_uint(s), __float_as_uint(q)}, r * 8, x.rs, STo + (nt * 2048 + tt * 32) * 8);
  }
}

DI void phase_oproj(Ctx& x, const int L, const Args& a, const f32x16 (&g2)[3]) {
  const int w = x.w;
  if (x.m < 14) {
    const int th = x.m / 7, ct = x.m % 7, tt0 = 4 * x.c + 2 * th, tt = tt0 + (w >> 1), nt = 2 * ct + (w & 1);
    u32x2 x0[4];
    if (L == 0) {
      const rsrc_t rx = mkrs(a.x, S * D * 2);
      sfor<0, 4>([&](auto i) { ld2i<16 * i>(x0[i], (x.l & 31) * 896 + (x.l >> 5) * 8, rx, tt * 32 * 896 + nt * 64); });
    } else {
      sfor<0, 4>([&](auto i) { ld2u<(i >> 1) * 1024 + (i & 1) * 8>(x0[i], x.vl16, x.rs, OFF_X + 2 * SZ_X + (tt * 28 + 2 * nt) * KB); });
    }
    await(x, FL_O(L, x.c, th), 8);
    stamp(x);
    u32x4 b[8][2];
    const unsigned s0 = OFF_O + L * SZ_O + (tt * 32 + 8 * w) * KB, s1 = OFF_O + L * SZ_O + ((tt0 + ((w >> 1) ^ 1)) * 32 + 8 * w) * KB;
    auto ld = [&](auto i) { ld4<(i & 3) * 1024>(b[i][0], x.vl16, x.rs, s0 + (i >> 2) * 4096); ld4<(i & 3) * 1024>(b[i][1], x.vl16, x.rs, s1 + (i >> 2) * 4096); };
    constexpr int WN = 5;
    sfor<0, WN>(ld);
    f32x16 acc[4];
    sfor<0, 8>([&](auto i) {
      constexpr int last = (i + WN - 1 < 7 ? i + WN - 1 : 7);
      wv<2 * (last - i) + 1>();
      m32a<A_WO + 8 * i, i == 0>(acc[0], b[i][0]); m32a<A_WO + 8 * i + 4, i == 0>(acc[1], b[i][0]);
      wv<2 * (last - i)>();
      m32a<A_WO + 8 * i, i == 0>(acc[2], b[i][1]); m32a<A_WO + 8 * i + 4, i == 0>(acc[3], b[i][1]);
      if constexpr (i + WN < 8) ld(std::integral_constant<int, i + WN>{});
    });
    nop<11>();
    sfor<0, 4>([&](auto k) { pin(acc[k]); pin(x0[k]); });
    stamp(x);
    reduce_put(x, acc); reduce_get(x, acc[0]);
    pin(acc[0]); fstamp(x);
    const u32x4 x2[2] = {u32x4{x0[0][0], x0[0][1], x0[1][0], x0[1][1]}, u32x4{x0[2][0], x0[2][1], x0[3][0], x0[3][1]}};
    res_epilogue<false>(x, acc[0], x2, OFF_X + (2 * L + 1) * SZ_X, OFF_ST + (2 * L + 1) * SZ_ST, tt, nt);
    fstamp(x);
    wv<0>();
    fstamp(x);
    signal(x, FL_X1(L, x.c), 1);
    fstamp(x);
  } else {
    stamp(x); stamp(x); fstamp(x); fstamp(x); fstamp(x); fstamp(x);
  }
#pragma unroll
  for (int j = 0; j < 12; ++j)
    *(f32x4*)(lds + (112 + 8 * (j >> 1) + 2 * w + (j & 1)) * KB + x.vl16) = f32x4{g2[j >> 2][4 * (j & 3)], g2[j >> 2][4 * (j & 3) + 1], g2[j >> 2][4 * (j & 3) + 2], g2[j >> 2][4 * (j & 3) + 3]};
}

constexpr int DWN = 7;
constexpr bool dn_half(int o) { return o == 4 || o == 5 || o == 14 || o == 15; }
struct DnSrc { unsigned s0, s1, l0, l1; };
DI DnSrc dn_src(const Ctx& x, int L) {
  const int tt0 = 4 * x.c + 2 * ((x.m / 7) & 1), tt = tt0 + (x.w >> 1), tto = tt0 + ((x.w >> 1) ^ 1);
  return DnSrc{OFF_H + L * SZ_H + (tt * 112 + 28 * x.w) * KB, OFF_H + L * SZ_H + (tto * 112 + 28 * x.w) * KB,
               OFF_HL + ((L * 64 + tt) * 16 + 4 * x.w) * KB, OFF_HL + ((L * 64 + tto) * 16 + 4 * x.w) * KB};
}
template <int O> DI void dn_ld(Ctx& x, const DnSrc& d, u32x4 (&b)[DWN][2]) {
  constexpr int i = dn_ks(O);
  if constexpr (dn_half(O)) {
    constexpr int imm = (O >= 14 ? 2048 : 0) + (O & 1) * 512;
    u32x2 lo, hi, lo1, hi1;
    ld2u<imm>(lo, x.l * 8, x.rs, d.l0); ld2u<imm + 1024>(hi, x.l * 8, x.rs, d.l0);
    ld2u<imm>(lo1, x.l * 8, x.rs, d.l1); ld2u<imm + 1024>(hi1, x.l * 8, x.rs, d.l1);
    b[O % DWN][0] = u32x4{lo[0], lo[1], hi[0], hi[1]}; b[O % DWN][1] = u32x4{lo1[0], lo1[1], hi1[0], hi1[1]};
  } else {
    ld4<(i & 3) * 1024>(b[O % DWN][0], x.vl16, x.rs, d.s0 + (i >> 2) * 4096); ld4<(i & 3) * 1024>(b[O % DWN][1], x.vl16, x.rs, d.s1 + (i >> 2) * 4096);
  }
}

DI void gu_tile(const f32x4 (&acc)[2][2], const u32x4 (&cs)[2], const float (&mean)[2], const float (&r2)[2], u32x2 (&hp)[2]) {
  const f32x4 cg = __builtin_bit_cast(f32x4, cs[0]), cu = __builtin_bit_cast(f32x4, cs[1]);
#pragma unroll
  for (int u = 0; u < 2; ++u) {
    const f32x2 nm = {-mean[u], -mean[u]}, rr = {r2[u], r2[u]};
#pragma unroll
    for (int e = 0; e < 2; ++e) {
      const f32x2 g = __builtin_elementwise_fma(nm, f32x2{cg[2 * e], cg[2 * e + 1]}, f32x2{acc[0][u][2 * e], acc[0][u][2 * e + 1]});
      const f32x2 v = __builtin_elementwise_fma(nm, f32x2{cu[2 * e], cu[2 * e + 1]}, f32x2{acc[1][u][2 * e], acc[1][u][2 * e + 1]});
      const f32x2 y = g * rr * v;
      hp[u][e] = cvtpk(y[0], y[1]);
    }
  }
}

DI void phase_gu(Ctx& x, const int L, u32x4 (&b)[DWN][2]) {
  const int w = x.w, l = x.l, g = l >> 4, c16 = l & 15, m = x.m, tt = 4 * x.c + w;
  if (L == 0 && wave0(x)) { unsigned v; ldflag(v, x.rs, OFF_FLAG + FL_W2); wv<0>(); recheck(x, v, FL_W2, 12); }
  await(x, FL_X1(L, x.c), 14);
  stamp(x);
  constexpr int NXV = 11;
  u32x4 xf[NXV][2]; f32x2 sp[2][4];
  const unsigned sx = OFF_X + (2 * L + 1) * SZ_X + tt * 28 * KB, st = OFF_ST + (2 * L + 1) * SZ_ST + tt * 32 * 8;
  auto ldx = [&](auto k, auto u) {
    if constexpr (k < NXV) ld4<(k & 1) * 2048 + 256 * u>(xf[k][u], x.vg16, x.rs, sx + (k >> 1) * 4096);
    else if constexpr (k < 14) lda<A_GX + 8 * (k - NXV) + 4 * u, (k & 1) * 2048 + 256 * u>(x.vg16, x.rs, sx + (k >> 1) * 4096);
    else { constexpr int i = k - 14; ld2<128 * u>(sp[u][i], (((i < 3 || g < 2) ? g + 4 * i : g) * 2048 + c16) * 8, x.rs, st); }
  };
  using I0 = std::integral_constant<int, 0>; using I1 = std::integral_constant<int, 1>;
  constexpr int XA = 3;
  sfor<0, XA>([&](auto k) { ldx(k, I0{}); ldx(k, I1{}); });
  const unsigned a0 = x.vl16, a1 = x.vl16 + 65536, a2 = x.vl16 + 131072;
  constexpr int DP = 6;
  u32x4 wf[8];
  auto rd = [&](auto ii) {
    constexpr int i = ii, slot = i >= 160 ? i - 160 : i;
    if constexpr (slot < 64) dsr<slot * 1024>(wf[i & 7], a0); else if constexpr (slot < 128) dsr<(slot - 64) * 1024>(wf[i & 7], a1); else dsr<(slot - 128) * 1024>(wf[i & 7], a2);
  };
  auto mm = [&](f32x4& c, const u32x4& a, auto k, auto u) {
    if constexpr (k < NXV) m16<k == 0>(c, a, xf[k][u]); else m16a<false, A_GX + 8 * (k - NXV) + 4 * u>(c, a);
  };
  const Two wd = base_wd(x);
  f32x4 aa[5][2][2], ch0, ch1;
  float mean[2], r2[2];
  sfor<0, DP>(rd);
  sfor<0, 140>([&](auto i) {
    constexpr int KS = i / 10, s = i % 10, t = s >> 1, mat = i & 1;
    using K = std::integral_constant<int, KS>;
    constexpr auto side = [](int k) { return (k >= 0 && k <= 3) || (k >= 5 && k <= 10); };
    constexpr auto all = [](int k) { return (k + XA < 14 ? 2 : 0) + ((k >= 0 && k <= 3) || (k >= 5 && k <= 9) ? 2 : 0) + (k == 5 ? 1 : 0) + (k >= 10 ? 3 : 0); };
    if constexpr (s == 0) {
      wv<(side(KS - 3) ? 1 : 0) + all(KS - 2) + all(KS - 1)>();
      if constexpr (KS == 5) asm volatile("s_barrier" ::: "memory");
    }
    if constexpr ((i & 1) == 0) { rd(std::integral_constant<int, i + DP>{}); rd(std::integral_constant<int, i + DP + 1>{}); wl<DP>(); }
    mm(aa[t][mat][0], wf[i & 7], K{}, I0{});
    if constexpr (s == 1 && KS + XA < 14) ldx(std::integral_constant<int, KS + XA>{}, I0{});
    if constexpr (s == 6 && KS + XA < 14) ldx(std::integral_constant<int, KS + XA>{}, I1{});
    if constexpr (KS <= 3 && s == 3) ldx(std::integral_constant<int, 14 + KS>{}, I0{});
    if constexpr (KS <= 3 && s == 8) ldx(std::integral_constant<int, 14 + KS>{}, I1{});
    if constexpr (KS == 5 && s == 3) asm volatile("buffer_load_dwordx4 %0, %2, %3, %4 offen sc1\n\tbuffer_load_dwordx4 %1, %2, %3, %4 offen offset:1024 sc1" : "=&v"(ch0), "=&v"(ch1) : "v"(x.vl16), "s"(x.rs), "s"(OFF_CSGU + m * 2048));
    if constexpr (KS == 11 && s == 2) { pin(ch0); pin(ch1); *(f32x4*)(lds + 40 * KB + x.vl16) = ch0 + ch1; }
    if constexpr (KS >= 5 && KS <= 9 && (s == 3 || s == 8) && !(KS == 5 && s == 3)) pf_wgu3<2 * (KS - 5) + (s == 8) - 1>(x);
    if constexpr (KS >= 10 && (s == 3 || s == 5 || s == 8) && 3 * (KS - 10) + (s == 3 ? 0 : (s == 5 ? 1 : 2)) < 10) {
      constexpr int f = 46 + 3 * (KS - 10) + (s == 3 ? 0 : (s == 5 ? 1 : 2));
      pf_wd<f, f + 1>(x, wd);
    }
    if constexpr ((KS == 10 || KS == 12) && s == 2) {
      constexpr int u = KS == 12;
      sfor<0, 4>([&](auto e) { pin(sp[u][e]); });
      float sm = sp[u][0][0] + sp[u][1][0] + sp[u][2][0], q = sp[u][0][1] + sp[u][1][1] + sp[u][2][1];
      if (g < 2) { sm += sp[u][3][0]; q += sp[u][3][1]; }
      sm = sum32(sum16(sm)); q = sum32(sum16(q));
      float rs; row_stats(sm, q, mean[u], rs); r2[u] = rs * rs;
      pin(mean[u]); pin(r2[u]);
    }
    mm(aa[t][mat][1], wf[i & 7], K{}, I1{}); SB();
  });
  fstamp(x);
  u32x4 cs[7][2];
  const unsigned csa = 40 * KB + g * 16;
  sfor<0, 10>([&](auto i) { dsr<64 * i>(cs[i >> 1][i & 1], csa); });
  wv<10>();
  asm volatile("s_barrier" ::: "memory");
  stamp(x);
  const unsigned H = OFF_H + L * SZ_H + tt * 112 * KB;
  const int gp = (m & 1) ? 7 * m + 1 : 7 * m;
  auto pair = [&](const f32x4 (&t0)[2][2], const f32x4 (&t1)[2][2], auto lt) {
    u32x2 h0[2], h1[2];
    gu_tile(t0, cs[lt], mean, r2, h0); gu_tile(t1, cs[lt + 1], mean, r2, h1);
    st4<0>(u32x4{h0[0][0], h0[0][1], h1[0][0], h1[0][1]}, x.vg16, x.rs, H + (gp + lt - 1) * 1024);
    st4<256>(u32x4{h0[1][0], h0[1][1], h1[1][0], h1[1][1]}, x.vg16, x.rs, H + (gp + lt - 1) * 1024);
  };
  {
    wl<0>();
    sfor<0, 20>([&](auto i) { pin(aa[i >> 2][(i >> 1) & 1][i & 1]); });
    sfor<0, 10>([&](auto i) { pin(cs[i >> 1][i & 1]); });
    u32x2 hl[2];
    gu_tile(aa[0], cs[0], mean, r2, hl);
    const unsigned sl = OFF_HL + ((L * 64 + tt) * 16 + m) * KB;
    st2<0>(hl[0], x.vg16 >> 1, x.rs, sl); st2<128>(hl[1], x.vg16 >> 1, x.rs, sl);
    pair(aa[1], aa[2], std::integral_constant<int, 1>{});
    pair(aa[3], aa[4], std::integral_constant<int, 3>{});
  }
  SB();
  sfor<10, 14>([&](auto i) { dsr<64 * i>(cs[i >> 1][i & 1], csa); });
  fstamp(x);
  f32x4 ab[2][2][2];
  unsigned fa;
  sfor<140, 196>([&](auto i) {
    constexpr int j = i - 140, KS = j / 4, t = (j % 4) >> 1, mat = i & 1;
    using K = std::integral_constant<int, KS>;
    if constexpr (j == 18) {
      fstamp(x);
      if (L == 0) wv<6>(); else wv<0>();
      asm volatile("s_barrier" ::: "memory");
      count(x, FL_H(L, x.c, 0), 1);
      SB();
      fstamp(x);
    }
    if constexpr ((i & 1) == 0) { if constexpr (i + DP < 196) { rd(std::integral_constant<int, i + DP>{}); rd(std::integral_constant<int, i + DP + 1>{}); wl<DP>(); } else wl<194 - i>(); }
    mm(ab[t][mat][0], wf[i & 7], K{}, I0{});
    if constexpr (j % 3 == 1 && j < 42) { if (L == 0) pf_wq<j / 3>(x); SB(); }
    if constexpr (j == 44) ldflag(fa, x.rs, OFF_FLAG + FL_H(L, x.c, 0));
    mm(ab[t][mat][1], wf[i & 7], K{}, I1{}); SB();
  });
  wv<0>();
  recheck(x, fa, FL_H(L, x.c, 0), 16);
  { const DnSrc d = dn_src(x, L); sfor<0, 3>([&](auto o) { dn_ld<o>(x, d, b); }); }
  stamp(x);
  sfor<0, 8>([&](auto i) { pin(ab[i >> 2][(i >> 1) & 1][i & 1]); });
  sfor<10, 14>([&](auto i) { pin(cs[i >> 1][i & 1]); });
  pair(ab[0], ab[1], std::integral_constant<int, 5>{});
}

DI void phase_down(Ctx& x, const int L, char* out, u32x4 (&b)[DWN][2]) {
  const int w = x.w;
  if (x.m >= 14) {
    wv<0>();
    sfor<0, 3>([&](auto o) { pin(b[o][0]); pin(b[o][1]); });
    signal(x, FL_H(L, x.c, 1), 1);
    stamp(x); fstamp(x); fstamp(x); stamp(x);
    return;
  }
  const int th = x.m / 7, ct = x.m % 7, tt = 4 * x.c + 2 * th + (w >> 1), nt = 2 * ct + (w & 1);
  stamp(x);
  constexpr int WN = DWN;
  u32x4 xr[2];
  const DnSrc d = dn_src(x, L);
  auto ld = [&](auto o) { dn_ld<o>(x, d, b); };
  sfor<3, WN>(ld);
  f32x16 acc[4];
  unsigned fb;
  sfor<0, 25>([&](auto i) {
    constexpr int last = i + WN - 1 < 27 ? i + WN - 1 : 27, ex = i <= 2 ? 2 : ((i >= 8 && i <= 13) ? 1 : ((i >= 21) ? 2 : 0));
    constexpr int later = [&] { int n = 0; for (int o = i + 1; o <= last; ++o) n += dn_half(o) ? 4 : 2; return n; }();
    wv<later + (dn_half(i) ? 2 : 1) + ex>();
    if constexpr (i == 3) signal(x, FL_H(L, x.c, 1), 1);
    m32a<A_WD + 8 * i, i == 0>(acc[0], b[i % WN][0]); m32a<A_WD + 8 * i + 4, i == 0>(acc[1], b[i % WN][0]);
    wv<later + ex>();
    m32a<A_WD + 8 * i, i == 0>(acc[2], b[i % WN][1]); m32a<A_WD + 8 * i + 4, i == 0>(acc[3], b[i % WN][1]);
    if constexpr (i == 7) ldflag(fb, x.rs, OFF_FLAG + FL_H(L, x.c, 1));
    if constexpr (i + WN == 20) { fstamp(x); wv<12>(); recheck(x, fb, FL_H(L, x.c, 1), 16); fstamp(x); }
    if constexpr (i + WN < 28) ld(std::integral_constant<int, i + WN>{});
    if constexpr (i + WN == 27) {
      const unsigned sr = OFF_X + (2 * L + 1) * SZ_X + (tt * 28 + 2 * nt) * KB;
      ld4<0>(xr[0], x.vl16, x.rs, sr); ld4<1024>(xr[1], x.vl16, x.rs, sr);
    }
  });
  wv<2>();
  sfor<1, 5>([&](auto t) {
    constexpr int k = t & 3;
    m32a<A_WD + 8 * 25 + 4 * (k & 1), false>(acc[k], b[25 % WN][k >> 1]);
    m32a<A_WD + 8 * 26 + 4 * (k & 1), false>(acc[k], b[26 % WN][k >> 1]);
    if constexpr (t > 1) { nop<(t == 2 ? 9 : 7)>(); red_put1<t - 1, 0>(x, acc[t - 1]); red_put1<t - 1, 1>(x, acc[t - 1]); }
    m32a<A_WD + 8 * 27 + 4 * (k & 1), false>(acc[k], b[27 % WN][k >> 1]);
    if constexpr (t > 1) { red_put1<t - 1, 2>(x, acc[t - 1]); red_put1<t - 1, 3>(x, acc[t - 1]); }
  });
  if (L == 0 && (x.m & 7) < 2) {
    ldl<4>(x.vl16, x.rs, OFF_WQ + ((16 + (x.m & 1)) * 28 + 7 * w) * KB, 7 * w * KB);
    ldl<3>(x.vl16, x.rs, OFF_WQ + ((16 + (x.m & 1)) * 28 + 7 * w + 4) * KB, (7 * w + 4) * KB);
  }
  nop<9>();
  pin(acc[0]);
  stamp(x);
  reduce_get(x, acc[0]);
  wv<0>();
  pin(xr[0]); pin(xr[1]);
  if (L > 0) {
    res_epilogue<true>(x, acc[0], xr, 0, 0, tt, nt, out);
  } else {
    res_epilogue<false>(x, acc[0], xr, OFF_X + 2 * SZ_X, OFF_ST + 2 * SZ_ST, tt, nt);
    wv<0>();
    signal(x, FL_X(x.c, th), 1);
  }
}

#define STAMP_SYNC
__global__ __launch_bounds__(256, 1) void k_main(Args a) {
  Ctx x;
  x.rs = mkrs(a.ws, WS_USED);
  x.w = __builtin_amdgcn_readfirstlane(threadIdx.x >> 6);
  asm volatile("s_nop 4" : "+s"(x.w));
  { const int b = blockIdx.x, i = b >> 4; x.c = 2 * (b & 7) + ((b >> 3) & 1); x.m = i < 4 ? (i & 1) + 8 * (i >> 1) : (i < 10 ? i - 2 : i); }
  f32x16 kf[3];
  { Ctx y = x; fresh(y); stamp(y); phase_pre(y, a, kf); STAMP_SYNC }
  for (int L = 0;; ++L) {
    f32x16 g2[3];
    int Lo = L; asm volatile("" : "+s"(Lo));
    { Ctx y = x; fresh(y); phase_att(y, Lo, kf, g2); stamp(y); STAMP_SYNC }
    { Ctx y = x; fresh(y); phase_oproj(y, Lo, a, g2); stamp(y); STAMP_SYNC }
    { Ctx y = x; u32x4 b[DWN][2]; fresh(y); phase_gu(y, Lo, b); stamp(y); fresh(y); phase_down(y, Lo, a.out, b); stamp(y); STAMP_SYNC }
    if (L == 1) break;
    { Ctx y = x; fresh(y); phase_front(y, kf); STAMP_SYNC }
  }
  fresh(x);
  if (blockIdx.x == 0 && wave0(x)) {
    get_tag(x);
    if (x.l == 0) st2<0>(u32x2{x.tlo, x.thi}, 0, x.rs, OFF_TAG);
  }
  stamp_flush(x);
  asm volatile(".Lk_end:");
}

extern "C" void kernel_launch(void* const* d_in, const int* in_sizes, int n_in, void* d_out, int out_size, void* d_ws, size_t ws_size,
                              hipStream_t stream) {
  Args a;
  a.x = (const char*)d_in[0]; a.wq = (const char*)d_in[1]; a.wk = (const char*)d_in[2]; a.wv = (const char*)d_in[3];
  a.wo = (const char*)d_in[4]; a.wg = (const char*)d_in[6]; a.wu = (const char*)d_in[7]; a.wd = (const char*)d_in[8];
  a.ws = (char*)d_ws; a.out = (char*)d_out;
  (void)hipFuncSetAttribute((const void*)k_main, hipFuncAttributeMaxDynamicSharedMemorySize, 160 * 1024);
  k_main<<<256, 256, 160 * 1024, stream>>>(a);
}
```

```cpp
#include <hip/hip_runtime.h>
#include <stdint.h>
#include <type_traits>

typedef float    f32x16 __attribute__((ext_vector_type(16)));
typedef float    f32x4  __attribute__((ext_vector_type(4)));
typedef float    f32x2  __attribute__((ext_vector_type(2)));
typedef unsigned u32x4  __attribute__((ext_vector_type(4)));
typedef unsigned u32x2  __attribute__((ext_vector_type(2)));
typedef __bf16   bf16x2 __attribute__((ext_vector_type(2)));
typedef __amdgpu_buffer_rsrc_t rsrc_t;
#define DI __device__ __forceinline__
#define GAS __attribute__((address_space(1)))
#define SB() __builtin_amdgcn_sched_barrier(0)

constexpr int S = 2048, D = 448, F = 1792;
constexpr unsigned KB = 1024;
constexpr unsigned OFF_FLAG = 0;
constexpr unsigned OFF_CSQ  = 64 * KB;
constexpr unsigned OFF_CSGU = OFF_CSQ + 4 * KB;
constexpr unsigned OFF_SLOT = OFF_CSGU + 32 * KB;
constexpr unsigned OFF_TAG  = OFF_SLOT + 32 * KB;
constexpr unsigned OFF_WQ   = 160 * KB;
constexpr unsigned OFF_WO   = OFF_WQ + 18 * 28 * KB;
constexpr unsigned OFF_WGU  = OFF_WO + 14 * 32 * KB;
constexpr unsigned OFF_WD   = OFF_WGU + 16 * 196 * KB;
constexpr unsigned SZ_X = 64 * 28 * KB, SZ_ST = 14 * 2048 * 8, SZ_O = 64 * 32 * KB, SZ_H = 64 * 112 * KB, SZ_KV = 64 * 4 * KB;
constexpr unsigned OFF_X    = OFF_WD + 14 * 112 * KB;
constexpr unsigned OFF_ST   = OFF_X + 4 * SZ_X;
constexpr unsigned OFF_O    = OFF_ST + 4 * SZ_ST;
constexpr unsigned OFF_H    = OFF_O + 2 * SZ_O;
constexpr unsigned OFF_HL   = OFF_H + 2 * SZ_H;
constexpr unsigned OFF_KV   = OFF_HL + 2 * 1024 * KB;
constexpr unsigned WS_USED  = OFF_KV + 2 * SZ_KV;
DI constexpr unsigned FL_X(int c, int th)         { return (c * 16 + th) * 256u; }
DI constexpr unsigned FL_O(int L, int c, int th)  { return (c * 16 + 2 + L * 2 + th) * 256u; }
DI constexpr unsigned FL_X1(int L, int c)         { return (c * 16 + 6 + L) * 256u; }
DI constexpr unsigned FL_H(int L, int c, int p)   { return (c * 16 + 8 + L * 2 + p) * 256u; }
DI constexpr unsigned FL_W(int c)                 { return (c * 16 + 12) * 256u; }
constexpr unsigned FL_W2 = 13 * 256u;

#define XAHEAD 3
#define OPWN 5
constexpr float QSCALE = 0.18898223650461362f * 1.4426950408889634f;
constexpr float M_LIMIT = 48.0f;

template <int I, int N, class Fn> DI void sfor(Fn&& f) { if constexpr (I < N) { f(std::integral_constant<int, I>{}); sfor<I + 1, N>(f); } }

DI unsigned cvtpk(float lo, float hi) { f32x2 v = {lo, hi}; bf16x2 b = __builtin_convertvector(v, bf16x2); return __builtin_bit_cast(unsigned, b); }
DI float bflo(unsigned u) { return __uint_as_float(u << 16); }
DI float bfhi(unsigned u) { return __uint_as_float(u & 0xffff0000u); }
DI float dot2(unsigned a, unsigned b, float c) { return __builtin_amdgcn_fdot2_f32_bf16(__builtin_bit_cast(bf16x2, a), __builtin_bit_cast(bf16x2, b), c, false); }
DI float sum32(float v) { auto r = __builtin_amdgcn_permlane32_swap(__float_as_uint(v), __float_as_uint(v), false, false); return __uint_as_float(r[0]) + __uint_as_float(r[1]); }
DI float sum16(float v) { auto r = __builtin_amdgcn_permlane16_swap(__float_as_uint(v), __float_as_uint(v), false, false); return __uint_as_float(r[0]) + __uint_as_float(r[1]); }
DI float max32(float v) { auto r = __builtin_amdgcn_permlane32_swap(__float_as_uint(v), __float_as_uint(v), false, false); return fmaxf(__uint_as_float(r[0]), __uint_as_float(r[1])); }

DI unsigned umax16(unsigned v) {
  v = max(v, (unsigned)__builtin_amdgcn_update_dpp(0, (int)v, 0x128, 0xf, 0xf, false));
  v = max(v, (unsigned)__builtin_amdgcn_update_dpp(0, (int)v, 0x124, 0xf, 0xf, false));
  v = max(v, (unsigned)__builtin_amdgcn_update_dpp(0, (int)v, 0x122, 0xf, 0xf, false));
  v = max(v, (unsigned)__builtin_amdgcn_update_dpp(0, (int)v, 0x121, 0xf, 0xf, false));
  return v;
}
DI unsigned umax64(unsigned v) {
  v = umax16(v);
  auto a = __builtin_amdgcn_permlane16_swap(v, v, false, false); v = max(a[0], a[1]);
  auto b = __builtin_amdgcn_permlane32_swap(v, v, false, false); return max(b[0], b[1]);
}

typedef short s16x4 __attribute__((ext_vector_type(4)));
DI u32x2 trread(const char* p) { return __builtin_bit_cast(u32x2, __builtin_amdgcn_ds_read_tr16_b64_v4i16((__attribute__((address_space(3))) s16x4*)p)); }
DI u32x4 trfrag(const char* lo, const char* hi) { const u32x2 a = trread(lo), b = trread(hi); return u32x4{a[0], a[1], b[0], b[1]}; }
DI void row_stats(float s, float q, float& mean, float& rstd) {
  mean = s * (1.0f / D);
  const float var = fmaxf(q * (1.0f / D) - mean * mean, 0.f);
  rstd = __builtin_amdgcn_rsqf(var + 1e-5f);
}
DI float fragsum(const u32x4& f, float s) {
#pragma unroll
  for (int d = 0; d < 4; ++d) s = dot2(f[d], 0x3f803f80u, s);
  return s;
}
struct Args { const char *x, *wq, *wk, *wv, *wo, *wg, *wu, *wd; char* ws; char* out; };

#define ACLOB "a0","a1","a2","a3","a4","a5","a6","a7","a8","a9","a10","a11","a12","a13","a14","a15","a16","a17","a18","a19","a20","a21","a22","a23","a24","a25","a26","a27","a28","a29","a30","a31","a32","a33","a34","a35","a36","a37","a38","a39","a40","a41","a42","a43","a44","a45","a46","a47","a48","a49","a50","a51","a52","a53","a54","a55","a56","a57","a58","a59","a60","a61","a62","a63","a64","a65","a66","a67","a68","a69","a70","a71","a72","a73","a74","a75","a76","a77","a78","a79","a80","a81","a82","a83","a84","a85","a86","a87","a88","a89","a90","a91","a92","a93","a94","a95","a96","a97","a98","a99","a100","a101","a102","a103","a104","a105","a106","a107","a108","a109","a110","a111","a112","a113","a114","a115","a116","a117","a118","a119","a120","a121","a122","a123","a124","a125","a126","a127","a128","a129","a130","a131","a132","a133","a134","a135","a136","a137","a138","a139","a140","a141","a142","a143","a144","a145","a146","a147","a148","a149","a150","a151","a152","a153","a154","a155","a156","a157","a158","a159","a160","a161","a162","a163","a164","a165","a166","a167","a168","a169","a170","a171","a172","a173","a174","a175","a176","a177","a178","a179","a180","a181","a182","a183","a184","a185","a186","a187","a188","a189","a190","a191","a192","a193","a194","a195","a196","a197","a198","a199","a200","a201","a202","a203","a204","a205","a206","a207","a208","a209","a210","a211","a212","a213","a214","a215","a216","a217","a218","a219","a220","a221","a222","a223","a224","a225","a226","a227","a228","a229","a230","a231","a232","a233","a234","a235","a236","a237","a238","a239","a240","a241","a242","a243","a244","a245","a246","a247","a248","a249","a250","a251","a252","a253","a254","a255"

template <int IMM> DI void ld4(u32x4& d, unsigned vo, rsrc_t rs, unsigned so) {
  asm volatile("buffer_load_dwordx4 %0, %1, %2, %3 offen offset:%c4 sc1" : "=v"(d) : "v"(vo), "s"(rs), "s"(so), "i"(IMM));
}
template <int IMM> DI void ld2(f32x2& d, unsigned vo, rsrc_t rs, unsigned so) {
  asm volatile("buffer_load_dwordx2 %0, %1, %2, %3 offen offset:%c4 sc1" : "=v"(d) : "v"(vo), "s"(rs), "s"(so), "i"(IMM));
}
template <int IMM> DI void ld4i(u32x4& d, unsigned vo, rsrc_t rs, unsigned so) {
  asm volatile("buffer_load_dwordx4 %0, %1, %2, %3 offen offset:%c4" : "=v"(d) : "v"(vo), "s"(rs), "s"(so), "i"(IMM));
}
template <int IMM> DI void ld2i(u32x2& d, unsigned vo, rsrc_t rs, unsigned so) {
  asm volatile("buffer_load_dwordx2 %0, %1, %2, %3 offen offset:%c4" : "=v"(d) : "v"(vo), "s"(rs), "s"(so), "i"(IMM));
}
template <int IMM> DI void ld2u(u32x2& d, unsigned vo, rsrc_t rs, unsigned so) {
  asm volatile("buffer_load_dwordx2 %0, %1, %2, %3 offen offset:%c4 sc1" : "=v"(d) : "v"(vo), "s"(rs), "s"(so), "i"(IMM));
}
template <int IMM> DI void ld4f(f32x4& d, unsigned vo, rsrc_t rs, unsigned so) {
  asm volatile("buffer_load_dwordx4 %0, %1, %2, %3 offen offset:%c4 sc1" : "=v"(d) : "v"(vo), "s"(rs), "s"(so), "i"(IMM));
}
DI void ld1(float& d, unsigned vo, rsrc_t rs, unsigned so) {
  asm volatile("buffer_load_dword %0, %1, %2, %3 offen sc1" : "=v"(d) : "v"(vo), "s"(rs), "s"(so));
}
template <int AR, int IMM> DI void lda(unsigned vo, rsrc_t rs, unsigned so) {
  asm volatile("buffer_load_dwordx4 a[%c3:%c4], %0, %1, %2 offen offset:%c5 sc1" :: "v"(vo), "s"(rs), "s"(so), "i"(AR), "i"(AR + 3), "i"(IMM) : ACLOB);
}
template <int N> DI void ldl(unsigned vo, rsrc_t rs, unsigned so, unsigned lds) {
  asm volatile("s_mov_b32 m0, %3\n\ts_nop 0\n\tbuffer_load_dwordx4 %0, %1, %2 offen sc1 lds" :: "v"(vo), "s"(rs), "s"(so), "s"(lds) : "memory");
  if constexpr (N > 1) asm volatile("buffer_load_dwordx4 %0, %1, %2 offen offset:1024 sc1 lds" :: "v"(vo), "s"(rs), "s"(so) : "memory");
  if constexpr (N > 2) asm volatile("buffer_load_dwordx4 %0, %1, %2 offen offset:2048 sc1 lds" :: "v"(vo), "s"(rs), "s"(so) : "memory");
  if constexpr (N > 3) asm volatile("buffer_load_dwordx4 %0, %1, %2 offen offset:3072 sc1 lds" :: "v"(vo), "s"(rs), "s"(so) : "memory");
}
template <int IMM> DI void st4(const u32x4& d, unsigned vo, rsrc_t rs, unsigned so) {
  asm volatile("buffer_store_dwordx4 %0, %1, %2, %3 offen offset:%c4 sc1\n\ts_nop 1" :: "v"(d), "v"(vo), "s"(rs), "s"(so), "i"(IMM) : "memory");
}
template <int IMM> DI void st4p(const u32x4& d, unsigned vo, rsrc_t rs, unsigned so) {
  asm volatile("buffer_store_dwordx4 %0, %1, %2, %3 offen offset:%c4\n\ts_nop 1" :: "v"(d), "v"(vo), "s"(rs), "s"(so), "i"(IMM) : "memory");
}
template <int IMM> DI void st2(const u32x2& d, unsigned vo, rsrc_t rs, unsigned so) {
  asm volatile("buffer_store_dwordx2 %0, %1, %2, %3 offen offset:%c4 sc1" :: "v"(d), "v"(vo), "s"(rs), "s"(so), "i"(IMM) : "memory");
}
DI void st1(float d, unsigned vo, rsrc_t rs, unsigned so) {
  asm volatile("buffer_store_dword %0, %1, %2, %3 offen sc1" :: "v"(d), "v"(vo), "s"(rs), "s"(so) : "memory");
}
template <int N> DI void wv() { asm volatile("s_waitcnt vmcnt(%c0)" :: "i"(N) : "memory"); SB(); }
template <int N> DI void wl() { asm volatile("s_waitcnt lgkmcnt(%c0)" :: "i"(N) : "memory"); SB(); }
template <class T> DI void pin(T& x) { asm volatile("" : "+v"(x)); }
DI void bar() { asm volatile("s_waitcnt lgkmcnt(0)\n\ts_barrier" ::: "memory"); }
template <int N> DI void nop() { asm volatile("s_nop %c0" :: "i"(N)); SB(); }

template <int OFF> DI void trd(u32x2& r, unsigned a) { asm volatile("ds_read_b64_tr_b16 %0, %1 offset:%c2" : "=v"(r) : "v"(a), "i"(OFF)); }
template <int OFF> DI void dsr(u32x4& r, unsigned a) { asm volatile("ds_read_b128 %0, %1 offset:%c2" : "=v"(r) : "v"(a), "i"(OFF)); }

DI void m32(f32x16& c, const u32x4& a, const u32x4& b)  { asm volatile("v_mfma_f32_32x32x16_bf16 %0, %1, %2, %0" : "+v"(c) : "v"(a), "v"(b)); }
DI void m32z(f32x16& c, const u32x4& a, const u32x4& b) { asm volatile("v_mfma_f32_32x32x16_bf16 %0, %1, %2, 0" : "=&v"(c) : "v"(a), "v"(b)); }
template <int AR, bool Z> DI void m32a(f32x16& c, const u32x4& b) {
  if constexpr (Z) asm volatile("v_mfma_f32_32x32x16_bf16 %0, a[%c2:%c3], %1, 0" : "=&v"(c) : "v"(b), "i"(AR), "i"(AR + 3));
  else             asm volatile("v_mfma_f32_32x32x16_bf16 %0, a[%c2:%c3], %1, %0" : "+v"(c) : "v"(b), "i"(AR), "i"(AR + 3));
}
template <int AR, bool Z> DI void m32b(f32x16& c, const u32x4& a) {
  if constexpr (Z) asm volatile("v_mfma_f32_32x32x16_bf16 %0, %1, a[%c2:%c3], 0" : "=&v"(c) : "v"(a), "i"(AR), "i"(AR + 3));
  else             asm volatile("v_mfma_f32_32x32x16_bf16 %0, %1, a[%c2:%c3], %0" : "+v"(c) : "v"(a), "i"(AR), "i"(AR + 3));
}
template <bool Z> DI void m16(f32x4& c, const u32x4& a, const u32x4& b) {
  if constexpr (Z) asm volatile("v_mfma_f32_16x16x32_bf16 %0, %1, %2, 0" : "=&v"(c) : "v"(a), "v"(b));
  else             asm volatile("v_mfma_f32_16x16x32_bf16 %0, %1, %2, %0" : "+v"(c) : "v"(a), "v"(b));
}

template <bool Z, int AR> DI void m16a(f32x4& c, const u32x4& a) {
  if constexpr (Z) asm volatile("v_mfma_f32_16x16x32_bf16 %0, %1, a[%c2:%c3], 0" : "=&v"(c) : "v"(a), "i"(AR), "i"(AR + 3));
  else             asm volatile("v_mfma_f32_16x16x32_bf16 %0, %1, a[%c2:%c3], %0" : "+v"(c) : "v"(a), "i"(AR), "i"(AR + 3));
}
DI void ldflag(unsigned& v, rsrc_t rs, unsigned so) { asm volatile("buffer_load_dword %0, off, %1, %2 sc1" : "=v"(v) : "s"(rs), "s"(so)); }

struct Ctx {
  rsrc_t rs;
  unsigned tlo, thi;
  unsigned vl16;
  unsigned vg16;
  int w, l, c, m;
};
DI void fresh(Ctx& x) {
  asm volatile("" : "+s"(x.w), "+s"(x.c), "+s"(x.m));
  asm volatile("v_mbcnt_lo_u32_b32 %0, -1, 0\n\tv_mbcnt_hi_u32_b32 %0, -1, %0" : "=v"(x.l));
  x.vl16 = x.l * 16;
  const int g = x.l >> 4;
  x.vg16 = ((g >> 1) * 64 + (g & 1) * 32 + (x.l & 15)) * 16;
}
extern __shared__ __attribute__((aligned(16))) char lds[];
constexpr unsigned SCR = 112 * KB;

DI void get_tag(Ctx& x) {
  unsigned lo, hi;
  asm volatile("v_accvgpr_read_b32 v255, a252\n\ts_nop 1\n\tv_readfirstlane_b32 %0, v255\n\tv_accvgpr_read_b32 v255, a253\n\ts_nop 1\n\tv_readfirstlane_b32 %1, v255\n\ts_nop 4"
               : "=s"(lo), "=s"(hi) :: "v255");
  const unsigned long long e = ((unsigned long long)hi << 32) | lo;
  unsigned long long n = e * 0x9E3779B97F4A7C15ull + 0x632BE59BD9B4E019ull;
  if (((n ^ e) >> 16) == 0) n += 0x10000;
  x.tlo = (unsigned)n; x.thi = (unsigned)(n >> 32);
}
DI bool wave0(const Ctx& x) { int w = x.w; asm volatile("" : "+s"(w)); return w == 0; }
DI void count(Ctx& x, unsigned fl, int n) {
  if (wave0(x)) {
    const unsigned long long mask = (1ull << n) - 1;
    unsigned long long keep;
    asm volatile("s_mov_b64 %0, exec\n\ts_mov_b64 exec, %5\n\tbuffer_atomic_add %1, %2, %3, %4 offen\n\ts_mov_b64 exec, %0"
                 : "=&s"(keep) : "v"(1u), "v"(x.l * 256), "s"(x.rs), "s"(OFF_FLAG + fl), "s"(mask) : "memory");
  }
}
DI void signal(Ctx& x, unsigned fl, int n) { bar(); count(x, fl, n); }
DI void await(Ctx& x, unsigned fl, unsigned target) {
  if (wave0(x)) {
#pragma nounroll
    for (unsigned spin = 0; spin < (1u << 18); ++spin) {
      unsigned v;
      asm volatile("buffer_load_dword %0, off, %1, %2 sc1\n\ts_waitcnt vmcnt(0)" : "=v"(v) : "s"(x.rs), "s"(OFF_FLAG + fl) : "memory");
      if ((unsigned)__builtin_amdgcn_readfirstlane(v) >= target) break;
      __builtin_amdgcn_s_sleep(1);
    }
  }
  bar();
}

DI void recheck(Ctx& x, unsigned v, unsigned fl, unsigned target) {
  pin(v);
#pragma nounroll
  for (unsigned spin = 0; (unsigned)__builtin_amdgcn_readfirstlane(v) < target && spin < (1u << 18); ++spin) {
    __builtin_amdgcn_s_sleep(2);
    asm volatile("buffer_load_dword %0, off, %1, %2 sc1\n\ts_waitcnt vmcnt(0)" : "=v"(v) : "s"(x.rs), "s"(OFF_FLAG + fl) : "memory");
  }
  SB();
}

DI void reduce_put(Ctx& x, const f32x16 (&acc)[4]) {
  sfor<1, 4>([&](auto k) {
    f32x4* p = (f32x4*)(lds + SCR + ((x.w ^ k) * 3 + k - 1) * 4096 + x.vl16);
#pragma unroll
    for (int q = 0; q < 4; ++q) p[q * 64] = f32x4{acc[k][4 * q], acc[k][4 * q + 1], acc[k][4 * q + 2], acc[k][4 * q + 3]};
  });
}
template <int K, int Q> DI void red_put1(Ctx& x, const f32x16& t) {
  asm volatile("ds_write_b128 %0, %1 offset:%c2" :: "v"(SCR + ((x.w ^ K) * 3 + K - 1) * 4096 + x.vl16),
               "v"(__builtin_shufflevector(t, t, 4 * Q, 4 * Q + 1, 4 * Q + 2, 4 * Q + 3)), "i"(Q * 1024) : "memory");
}
DI void reduce_get(Ctx& x, f32x16& own) {
  bar();
  const f32x4* p = (const f32x4*)(lds + SCR + x.w * 3 * 4096 + x.vl16);
#pragma unroll
  for (int k = 0; k < 3; ++k)
#pragma unroll
    for (int q = 0; q < 4; ++q) {
      f32x4 v = p[k * 256 + q * 64];
#pragma unroll
      for (int i = 0; i < 4; ++i) own[4 * q + i] += v[i];
    }
}

constexpr int A_WO = 184, A_WD = 0, A_GX = 224;
constexpr int dn_ks(int o) { return o < 20 ? 2 * ((o >> 1) % 5 + ((o >> 1) % 5 >= 2) + 7 * (o / 10)) + (o & 1) : 2 * (((o - 20) >> 1) * 4 - ((o - 20) >> 2) + 2) + (o & 1); }
static_assert(dn_ks(0) == 0 && dn_ks(4) == 6 && dn_ks(9) == 11 && dn_ks(10) == 14 && dn_ks(19) == 25 && dn_ks(20) == 4 && dn_ks(22) == 12 && dn_ks(24) == 18 && dn_ks(27) == 27, "");
struct Two { unsigned s0, s1; };
constexpr int SL_WQ = 56;
template <int J> DI void pf_wq(Ctx& x) {
  ldl<1>(x.vl16, x.rs, OFF_WQ + ((x.m & 7) * 56 + 14 * x.w + J) * KB, (SL_WQ + 14 * x.w + J) * KB);
}
DI Two base_wo(const Ctx& x) { const int ct = x.m % 7;
  return Two{OFF_WO + ((2 * ct + (x.w & 1)) * 32 + 8 * x.w) * KB, OFF_WO + ((2 * ct + ((x.w & 1) ^ 1)) * 32 + 8 * x.w) * KB}; }
DI void pf_wo(Ctx& x) {
  const Two b = base_wo(x);
  sfor<0, 8>([&](auto i) {
    lda<A_WO + 8 * i, (i & 3) * 1024>(x.vl16, x.rs, b.s0 + (i >> 2) * 4096);
    lda<A_WO + 8 * i + 4, (i & 3) * 1024>(x.vl16, x.rs, b.s1 + (i >> 2) * 4096);
  });
}
DI Two base_wd(const Ctx& x) { const int ct = x.m % 7;
  return Two{OFF_WD + ((2 * ct + (x.w & 1)) * 112 + 28 * x.w) * KB, OFF_WD + ((2 * ct + ((x.w & 1) ^ 1)) * 112 + 28 * x.w) * KB}; }
template <int I0, int I1> DI void pf_wd(Ctx& x, const Two& b) {
  sfor<I0, I1>([&](auto f) {
    constexpr int i = f >> 1;
    lda<A_WD + 4 * f, (i & 3) * 1024>(x.vl16, x.rs, ((f & 1) ? b.s1 : b.s0) + (i >> 2) * 4096);
  });
}
DI void pf_wgu1(Ctx& x) {
  for (int t = 0; t < 14; ++t) ldl<2>(x.vl16, x.rs, OFF_WGU + (x.m * 196 + 8 * t + 2 * x.w) * KB, (8 * t + 2 * x.w) * KB);
}
template <int J> DI void pf_wgu3(Ctx& x) {
  ldl<1>(x.vl16, x.rs, OFF_WGU + (x.m * 196 + 160 + 9 * x.w + J) * KB, (9 * x.w + J) * KB);
}

constexpr unsigned S_QP = SCR, S_KP = SCR + 16384, S_QF = SCR + 24576, S_ST = SCR + 32768, S_CS = SCR + 33792, S_KM = SCR + 36864, S_TR = SCR + 40960;
DI unsigned kv_finish(Ctx& x, const float (&raw)[16], const f32x4 (&cs)[4], float mean, float rstd, bool isv, unsigned so, int slot) {
  const int h = x.l >> 5, r = x.l & 31;
  u32x4 y[2];
#pragma unroll
  for (int d = 0; d < 8; ++d)
    y[d >> 2][d & 3] = cvtpk((raw[2 * d] - mean * cs[d >> 1][2 * (d & 1)]) * rstd, (raw[2 * d + 1] - mean * cs[d >> 1][2 * (d & 1) + 1]) * rstd);
  if (h) { y[1][2] = 0; y[1][3] = 0; }
  unsigned nb = 0;
  if (!isv) {
    float n2 = 0.f;
#pragma unroll
    for (int d = 0; d < 8; ++d) n2 = dot2(y[d >> 2][d & 3], y[d >> 2][d & 3], n2);
    n2 = sum32(n2);
    nb = umax64(n2 < 3.0e38f ? __float_as_uint(n2) : 0x7f800000u);
    nb = cvtpk(1.01f * __builtin_amdgcn_sqrtf(__uint_as_float(nb)), 0.f);
    if (h) y[1][2] = 0x3f80u;
  } else {
    if (h) y[1][2] = 0x3f80u;
    char* im = lds + S_TR + slot * 2048;
#pragma unroll
    for (int a = 0; a < 4; ++a) *(u32x2*)(im + r * 64 + 16 * a + 8 * h) = u32x2{y[a >> 1][2 * (a & 1)], y[a >> 1][2 * (a & 1) + 1]};
    asm volatile("s_waitcnt lgkmcnt(0)" ::: "memory");
    const char* tb = im + (4 * h + ((x.l >> 2) & 3)) * 64 + ((x.l >> 4) & 1) * 32 + (x.l & 3) * 8;
    y[0] = trfrag(tb, tb + 512); y[1] = trfrag(tb + 1024, tb + 1536);
  }
  st4<0>(y[0], x.vl16, x.rs, so); st4<1024>(y[1], x.vl16, x.rs, so);
  return nb;
}
DI void kv_publish(Ctx& x, int L, int p, unsigned km) {
  get_tag(x);
  st2<0>(u32x2{(x.tlo & 0xffff0000u) | km, x.thi}, x.l < 16 ? x.l * 1024 : ~0u, x.rs, OFF_SLOT + (L * 2048 + p) * 8);
}
DI void q_finish(Ctx& x, const f32x16& qa, const f32x4 (&cq)[4], float mean, float rstd) {
  const int h = x.l >> 5;
  u32x4 qf[2];
  const f32x4* p = (const f32x4*)(lds + S_QP + x.w * 4096 + x.vl16);
  const float sc = rstd * QSCALE;
#pragma unroll
  for (int q = 0; q < 4; ++q) {
    const f32x4 o = p[q * 64];
    float v[4];
#pragma unroll
    for (int i = 0; i < 4; ++i) v[i] = (qa[4 * q + i] + o[i] - mean * cq[q][i]) * sc;
    qf[q >> 1][2 * (q & 1)] = cvtpk(v[0], v[1]); qf[q >> 1][2 * (q & 1) + 1] = cvtpk(v[2], v[3]);
  }
  if (h) { qf[1][2] = 0; qf[1][3] = 0; }
  float qn2 = 0.f, qn3 = 0.f;
#pragma unroll
  for (int d = 0; d < 4; ++d) { qn2 = dot2(qf[0][d], qf[0][d], qn2); qn3 = dot2(qf[1][d], qf[1][d], qn3); }
  qn2 = sum32(qn2 + qn3);
  if (h) qf[1][2] = cvtpk(1.01f * __builtin_amdgcn_sqrtf(qn2), 0.f);
  ((u32x4*)(lds + S_QF + x.w * 2048 + x.vl16))[0] = qf[0];
  ((u32x4*)(lds + S_QF + x.w * 2048 + x.vl16))[64] = qf[1];
}
DI void put_tile(char* p, const f32x16& t) {
#pragma unroll
  for (int q = 0; q < 4; ++q) ((f32x4*)p)[q * 64] = f32x4{t[4 * q], t[4 * q + 1], t[4 * q + 2], t[4 * q + 3]};
}

constexpr unsigned IMG_Q = 0, IMG_T = 56 * KB;
DI rsrc_t mkrs(const char* p, unsigned n) { return __builtin_amdgcn_make_buffer_rsrc((char*)p, 0, n, 0x00020000); }

DI void ask_slots(Ctx& x, int L) {
  if (wave0(x)) asm volatile("buffer_load_dwordx4 a[248:251], %0, %1, %2 offen sc1" :: "v"(x.vl16), "s"(x.rs), "s"(OFF_SLOT + (L * 16 + x.c) * 1024) : ACLOB);
}
DI void kv_wait(Ctx& x, int L) {
  wv<0>();
  if (wave0(x)) {
    get_tag(x);
    unsigned s0, s1, s2, s3;
#pragma nounroll
    for (unsigned spin = 0; spin < (1u << 18); ++spin) {
      asm volatile("v_accvgpr_read_b32 %0, a248\n\tv_accvgpr_read_b32 %1, a249\n\tv_accvgpr_read_b32 %2, a250\n\tv_accvgpr_read_b32 %3, a251" : "=v"(s0), "=v"(s1), "=v"(s2), "=v"(s3));
      if (__all(s1 == x.thi && s3 == x.thi && (((s0 ^ x.tlo) | (s2 ^ x.tlo)) >> 16) == 0)) break;
      __builtin_amdgcn_s_sleep(2);
      ask_slots(x, L); wv<0>();
    }
    const unsigned km = umax64(max(s0 << 16, s2 << 16));
    if (x.l == 0) ((unsigned*)(lds + S_KM))[4] = km;
  }
  bar();
}
DI void ask_kv(Ctx& x, int L, f32x16 (&kf)[3]) {
  const unsigned kvs = OFF_KV + L * SZ_KV + 16 * x.w * 4096;
  asm volatile(".irp i,0,1,2,3\n\tbuffer_load_dwordx4 v[128+4*\\i:131+4*\\i], %3, %4, %5 offen offset:1024*\\i sc1\n\t.endr\n\t"
               ".irp i,0,1,2,3\n\tbuffer_load_dwordx4 v[144+4*\\i:147+4*\\i], %3, %4, %6 offen offset:1024*\\i sc1\n\t.endr\n\t"
               ".irp i,0,1,2,3\n\tbuffer_load_dwordx4 v[160+4*\\i:163+4*\\i], %3, %4, %7 offen offset:1024*\\i sc1\n\t.endr"
               : "={v[128:143]}"(kf[0]), "={v[144:159]}"(kf[1]), "={v[160:175]}"(kf[2]) : "{v204}"(x.vl16), "s"(x.rs), "s"(kvs), "s"(kvs + 4096), "s"(kvs + 8192));
}

DI void phase_pre(Ctx& x, const Args& a, f32x16 (&kf)[3]) {
  const int w = x.w, l = x.l, h = l >> 5, r = l & 31;
  const int th = x.m >> 3, hp = x.m & 7, tl = w & 1, kh = w >> 1, tt = 4 * x.c + 2 * th + tl, wi = 2 * x.c + th;
  const bool kvp = hp < 2;
  { unsigned long long pc; unsigned len;
    asm volatile("s_getpc_b64 %0\n.Lk_begin:\n\ts_mov_b32 %1, .Lk_end-.Lk_begin" : "=s"(pc), "=s"(len));
    const rsrc_t rc = mkrs((const char*)pc, len);
    asm volatile("buffer_load_dwordx4 a[112:115], %0, %1, %2 offen\n\tbuffer_load_dwordx4 a[112:115], %0, %1, %2 offen offset:1024"
                 :: "v"(x.vl16), "s"(rc), "s"((unsigned)(hp * 8 + 2 * w) * KB) : ACLOB); }
  if (x.m == 2) asm volatile("buffer_load_dword a116, %0, %1, 0 offen sc1" :: "v"(l < 16 ? OFF_FLAG + (x.c * 16 + l) * 256 : OFF_SLOT + ((l >> 3) & 1) * 16384 + x.c * 1024 + (l & 7) * 128), "s"(x.rs) : ACLOB);
  u32x4 xf[14];
  { const rsrc_t rx = mkrs(a.x, S * D * 2);
    sfor<0, 14>([&](auto i) { ld4i<32 * i>(xf[i], r * 896 + h * 16, rx, tt * 32 * 896 + kh * 448); }); }
  asm volatile("buffer_load_dwordx2 a[252:253], off, %0, %1 sc1" :: "s"(x.rs), "s"(OFF_TAG) : ACLOB);
  const int task = wi * 6 + hp - 2;
  const rsrc_t rq = mkrs(a.wq, D * D * 2);
  auto ask_q = [&](auto i) {
    ldl<1>((8 * w + (l >> 3)) * 896 + (l & 7) * 16, rq, hp * 112 + i * 32 * 896, IMG_Q + (4 * i + w) * KB);
  };
  auto ask_t = [&](const char* tp, unsigned tn, unsigned ts, unsigned tb) {
    const rsrc_t rt = mkrs(tp, tn);
    const unsigned vo = (8 * w + (l >> 3)) * ts + (l & 7) * 16;
    sfor<0, 7>([&](auto i) { ldl<1>(vo, rt, tb + i * 32 * ts, IMG_T + (4 * i + w) * KB); });
  };
  const int tco = ((l >> 4) & 1) * 32 + (l & 3) * 8;
  const int sro = 4 * h + ((l >> 2) & 3);
  if (kvp) ask_t(hp ? a.wv : a.wk, D * 56, 128, 0);
  else if (task < 112) ask_t(task < 56 ? a.wg : a.wu, D * F * 2, F * 2, (task & 1) * 224 * F * 2 + ((task % 56) >> 1) * 128);
  else if (task < 168) ask_t(a.wd, D * F * 2, 896, ((task - 112) / 7) * 224 * 896 + ((task - 112) % 7) * 128);
  else ask_t(a.wo, task < 182 ? D * D * 2 : 0, 896, ((task - 168) / 7) * 224 * 896 + ((task - 168) % 7) * 128);
  wv<8>();
  {
    float s = 0.f, q = 0.f, s1 = 0.f, q1 = 0.f;
    sfor<0, 14>([&](auto i) {
      if (!kvp) ask_q(i);
      pin(xf[i]);
      s = dot2(xf[i][0], 0x3f803f80u, s); q = dot2(xf[i][0], xf[i][0], q); s1 = dot2(xf[i][1], 0x3f803f80u, s1); q1 = dot2(xf[i][1], xf[i][1], q1);
      s = dot2(xf[i][2], 0x3f803f80u, s); q = dot2(xf[i][2], xf[i][2], q); s1 = dot2(xf[i][3], 0x3f803f80u, s1); q1 = dot2(xf[i][3], xf[i][3], q1);
      pin(s); pin(q); pin(s1); pin(q1);
    });
    s = sum32(s + s1); q = sum32(q + q1);
    if (h == 0) ((f32x2*)(lds + S_ST))[w * 32 + r] = f32x2{s, q};
  }
  if (kvp) wv<0>(); else wv<14>();
  bar();
  if (!kvp) {
    if (task < 112) {
      const int g = l >> 4, mat = task >= 56, gt = 4 * ((task % 56) >> 1) + w, m = gt / 7, j = gt % 7, lt = (m & 1) ? j : (j == 6 ? 0 : j + 1);
      const char* tb = lds + IMG_T + (16 * (g >> 1) + 4 * (g & 1) + ((l >> 2) & 3)) * 128 + 32 * w + (l & 3) * 8;
      const unsigned st = lt < 5 ? 10 * KB : 4 * KB, so = OFF_WGU + (m * 196 + (lt < 5 ? lt * 2 + mat : 140 + (lt - 5) * 2 + mat)) * KB + (task & 1) * 7 * st;
      u32x4 f[7];
      sfor<0, 7>([&](auto i) { f[i] = trfrag(tb + i * 4096, tb + i * 4096 + 1024); });
      float cs = 0.f;
      sfor<0, 7>([&](auto i) { st4<0>(f[i], x.vl16, x.rs, so + i * st); cs = fragsum(f[i], cs); });
      cs = sum32(sum16(cs));
      st1(cs, l < 16 ? l * 4 : ~0u, x.rs, OFF_CSGU + m * 2048 + (task & 1) * 1024 + (lt * 2 + mat) * 64);
    } else if (task < 168) {
      const int nt = 2 * ((task - 112) % 7) + (w & 1), rb = (task - 112) / 7;
      const char* tb = lds + IMG_T + sro * 128 + 64 * (w & 1) + tco;
      const unsigned so = OFF_WD + (nt * 112 + 28 * (rb >> 1)) * KB, sa = so + (rb & 1) * 10 * KB, sb = so + (20 + (rb & 1) * 4) * KB;
      u32x4 f[7];
      if (w < 2) {
        sfor<0, 7>([&](auto i) { const char* p = tb + (32 * (i >> 1) + 8 * (i & 1)) * 128; f[i] = trfrag(p, p + 2048); });
        sfor<0, 7>([&](auto i) { st4<0>(f[i], x.vl16, x.rs, i < 4 ? sa + i * KB : (i < 6 ? sb + (i - 4) * KB : sa + 4 * KB)); });
      } else {
        sfor<0, 7>([&](auto i) { const char* p = tb + (32 * ((i + 7) >> 1) + 8 * ((i + 7) & 1)) * 128; f[i] = trfrag(p, p + 2048); });
        sfor<0, 7>([&](auto i) { st4<0>(f[i], x.vl16, x.rs, i < 5 ? sa + (5 + i) * KB : sb + (i - 3) * KB); });
      }
      st1(0.f, ~0u, x.rs, 0);
    } else {
      const int nt = 2 * ((task - 168) % 7) + (w & 1);
      const char* tb = lds + IMG_T + (112 * (w >> 1) + sro) * 128 + 64 * (w & 1) + tco;
      const unsigned vo = task < 182 ? x.vl16 : ~0u - 4096;
      u32x4 f[8];
      sfor<0, 8>([&](auto i) { const char* p = tb + (28 * (i >> 1) + 16 * (i & 1)) * 128; f[i] = trfrag(p, p + 1024); if ((i & 1) && h) { f[i][2] = 0; f[i][3] = 0; } });
      sfor<0, 8>([&](auto i) { st4<(i & 3) * 1024>(f[i], vo, x.rs, OFF_WO + (nt * 32 + 16 * ((task - 168) / 7) + 8 * (w >> 1) + (i & ~3)) * KB); });
    }
  }
  if (!kvp) { wv<8>(); bar(); }
  float mean, rstd;
  { const f32x2 p0 = ((const f32x2*)(lds + S_ST))[tl * 32 + r], p1 = ((const f32x2*)(lds + S_ST))[(tl + 2) * 32 + r];
    row_stats(p0[0] + p1[0], p0[1] + p1[1], mean, rstd); }
  float* csw = (float*)(lds + S_CS);
  const int tro = 8 * h + ((l >> 2) & 3);
  u32x4 ones = {0x3f803f80u, 0x3f803f80u, 0x3f803f80u, 0x3f803f80u};
  pin(ones);
  if (kvp) {
    f32x16 kv, cs;
    const unsigned tb = IMG_T + (224 * kh + tro) * 56 + tco;
    u32x2 f[14][2];
    auto rd = [&](auto i) { trd<i * 896>(f[i][0], tb); trd<i * 896 + 224>(f[i][1], tb); };
    sfor<0, 7>(rd);
    sfor<0, 14>([&](auto i) {
      if constexpr (i + 7 < 14) { wl<12>(); rd(std::integral_constant<int, i + 7>{}); } else wl<2 * (13 - i)>();
      pin(f[i][0]); pin(f[i][1]);
      u32x4 g = {f[i][0][0], f[i][0][1], f[i][1][0], f[i][1][1]};
      pin(g); nop<0>();
      if constexpr (i == 0) { m32z(kv, g, xf[i]); m32z(cs, g, ones); } else { m32(kv, g, xf[i]); m32(cs, g, ones); }
    });
    nop<11>(); pin(kv); pin(cs);
    if (r == 0) sfor<0, 4>([&](auto q) { *(f32x4*)(csw + (w * 3 + 2) * 32 + 8 * q + 4 * h) = f32x4{cs[4 * q], cs[4 * q + 1], cs[4 * q + 2], cs[4 * q + 3]}; });
    if (kh) put_tile(lds + S_KP + tl * 4096 + x.vl16, kv);
    bar();
    if (!kh) {
      if (x.m == 0 && w == 0) st1(0.f, l < 16 ? l * 256 : ~0u, x.rs, OFF_FLAG + x.c * 4096);
      float raw[16]; f32x4 c4[4];
      sfor<0, 4>([&](auto q) {
        const f32x4 o = ((const f32x4*)(lds + S_KP + tl * 4096 + x.vl16))[q * 64];
        c4[q] = *(const f32x4*)(csw + (w * 3 + 2) * 32 + 8 * q + 4 * h) + *(const f32x4*)(csw + ((w + 2) * 3 + 2) * 32 + 8 * q + 4 * h);
#pragma unroll
        for (int i = 0; i < 4; ++i) raw[4 * q + i] = kv[4 * q + i] + o[i];
      });
      const unsigned km = kv_finish(x, raw, c4, mean, rstd, hp == 1, OFF_KV + (tt * 4 + 2 * hp) * KB, tl);
      wv<0>();
      kv_publish(x, 0, 2 * tt + hp, km);
      if (tt == 0 && r == 0) sfor<0, 4>([&](auto q) { st4<32 * q>(__builtin_bit_cast(u32x4, c4[q]), h * 16, x.rs, OFF_CSQ + (16 + hp) * 128); });
    } else {
      sfor<0, 28>([&](auto i) { ldl<1>((16 * tl + 8 * (i & 1) + (l >> 3)) * 896 + (l & 7) * 16, rq, hp * 112 + (i >> 1) * 32 * 896, IMG_Q + (4 * (i >> 1) + 2 * tl + (i & 1)) * KB); });
      wv<0>();
    }
    bar();
  }
  f32x16 qa;
  {
    f32x16 q1, q2, cs;
    const unsigned t1 = IMG_Q + (224 * kh + tro) * 128 + tco + 56 * tl, t2 = IMG_Q + (224 * kh + tro) * 128 + tco + 56 * (tl ^ 1);
    u32x2 f[14][4];
    auto rd = [&](auto i) { trd<i * 2048>(f[i][0], t1); trd<i * 2048 + 512>(f[i][1], t1); trd<i * 2048>(f[i][2], t2); trd<i * 2048 + 512>(f[i][3], t2); };
    wl<0>();
    sfor<0, 3>(rd);
    sfor<0, 14>([&](auto i) {
      if constexpr (i + 3 < 14) { wl<8>(); rd(std::integral_constant<int, i + 3>{}); } else wl<4 * (13 - i)>();
      sfor<0, 4>([&](auto e) { pin(f[i][e]); });
      u32x4 g1 = {f[i][0][0], f[i][0][1], f[i][1][0], f[i][1][1]}, g2 = {f[i][2][0], f[i][2][1], f[i][3][0], f[i][3][1]};
      pin(g1); pin(g2); nop<0>();
      if constexpr (i == 0) { m32z(q1, g1, xf[i]); m32z(q2, g2, xf[i]); m32z(cs, g1, ones); } else { m32(q1, g1, xf[i]); m32(q2, g2, xf[i]); m32(cs, g1, ones); }
      if constexpr (i == 10) { ask_slots(x, 0); SB(); }
    });
    nop<11>(); pin(q1); pin(q2); pin(cs);
    if (r == 0) sfor<0, 4>([&](auto q) { *(f32x4*)(csw + (w * 3) * 32 + 8 * q + 4 * h) = f32x4{cs[4 * q], cs[4 * q + 1], cs[4 * q + 2], cs[4 * q + 3]}; });
    if (kh == tl) { put_tile(lds + S_QP + (w ^ 2) * 4096 + x.vl16, q2); qa = q1; } else { put_tile(lds + S_QP + (w ^ 2) * 4096 + x.vl16, q1); qa = q2; }
  }
  if (th == 0 && x.c < 14) {
    const int f = 4 * x.c + w, hd = f >= 28, ks = f - 28 * hd;
    const char* tb = lds + IMG_Q + (16 * ks + sro) * 128 + tco + 56 * hd;
    st4<0>(trfrag(tb, tb + 1024), x.vl16, x.rs, OFF_WQ + (hp * 56 + f) * KB);
  }
  if (kvp && th == 1 && x.c < 7) {
    const char* tb = lds + IMG_T + (16 * (4 * x.c + w) + sro) * 56 + tco;
    st4<0>(trfrag(tb, tb + 448), x.vl16, x.rs, OFF_WQ + ((16 + hp) * 28 + 4 * x.c + w) * KB);
  }
  kv_wait(x, 0);
  count(x, FL_W(x.c), 1);
  ask_kv(x, 0, kf);
  const bool own = x.c == hp && th == 0;
  {
    f32x4 cq[4];
    sfor<0, 4>([&](auto q) {
      cq[q] = *(const f32x4*)(csw + (kh * 3) * 32 + 8 * q + 4 * h) + *(const f32x4*)(csw + ((kh + 2) * 3) * 32 + 8 * q + 4 * h);
      if (own && tl == 0 && r == 0) st4<32 * q>(__builtin_bit_cast(u32x4, cq[q]), h * 16, x.rs, OFF_CSQ + (2 * hp + kh) * 128);
    });
    q_finish(x, qa, cq, mean, rstd);
  }
}

DI void phase_front(Ctx& x, f32x16 (&kf)[3]) {
  const int w = x.w, l = x.l, h = l >> 5, r = l & 31;
  const int th = x.m >> 3, hp = x.m & 7, tl = w & 1, kh = w >> 1, tt = 4 * x.c + 2 * th + tl;
  const unsigned KV = OFF_KV + SZ_KV;
  {
    f32x4 cq[4], ck[4];
    sfor<0, 4>([&](auto q) { ld4f<32 * q>(cq[q], h * 16, x.rs, OFF_CSQ + (2 * hp + kh) * 128); ld4f<32 * q>(ck[q], h * 16, x.rs, OFF_CSQ + (16 + (hp & 1)) * 128); });
    wv<0>();
    await(x, FL_X(x.c, th), 7);
    u32x4 xf[14]; f32x2 sp[7];
    {
      const unsigned so = OFF_X + 2 * SZ_X + (tt * 28 + 14 * kh) * KB;
      sfor<0, 7>([&](auto i) { ld4<(i & 3) * 1024>(xf[i], x.vl16, x.rs, so + (i >> 2) * 4096); });
      sfor<0, 7>([&](auto i) { ld2<0>(sp[i], (h * 7 * 2048 + r) * 8, x.rs, OFF_ST + 2 * SZ_ST + (i * 2048 + tt * 32) * 8); });
      sfor<7, 14>([&](auto i) { ld4<(i & 3) * 1024>(xf[i], x.vl16, x.rs, so + (i >> 2) * 4096); });
    }
    float mean, rstd;
    auto stats = [&]() {
      sfor<0, 7>([&](auto i) { pin(sp[i]); });
      sfor<0, 4>([&](auto i) { pin(cq[i]); pin(ck[i]); });
      float s = 0.f, q = 0.f;
#pragma unroll
      for (int i = 0; i < 7; ++i) { s += sp[i][0]; q += sp[i][1]; }
      row_stats(sum32(s), sum32(q), mean, rstd);
      pin(mean); pin(rstd); SB();
    };
    f32x16 qa, qb;
    u32x4 wa[14], wb[14];
    const unsigned pa = x.vl16 + (SL_WQ + 28 * kh + 14 * kh) * KB, pb = x.vl16 + (SL_WQ + 28 * (kh ^ 1) + 14 * kh) * KB;
    if (hp < 2) {
      f32x16 kv;
      sfor<0, 14>([&](auto i) { dsr<i * 1024>(wa[i], x.vl16 + 14 * kh * KB); });
      sfor<0, 14>([&](auto i) {
        wv<(i < 7 ? 20 : 13) - i>(); wl<13 - i>();
        if constexpr (i == 0) m32z(kv, wa[i], xf[i]); else m32(kv, wa[i], xf[i]);
        if constexpr (i == 7) stats();
      });
      nop<11>(); pin(kv);
      if (kh) put_tile(lds + S_KP + tl * 4096 + x.vl16, kv);
      bar();
      unsigned km = 0;
      if (!kh) {
        float raw[16];
        sfor<0, 4>([&](auto q) {
          const f32x4 o = ((const f32x4*)(lds + S_KP + tl * 4096 + x.vl16))[q * 64];
#pragma unroll
          for (int i = 0; i < 4; ++i) raw[4 * q + i] = kv[4 * q + i] + o[i];
        });
        km = kv_finish(x, raw, ck, mean, rstd, hp == 1, KV + (tt * 4 + 2 * hp) * KB, tl);
      }
      wl<0>();
      sfor<0, 7>([&](auto i) { dsr<i * 1024>(wb[i], pb); dsr<i * 1024>(wa[i], pa); });
      sfor<0, 14>([&](auto i) {
        if constexpr (i < 7) wl<12>(); else wl<2 * (13 - i)>();
        if constexpr (i == 0) { m32z(qb, wb[i], xf[i]); m32z(qa, wa[i], xf[i]); } else { m32(qb, wb[i], xf[i]); m32(qa, wa[i], xf[i]); }
        if constexpr (i < 7) { dsr<(i + 7) * 1024>(wb[i + 7], pb); dsr<(i + 7) * 1024>(wa[i + 7], pa); }
        if constexpr (i == 8) { if (!kh) { wv<0>(); kv_publish(x, 1, 2 * tt + hp, km); } SB(); }
      });
    } else {
      sfor<0, 7>([&](auto i) { dsr<i * 1024>(wb[i], pb); dsr<i * 1024>(wa[i], pa); });
      sfor<0, 14>([&](auto i) {
        wv<(i < 7 ? 20 : 13) - i>();
        if constexpr (i < 7) wl<12>(); else wl<2 * (13 - i)>();
        if constexpr (i == 0) { m32z(qb, wb[i], xf[i]); m32z(qa, wa[i], xf[i]); } else { m32(qb, wb[i], xf[i]); m32(qa, wa[i], xf[i]); }
        if constexpr (i < 7) { dsr<(i + 7) * 1024>(wb[i + 7], pb); dsr<(i + 7) * 1024>(wa[i + 7], pa); }
        if constexpr (i == 7) stats();
      });
    }
    nop<11>();
    pin(qa); pin(qb);
    put_tile(lds + S_QP + (w ^ 2) * 4096 + x.vl16, qb);
    bar();
    q_finish(x, qa, cq, mean, rstd);
    ask_slots(x, 1);
    kv_wait(x, 1);
    ask_kv(x, 1, kf);
  }
}

DI void phase_att(Ctx& x, const int L, f32x16 (&kf)[3], f32x16 (&g2)[3]) {
  const int w = x.w, l = x.l, h = l >> 5, r = l & 31;
  const int th = x.m >> 3, hp = x.m & 7, tl = w & 1, kh = w >> 1, tt = 4 * x.c + 2 * th + tl;
  const unsigned kvs = OFF_KV + L * SZ_KV + 16 * w * 4096;
  if (L == 0) bar();
  unsigned kma = S_KM + 16; asm volatile("" : "+s"(kma));
  const float kmx = *(const float*)(lds + kma);
  u32x4 q[4][2];
#pragma unroll
  for (int k = 0; k < 4; ++k) {
    q[k][0] = ((const u32x4*)(lds + S_QF + (w ^ k) * 2048 + x.vl16))[0];
    q[k][1] = ((const u32x4*)(lds + S_QF + (w ^ k) * 2048 + x.vl16))[64];
  }
  bool safe = true;
  {
#pragma unroll
    for (int k = 0; k < 4; ++k) {
      const float mrow = bflo(q[k][1][2]) * kmx + 0.01f;
      safe = safe && (!h || mrow <= M_LIMIT);
      if (h) q[k][1][2] = cvtpk(-mrow, 0.f);
    }
  }
  const bool fast = __all(safe);
  f32x16 own;
  if (fast) {
    unsigned so_kv = kvs + 3 * 4096, so_w = OFF_WGU + (x.m * 196 + 2 * w) * KB, so_l = 2 * w * KB, spin = 1u << 16, fvo = (l & 15) * 4096;
    Two ab = base_wo(x);
    const Two wd = base_wd(x);
    asm volatile(
      ".macro GRP s, pa, pb\n\t"
      "v_exp_f32 v200, v[\\s]\n\tv_exp_f32 v201, v[\\s+1]\n\tv_cvt_pk_bf16_f32 v[\\pa], v202, v203\n\t"
      "v_exp_f32 v202, v[\\s+2]\n\tv_exp_f32 v203, v[\\s+3]\n\tv_cvt_pk_bf16_f32 v[\\pb], v200, v201\n\t"
      ".endm\n\t"
      ".macro MEM kind, i, ld\n\t"
      ".if \\kind == 1\n\t"
      "buffer_load_dwordx4 v[128+16*\\ld+4*\\i:128+16*\\ld+4*\\i+3], %[vo], %[rs], %[skv] offen offset:1024*\\i sc1\n\t"
      ".elseif \\kind == 5\n\t"
      "ds_write_b128 v205, v[4*\\i:4*\\i+3] offset:1024*\\i\n\t"
      ".elseif \\kind == 6\n\t"
      "ds_write_b128 v205, v[4:7] offset:1024\n\t"
      ".elseif \\kind == 7\n\t"
      "ds_write_b128 v205, v[12:15] offset:3072\n\t"
      ".elseif \\kind == 2\n\t"
      "buffer_load_dword v206, v205, %[rs], 0 offen offset:3072 sc1\n\t"
      ".elseif \\kind == 3\n\t"
      ".if att_n < 16 || att_n >= 56\n\t"
      ".if att_n < 102\n\t"
      ".if att_n < 16\n\t.set att_r, 184+4*att_n\n\t.else\n\t.set att_r, 4*(att_n-56)\n\t.endif\n\t"
      ".if att_n & 1\n\t"
      "buffer_load_dwordx4 a[att_r:att_r+3], %[vo], %[rs], %[ab1] offen offset:1024*((att_n>>1)&3) sc1\n\t"
      ".else\n\t"
      "buffer_load_dwordx4 a[att_r:att_r+3], %[vo], %[rs], %[ab0] offen offset:1024*((att_n>>1)&3) sc1\n\t"
      ".endif\n\t"
      ".if att_n == 15\n\t"
      "s_mov_b32 %[ab0], %[wd0]\n\ts_mov_b32 %[ab1], %[wd1]\n\t"
      ".elseif (att_n & 7) == 7\n\t"
      "s_add_u32 %[ab0], %[ab0], 4096\n\ts_add_u32 %[ab1], %[ab1], 4096\n\t"
      ".endif\n\t"
      ".endif\n\t"
      ".elseif att_n < 44\n\t"
      ".if att_n & 1\n\t"
      "buffer_load_dwordx4 %[vo], %[rs], %[sw] offen offset:1024 sc1 lds\n\t"
      "s_add_u32 %[sw], %[sw], 8192\n\ts_add_u32 m0, m0, 8192\n\t"
      ".else\n\t"
      "buffer_load_dwordx4 %[vo], %[rs], %[sw] offen sc1 lds\n\t"
      ".endif\n\t"
      ".else\n\t"
      "buffer_load_dwordx4 v[208+4*(att_n-44):208+4*(att_n-44)+3], %[vo], %[rs], %[sw] offen offset:1024*(att_n&1) sc1\n\t"
      ".if att_n & 1\n\ts_add_u32 %[sw], %[sw], 8192\n\t.endif\n\t"
      ".endif\n\t"
      ".set att_n, att_n+1\n\t"
      ".endif\n\t"
      ".endm\n\t"
      ".macro STEP sc, sn, k, nk, pk, tk, nt, pt, ld, m0k, m0i, m1k, m2k, m2i, m3k\n\t"
      "v_mfma_f32_32x32x16_bf16 v[\\sn:\\sn+15], v[128+16*\\nt:128+16*\\nt+3], v[64+8*\\nk:64+8*\\nk+3], 0\n\t"
      "MEM \\m0k, \\m0i, \\ld\n\t"
      "GRP \\sc, 199, 192\n\t"
      "v_mfma_f32_32x32x16_bf16 v[\\sn:\\sn+15], v[132+16*\\nt:132+16*\\nt+3], v[68+8*\\nk:68+8*\\nk+3], v[\\sn:\\sn+15]\n\t"
      "MEM \\m1k, 0, 0\n\t"
      "GRP \\sc+4, 193, 194\n\t"
      "v_mfma_f32_32x32x16_bf16 v[16*\\pk:16*\\pk+15], v[140+16*\\pt:140+16*\\pt+3], v[196:199], v[16*\\pk:16*\\pk+15]\n\t"
      "MEM \\m2k, \\m2i, \\ld\n\t"
      "GRP \\sc+8, 195, 196\n\t"
      "v_mfma_f32_32x32x16_bf16 v[16*\\k:16*\\k+15], v[136+16*\\tk:136+16*\\tk+3], v[192:195], v[16*\\k:16*\\k+15]\n\t"
      "MEM \\m3k, 0, 0\n\t"
      "GRP \\sc+12, 197, 198\n\t"
      ".endm\n\t"
      ".macro TILE tk, nx, pv, on, wait, p, fl=0, q=1\n\t"
      "STEP 96, 112, 0, 1, 3, \\tk, \\tk, \\pv, \\pv, \\p, 0, \\q*\\p, \\p, 0, \\fl\n\t"
      "STEP 112, 96, 1, 2, 0, \\tk, \\tk, \\tk, \\pv, \\on, 0, \\p, \\on, 1, 0\n\t"
      "STEP 96, 112, 2, 3, 1, \\tk, \\tk, \\tk, \\pv, \\on, 2, \\p, \\on, 3, 0\n\t"
      ".if \\on\n\ts_add_u32 %[skv], %[skv], 4096\n\t.endif\n\t"
      "s_waitcnt vmcnt(\\wait)\n\t"
      "STEP 112, 96, 3, 0, 2, \\tk, \\nx, \\tk, \\pv, \\p, 0, \\p, \\p, 0, 0\n\t"
      ".endm\n\t"
      ".set att_n, 0\n\t"
      "s_mov_b32 m0, %[sl]\n\t"
      "v_mov_b32 v196, 0\n\tv_mov_b32 v197, 0\n\tv_mov_b32 v198, 0\n\tv_mov_b32 v199, 0\n\tv_mov_b32 v202, 0\n\tv_mov_b32 v203, 0\n\t"
      "v_mov_b32 v188, 0\n\tv_mov_b32 v189, 0\n\tv_mov_b32 v190, 0\n\tv_mov_b32 v191, 0\n\t"
      "v_mfma_f32_32x32x16_bf16 v[0:15], v[196:199], v[196:199], 0\n\t"
      "v_mfma_f32_32x32x16_bf16 v[16:31], v[196:199], v[196:199], 0\n\t"
      "v_mfma_f32_32x32x16_bf16 v[32:47], v[196:199], v[196:199], 0\n\t"
      "v_mfma_f32_32x32x16_bf16 v[48:63], v[196:199], v[196:199], 0\n\t"
      "s_waitcnt vmcnt(8)\n\t"
      "v_mfma_f32_32x32x16_bf16 v[96:111], v[128:131], v[64:67], 0\n\t"
      "v_mfma_f32_32x32x16_bf16 v[96:111], v[132:135], v[68:71], v[96:111]\n\t"
      "s_nop 11\n\t"
      "s_cmp_lg_u32 %[lay], 0\n\t"
      "s_cbranch_scc1 4f\n\t"
      "TILE 0, 1, 3, 1, 8, 0\n\t"
      "TILE 1, 2, 0, 1, 8, 0\n\tTILE 2, 3, 1, 1, 9, 0, 2\n\t"
      "s_waitcnt vmcnt(4)\n\t"
      "1:\n\t"
      "v_cmp_gt_u32 vcc, 16, v206\n\t"
      "s_and_b64 vcc, exec, vcc\n\t"
      "s_cbranch_vccz 2f\n\t"
      "s_sleep 24\n\t"
      "buffer_load_dword v206, v205, %[rs], 0 offen offset:3072 sc1\n\t"
      "s_sub_u32 %[spin], %[spin], 1\n\t"
      "s_waitcnt vmcnt(0)\n\t"
      "s_cbranch_scc0 1b\n\t"
      "2:\n\t"
      "s_barrier\n\t"
      "TILE 3, 0, 2, 1, 14, 3\n\t"
      "TILE 0, 1, 3, 1, 21, 3\n\tTILE 1, 2, 0, 1, 24, 3\n\tTILE 2, 3, 1, 1, 24, 3\n\tTILE 3, 0, 2, 1, 24, 3\n\t"
      "TILE 0, 1, 3, 1, 24, 3\n\tTILE 1, 2, 0, 1, 24, 3\n\tTILE 2, 3, 1, 1, 24, 3\n\tTILE 3, 0, 2, 1, 24, 3\n\t"
      "TILE 0, 1, 3, 1, 24, 3\n\tTILE 1, 2, 0, 0, 20, 3\n\tTILE 2, 3, 1, 0, 16, 3\n\t"
      "STEP 96, 112, 0, 1, 3, 3, 3, 2, 0, 3, 0, 3, 3, 0, 3\n\t"
      "STEP 112, 96, 1, 2, 0, 3, 3, 3, 0, 3, 0, 3, 0, 0, 0\n\t"
      "s_branch 5f\n\t"
      "4:\n\t"
      ".set att_n, 0\n\t"
      "TILE 0, 1, 3, 1, 12, 3, 0, 0\n\tTILE 1, 2, 0, 1, 19, 3, 0, 0\n\tTILE 2, 3, 1, 1, 22, 3, 0, 0\n\t"
      "s_barrier\n\t"
      "TILE 3, 0, 2, 1, 22, 3, 0, 0\n\t"
      "TILE 0, 1, 3, 1, 22, 3, 0, 0\n\tTILE 1, 2, 0, 1, 22, 3, 0, 0\n\tTILE 2, 3, 1, 1, 22, 3, 0, 0\n\tTILE 3, 0, 2, 1, 22, 3, 0, 0\n\t"
      "TILE 0, 1, 3, 1, 22, 3, 0, 0\n\tTILE 1, 2, 0, 1, 22, 3, 0, 0\n\tTILE 2, 3, 1, 1, 22, 3, 0, 0\n\tTILE 3, 0, 2, 1, 22, 3, 0, 0\n\t"
      "TILE 0, 1, 3, 1, 22, 3, 0, 0\n\tTILE 1, 2, 0, 0, 18, 3, 0, 0\n\tTILE 2, 3, 1, 0, 14, 3, 0, 0\n\t"
      "STEP 96, 112, 0, 1, 3, 3, 3, 2, 0, 0, 0, 0, 0, 0, 0\n\t"
      "STEP 112, 96, 1, 2, 0, 3, 3, 3, 0, 0, 0, 0, 0, 0, 0\n\t"
      "5:\n\t"
      "v_add_u32 v205, %[d1], v204\n\tv_add_u32 v206, %[d2], v204\n\t"
      "STEP 96, 112, 2, 3, 1, 3, 3, 3, 0, 5, 0, 6, 5, 2, 7\n\t"
      "ds_write_b128 v206, v[16:19]\n\t"
      "GRP 112, 199, 192\n\t"
      "ds_write_b128 v206, v[20:23] offset:1024\n\t"
      "GRP 116, 193, 194\n\t"
      "v_mfma_f32_32x32x16_bf16 v[32:47], v[188:191], v[196:199], v[32:47]\n\t"
      "ds_write_b128 v206, v[24:27] offset:2048\n\t"
      "GRP 120, 195, 196\n\t"
      "v_mfma_f32_32x32x16_bf16 v[48:63], v[184:187], v[192:195], v[48:63]\n\t"
      "ds_write_b128 v206, v[28:31] offset:3072\n\t"
      "GRP 124, 197, 198\n\t"
      "v_add_u32 v200, %[d3], v204\n\t"
      "v_cvt_pk_bf16_f32 v199, v202, v203\n\t"
      "s_nop 1\n\t"
      "v_mfma_f32_32x32x16_bf16 v[48:63], v[188:191], v[196:199], v[48:63]\n\t"
      "ds_write_b128 v200, v[32:35]\n\tds_write_b128 v200, v[36:39] offset:1024\n\tds_write_b128 v200, v[40:43] offset:2048\n\tds_write_b128 v200, v[44:47] offset:3072\n\t"
      "s_nop 7\n\t"
      ".purgem GRP\n\t.purgem MEM\n\t.purgem STEP\n\t.purgem TILE"
      : "={v[48:63]}"(own), "+{v[128:143]}"(kf[0]), "+{v[144:159]}"(kf[1]), "+{v[160:175]}"(kf[2]),
        "={v[208:223]}"(g2[0]), "={v[224:239]}"(g2[1]), "={v[240:255]}"(g2[2]),
        [skv] "+s"(so_kv), [sw] "+s"(so_w), [ab0] "+s"(ab.s0), [ab1] "+s"(ab.s1), [spin] "+s"(spin), "+{v205}"(fvo)
      : "{v[64:67]}"(q[1][0]), "{v[68:71]}"(q[1][1]), "{v[72:75]}"(q[2][0]), "{v[76:79]}"(q[2][1]),
        "{v[80:83]}"(q[3][0]), "{v[84:87]}"(q[3][1]), "{v[88:91]}"(q[0][0]), "{v[92:95]}"(q[0][1]),
        [vo] "{v204}"(x.vl16), [d1] "s"(SCR + (w ^ 1) * 3 * 4096), [d2] "s"(SCR + ((w ^ 2) * 3 + 1) * 4096), [d3] "s"(SCR + ((w ^ 3) * 3 + 2) * 4096), [rs] "s"(x.rs), [sl] "s"(so_l), [lay] "s"(L), [wd0] "s"(wd.s0), [wd1] "s"(wd.s1)
      : "memory", "scc", "vcc",
        "v0","v1","v2","v3","v4","v5","v6","v7","v8","v9","v10","v11","v12","v13","v14","v15","v16","v17","v18","v19","v20","v21","v22","v23",
        "v24","v25","v26","v27","v28","v29","v30","v31","v32","v33","v34","v35","v36","v37","v38","v39","v40","v41","v42","v43","v44","v45","v46","v47",
        "v96","v97","v98","v99","v100","v101","v102","v103","v104","v105","v106","v107","v108","v109","v110","v111",
        "v112","v113","v114","v115","v116","v117","v118","v119","v120","v121","v122","v123","v124","v125","v126","v127",
        "v176","v177","v178","v179","v180","v181","v182","v183","v184","v185","v186","v187","v188","v189","v190","v191",
        "v192","v193","v194","v195","v196","v197","v198","v199","v200","v201","v202","v203","v206", ACLOB);
  } else {
    f32x16 o[4];
    wv<0>();
    pin(kf[0]); pin(kf[1]); pin(kf[2]);
    float mx[4];
#pragma unroll
    for (int k = 0; k < 4; ++k) {
      mx[k] = -3.0e38f; if (h) q[k][1][2] = 0; pin(q[k][1]);
#pragma unroll
      for (int i = 0; i < 16; ++i) o[k][i] = 0.f;
    }
#pragma nounroll
    for (unsigned spin = 0; spin < (1u << 16); ++spin) {
      float v;
      ld1(v, (l & 15) * 4096, x.rs, OFF_FLAG + FL_W(0)); wv<0>(); pin(v);
      if (__all(__float_as_uint(v) >= 16u)) break;
      __builtin_amdgcn_s_sleep(24);
    }
    pf_wo(x);
    pf_wgu1(x);
    sfor<0, 12>([&](auto j) {
      u32x4 t;
      ld4<1024 * (j & 1)>(t, x.vl16, x.rs, OFF_WGU + (x.m * 196 + 112 + 8 * (j >> 1) + 2 * w) * KB); wv<0>(); pin(t);
#pragma unroll
      for (int i = 0; i < 4; ++i) g2[j >> 2][4 * (j & 3) + i] = __uint_as_float(t[i]);
    });
    pf_wd<0, 46>(x, base_wd(x));
    for (int tk = 0; tk < 16; ++tk) {
      u32x4 kf[4];
      sfor<0, 4>([&](auto i) { ld4<i * 1024>(kf[i], x.vl16, x.rs, kvs + tk * 4096); });
      wv<0>();
      sfor<0, 4>([&](auto k) {
        f32x16 s;
        nop<1>();
        m32z(s, kf[0], q[k][0]); m32(s, kf[1], q[k][1]);
        nop<11>(); pin(s);
        float tm = s[0];
#pragma unroll
        for (int i = 1; i < 16; ++i) tm = fmaxf(tm, s[i]);
        tm = max32(tm);
        const float mn = fmaxf(mx[k], tm), al = __builtin_amdgcn_exp2f(mx[k] - mn);
        mx[k] = mn;
#pragma unroll
        for (int i = 0; i < 16; ++i) o[k][i] *= al;
        u32x4 p0, p1;
#pragma unroll
        for (int d = 0; d < 4; ++d) {
          p0[d] = cvtpk(__builtin_amdgcn_exp2f(s[2 * d] - mn), __builtin_amdgcn_exp2f(s[2 * d + 1] - mn));
          p1[d] = cvtpk(__builtin_amdgcn_exp2f(s[8 + 2 * d] - mn), __builtin_amdgcn_exp2f(s[9 + 2 * d] - mn));
        }
        pin(p0); pin(p1); pin(o[k]);
        nop<1>();
        m32(o[k], kf[2], p0); m32(o[k], kf[3], p1);
        nop<11>(); pin(o[k]);
      });
    }
    bar();
    float* mm = (float*)(lds + SCR);
#pragma unroll
    for (int k = 0; k < 4; ++k) if (h == 0) mm[((w ^ k) * 4 + w) * 32 + r] = mx[k];
    bar();
#pragma unroll
    for (int k = 0; k < 4; ++k) {
      const float* p = mm + (w ^ k) * 128 + r;
      const float g = fmaxf(fmaxf(p[0], p[32]), fmaxf(p[64], p[96])), f = __builtin_amdgcn_exp2f(mx[k] - g);
#pragma unroll
      for (int i = 0; i < 16; ++i) o[k][i] *= f;
    }
    bar();
    reduce_put(x, o);
    own = o[0];
  }
  reduce_get(x, own);
  pin(own);
  {
    auto lr = __builtin_amdgcn_permlane32_swap(__float_as_uint(own[12]), __float_as_uint(own[12]), false, false);
    const float inv = __builtin_amdgcn_rcpf(__uint_as_float(lr[1]));
    u32x4 a, b;
#pragma unroll
    for (int d = 0; d < 4; ++d) { a[d] = cvtpk(own[2 * d] * inv, own[2 * d + 1] * inv); b[d] = cvtpk(own[8 + 2 * d] * inv, own[9 + 2 * d] * inv); }
    if (h) { b[2] = 0; b[3] = 0; }
    const unsigned so = OFF_O + L * SZ_O + (tt * 32 + 2 * (2 * hp + kh)) * KB;
    st4<0>(a, x.vl16, x.rs, so); st4<1024>(b, x.vl16, x.rs, so);
  }
  wv<0>();
  signal(x, FL_O(L, x.c, th), 1);
  if (L == 0 && th == 0) {
    if (x.c == hp) count(x, FL_W2, 1);
  }
}

template <bool FINAL> DI void res_epilogue(Ctx& x, const f32x16& acc, const u32x4 (&xr)[2], unsigned Xo, unsigned STo, int tt, int nt, char* out = nullptr) {
  const int h = x.l >> 5, r = x.l & 31;
  u32x4 y[2];
#pragma unroll
  for (int d = 0; d < 8; ++d) y[d >> 2][d & 3] = cvtpk(acc[2 * d] + bflo(xr[d >> 2][d & 3]), acc[2 * d + 1] + bfhi(xr[d >> 2][d & 3]));
  if constexpr (FINAL) {
#pragma unroll
    for (int s = 0; s < 2; ++s) {
      auto a = __builtin_amdgcn_permlane32_swap(y[s][0], y[s][2], false, false);
      auto b = __builtin_amdgcn_permlane32_swap(y[s][1], y[s][3], false, false);
      y[s] = u32x4{a[0], b[0], a[1], b[1]};
    }
    const unsigned vo = r * (D * 2) + h * 16, so = tt * 32 * D * 2 + nt * 64;
    const rsrc_t ro = mkrs(out, S * D * 2);
    st4p<0>(y[0], vo, ro, so); st4p<32>(y[1], vo, ro, so);
  } else {
    const unsigned so = Xo + (tt * 28 + 2 * nt) * KB;
    st4<0>(y[0], x.vl16, x.rs, so); st4<1024>(y[1], x.vl16, x.rs, so);
    float s = 0.f, q = 0.f, s1 = 0.f, q1 = 0.f;
#pragma unroll
    for (int d = 0; d < 4; ++d) {
      s = dot2(y[0][d], 0x3f803f80u, s); q = dot2(y[0][d], y[0][d], q); s1 = dot2(y[1][d], 0x3f803f80u, s1); q1 = dot2(y[1][d], y[1][d], q1);
    }
    s = sum32(s + s1); q = sum32(q + q1);
    if (h == 0) st2<0>(u32x2{__float_as_uint(s), __float_as_uint(q)}, r * 8, x.rs, STo + (nt * 2048 + tt * 32) * 8);
  }
}

DI void phase_oproj(Ctx& x, const int L, const Args& a, const f32x16 (&g2)[3]) {
  const int w = x.w;
  if (x.m < 14) {
    const int th = x.m / 7, ct = x.m % 7, tt0 = 4 * x.c + 2 * th, tt = tt0 + (w >> 1), nt = 2 * ct + (w & 1);
    u32x2 x0[4];
    if (L == 0) {
      const rsrc_t rx = mkrs(a.x, S * D * 2);
      sfor<0, 4>([&](auto i) { ld2i<16 * i>(x0[i], (x.l & 31) * 896 + (x.l >> 5) * 8, rx, tt * 32 * 896 + nt * 64); });
    } else {
      sfor<0, 4>([&](auto i) { ld2u<(i >> 1) * 1024 + (i & 1) * 8>(x0[i], x.vl16, x.rs, OFF_X + 2 * SZ_X + (tt * 28 + 2 * nt) * KB); });
    }
    await(x, FL_O(L, x.c, th), 8);
    u32x4 b[8][2];
    const unsigned s0 = OFF_O + L * SZ_O + (tt * 32 + 8 * w) * KB, s1 = OFF_O + L * SZ_O + ((tt0 + ((w >> 1) ^ 1)) * 32 + 8 * w) * KB;
    auto ld = [&](auto i) { ld4<(i & 3) * 1024>(b[i][0], x.vl16, x.rs, s0 + (i >> 2) * 4096); ld4<(i & 3) * 1024>(b[i][1], x.vl16, x.rs, s1 + (i >> 2) * 4096); };
    constexpr int WN = OPWN;
    sfor<0, WN>(ld);
    f32x16 acc[4];
    sfor<0, 8>([&](auto i) {
      constexpr int last = (i + WN - 1 < 7 ? i + WN - 1 : 7);
      wv<2 * (last - i) + 1>();
      m32a<A_WO + 8 * i, i == 0>(acc[0], b[i][0]); m32a<A_WO + 8 * i + 4, i == 0>(acc[1], b[i][0]);
      wv<2 * (last - i)>();
      m32a<A_WO + 8 * i, i == 0>(acc[2], b[i][1]); m32a<A_WO + 8 * i + 4, i == 0>(acc[3], b[i][1]);
      if constexpr (i + WN < 8) ld(std::integral_constant<int, i + WN>{});
    });
    nop<11>();
    sfor<0, 4>([&](auto k) { pin(acc[k]); pin(x0[k]); });
    reduce_put(x, acc); reduce_get(x, acc[0]);
    pin(acc[0]);
    const u32x4 x2[2] = {u32x4{x0[0][0], x0[0][1], x0[1][0], x0[1][1]}, u32x4{x0[2][0], x0[2][1], x0[3][0], x0[3][1]}};
    res_epilogue<false>(x, acc[0], x2, OFF_X + (2 * L + 1) * SZ_X, OFF_ST + (2 * L + 1) * SZ_ST, tt, nt);
    wv<0>();
    signal(x, FL_X1(L, x.c), 1);
  }
#pragma unroll
  for (int j = 0; j < 12; ++j)
    *(f32x4*)(lds + (112 + 8 * (j >> 1) + 2 * w + (j & 1)) * KB + x.vl16) = f32x4{g2[j >> 2][4 * (j & 3)], g2[j >> 2][4 * (j & 3) + 1], g2[j >> 2][4 * (j & 3) + 2], g2[j >> 2][4 * (j & 3) + 3]};
}

constexpr int DWN = 7;
constexpr bool dn_half(int o) { return o == 4 || o == 5 || o == 14 || o == 15; }
struct DnSrc { unsigned s0, s1, l0, l1; };
DI DnSrc dn_src(const Ctx& x, int L) {
  const int tt0 = 4 * x.c + 2 * ((x.m / 7) & 1), tt = tt0 + (x.w >> 1), tto = tt0 + ((x.w >> 1) ^ 1);
  return DnSrc{OFF_H + L * SZ_H + (tt * 112 + 28 * x.w) * KB, OFF_H + L * SZ_H + (tto * 112 + 28 * x.w) * KB,
               OFF_HL + ((L * 64 + tt) * 16 + 4 * x.w) * KB, OFF_HL + ((L * 64 + tto) * 16 + 4 * x.w) * KB};
}
template <int O> DI void dn_ld(Ctx& x, const DnSrc& d, u32x4 (&b)[DWN][2]) {
  constexpr int i = dn_ks(O);
  if constexpr (dn_half(O)) {
    constexpr int imm = (O >= 14 ? 2048 : 0) + (O & 1) * 512;
    u32x2 lo, hi, lo1, hi1;
    ld2u<imm>(lo, x.l * 8, x.rs, d.l0); ld2u<imm + 1024>(hi, x.l * 8, x.rs, d.l0);
    ld2u<imm>(lo1, x.l * 8, x.rs, d.l1); ld2u<imm + 1024>(hi1, x.l * 8, x.rs, d.l1);
    b[O % DWN][0] = u32x4{lo[0], lo[1], hi[0], hi[1]}; b[O % DWN][1] = u32x4{lo1[0], lo1[1], hi1[0], hi1[1]};
  } else {
    ld4<(i & 3) * 1024>(b[O % DWN][0], x.vl16, x.rs, d.s0 + (i >> 2) * 4096); ld4<(i & 3) * 1024>(b[O % DWN][1], x.vl16, x.rs, d.s1 + (i >> 2) * 4096);
  }
}

DI void gu_tile(const f32x4 (&acc)[2][2], const u32x4 (&cs)[2], const float (&mean)[2], const float (&r2)[2], u32x2 (&hp)[2]) {
  const f32x4 cg = __builtin_bit_cast(f32x4, cs[0]), cu = __builtin_bit_cast(f32x4, cs[1]);
#pragma unroll
  for (int u = 0; u < 2; ++u) {
    const f32x2 nm = {-mean[u], -mean[u]}, rr = {r2[u], r2[u]};
#pragma unroll
    for (int e = 0; e < 2; ++e) {
      const f32x2 g = __builtin_elementwise_fma(nm, f32x2{cg[2 * e], cg[2 * e + 1]}, f32x2{acc[0][u][2 * e], acc[0][u][2 * e + 1]});
      const f32x2 v = __builtin_elementwise_fma(nm, f32x2{cu[2 * e], cu[2 * e + 1]}, f32x2{acc[1][u][2 * e], acc[1][u][2 * e + 1]});
      const f32x2 y = g * rr * v;
      hp[u][e] = cvtpk(y[0], y[1]);
    }
  }
}

DI void phase_gu(Ctx& x, const int L, u32x4 (&b)[DWN][2]) {
  const int w = x.w, l = x.l, g = l >> 4, c16 = l & 15, m = x.m, tt = 4 * x.c + w;
  if (L == 0 && wave0(x)) { unsigned v; ldflag(v, x.rs, OFF_FLAG + FL_W2); wv<0>(); recheck(x, v, FL_W2, 8); }
  await(x, FL_X1(L, x.c), 14);
  constexpr int NXV = 11;
  u32x4 xf[NXV][2]; f32x2 sp[2][4];
  const unsigned sx = OFF_X + (2 * L + 1) * SZ_X + tt * 28 * KB, st = OFF_ST + (2 * L + 1) * SZ_ST + tt * 32 * 8;
  auto ldx = [&](auto k, auto u) {
    if constexpr (k < NXV) ld4<(k & 1) * 2048 + 256 * u>(xf[k][u], x.vg16, x.rs, sx + (k >> 1) * 4096);
    else if constexpr (k < 14) lda<A_GX + 8 * (k - NXV) + 4 * u, (k & 1) * 2048 + 256 * u>(x.vg16, x.rs, sx + (k >> 1) * 4096);
    else { constexpr int i = k - 14; ld2<128 * u>(sp[u][i], (((i < 3 || g < 2) ? g + 4 * i : g) * 2048 + c16) * 8, x.rs, st); }
  };
  using I0 = std::integral_constant<int, 0>; using I1 = std::integral_constant<int, 1>;
  constexpr int XA = XAHEAD;
  sfor<0, XA>([&](auto k) { ldx(k, I0{}); ldx(k, I1{}); });
  const unsigned a0 = x.vl16, a1 = x.vl16 + 65536, a2 = x.vl16 + 131072;
  constexpr int DP = 6;
  u32x4 wf[8];
  auto rd = [&](auto ii) {
    constexpr int i = ii, slot = i >= 160 ? i - 160 : i;
    if constexpr (slot < 64) dsr<slot * 1024>(wf[i & 7], a0); else if constexpr (slot < 128) dsr<(slot - 64) * 1024>(wf[i & 7], a1); else dsr<(slot - 128) * 1024>(wf[i & 7], a2);
  };
  auto mm = [&](f32x4& c, const u32x4& a, auto k, auto u) {
    if constexpr (k < NXV) m16<k == 0>(c, a, xf[k][u]); else m16a<false, A_GX + 8 * (k - NXV) + 4 * u>(c, a);
  };
  const Two wd = base_wd(x);
  f32x4 aa[5][2][2], ch0, ch1;
  float mean[2], r2[2];
  sfor<0, DP>(rd);
  sfor<0, 140>([&](auto i) {
    constexpr int KS = i / 10, s = i % 10, t = s >> 1, mat = i & 1;
    using K = std::integral_constant<int, KS>;
    constexpr auto side = [](int k) { return (k >= 0 && k <= 3) || (k >= 5 && k <= 12); };
    constexpr auto all = [](int k) { return (k + XA < 14 ? 2 : 0) + ((k >= 0 && k <= 3) || (k >= 5 && k <= 9) ? 2 : 0) + (k == 5 ? 1 : 0) + (k >= 10 ? 3 : 0); };
    if constexpr (s == 0) {
      constexpr int cnt = [&] { int n = side(KS - XA) ? 1 : 0; for (int k = KS - XA + 1; k < KS; ++k) n += all(k); return n; }();
      wv<cnt>();
      if constexpr (KS == 5) asm volatile("s_barrier" ::: "memory");
    }
    if constexpr ((i & 1) == 0) { rd(std::integral_constant<int, i + DP>{}); rd(std::integral_constant<int, i + DP + 1>{}); wl<DP>(); }
    mm(aa[t][mat][0], wf[i & 7], K{}, I0{});
    if constexpr (s == 1 && KS + XA < 14) ldx(std::integral_constant<int, KS + XA>{}, I0{});
    if constexpr (s == 6 && KS + XA < 14) ldx(std::integral_constant<int, KS + XA>{}, I1{});
    if constexpr (KS <= 3 && s == 3) ldx(std::integral_constant<int, 14 + KS>{}, I0{});
    if constexpr (KS <= 3 && s == 8) ldx(std::integral_constant<int, 14 + KS>{}, I1{});
    if constexpr (KS == 5 && s == 3) asm volatile("buffer_load_dwordx4 %0, %2, %3, %4 offen sc1\n\tbuffer_load_dwordx4 %1, %2, %3, %4 offen offset:1024 sc1" : "=&v"(ch0), "=&v"(ch1) : "v"(x.vl16), "s"(x.rs), "s"(OFF_CSGU + m * 2048));
    if constexpr (KS == 11 && s == 2) { pin(ch0); pin(ch1); *(f32x4*)(lds + 40 * KB + x.vl16) = ch0 + ch1; }
    if constexpr (KS >= 5 && KS <= 9 && (s == 3 || s == 8) && !(KS == 5 && s == 3)) pf_wgu3<2 * (KS - 5) + (s == 8) - 1>(x);
    if constexpr (KS >= 10 && (s == 3 || s == 5 || s == 8) && 3 * (KS - 10) + (s == 3 ? 0 : (s == 5 ? 1 : 2)) < 10) {
      constexpr int f = 46 + 3 * (KS - 10) + (s == 3 ? 0 : (s == 5 ? 1 : 2));
      pf_wd<f, f + 1>(x, wd);
    }
    if constexpr ((KS == 10 || KS == 12) && s == 2) {
      constexpr int u = KS == 12;
      sfor<0, 4>([&](auto e) { pin(sp[u][e]); });
      float sm = sp[u][0][0] + sp[u][1][0] + sp[u][2][0], q = sp[u][0][1] + sp[u][1][1] + sp[u][2][1];
      if (g < 2) { sm += sp[u][3][0]; q += sp[u][3][1]; }
      sm = sum32(sum16(sm)); q = sum32(sum16(q));
      float rs; row_stats(sm, q, mean[u], rs); r2[u] = rs * rs;
      pin(mean[u]); pin(r2[u]);
    }
    mm(aa[t][mat][1], wf[i & 7], K{}, I1{}); SB();
  });
  u32x4 cs[7][2];
  const unsigned csa = 40 * KB + g * 16;
  sfor<0, 10>([&](auto i) { dsr<64 * i>(cs[i >> 1][i & 1], csa); });
  wv<10>();
  asm volatile("s_barrier" ::: "memory");
  const unsigned H = OFF_H + L * SZ_H + tt * 112 * KB;
  const int gp = (m & 1) ? 7 * m + 1 : 7 * m;
  auto pair = [&](const f32x4 (&t0)[2][2], const f32x4 (&t1)[2][2], auto lt) {
    u32x2 h0[2], h1[2];
    gu_tile(t0, cs[lt], mean, r2, h0); gu_tile(t1, cs[lt + 1], mean, r2, h1);
    st4<0>(u32x4{h0[0][0], h0[0][1], h1[0][0], h1[0][1]}, x.vg16, x.rs, H + (gp + lt - 1) * 1024);
    st4<256>(u32x4{h0[1][0], h0[1][1], h1[1][0], h1[1][1]}, x.vg16, x.rs, H + (gp + lt - 1) * 1024);
  };
  {
    wl<0>();
    sfor<0, 20>([&](auto i) { pin(aa[i >> 2][(i >> 1) & 1][i & 1]); });
    sfor<0, 10>([&](auto i) { pin(cs[i >> 1][i & 1]); });
    u32x2 hl[2];
    gu_tile(aa[0], cs[0], mean, r2, hl);
    const unsigned sl = OFF_HL + ((L * 64 + tt) * 16 + m) * KB;
    st2<0>(hl[0], x.vg16 >> 1, x.rs, sl); st2<128>(hl[1], x.vg16 >> 1, x.rs, sl);
    pair(aa[1], aa[2], std::integral_constant<int, 1>{});
    pair(aa[3], aa[4], std::integral_constant<int, 3>{});
  }
  SB();
  sfor<10, 14>([&](auto i) { dsr<64 * i>(cs[i >> 1][i & 1], csa); });
  f32x4 ab[2][2][2];
  unsigned fa;
  sfor<140, 196>([&](auto i) {
    constexpr int j = i - 140, KS = j / 4, t = (j % 4) >> 1, mat = i & 1;
    using K = std::integral_constant<int, KS>;
    if constexpr (j == 18) {
      if (L == 0) wv<6>(); else wv<0>();
      asm volatile("s_barrier" ::: "memory");
      count(x, FL_H(L, x.c, 0), 1);
      SB();
    }
    if constexpr ((i & 1) == 0) { if constexpr (i + DP < 196) { rd(std::integral_constant<int, i + DP>{}); rd(std::integral_constant<int, i + DP + 1>{}); wl<DP>(); } else wl<194 - i>(); }
    mm(ab[t][mat][0], wf[i & 7], K{}, I0{});
    if constexpr (j % 3 == 1 && j < 42) { if (L == 0) pf_wq<j / 3>(x); SB(); }
    if constexpr (j == 44) ldflag(fa, x.rs, OFF_FLAG + FL_H(L, x.c, 0));
    mm(ab[t][mat][1], wf[i & 7], K{}, I1{}); SB();
  });
  wv<0>();
  recheck(x, fa, FL_H(L, x.c, 0), 16);
  { const DnSrc d = dn_src(x, L); sfor<0, 3>([&](auto o) { dn_ld<o>(x, d, b); }); }
  sfor<0, 8>([&](auto i) { pin(ab[i >> 2][(i >> 1) & 1][i & 1]); });
  sfor<10, 14>([&](auto i) { pin(cs[i >> 1][i & 1]); });
  pair(ab[0], ab[1], std::integral_constant<int, 5>{});
}

DI void phase_down(Ctx& x, const int L, char* out, u32x4 (&b)[DWN][2]) {
  const int w = x.w;
  if (x.m >= 14) {
    wv<0>();
    sfor<0, 3>([&](auto o) { pin(b[o][0]); pin(b[o][1]); });
    signal(x, FL_H(L, x.c, 1), 1);
    return;
  }
  const int th = x.m / 7, ct = x.m % 7, tt = 4 * x.c + 2 * th + (w >> 1), nt = 2 * ct + (w & 1);
  constexpr int WN = DWN;
  u32x4 xr[2];
  const DnSrc d = dn_src(x, L);
  auto ld = [&](auto o) { dn_ld<o>(x, d, b); };
  sfor<3, WN>(ld);
  f32x16 acc[4];
  unsigned fb;
  sfor<0, 25>([&](auto i) {
    constexpr int last = i + WN - 1 < 27 ? i + WN - 1 : 27, ex = i <= 2 ? 2 : ((i >= 8 && i <= 13) ? 1 : ((i >= 21) ? 2 : 0));
    constexpr int later = [&] { int n = 0; for (int o = i + 1; o <= last; ++o) n += dn_half(o) ? 4 : 2; return n; }();
    wv<later + (dn_half(i) ? 2 : 1) + ex>();
    if constexpr (i == 3) signal(x, FL_H(L, x.c, 1), 1);
    m32a<A_WD + 8 * i, i == 0>(acc[0], b[i % WN][0]); m32a<A_WD + 8 * i + 4, i == 0>(acc[1], b[i % WN][0]);
    wv<later + ex>();
    m32a<A_WD + 8 * i, i == 0>(acc[2], b[i % WN][1]); m32a<A_WD + 8 * i + 4, i == 0>(acc[3], b[i % WN][1]);
    if constexpr (i == 7) ldflag(fb, x.rs, OFF_FLAG + FL_H(L, x.c, 1));
    if constexpr (i + WN == 20) { wv<12>(); recheck(x, fb, FL_H(L, x.c, 1), 16); }
    if constexpr (i + WN < 28) ld(std::integral_constant<int, i + WN>{});
    if constexpr (i + WN == 27) {
      const unsigned sr = OFF_X + (2 * L + 1) * SZ_X + (tt * 28 + 2 * nt) * KB;
      ld4<0>(xr[0], x.vl16, x.rs, sr); ld4<1024>(xr[1], x.vl16, x.rs, sr);
    }
  });
  wv<2>();
  sfor<1, 5>([&](auto t) {
    constexpr int k = t & 3;
    m32a<A_WD + 8 * 25 + 4 * (k & 1), false>(acc[k], b[25 % WN][k >> 1]);
    m32a<A_WD + 8 * 26 + 4 * (k & 1), false>(acc[k], b[26 % WN][k >> 1]);
    if constexpr (t > 1) { nop<(t == 2 ? 9 : 7)>(); red_put1<t - 1, 0>(x, acc[t - 1]); red_put1<t - 1, 1>(x, acc[t - 1]); }
    m32a<A_WD + 8 * 27 + 4 * (k & 1), false>(acc[k], b[27 % WN][k >> 1]);
    if constexpr (t > 1) { red_put1<t - 1, 2>(x, acc[t - 1]); red_put1<t - 1, 3>(x, acc[t - 1]); }
  });
  if (L == 0 && (x.m & 7) < 2) {
    ldl<4>(x.vl16, x.rs, OFF_WQ + ((16 + (x.m & 1)) * 28 + 7 * w) * KB, 7 * w * KB);
    ldl<3>(x.vl16, x.rs, OFF_WQ + ((16 + (x.m & 1)) * 28 + 7 * w + 4) * KB, (7 * w + 4) * KB);
  }
  nop<9>();
  pin(acc[0]);
  reduce_get(x, acc[0]);
  wv<0>();
  pin(xr[0]); pin(xr[1]);
  if (L > 0) {
    res_epilogue<true>(x, acc[0], xr, 0, 0, tt, nt, out);
  } else {
    res_epilogue<false>(x, acc[0], xr, OFF_X + 2 * SZ_X, OFF_ST + 2 * SZ_ST, tt, nt);
    wv<0>();
    signal(x, FL_X(x.c, th), 1);
  }
}

__global__ __launch_bounds__(256, 1) void k_main(Args a) {
  Ctx x;
  x.rs = mkrs(a.ws, WS_USED);
  x.w = __builtin_amdgcn_readfirstlane(threadIdx.x >> 6);
  asm volatile("s_nop 4" : "+s"(x.w));
  { const int b = blockIdx.x, i = b >> 4; x.c = 2 * (b & 7) + ((b >> 3) & 1); x.m = i < 4 ? (i & 1) + 8 * (i >> 1) : (i < 10 ? i - 2 : i); }
  f32x16 kf[3];
  { Ctx y = x; fresh(y); phase_pre(y, a, kf); }
  for (int L = 0;; ++L) {
    f32x16 g2[3];
    int Lo = L; asm volatile("" : "+s"(Lo));
    { Ctx y = x; fresh(y); phase_att(y, Lo, kf, g2); }
    { Ctx y = x; fresh(y); phase_oproj(y, Lo, a, g2); }
    { Ctx y = x; u32x4 b[DWN][2]; fresh(y); phase_gu(y, Lo, b); fresh(y); phase_down(y, Lo, a.out, b); }
    if (L == 1) break;
    { Ctx y = x; fresh(y); phase_front(y, kf); }
  }
  fresh(x);
  if (blockIdx.x == 0 && wave0(x)) {
    get_tag(x);
    if (x.l == 0) st2<0>(u32x2{x.tlo, x.thi}, 0, x.rs, OFF_TAG);
  }
  asm volatile(".Lk_end:");
}

extern "C" void kernel_launch(void* const* d_in, const int* in_sizes, int n_in, void* d_out, int out_size, void* d_ws, size_t ws_size,
                              hipStream_t stream) {
  Args a;
  a.x = (const char*)d_in[0]; a.wq = (const char*)d_in[1]; a.wk = (const char*)d_in[2]; a.wv = (const char*)d_in[3];
  a.wo = (const char*)d_in[4]; a.wg = (const char*)d_in[6]; a.wu = (const char*)d_in[7]; a.wd = (const char*)d_in[8];
  a.ws = (char*)d_ws; a.out = (char*)d_out;
  (void)hipFuncSetAttribute((const void*)k_main, hipFuncAttributeMaxDynamicSharedMemorySize, 160 * 1024);
  k_main<<<256, 256, 160 * 1024, stream>>>(a);
}
```

```cpp
#include <hip/hip_runtime.h>
#include <stdint.h>
#include <type_traits>

typedef float    f32x16 __attribute__((ext_vector_type(16)));
typedef float    f32x4  __attribute__((ext_vector_type(4)));
typedef float    f32x2  __attribute__((ext_vector_type(2)));
typedef unsigned u32x4  __attribute__((ext_vector_type(4)));
typedef unsigned u32x2  __attribute__((ext_vector_type(2)));
typedef __bf16   bf16x2 __attribute__((ext_vector_type(2)));
typedef __amdgpu_buffer_rsrc_t rsrc_t;
#define DI __device__ __forceinline__
#define GAS __attribute__((address_space(1)))
#define SB() __builtin_amdgcn_sched_barrier(0)

constexpr int S = 2048, D = 448, F = 1792;
constexpr unsigned KB = 1024;
constexpr unsigned OFF_FLAG = 0;
constexpr unsigned OFF_CSQ  = 64 * KB;
constexpr unsigned OFF_CSGU = OFF_CSQ + 4 * KB;
constexpr unsigned OFF_SLOT = OFF_CSGU + 32 * KB;
constexpr unsigned OFF_TAG  = OFF_SLOT + 32 * KB;
constexpr unsigned OFF_WQ   = 160 * KB;
constexpr unsigned OFF_WO   = OFF_WQ + 18 * 28 * KB;
constexpr unsigned OFF_WGU  = OFF_WO + 14 * 32 * KB;
constexpr unsigned OFF_WD   = OFF_WGU + 16 * 196 * KB;
constexpr unsigned SZ_X = 64 * 28 * KB, SZ_ST = 14 * 2048 * 8, SZ_O = 64 * 32 * KB, SZ_H = 64 * 112 * KB, SZ_KV = 64 * 4 * KB;
constexpr unsigned OFF_X    = OFF_WD + 14 * 112 * KB;
constexpr unsigned OFF_ST   = OFF_X + 4 * SZ_X;
constexpr unsigned OFF_O    = OFF_ST + 4 * SZ_ST;
constexpr unsigned OFF_H    = OFF_O + 2 * SZ_O;
constexpr unsigned OFF_HL   = OFF_H + 2 * SZ_H;
constexpr unsigned OFF_KV   = OFF_HL + 2 * 1024 * KB;
constexpr unsigned WS_USED  = OFF_KV + 2 * SZ_KV;
DI constexpr unsigned FL_X(int c, int th)         { return (c * 16 + th) * 256u; }
DI constexpr unsigned FL_O(int L, int c, int th)  { return (c * 16 + 2 + L * 2 + th) * 256u; }
DI constexpr unsigned FL_X1(int L, int c)         { return (c * 16 + 6 + L) * 256u; }
DI constexpr unsigned FL_H(int L, int c, int p)   { return (c * 16 + 8 + L * 2 + p) * 256u; }
DI constexpr unsigned FL_W(int c)                 { return (c * 16 + 12) * 256u; }
constexpr unsigned FL_W2 = 13 * 256u;

#define XAHEAD 3
#define OPWN 5
constexpr float QSCALE = 0.18898223650461362f * 1.4426950408889634f;
constexpr float M_LIMIT = 48.0f;

template <int I, int N, class Fn> DI void sfor(Fn&& f) { if constexpr (I < N) { f(std::integral_constant<int, I>{}); sfor<I + 1, N>(f); } }

DI unsigned cvtpk(float lo, float hi) { f32x2 v = {lo, hi}; bf16x2 b = __builtin_convertvector(v, bf16x2); return __builtin_bit_cast(unsigned, b); }
DI float bflo(unsigned u) { return __uint_as_float(u << 16); }
DI float bfhi(unsigned u) { return __uint_as_float(u & 0xffff0000u); }
DI float dot2(unsigned a, unsigned b, float c) { return __builtin_amdgcn_fdot2_f32_bf16(__builtin_bit_cast(bf16x2, a), __builtin_bit_cast(bf16x2, b), c, false); }
DI float sum32(float v) { auto r = __builtin_amdgcn_permlane32_swap(__float_as_uint(v), __float_as_uint(v), false, false); return __uint_as_float(r[0]) + __uint_as_float(r[1]); }
DI float sum16(float v) { auto r = __builtin_amdgcn_permlane16_swap(__float_as_uint(v), __float_as_uint(v), false, false); return __uint_as_float(r[0]) + __uint_as_float(r[1]); }
DI float max32(float v) { auto r = __builtin_amdgcn_permlane32_swap(__float_as_uint(v), __float_as_uint(v), false, false); return fmaxf(__uint_as_float(r[0]), __uint_as_float(r[1])); }

DI unsigned umax16(unsigned v) {
  v = max(v, (unsigned)__builtin_amdgcn_update_dpp(0, (int)v, 0x128, 0xf, 0xf, false));
  v = max(v, (unsigned)__builtin_amdgcn_update_dpp(0, (int)v, 0x124, 0xf, 0xf, false));
  v = max(v, (unsigned)__builtin_amdgcn_update_dpp(0, (int)v, 0x122, 0xf, 0xf, false));
  v = max(v, (unsigned)__builtin_amdgcn_update_dpp(0, (int)v, 0x121, 0xf, 0xf, false));
  return v;
}
DI unsigned umax64(unsigned v) {
  v = umax16(v);
  auto a = __builtin_amdgcn_permlane16_swap(v, v, false, false); v = max(a[0], a[1]);
  auto b = __builtin_amdgcn_permlane32_swap(v, v, false, false); return max(b[0], b[1]);
}

typedef short s16x4 __attribute__((ext_vector_type(4)));
DI u32x2 trread(const char* p) { return __builtin_bit_cast(u32x2, __builtin_amdgcn_ds_read_tr16_b64_v4i16((__attribute__((address_space(3))) s16x4*)p)); }
DI u32x4 trfrag(const char* lo, const char* hi) { const u32x2 a = trread(lo), b = trread(hi); return u32x4{a[0], a[1], b[0], b[1]}; }
DI void row_stats(float s, float q, float& mean, float& rstd) {
  mean = s * (1.0f / D);
  const float var = fmaxf(q * (1.0f / D) - mean * mean, 0.f);
  rstd = __builtin_amdgcn_rsqf(var + 1e-5f);
}
DI float fragsum(const u32x4& f, float s) {
#pragma unroll
  for (int d = 0; d < 4; ++d) s = dot2(f[d], 0x3f803f80u, s);
  return s;
}
struct Args { const char *x, *wq, *wk, *wv, *wo, *wg, *wu, *wd; char* ws; char* out; };

#define ACLOB "a0","a1","a2","a3","a4","a5","a6","a7","a8","a9","a10","a11","a12","a13","a14","a15","a16","a17","a18","a19","a20","a21","a22","a23","a24","a25","a26","a27","a28","a29","a30","a31","a32","a33","a34","a35","a36","a37","a38","a39","a40","a41","a42","a43","a44","a45","a46","a47","a48","a49","a50","a51","a52","a53","a54","a55","a56","a57","a58","a59","a60","a61","a62","a63","a64","a65","a66","a67","a68","a69","a70","a71","a72","a73","a74","a75","a76","a77","a78","a79","a80","a81","a82","a83","a84","a85","a86","a87","a88","a89","a90","a91","a92","a93","a94","a95","a96","a97","a98","a99","a100","a101","a102","a103","a104","a105","a106","a107","a108","a109","a110","a111","a112","a113","a114","a115","a116","a117","a118","a119","a120","a121","a122","a123","a124","a125","a126","a127","a128","a129","a130","a131","a132","a133","a134","a135","a136","a137","a138","a139","a140","a141","a142","a143","a144","a145","a146","a147","a148","a149","a150","a151","a152","a153","a154","a155","a156","a157","a158","a159","a160","a161","a162","a163","a164","a165","a166","a167","a168","a169","a170","a171","a172","a173","a174","a175","a176","a177","a178","a179","a180","a181","a182","a183","a184","a185","a186","a187","a188","a189","a190","a191","a192","a193","a194","a195","a196","a197","a198","a199","a200","a201","a202","a203","a204","a205","a206","a207","a208","a209","a210","a211","a212","a213","a214","a215","a216","a217","a218","a219","a220","a221","a222","a223","a224","a225","a226","a227","a228","a229","a230","a231","a232","a233","a234","a235","a236","a237","a238","a239","a240","a241","a242","a243","a244","a245","a246","a247","a248","a249","a250","a251","a252","a253","a254","a255"

template <int IMM> DI void ld4(u32x4& d, unsigned vo, rsrc_t rs, unsigned so) {
  asm volatile("buffer_load_dwordx4 %0, %1, %2, %3 offen offset:%c4 sc1" : "=v"(d) : "v"(vo), "s"(rs), "s"(so), "i"(IMM));
}
template <int IMM> DI void ld2(f32x2& d, unsigned vo, rsrc_t rs, unsigned so) {
  asm volatile("buffer_load_dwordx2 %0, %1, %2, %3 offen offset:%c4 sc1" : "=v"(d) : "v"(vo), "s"(rs), "s"(so), "i"(IMM));
}
template <int IMM> DI void ld4i(u32x4& d, unsigned vo, rsrc_t rs, unsigned so) {
  asm volatile("buffer_load_dwordx4 %0, %1, %2, %3 offen offset:%c4" : "=v"(d) : "v"(vo), "s"(rs), "s"(so), "i"(IMM));
}
template <int IMM> DI void ld2i(u32x2& d, unsigned vo, rsrc_t rs, unsigned so) {
  asm volatile("buffer_load_dwordx2 %0, %1, %2, %3 offen offset:%c4" : "=v"(d) : "v"(vo), "s"(rs), "s"(so), "i"(IMM));
}
template <int IMM> DI void ld2u(u32x2& d, unsigned vo, rsrc_t rs, unsigned so) {
  asm volatile("buffer_load_dwordx2 %0, %1, %2, %3 offen offset:%c4 sc1" : "=v"(d) : "v"(vo), "s"(rs), "s"(so), "i"(IMM));
}
template <int IMM> DI void ld4f(f32x4& d, unsigned vo, rsrc_t rs, unsigned so) {
  asm volatile("buffer_load_dwordx4 %0, %1, %2, %3 offen offset:%c4 sc1" : "=v"(d) : "v"(vo), "s"(rs), "s"(so), "i"(IMM));
}
DI void ld1(float& d, unsigned vo, rsrc_t rs, unsigned so) {
  asm volatile("buffer_load_dword %0, %1, %2, %3 offen sc1" : "=v"(d) : "v"(vo), "s"(rs), "s"(so));
}
template <int AR, int IMM> DI void lda(unsigned vo, rsrc_t rs, unsigned so) {
  asm volatile("buffer_load_dwordx4 a[%c3:%c4], %0, %1, %2 offen offset:%c5 sc1" :: "v"(vo), "s"(rs), "s"(so), "i"(AR), "i"(AR + 3), "i"(IMM) : ACLOB);
}
template <int N> DI void ldl(unsigned vo, rsrc_t rs, unsigned so, unsigned lds) {
  asm volatile("s_mov_b32 m0, %3\n\ts_nop 0\n\tbuffer_load_dwordx4 %0, %1, %2 offen sc1 lds" :: "v"(vo), "s"(rs), "s"(so), "s"(lds) : "memory");
  if constexpr (N > 1) asm volatile("buffer_load_dwordx4 %0, %1, %2 offen offset:1024 sc1 lds" :: "v"(vo), "s"(rs), "s"(so) : "memory");
  if constexpr (N > 2) asm volatile("buffer_load_dwordx4 %0, %1, %2 offen offset:2048 sc1 lds" :: "v"(vo), "s"(rs), "s"(so) : "memory");
  if constexpr (N > 3) asm volatile("buffer_load_dwordx4 %0, %1, %2 offen offset:3072 sc1 lds" :: "v"(vo), "s"(rs), "s"(so) : "memory");
}
template <int IMM> DI void st4(const u32x4& d, unsigned vo, rsrc_t rs, unsigned so) {
  asm volatile("buffer_store_dwordx4 %0, %1, %2, %3 offen offset:%c4 sc1\n\ts_nop 1" :: "v"(d), "v"(vo), "s"(rs), "s"(so), "i"(IMM) : "memory");
}
template <int IMM> DI void st4p(const u32x4& d, unsigned vo, rsrc_t rs, unsigned so) {
  asm volatile("buffer_store_dwordx4 %0, %1, %2, %3 offen offset:%c4\n\ts_nop 1" :: "v"(d), "v"(vo), "s"(rs), "s"(so), "i"(IMM) : "memory");
}
template <int IMM> DI void st2(const u32x2& d, unsigned vo, rsrc_t rs, unsigned so) {
  asm volatile("buffer_store_dwordx2 %0, %1, %2, %3 offen offset:%c4 sc1" :: "v"(d), "v"(vo), "s"(rs), "s"(so), "i"(IMM) : "memory");
}
DI void st1(float d, unsigned vo, rsrc_t rs, unsigned so) {
  asm volatile("buffer_store_dword %0, %1, %2, %3 offen sc1" :: "v"(d), "v"(vo), "s"(rs), "s"(so) : "memory");
}
template <int N> DI void wv() { asm volatile("s_waitcnt vmcnt(%c0)" :: "i"(N) : "memory"); SB(); }
template <int N> DI void wl() { asm volatile("s_waitcnt lgkmcnt(%c0)" :: "i"(N) : "memory"); SB(); }
template <class T> DI void pin(T& x) { asm volatile("" : "+v"(x)); }
DI void bar() { asm volatile("s_waitcnt lgkmcnt(0)\n\ts_barrier" ::: "memory"); }
template <int N> DI void nop() { asm volatile("s_nop %c0" :: "i"(N)); SB(); }

template <int OFF> DI void trd(u32x2& r, unsigned a) { asm volatile("ds_read_b64_tr_b16 %0, %1 offset:%c2" : "=v"(r) : "v"(a), "i"(OFF)); }
template <int OFF> DI void dsr(u32x4& r, unsigned a) { asm volatile("ds_read_b128 %0, %1 offset:%c2" : "=v"(r) : "v"(a), "i"(OFF)); }

DI void m32(f32x16& c, const u32x4& a, const u32x4& b)  { asm volatile("v_mfma_f32_32x32x16_bf16 %0, %1, %2, %0" : "+v"(c) : "v"(a), "v"(b)); }
DI void m32z(f32x16& c, const u32x4& a, const u32x4& b) { asm volatile("v_mfma_f32_32x32x16_bf16 %0, %1, %2, 0" : "=&v"(c) : "v"(a), "v"(b)); }
template <int AR, bool Z> DI void m32a(f32x16& c, const u32x4& b) {
  if constexpr (Z) asm volatile("v_mfma_f32_32x32x16_bf16 %0, a[%c2:%c3], %1, 0" : "=&v"(c) : "v"(b), "i"(AR), "i"(AR + 3));
  else             asm volatile("v_mfma_f32_32x32x16_bf16 %0, a[%c2:%c3], %1, %0" : "+v"(c) : "v"(b), "i"(AR), "i"(AR + 3));
}
template <int AR, bool Z> DI void m32b(f32x16& c, const u32x4& a) {
  if constexpr (Z) asm volatile("v_mfma_f32_32x32x16_bf16 %0, %1, a[%c2:%c3], 0" : "=&v"(c) : "v"(a), "i"(AR), "i"(AR + 3));
  else             asm volatile("v_mfma_f32_32x32x16_bf16 %0, %1, a[%c2:%c3], %0" : "+v"(c) : "v"(a), "i"(AR), "i"(AR + 3));
}
template <bool Z> DI void m16(f32x4& c, const u32x4& a, const u32x4& b) {
  if constexpr (Z) asm volatile("v_mfma_f32_16x16x32_bf16 %0, %1, %2, 0" : "=&v"(c) : "v"(a), "v"(b));
  else             asm volatile("v_mfma_f32_16x16x32_bf16 %0, %1, %2, %0" : "+v"(c) : "v"(a), "v"(b));
}

template <bool Z, int AR> DI void m16a(f32x4& c, const u32x4& a) {
  if constexpr (Z) asm volatile("v_mfma_f32_16x16x32_bf16 %0, %1, a[%c2:%c3], 0" : "=&v"(c) : "v"(a), "i"(AR), "i"(AR + 3));
  else             asm volatile("v_mfma_f32_16x16x32_bf16 %0, %1, a[%c2:%c3], %0" : "+v"(c) : "v"(a), "i"(AR), "i"(AR + 3));
}
DI void ldflag(unsigned& v, rsrc_t rs, unsigned so) { asm volatile("buffer_load_dword %0, off, %1, %2 sc1" : "=v"(v) : "s"(rs), "s"(so)); }

struct Ctx {
  rsrc_t rs;
  unsigned tlo, thi;
  unsigned vl16;
  unsigned vg16;
  int w, l, c, m;
};
DI void fresh(Ctx& x) {
  asm volatile("" : "+s"(x.w), "+s"(x.c), "+s"(x.m));
  asm volatile("v_mbcnt_lo_u32_b32 %0, -1, 0\n\tv_mbcnt_hi_u32_b32 %0, -1, %0" : "=v"(x.l));
  x.vl16 = x.l * 16;
  const int g = x.l >> 4;
  x.vg16 = ((g >> 1) * 64 + (g & 1) * 32 + (x.l & 15)) * 16;
}
extern __shared__ __attribute__((aligned(16))) char lds[];
constexpr unsigned SCR = 112 * KB;

DI void get_tag(Ctx& x) {
  unsigned lo, hi;
  asm volatile("v_accvgpr_read_b32 v255, a252\n\ts_nop 1\n\tv_readfirstlane_b32 %0, v255\n\tv_accvgpr_read_b32 v255, a253\n\ts_nop 1\n\tv_readfirstlane_b32 %1, v255\n\ts_nop 4"
               : "=s"(lo), "=s"(hi) :: "v255");
  const unsigned long long e = ((unsigned long long)hi << 32) | lo;
  unsigned long long n = e * 0x9E3779B97F4A7C15ull + 0x632BE59BD9B4E019ull;
  if (((n ^ e) >> 16) == 0) n += 0x10000;
  x.tlo = (unsigned)n; x.thi = (unsigned)(n >> 32);
}
DI bool wave0(const Ctx& x) { int w = x.w; asm volatile("" : "+s"(w)); return w == 0; }
DI void count(Ctx& x, unsigned fl, int n) {
  if (wave0(x)) {
    const unsigned long long mask = (1ull << n) - 1;
    unsigned long long keep;
    asm volatile("s_mov_b64 %0, exec\n\ts_mov_b64 exec, %5\n\tbuffer_atomic_add %1, %2, %3, %4 offen\n\ts_mov_b64 exec, %0"
                 : "=&s"(keep) : "v"(1u), "v"(x.l * 256), "s"(x.rs), "s"(OFF_FLAG + fl), "s"(mask) : "memory");
  }
}
DI void signal(Ctx& x, unsigned fl, int n) { bar(); count(x, fl, n); }
DI void await(Ctx& x, unsigned fl, unsigned target) {
  if (wave0(x)) {
#pragma nounroll
    for (unsigned spin = 0; spin < (1u << 18); ++spin) {
      unsigned v;
      asm volatile("buffer_load_dword %0, off, %1, %2 sc1\n\ts_waitcnt vmcnt(0)" : "=v"(v) : "s"(x.rs), "s"(OFF_FLAG + fl) : "memory");
      if ((unsigned)__builtin_amdgcn_readfirstlane(v) >= target) break;
      __builtin_amdgcn_s_sleep(1);
    }
  }
  bar();
}

DI void recheck(Ctx& x, unsigned v, unsigned fl, unsigned target) {
  pin(v);
#pragma nounroll
  for (unsigned spin = 0; (unsigned)__builtin_amdgcn_readfirstlane(v) < target && spin < (1u << 18); ++spin) {
    __builtin_amdgcn_s_sleep(2);
    asm volatile("buffer_load_dword %0, off, %1, %2 sc1\n\ts_waitcnt vmcnt(0)" : "=v"(v) : "s"(x.rs), "s"(OFF_FLAG + fl) : "memory");
  }
  SB();
}

DI void reduce_put(Ctx& x, const f32x16 (&acc)[4]) {
  sfor<1, 4>([&](auto k) {
    f32x4* p = (f32x4*)(lds + SCR + ((x.w ^ k) * 3 + k - 1) * 4096 + x.vl16);
#pragma unroll
    for (int q = 0; q < 4; ++q) p[q * 64] = f32x4{acc[k][4 * q], acc[k][4 * q + 1], acc[k][4 * q + 2], acc[k][4 * q + 3]};
  });
}
template <int K, int Q> DI void red_put1(Ctx& x, const f32x16& t) {
  asm volatile("ds_write_b128 %0, %1 offset:%c2" :: "v"(SCR + ((x.w ^ K) * 3 + K - 1) * 4096 + x.vl16),
               "v"(__builtin_shufflevector(t, t, 4 * Q, 4 * Q + 1, 4 * Q + 2, 4 * Q + 3)), "i"(Q * 1024) : "memory");
}
DI void reduce_get(Ctx& x, f32x16& own) {
  bar();
  const f32x4* p = (const f32x4*)(lds + SCR + x.w * 3 * 4096 + x.vl16);
#pragma unroll
  for (int k = 0; k < 3; ++k)
#pragma unroll
    for (int q = 0; q < 4; ++q) {
      f32x4 v = p[k * 256 + q * 64];
#pragma unroll
      for (int i = 0; i < 4; ++i) own[4 * q + i] += v[i];
    }
}

constexpr int A_WO = 184, A_WD = 0, A_GX = 224;
constexpr int dn_ks(int o) { return o < 20 ? 2 * ((o >> 1) % 5 + ((o >> 1) % 5 >= 2) + 7 * (o / 10)) + (o & 1) : 2 * (((o - 20) >> 1) * 4 - ((o - 20) >> 2) + 2) + (o & 1); }
static_assert(dn_ks(0) == 0 && dn_ks(4) == 6 && dn_ks(9) == 11 && dn_ks(10) == 14 && dn_ks(19) == 25 && dn_ks(20) == 4 && dn_ks(22) == 12 && dn_ks(24) == 18 && dn_ks(27) == 27, "");
struct Two { unsigned s0, s1; };
constexpr int SL_WQ = 56;
template <int J> DI void pf_wq(Ctx& x) {
  ldl<1>(x.vl16, x.rs, OFF_WQ + ((x.m & 7) * 56 + 14 * x.w + J) * KB, (SL_WQ + 14 * x.w + J) * KB);
}
DI Two base_wo(const Ctx& x) { const int ct = x.m % 7;
  return Two{OFF_WO + ((2 * ct + (x.w & 1)) * 32 + 8 * x.w) * KB, OFF_WO + ((2 * ct + ((x.w & 1) ^ 1)) * 32 + 8 * x.w) * KB}; }
DI void pf_wo(Ctx& x) {
  const Two b = base_wo(x);
  sfor<0, 8>([&](auto i) {
    lda<A_WO + 8 * i, (i & 3) * 1024>(x.vl16, x.rs, b.s0 + (i >> 2) * 4096);
    lda<A_WO + 8 * i + 4, (i & 3) * 1024>(x.vl16, x.rs, b.s1 + (i >> 2) * 4096);
  });
}
DI Two base_wd(const Ctx& x) { const int ct = x.m % 7;
  return Two{OFF_WD + ((2 * ct + (x.w & 1)) * 112 + 28 * x.w) * KB, OFF_WD + ((2 * ct + ((x.w & 1) ^ 1)) * 112 + 28 * x.w) * KB}; }
template <int I0, int I1> DI void pf_wd(Ctx& x, const Two& b) {
  sfor<I0, I1>([&](auto f) {
    constexpr int i = f >> 1;
    lda<A_WD + 4 * f, (i & 3) * 1024>(x.vl16, x.rs, ((f & 1) ? b.s1 : b.s0) + (i >> 2) * 4096);
  });
}
DI void pf_wgu1(Ctx& x) {
  for (int t = 0; t < 14; ++t) ldl<2>(x.vl16, x.rs, OFF_WGU + (x.m * 196 + 8 * t + 2 * x.w) * KB, (8 * t + 2 * x.w) * KB);
}
template <int J> DI void pf_wgu3(Ctx& x) {
  ldl<1>(x.vl16, x.rs, OFF_WGU + (x.m * 196 + 160 + 9 * x.w + J) * KB, (9 * x.w + J) * KB);
}

constexpr unsigned S_QP = SCR, S_KP = SCR + 16384, S_QF = SCR + 24576, S_ST = SCR + 32768, S_CS = SCR + 33792, S_KM = SCR + 36864, S_TR = SCR + 40960;
DI unsigned kv_finish(Ctx& x, const float (&raw)[16], const f32x4 (&cs)[4], float mean, float rstd, bool isv, unsigned so, int slot) {
  const int h = x.l >> 5, r = x.l & 31;
  u32x4 y[2];
#pragma unroll
  for (int d = 0; d < 8; ++d)
    y[d >> 2][d & 3] = cvtpk((raw[2 * d] - mean * cs[d >> 1][2 * (d & 1)]) * rstd, (raw[2 * d + 1] - mean * cs[d >> 1][2 * (d & 1) + 1]) * rstd);
  if (h) { y[1][2] = 0; y[1][3] = 0; }
  unsigned nb = 0;
  if (!isv) {
    float n2 = 0.f;
#pragma unroll
    for (int d = 0; d < 8; ++d) n2 = dot2(y[d >> 2][d & 3], y[d >> 2][d & 3], n2);
    n2 = sum32(n2);
    nb = umax64(n2 < 3.0e38f ? __float_as_uint(n2) : 0x7f800000u);
    nb = cvtpk(1.01f * __builtin_amdgcn_sqrtf(__uint_as_float(nb)), 0.f);
    if (h) y[1][2] = 0x3f80u;
  } else {
    if (h) y[1][2] = 0x3f80u;
    char* im = lds + S_TR + slot * 2048;
#pragma unroll
    for (int a = 0; a < 4; ++a) *(u32x2*)(im + r * 64 + 16 * a + 8 * h) = u32x2{y[a >> 1][2 * (a & 1)], y[a >> 1][2 * (a & 1) + 1]};
    asm volatile("s_waitcnt lgkmcnt(0)" ::: "memory");
    const char* tb = im + (4 * h + ((x.l >> 2) & 3)) * 64 + ((x.l >> 4) & 1) * 32 + (x.l & 3) * 8;
    y[0] = trfrag(tb, tb + 512); y[1] = trfrag(tb + 1024, tb + 1536);
  }
  st4<0>(y[0], x.vl16, x.rs, so); st4<1024>(y[1], x.vl16, x.rs, so);
  return nb;
}
DI void kv_publish(Ctx& x, int L, int p, unsigned km) {
  get_tag(x);
  st2<0>(u32x2{(x.tlo & 0xffff0000u) | km, x.thi}, x.l < 16 ? x.l * 1024 : ~0u, x.rs, OFF_SLOT + (L * 2048 + p) * 8);
}
DI void q_finish(Ctx& x, const f32x16& qa, const f32x4 (&cq)[4], float mean, float rstd) {
  const int h = x.l >> 5;
  u32x4 qf[2];
  const f32x4* p = (const f32x4*)(lds + S_QP + x.w * 4096 + x.vl16);
  const float sc = rstd * QSCALE;
#pragma unroll
  for (int q = 0; q < 4; ++q) {
    const f32x4 o = p[q * 64];
    float v[4];
#pragma unroll
    for (int i = 0; i < 4; ++i) v[i] = (qa[4 * q + i] + o[i] - mean * cq[q][i]) * sc;
    qf[q >> 1][2 * (q & 1)] = cvtpk(v[0], v[1]); qf[q >> 1][2 * (q & 1) + 1] = cvtpk(v[2], v[3]);
  }
  if (h) { qf[1][2] = 0; qf[1][3] = 0; }
  float qn2 = 0.f, qn3 = 0.f;
#pragma unroll
  for (int d = 0; d < 4; ++d) { qn2 = dot2(qf[0][d], qf[0][d], qn2); qn3 = dot2(qf[1][d], qf[1][d], qn3); }
  qn2 = sum32(qn2 + qn3);
  if (h) qf[1][2] = cvtpk(1.01f * __builtin_amdgcn_sqrtf(qn2), 0.f);
  ((u32x4*)(lds + S_QF + x.w * 2048 + x.vl16))[0] = qf[0];
  ((u32x4*)(lds + S_QF + x.w * 2048 + x.vl16))[64] = qf[1];
}
DI void put_tile(char* p, const f32x16& t) {
#pragma unroll
  for (int q = 0; q < 4; ++q) ((f32x4*)p)[q * 64] = f32x4{t[4 * q], t[4 * q + 1], t[4 * q + 2], t[4 * q + 3]};
}

constexpr unsigned IMG_Q = 0, IMG_T = 56 * KB;
DI rsrc_t mkrs(const char* p, unsigned n) { return __builtin_amdgcn_make_buffer_rsrc((char*)p, 0, n, 0x00020000); }

DI void ask_slots(Ctx& x, int L) {
  if (wave0(x)) asm volatile("buffer_load_dwordx4 a[248:251], %0, %1, %2 offen sc1" :: "v"(x.vl16), "s"(x.rs), "s"(OFF_SLOT + (L * 16 + x.c) * 1024) : ACLOB);
}
DI void kv_wait(Ctx& x, int L) {
  wv<0>();
  if (wave0(x)) {
    get_tag(x);
    unsigned s0, s1, s2, s3;
#pragma nounroll
    for (unsigned spin = 0; spin < (1u << 18); ++spin) {
      asm volatile("v_accvgpr_read_b32 %0, a248\n\tv_accvgpr_read_b32 %1, a249\n\tv_accvgpr_read_b32 %2, a250\n\tv_accvgpr_read_b32 %3, a251" : "=v"(s0), "=v"(s1), "=v"(s2), "=v"(s3));
      if (__all(s1 == x.thi && s3 == x.thi && (((s0 ^ x.tlo) | (s2 ^ x.tlo)) >> 16) == 0)) break;
      __builtin_amdgcn_s_sleep(2);
      ask_slots(x, L); wv<0>();
    }
    const unsigned km = umax64(max(s0 << 16, s2 << 16));
    if (x.l == 0) ((unsigned*)(lds + S_KM))[4] = km;
  }
  bar();
}
DI void ask_kv(Ctx& x, int L, f32x16 (&kf)[3]) {
  const unsigned kvs = OFF_KV + L * SZ_KV + 16 * x.w * 4096;
  asm volatile(".irp i,0,1,2,3\n\tbuffer_load_dwordx4 v[128+4*\\i:131+4*\\i], %3, %4, %5 offen offset:1024*\\i sc1\n\t.endr\n\t"
               ".irp i,0,1,2,3\n\tbuffer_load_dwordx4 v[144+4*\\i:147+4*\\i], %3, %4, %6 offen offset:1024*\\i sc1\n\t.endr\n\t"
               ".irp i,0,1,2,3\n\tbuffer_load_dwordx4 v[160+4*\\i:163+4*\\i], %3, %4, %7 offen offset:1024*\\i sc1\n\t.endr"
               : "={v[128:143]}"(kf[0]), "={v[144:159]}"(kf[1]), "={v[160:175]}"(kf[2]) : "{v204}"(x.vl16), "s"(x.rs), "s"(kvs), "s"(kvs + 4096), "s"(kvs + 8192));
}

DI void phase_pre(Ctx& x, const Args& a, f32x16 (&kf)[3]) {
  const int w = x.w, l = x.l, h = l >> 5, r = l & 31;
  const int th = x.m >> 3, hp = x.m & 7, tl = w & 1, kh = w >> 1, tt = 4 * x.c + 2 * th + tl, wi = 2 * x.c + th;
  const bool kvp = hp < 2;
  u32x4 xf[14];
  { const rsrc_t rx = mkrs(a.x, S * D * 2);
    sfor<0, 14>([&](auto i) { ld4i<32 * i>(xf[i], r * 896 + h * 16, rx, tt * 32 * 896 + kh * 448); }); }
  if (x.m == 2) asm volatile("buffer_load_dword a116, %0, %1, 0 offen sc1" :: "v"(l < 16 ? OFF_FLAG + (x.c * 16 + l) * 256 : OFF_SLOT + ((l >> 3) & 1) * 16384 + x.c * 1024 + (l & 7) * 128), "s"(x.rs) : ACLOB);
  asm volatile("buffer_load_dwordx2 a[252:253], off, %0, %1 sc1" :: "s"(x.rs), "s"(OFF_TAG) : ACLOB);
  const int task = wi * 6 + hp - 2;
  const rsrc_t rq = mkrs(a.wq, D * D * 2);
  auto ask_q = [&](auto i) {
    ldl<1>((8 * w + (l >> 3)) * 896 + (l & 7) * 16, rq, hp * 112 + i * 32 * 896, IMG_Q + (4 * i + w) * KB);
  };
  auto ask_t = [&](const char* tp, unsigned tn, unsigned ts, unsigned tb) {
    const rsrc_t rt = mkrs(tp, tn);
    const unsigned vo = (8 * w + (l >> 3)) * ts + (l & 7) * 16;
    sfor<0, 7>([&](auto i) { ldl<1>(vo, rt, tb + i * 32 * ts, IMG_T + (4 * i + w) * KB); });
  };
  const int tco = ((l >> 4) & 1) * 32 + (l & 3) * 8;
  const int sro = 4 * h + ((l >> 2) & 3);
  if (kvp) ask_t(hp ? a.wv : a.wk, D * 56, 128, 0);
  else if (task < 112) ask_t(task < 56 ? a.wg : a.wu, D * F * 2, F * 2, (task & 1) * 224 * F * 2 + ((task % 56) >> 1) * 128);
  else if (task < 168) ask_t(a.wd, D * F * 2, 896, ((task - 112) / 7) * 224 * 896 + ((task - 112) % 7) * 128);
  else ask_t(a.wo, task < 182 ? D * D * 2 : 0, 896, ((task - 168) / 7) * 224 * 896 + ((task - 168) % 7) * 128);
  wv<8>();
  {
    float s = 0.f, q = 0.f, s1 = 0.f, q1 = 0.f;
    sfor<0, 14>([&](auto i) {
      if (!kvp) ask_q(i);
      pin(xf[i]);
      s = dot2(xf[i][0], 0x3f803f80u, s); q = dot2(xf[i][0], xf[i][0], q); s1 = dot2(xf[i][1], 0x3f803f80u, s1); q1 = dot2(xf[i][1], xf[i][1], q1);
      s = dot2(xf[i][2], 0x3f803f80u, s); q = dot2(xf[i][2], xf[i][2], q); s1 = dot2(xf[i][3], 0x3f803f80u, s1); q1 = dot2(xf[i][3], xf[i][3], q1);
      pin(s); pin(q); pin(s1); pin(q1);
    });
    s = sum32(s + s1); q = sum32(q + q1);
    if (h == 0) ((f32x2*)(lds + S_ST))[w * 32 + r] = f32x2{s, q};
  }
  if (kvp) wv<0>(); else wv<14>();
  bar();
  if (!kvp) {
    if (task < 112) {
      const int g = l >> 4, mat = task >= 56, gt = 4 * ((task % 56) >> 1) + w, m = gt / 7, j = gt % 7, lt = (m & 1) ? j : (j == 6 ? 0 : j + 1);
      const char* tb = lds + IMG_T + (16 * (g >> 1) + 4 * (g & 1) + ((l >> 2) & 3)) * 128 + 32 * w + (l & 3) * 8;
      const unsigned st = lt < 5 ? 10 * KB : 4 * KB, so = OFF_WGU + (m * 196 + (lt < 5 ? lt * 2 + mat : 140 + (lt - 5) * 2 + mat)) * KB + (task & 1) * 7 * st;
      u32x4 f[7];
      sfor<0, 7>([&](auto i) { f[i] = trfrag(tb + i * 4096, tb + i * 4096 + 1024); });
      float cs = 0.f;
      sfor<0, 7>([&](auto i) { st4<0>(f[i], x.vl16, x.rs, so + i * st); cs = fragsum(f[i], cs); });
      cs = sum32(sum16(cs));
      st1(cs, l < 16 ? l * 4 : ~0u, x.rs, OFF_CSGU + m * 2048 + (task & 1) * 1024 + (lt * 2 + mat) * 64);
    } else if (task < 168) {
      const int nt = 2 * ((task - 112) % 7) + (w & 1), rb = (task - 112) / 7;
      const char* tb = lds + IMG_T + sro * 128 + 64 * (w & 1) + tco;
      const unsigned so = OFF_WD + (nt * 112 + 28 * (rb >> 1)) * KB, sa = so + (rb & 1) * 10 * KB, sb = so + (20 + (rb & 1) * 4) * KB;
      u32x4 f[7];
      if (w < 2) {
        sfor<0, 7>([&](auto i) { const char* p = tb + (32 * (i >> 1) + 8 * (i & 1)) * 128; f[i] = trfrag(p, p + 2048); });
        sfor<0, 7>([&](auto i) { st4<0>(f[i], x.vl16, x.rs, i < 4 ? sa + i * KB : (i < 6 ? sb + (i - 4) * KB : sa + 4 * KB)); });
      } else {
        sfor<0, 7>([&](auto i) { const char* p = tb + (32 * ((i + 7) >> 1) + 8 * ((i + 7) & 1)) * 128; f[i] = trfrag(p, p + 2048); });
        sfor<0, 7>([&](auto i) { st4<0>(f[i], x.vl16, x.rs, i < 5 ? sa + (5 + i) * KB : sb + (i - 3) * KB); });
      }
      st1(0.f, ~0u, x.rs, 0);
    } else {
      const int nt = 2 * ((task - 168) % 7) + (w & 1);
      const char* tb = lds + IMG_T + (112 * (w >> 1) + sro) * 128 + 64 * (w & 1) + tco;
      const unsigned vo = task < 182 ? x.vl16 : ~0u - 4096;
      u32x4 f[8];
      sfor<0, 8>([&](auto i) { const char* p = tb + (28 * (i >> 1) + 16 * (i & 1)) * 128; f[i] = trfrag(p, p + 1024); if ((i & 1) && h) { f[i][2] = 0; f[i][3] = 0; } });
      sfor<0, 8>([&](auto i) { st4<(i & 3) * 1024>(f[i], vo, x.rs, OFF_WO + (nt * 32 + 16 * ((task - 168) / 7) + 8 * (w >> 1) + (i & ~3)) * KB); });
    }
  }
  if (!kvp) { wv<8>(); bar(); }
  float mean, rstd;
  { const f32x2 p0 = ((const f32x2*)(lds + S_ST))[tl * 32 + r], p1 = ((const f32x2*)(lds + S_ST))[(tl + 2) * 32 + r];
    row_stats(p0[0] + p1[0], p0[1] + p1[1], mean, rstd); }
  float* csw = (float*)(lds + S_CS);
  const int tro = 8 * h + ((l >> 2) & 3);
  u32x4 ones = {0x3f803f80u, 0x3f803f80u, 0x3f803f80u, 0x3f803f80u};
  pin(ones);
  if (kvp) {
    f32x16 kv, cs;
    const unsigned tb = IMG_T + (224 * kh + tro) * 56 + tco;
    u32x2 f[14][2];
    auto rd = [&](auto i) { trd<i * 896>(f[i][0], tb); trd<i * 896 + 224>(f[i][1], tb); };
    sfor<0, 7>(rd);
    sfor<0, 14>([&](auto i) {
      if constexpr (i + 7 < 14) { wl<12>(); rd(std::integral_constant<int, i + 7>{}); } else wl<2 * (13 - i)>();
      pin(f[i][0]); pin(f[i][1]);
      u32x4 g = {f[i][0][0], f[i][0][1], f[i][1][0], f[i][1][1]};
      pin(g); nop<0>();
      if constexpr (i == 0) { m32z(kv, g, xf[i]); m32z(cs, g, ones); } else { m32(kv, g, xf[i]); m32(cs, g, ones); }
    });
    nop<11>(); pin(kv); pin(cs);
    if (r == 0) sfor<0, 4>([&](auto q) { *(f32x4*)(csw + (w * 3 + 2) * 32 + 8 * q + 4 * h) = f32x4{cs[4 * q], cs[4 * q + 1], cs[4 * q + 2], cs[4 * q + 3]}; });
    if (kh) put_tile(lds + S_KP + tl * 4096 + x.vl16, kv);
    bar();
    if (!kh) {
      if (x.m == 0 && w == 0) st1(0.f, l < 16 ? l * 256 : ~0u, x.rs, OFF_FLAG + x.c * 4096);
      float raw[16]; f32x4 c4[4];
      sfor<0, 4>([&](auto q) {
        const f32x4 o = ((const f32x4*)(lds + S_KP + tl * 4096 + x.vl16))[q * 64];
        c4[q] = *(const f32x4*)(csw + (w * 3 + 2) * 32 + 8 * q + 4 * h) + *(const f32x4*)(csw + ((w + 2) * 3 + 2) * 32 + 8 * q + 4 * h);
#pragma unroll
        for (int i = 0; i < 4; ++i) raw[4 * q + i] = kv[4 * q + i] + o[i];
      });
      const unsigned km = kv_finish(x, raw, c4, mean, rstd, hp == 1, OFF_KV + (tt * 4 + 2 * hp) * KB, tl);
      wv<0>();
      kv_publish(x, 0, 2 * tt + hp, km);
      if (tt == 0 && r == 0) sfor<0, 4>([&](auto q) { st4<32 * q>(__builtin_bit_cast(u32x4, c4[q]), h * 16, x.rs, OFF_CSQ + (16 + hp) * 128); });
    } else {
      sfor<0, 28>([&](auto i) { ldl<1>((16 * tl + 8 * (i & 1) + (l >> 3)) * 896 + (l & 7) * 16, rq, hp * 112 + (i >> 1) * 32 * 896, IMG_Q + (4 * (i >> 1) + 2 * tl + (i & 1)) * KB); });
      wv<0>();
    }
    bar();
  }
  f32x16 qa;
  {
    f32x16 q1, q2, cs;
    const unsigned t1 = IMG_Q + (224 * kh + tro) * 128 + tco + 56 * tl, t2 = IMG_Q + (224 * kh + tro) * 128 + tco + 56 * (tl ^ 1);
    u32x2 f[14][4];
    auto rd = [&](auto i) { trd<i * 2048>(f[i][0], t1); trd<i * 2048 + 512>(f[i][1], t1); trd<i * 2048>(f[i][2], t2); trd<i * 2048 + 512>(f[i][3], t2); };
    wl<0>();
    sfor<0, 3>(rd);
    sfor<0, 14>([&](auto i) {
      if constexpr (i + 3 < 14) { wl<8>(); rd(std::integral_constant<int, i + 3>{}); } else wl<4 * (13 - i)>();
      sfor<0, 4>([&](auto e) { pin(f[i][e]); });
      u32x4 g1 = {f[i][0][0], f[i][0][1], f[i][1][0], f[i][1][1]}, g2 = {f[i][2][0], f[i][2][1], f[i][3][0], f[i][3][1]};
      pin(g1); pin(g2); nop<0>();
      if constexpr (i == 0) { m32z(q1, g1, xf[i]); m32z(q2, g2, xf[i]); m32z(cs, g1, ones); } else { m32(q1, g1, xf[i]); m32(q2, g2, xf[i]); m32(cs, g1, ones); }
      if constexpr (i == 10) { ask_slots(x, 0); SB(); }
    });
    nop<11>(); pin(q1); pin(q2); pin(cs);
    if (r == 0) sfor<0, 4>([&](auto q) { *(f32x4*)(csw + (w * 3) * 32 + 8 * q + 4 * h) = f32x4{cs[4 * q], cs[4 * q + 1], cs[4 * q + 2], cs[4 * q + 3]}; });
    if (kh == tl) { put_tile(lds + S_QP + (w ^ 2) * 4096 + x.vl16, q2); qa = q1; } else { put_tile(lds + S_QP + (w ^ 2) * 4096 + x.vl16, q1); qa = q2; }
  }
  if (th == 0 && x.c < 14) {
    const int f = 4 * x.c + w, hd = f >= 28, ks = f - 28 * hd;
    const char* tb = lds + IMG_Q + (16 * ks + sro) * 128 + tco + 56 * hd;
    st4<0>(trfrag(tb, tb + 1024), x.vl16, x.rs, OFF_WQ + (hp * 56 + f) * KB);
  }
  if (kvp && th == 1 && x.c < 7) {
    const char* tb = lds + IMG_T + (16 * (4 * x.c + w) + sro) * 56 + tco;
    st4<0>(trfrag(tb, tb + 448), x.vl16, x.rs, OFF_WQ + ((16 + hp) * 28 + 4 * x.c + w) * KB);
  }
  kv_wait(x, 0);
  count(x, FL_W(x.c), 1);
  ask_kv(x, 0, kf);
  const bool own = x.c == hp && th == 0;
  {
    f32x4 cq[4];
    sfor<0, 4>([&](auto q) {
      cq[q] = *(const f32x4*)(csw + (kh * 3) * 32 + 8 * q + 4 * h) + *(const f32x4*)(csw + ((kh + 2) * 3) * 32 + 8 * q + 4 * h);
      if (own && tl == 0 && r == 0) st4<32 * q>(__builtin_bit_cast(u32x4, cq[q]), h * 16, x.rs, OFF_CSQ + (2 * hp + kh) * 128);
    });
    q_finish(x, qa, cq, mean, rstd);
  }
}

DI void phase_front(Ctx& x, f32x16 (&kf)[3]) {
  const int w = x.w, l = x.l, h = l >> 5, r = l & 31;
  const int th = x.m >> 3, hp = x.m & 7, tl = w & 1, kh = w >> 1, tt = 4 * x.c + 2 * th + tl;
  const unsigned KV = OFF_KV + SZ_KV;
  {
    f32x4 cq[4], ck[4];
    sfor<0, 4>([&](auto q) { ld4f<32 * q>(cq[q], h * 16, x.rs, OFF_CSQ + (2 * hp + kh) * 128); ld4f<32 * q>(ck[q], h * 16, x.rs, OFF_CSQ + (16 + (hp & 1)) * 128); });
    wv<0>();
    await(x, FL_X(x.c, th), 7);
    u32x4 xf[14]; f32x2 sp[7];
    {
      const unsigned so = OFF_X + 2 * SZ_X + (tt * 28 + 14 * kh) * KB;
      sfor<0, 7>([&](auto i) { ld4<(i & 3) * 1024>(xf[i], x.vl16, x.rs, so + (i >> 2) * 4096); });
      sfor<0, 7>([&](auto i) { ld2<0>(sp[i], (h * 7 * 2048 + r) * 8, x.rs, OFF_ST + 2 * SZ_ST + (i * 2048 + tt * 32) * 8); });
      sfor<7, 14>([&](auto i) { ld4<(i & 3) * 1024>(xf[i], x.vl16, x.rs, so + (i >> 2) * 4096); });
    }
    float mean, rstd;
    auto stats = [&]() {
      sfor<0, 7>([&](auto i) { pin(sp[i]); });
      sfor<0, 4>([&](auto i) { pin(cq[i]); pin(ck[i]); });
      float s = 0.f, q = 0.f;
#pragma unroll
      for (int i = 0; i < 7; ++i) { s += sp[i][0]; q += sp[i][1]; }
      row_stats(sum32(s), sum32(q), mean, rstd);
      pin(mean); pin(rstd); SB();
    };
    f32x16 qa, qb;
    u32x4 wa[14], wb[14];
    const unsigned pa = x.vl16 + (SL_WQ + 28 * kh + 14 * kh) * KB, pb = x.vl16 + (SL_WQ + 28 * (kh ^ 1) + 14 * kh) * KB;
    if (hp < 2) {
      f32x16 kv;
      sfor<0, 14>([&](auto i) { dsr<i * 1024>(wa[i], x.vl16 + 14 * kh * KB); });
      sfor<0, 14>([&](auto i) {
        wv<(i < 7 ? 20 : 13) - i>(); wl<13 - i>();
        if constexpr (i == 0) m32z(kv, wa[i], xf[i]); else m32(kv, wa[i], xf[i]);
        if constexpr (i == 7) stats();
      });
      nop<11>(); pin(kv);
      if (kh) put_tile(lds + S_KP + tl * 4096 + x.vl16, kv);
      bar();
      unsigned km = 0;
      if (!kh) {
        float raw[16];
        sfor<0, 4>([&](auto q) {
          const f32x4 o = ((const f32x4*)(lds + S_KP + tl * 4096 + x.vl16))[q * 64];
#pragma unroll
          for (int i = 0; i < 4; ++i) raw[4 * q + i] = kv[4 * q + i] + o[i];
        });
        km = kv_finish(x, raw, ck, mean, rstd, hp == 1, KV + (tt * 4 + 2 * hp) * KB, tl);
      }
      wl<0>();
      sfor<0, 7>([&](auto i) { dsr<i * 1024>(wb[i], pb); dsr<i * 1024>(wa[i], pa); });
      sfor<0, 14>([&](auto i) {
        if constexpr (i < 7) wl<12>(); else wl<2 * (13 - i)>();
        if constexpr (i == 0) { m32z(qb, wb[i], xf[i]); m32z(qa, wa[i], xf[i]); } else { m32(qb, wb[i], xf[i]); m32(qa, wa[i], xf[i]); }
        if constexpr (i < 7) { dsr<(i + 7) * 1024>(wb[i + 7], pb); dsr<(i + 7) * 1024>(wa[i + 7], pa); }
        if constexpr (i == 8) { if (!kh) { wv<0>(); kv_publish(x, 1, 2 * tt + hp, km); } SB(); }
      });
    } else {
      sfor<0, 7>([&](auto i) { dsr<i * 1024>(wb[i], pb); dsr<i * 1024>(wa[i], pa); });
      sfor<0, 14>([&](auto i) {
        wv<(i < 7 ? 20 : 13) - i>();
        if constexpr (i < 7) wl<12>(); else wl<2 * (13 - i)>();
        if constexpr (i == 0) { m32z(qb, wb[i], xf[i]); m32z(qa, wa[i], xf[i]); } else { m32(qb, wb[i], xf[i]); m32(qa, wa[i], xf[i]); }
        if constexpr (i < 7) { dsr<(i + 7) * 1024>(wb[i + 7], pb); dsr<(i + 7) * 1024>(wa[i + 7], pa); }
        if constexpr (i == 7) stats();
      });
    }
    nop<11>();
    pin(qa); pin(qb);
    put_tile(lds + S_QP + (w ^ 2) * 4096 + x.vl16, qb);
    bar();
    q_finish(x, qa, cq, mean, rstd);
    ask_slots(x, 1);
    kv_wait(x, 1);
    ask_kv(x, 1, kf);
  }
}

DI void phase_att(Ctx& x, const int L, f32x16 (&kf)[3], f32x16 (&g2)[3]) {
  const int w = x.w, l = x.l, h = l >> 5, r = l & 31;
  const int th = x.m >> 3, hp = x.m & 7, tl = w & 1, kh = w >> 1, tt = 4 * x.c + 2 * th + tl;
  const unsigned kvs = OFF_KV + L * SZ_KV + 16 * w * 4096;
  if (L == 0) bar();
  unsigned kma = S_KM + 16; asm volatile("" : "+s"(kma));
  const float kmx = *(const float*)(lds + kma);
  u32x4 q[4][2];
#pragma unroll
  for (int k = 0; k < 4; ++k) {
    q[k][0] = ((const u32x4*)(lds + S_QF + (w ^ k) * 2048 + x.vl16))[0];
    q[k][1] = ((const u32x4*)(lds + S_QF + (w ^ k) * 2048 + x.vl16))[64];
  }
  bool safe = true;
  {
#pragma unroll
    for (int k = 0; k < 4; ++k) {
      const float mrow = bflo(q[k][1][2]) * kmx + 0.01f;
      safe = safe && (!h || mrow <= M_LIMIT);
      if (h) q[k][1][2] = cvtpk(-mrow, 0.f);
    }
  }
  const bool fast = __all(safe);
  f32x16 own;
  if (fast) {
    unsigned so_kv = kvs + 3 * 4096, so_w = OFF_WGU + (x.m * 196 + 2 * w) * KB, so_l = 2 * w * KB, spin = 1u << 16, fvo = (l & 15) * 4096;
    Two ab = base_wo(x);
    const Two wd = base_wd(x);
    asm volatile(
      ".macro GRP s, pa, pb\n\t"
      "v_exp_f32 v200, v[\\s]\n\tv_exp_f32 v201, v[\\s+1]\n\tv_cvt_pk_bf16_f32 v[\\pa], v202, v203\n\t"
      "v_exp_f32 v202, v[\\s+2]\n\tv_exp_f32 v203, v[\\s+3]\n\tv_cvt_pk_bf16_f32 v[\\pb], v200, v201\n\t"
      ".endm\n\t"
      ".macro MEM kind, i, ld\n\t"
      ".if \\kind == 1\n\t"
      "buffer_load_dwordx4 v[128+16*\\ld+4*\\i:128+16*\\ld+4*\\i+3], %[vo], %[rs], %[skv] offen offset:1024*\\i sc1\n\t"
      ".elseif \\kind == 5\n\t"
      "ds_write_b128 v205, v[4*\\i:4*\\i+3] offset:1024*\\i\n\t"
      ".elseif \\kind == 6\n\t"
      "ds_write_b128 v205, v[4:7] offset:1024\n\t"
      ".elseif \\kind == 7\n\t"
      "ds_write_b128 v205, v[12:15] offset:3072\n\t"
      ".elseif \\kind == 2\n\t"
      "buffer_load_dword v206, v205, %[rs], 0 offen offset:3072 sc1\n\t"
      ".elseif \\kind == 3\n\t"
      ".if att_n < 16 || att_n >= 56\n\t"
      ".if att_n < 102\n\t"
      ".if att_n < 16\n\t.set att_r, 184+4*att_n\n\t.else\n\t.set att_r, 4*(att_n-56)\n\t.endif\n\t"
      ".if att_n & 1\n\t"
      "buffer_load_dwordx4 a[att_r:att_r+3], %[vo], %[rs], %[ab1] offen offset:1024*((att_n>>1)&3) sc1\n\t"
      ".else\n\t"
      "buffer_load_dwordx4 a[att_r:att_r+3], %[vo], %[rs], %[ab0] offen offset:1024*((att_n>>1)&3) sc1\n\t"
      ".endif\n\t"
      ".if att_n == 15\n\t"
      "s_mov_b32 %[ab0], %[wd0]\n\ts_mov_b32 %[ab1], %[wd1]\n\t"
      ".elseif (att_n & 7) == 7\n\t"
      "s_add_u32 %[ab0], %[ab0], 4096\n\ts_add_u32 %[ab1], %[ab1], 4096\n\t"
      ".endif\n\t"
      ".endif\n\t"
      ".elseif att_n < 44\n\t"
      ".if att_n & 1\n\t"
      "buffer_load_dwordx4 %[vo], %[rs], %[sw] offen offset:1024 sc1 lds\n\t"
      "s_add_u32 %[sw], %[sw], 8192\n\ts_add_u32 m0, m0, 8192\n\t"
      ".else\n\t"
      "buffer_load_dwordx4 %[vo], %[rs], %[sw] offen sc1 lds\n\t"
      ".endif\n\t"
      ".else\n\t"
      "buffer_load_dwordx4 v[208+4*(att_n-44):208+4*(att_n-44)+3], %[vo], %[rs], %[sw] offen offset:1024*(att_n&1) sc1\n\t"
      ".if att_n & 1\n\ts_add_u32 %[sw], %[sw], 8192\n\t.endif\n\t"
      ".endif\n\t"
      ".set att_n, att_n+1\n\t"
      ".endif\n\t"
      ".endm\n\t"
      ".macro STEP sc, sn, k, nk, pk, tk, nt, pt, ld, m0k, m0i, m1k, m2k, m2i, m3k\n\t"
      "v_mfma_f32_32x32x16_bf16 v[\\sn:\\sn+15], v[128+16*\\nt:128+16*\\nt+3], v[64+8*\\nk:64+8*\\nk+3], 0\n\t"
      "MEM \\m0k, \\m0i, \\ld\n\t"
      "GRP \\sc, 199, 192\n\t"
      "v_mfma_f32_32x32x16_bf16 v[\\sn:\\sn+15], v[132+16*\\nt:132+16*\\nt+3], v[68+8*\\nk:68+8*\\nk+3], v[\\sn:\\sn+15]\n\t"
      "MEM \\m1k, 0, 0\n\t"
      "GRP \\sc+4, 193, 194\n\t"
      "v_mfma_f32_32x32x16_bf16 v[16*\\pk:16*\\pk+15], v[140+16*\\pt:140+16*\\pt+3], v[196:199], v[16*\\pk:16*\\pk+15]\n\t"
      "MEM \\m2k, \\m2i, \\ld\n\t"
      "GRP \\sc+8, 195, 196\n\t"
      "v_mfma_f32_32x32x16_bf16 v[16*\\k:16*\\k+15], v[136+16*\\tk:136+16*\\tk+3], v[192:195], v[16*\\k:16*\\k+15]\n\t"
      "MEM \\m3k, 0, 0\n\t"
      "GRP \\sc+12, 197, 198\n\t"
      ".endm\n\t"
      ".macro TILE tk, nx, pv, on, wait, p, fl=0\n\t"
      "STEP 96, 112, 0, 1, 3, \\tk, \\tk, \\pv, \\pv, \\p, 0, \\p, \\p, 0, \\fl\n\t"
      "STEP 112, 96, 1, 2, 0, \\tk, \\tk, \\tk, \\pv, \\on, 0, \\p, \\on, 1, 0\n\t"
      "STEP 96, 112, 2, 3, 1, \\tk, \\tk, \\tk, \\pv, \\on, 2, \\p, \\on, 3, 0\n\t"
      ".if \\on\n\ts_add_u32 %[skv], %[skv], 4096\n\t.endif\n\t"
      "s_waitcnt vmcnt(\\wait)\n\t"
      "STEP 112, 96, 3, 0, 2, \\tk, \\nx, \\tk, \\pv, \\p, 0, \\p, \\p, 0, 0\n\t"
      ".endm\n\t"
      ".set att_n, 0\n\t"
      "s_mov_b32 m0, %[sl]\n\t"
      "v_mov_b32 v196, 0\n\tv_mov_b32 v197, 0\n\tv_mov_b32 v198, 0\n\tv_mov_b32 v199, 0\n\tv_mov_b32 v202, 0\n\tv_mov_b32 v203, 0\n\t"
      "v_mov_b32 v188, 0\n\tv_mov_b32 v189, 0\n\tv_mov_b32 v190, 0\n\tv_mov_b32 v191, 0\n\t"
      "v_mfma_f32_32x32x16_bf16 v[0:15], v[196:199], v[196:199], 0\n\t"
      "v_mfma_f32_32x32x16_bf16 v[16:31], v[196:199], v[196:199], 0\n\t"
      "v_mfma_f32_32x32x16_bf16 v[32:47], v[196:199], v[196:199], 0\n\t"
      "v_mfma_f32_32x32x16_bf16 v[48:63], v[196:199], v[196:199], 0\n\t"
      "s_waitcnt vmcnt(8)\n\t"
      "v_mfma_f32_32x32x16_bf16 v[96:111], v[128:131], v[64:67], 0\n\t"
      "v_mfma_f32_32x32x16_bf16 v[96:111], v[132:135], v[68:71], v[96:111]\n\t"
      "s_nop 11\n\t"
      "TILE 0, 1, 3, 1, 8, 0\n\t"
      "TILE 1, 2, 0, 1, 8, 0\n\tTILE 2, 3, 1, 1, 9, 0, 2\n\t"
      "s_waitcnt vmcnt(4)\n\t"
      "1:\n\t"
      "v_cmp_gt_u32 vcc, 16, v206\n\t"
      "s_and_b64 vcc, exec, vcc\n\t"
      "s_cbranch_vccz 2f\n\t"
      "s_sleep 24\n\t"
      "buffer_load_dword v206, v205, %[rs], 0 offen offset:3072 sc1\n\t"
      "s_sub_u32 %[spin], %[spin], 1\n\t"
      "s_waitcnt vmcnt(0)\n\t"
      "s_cbranch_scc0 1b\n\t"
      "2:\n\t"
      "s_barrier\n\t"
      "TILE 3, 0, 2, 1, 14, 3\n\t"
      "TILE 0, 1, 3, 1, 21, 3\n\tTILE 1, 2, 0, 1, 24, 3\n\tTILE 2, 3, 1, 1, 24, 3\n\tTILE 3, 0, 2, 1, 24, 3\n\t"
      "TILE 0, 1, 3, 1, 24, 3\n\tTILE 1, 2, 0, 1, 24, 3\n\tTILE 2, 3, 1, 1, 24, 3\n\tTILE 3, 0, 2, 1, 24, 3\n\t"
      "TILE 0, 1, 3, 1, 24, 3\n\tTILE 1, 2, 0, 0, 20, 3\n\tTILE 2, 3, 1, 0, 16, 3\n\t"
      "v_add_u32 v205, %[d1], v204\n\tv_add_u32 v206, %[d2], v204\n\t"
      "STEP 96, 112, 0, 1, 3, 3, 3, 2, 0, 3, 0, 3, 3, 0, 3\n\t"
      "STEP 112, 96, 1, 2, 0, 3, 3, 3, 0, 3, 0, 3, 0, 0, 0\n\t"
      "STEP 96, 112, 2, 3, 1, 3, 3, 3, 0, 5, 0, 6, 5, 2, 7\n\t"
      "ds_write_b128 v206, v[16:19]\n\t"
      "GRP 112, 199, 192\n\t"
      "ds_write_b128 v206, v[20:23] offset:1024\n\t"
      "GRP 116, 193, 194\n\t"
      "v_mfma_f32_32x32x16_bf16 v[32:47], v[188:191], v[196:199], v[32:47]\n\t"
      "ds_write_b128 v206, v[24:27] offset:2048\n\t"
      "GRP 120, 195, 196\n\t"
      "v_mfma_f32_32x32x16_bf16 v[48:63], v[184:187], v[192:195], v[48:63]\n\t"
      "ds_write_b128 v206, v[28:31] offset:3072\n\t"
      "GRP 124, 197, 198\n\t"
      "v_add_u32 v200, %[d3], v204\n\t"
      "v_cvt_pk_bf16_f32 v199, v202, v203\n\t"
      "s_nop 1\n\t"
      "v_mfma_f32_32x32x16_bf16 v[48:63], v[188:191], v[196:199], v[48:63]\n\t"
      "ds_write_b128 v200, v[32:35]\n\tds_write_b128 v200, v[36:39] offset:1024\n\tds_write_b128 v200, v[40:43] offset:2048\n\tds_write_b128 v200, v[44:47] offset:3072\n\t"
      "s_nop 7\n\t"
      ".purgem GRP\n\t.purgem MEM\n\t.purgem STEP\n\t.purgem TILE"
      : "={v[48:63]}"(own), "+{v[128:143]}"(kf[0]), "+{v[144:159]}"(kf[1]), "+{v[160:175]}"(kf[2]),
        "={v[208:223]}"(g2[0]), "={v[224:239]}"(g2[1]), "={v[240:255]}"(g2[2]),
        [skv] "+s"(so_kv), [sw] "+s"(so_w), [ab0] "+s"(ab.s0), [ab1] "+s"(ab.s1), [spin] "+s"(spin), "+{v205}"(fvo)
      : "{v[64:67]}"(q[1][0]), "{v[68:71]}"(q[1][1]), "{v[72:75]}"(q[2][0]), "{v[76:79]}"(q[2][1]),
        "{v[80:83]}"(q[3][0]), "{v[84:87]}"(q[3][1]), "{v[88:91]}"(q[0][0]), "{v[92:95]}"(q[0][1]),
        [vo] "{v204}"(x.vl16), [d1] "s"(SCR + (w ^ 1) * 3 * 4096), [d2] "s"(SCR + ((w ^ 2) * 3 + 1) * 4096), [d3] "s"(SCR + ((w ^ 3) * 3 + 2) * 4096), [rs] "s"(x.rs), [sl] "s"(so_l), [wd0] "s"(wd.s0), [wd1] "s"(wd.s1)
      : "memory", "scc", "vcc",
        "v0","v1","v2","v3","v4","v5","v6","v7","v8","v9","v10","v11","v12","v13","v14","v15","v16","v17","v18","v19","v20","v21","v22","v23",
        "v24","v25","v26","v27","v28","v29","v30","v31","v32","v33","v34","v35","v36","v37","v38","v39","v40","v41","v42","v43","v44","v45","v46","v47",
        "v96","v97","v98","v99","v100","v101","v102","v103","v104","v105","v106","v107","v108","v109","v110","v111",
        "v112","v113","v114","v115","v116","v117","v118","v119","v120","v121","v122","v123","v124","v125","v126","v127",
        "v176","v177","v178","v179","v180","v181","v182","v183","v184","v185","v186","v187","v188","v189","v190","v191",
        "v192","v193","v194","v195","v196","v197","v198","v199","v200","v201","v202","v203","v206", ACLOB);
  } else {
    f32x16 o[4];
    wv<0>();
    pin(kf[0]); pin(kf[1]); pin(kf[2]);
    float mx[4];
#pragma unroll
    for (int k = 0; k < 4; ++k) {
      mx[k] = -3.0e38f; if (h) q[k][1][2] = 0; pin(q[k][1]);
#pragma unroll
      for (int i = 0; i < 16; ++i) o[k][i] = 0.f;
    }
#pragma nounroll
    for (unsigned spin = 0; spin < (1u << 16); ++spin) {
      float v;
      ld1(v, (l & 15) * 4096, x.rs, OFF_FLAG + FL_W(0)); wv<0>(); pin(v);
      if (__all(__float_as_uint(v) >= 16u)) break;
      __builtin_amdgcn_s_sleep(24);
    }
    pf_wo(x);
    pf_wgu1(x);
    sfor<0, 12>([&](auto j) {
      u32x4 t;
      ld4<1024 * (j & 1)>(t, x.vl16, x.rs, OFF_WGU + (x.m * 196 + 112 + 8 * (j >> 1) + 2 * w) * KB); wv<0>(); pin(t);
#pragma unroll
      for (int i = 0; i < 4; ++i) g2[j >> 2][4 * (j & 3) + i] = __uint_as_float(t[i]);
    });
    pf_wd<0, 46>(x, base_wd(x));
    for (int tk = 0; tk < 16; ++tk) {
      u32x4 kf[4];
      sfor<0, 4>([&](auto i) { ld4<i * 1024>(kf[i], x.vl16, x.rs, kvs + tk * 4096); });
      wv<0>();
      sfor<0, 4>([&](auto k) {
        f32x16 s;
        nop<1>();
        m32z(s, kf[0], q[k][0]); m32(s, kf[1], q[k][1]);
        nop<11>(); pin(s);
        float tm = s[0];
#pragma unroll
        for (int i = 1; i < 16; ++i) tm = fmaxf(tm, s[i]);
        tm = max32(tm);
        const float mn = fmaxf(mx[k], tm), al = __builtin_amdgcn_exp2f(mx[k] - mn);
        mx[k] = mn;
#pragma unroll
        for (int i = 0; i < 16; ++i) o[k][i] *= al;
        u32x4 p0, p1;
#pragma unroll
        for (int d = 0; d < 4; ++d) {
          p0[d] = cvtpk(__builtin_amdgcn_exp2f(s[2 * d] - mn), __builtin_amdgcn_exp2f(s[2 * d + 1] - mn));
          p1[d] = cvtpk(__builtin_amdgcn_exp2f(s[8 + 2 * d] - mn), __builtin_amdgcn_exp2f(s[9 + 2 * d] - mn));
        }
        pin(p0); pin(p1); pin(o[k]);
        nop<1>();
        m32(o[k], kf[2], p0); m32(o[k], kf[3], p1);
        nop<11>(); pin(o[k]);
      });
    }
    bar();
    float* mm = (float*)(lds + SCR);
#pragma unroll
    for (int k = 0; k < 4; ++k) if (h == 0) mm[((w ^ k) * 4 + w) * 32 + r] = mx[k];
    bar();
#pragma unroll
    for (int k = 0; k < 4; ++k) {
      const float* p = mm + (w ^ k) * 128 + r;
      const float g = fmaxf(fmaxf(p[0], p[32]), fmaxf(p[64], p[96])), f = __builtin_amdgcn_exp2f(mx[k] - g);
#pragma unroll
      for (int i = 0; i < 16; ++i) o[k][i] *= f;
    }
    bar();
    reduce_put(x, o);
    own = o[0];
  }
  reduce_get(x, own);
  pin(own);
  {
    auto lr = __builtin_amdgcn_permlane32_swap(__float_as_uint(own[12]), __float_as_uint(own[12]), false, false);
    const float inv = __builtin_amdgcn_rcpf(__uint_as_float(lr[1]));
    u32x4 a, b;
#pragma unroll
    for (int d = 0; d < 4; ++d) { a[d] = cvtpk(own[2 * d] * inv, own[2 * d + 1] * inv); b[d] = cvtpk(own[8 + 2 * d] * inv, own[9 + 2 * d] * inv); }
    if (h) { b[2] = 0; b[3] = 0; }
    const unsigned so = OFF_O + L * SZ_O + (tt * 32 + 2 * (2 * hp + kh)) * KB;
    st4<0>(a, x.vl16, x.rs, so); st4<1024>(b, x.vl16, x.rs, so);
  }
  wv<0>();
  signal(x, FL_O(L, x.c, th), 1);
  if (L == 0 && th == 0) {
    if (x.c == hp) count(x, FL_W2, 1);
  }
}

template <bool FINAL> DI void res_epilogue(Ctx& x, const f32x16& acc, const u32x4 (&xr)[2], unsigned Xo, unsigned STo, int tt, int nt, char* out = nullptr) {
  const int h = x.l >> 5, r = x.l & 31;
  u32x4 y[2];
#pragma unroll
  for (int d = 0; d < 8; ++d) y[d >> 2][d & 3] = cvtpk(acc[2 * d] + bflo(xr[d >> 2][d & 3]), acc[2 * d + 1] + bfhi(xr[d >> 2][d & 3]));
  if constexpr (FINAL) {
#pragma unroll
    for (int s = 0; s < 2; ++s) {
      auto a = __builtin_amdgcn_permlane32_swap(y[s][0], y[s][2], false, false);
      auto b = __builtin_amdgcn_permlane32_swap(y[s][1], y[s][3], false, false);
      y[s] = u32x4{a[0], b[0], a[1], b[1]};
    }
    const unsigned vo = r * (D * 2) + h * 16, so = tt * 32 * D * 2 + nt * 64;
    const rsrc_t ro = mkrs(out, S * D * 2);
    st4p<0>(y[0], vo, ro, so); st4p<32>(y[1], vo, ro, so);
  } else {
    const unsigned so = Xo + (tt * 28 + 2 * nt) * KB;
    st4<0>(y[0], x.vl16, x.rs, so); st4<1024>(y[1], x.vl16, x.rs, so);
    float s = 0.f, q = 0.f, s1 = 0.f, q1 = 0.f;
#pragma unroll
    for (int d = 0; d < 4; ++d) {
      s = dot2(y[0][d], 0x3f803f80u, s); q = dot2(y[0][d], y[0][d], q); s1 = dot2(y[1][d], 0x3f803f80u, s1); q1 = dot2(y[1][d], y[1][d], q1);
    }
    s = sum32(s + s1); q = sum32(q + q1);
    if (h == 0) st2<0>(u32x2{__float_as_uint(s), __float_as_uint(q)}, r * 8, x.rs, STo + (nt * 2048 + tt * 32) * 8);
  }
}

DI void phase_oproj(Ctx& x, const int L, const Args& a, const f32x16 (&g2)[3]) {
  const int w = x.w;
  if (x.m < 14) {
    const int th = x.m / 7, ct = x.m % 7, tt0 = 4 * x.c + 2 * th, tt = tt0 + (w >> 1), nt = 2 * ct + (w & 1);
    u32x2 x0[4];
    if (L == 0) {
      const rsrc_t rx = mkrs(a.x, S * D * 2);
      sfor<0, 4>([&](auto i) { ld2i<16 * i>(x0[i], (x.l & 31) * 896 + (x.l >> 5) * 8, rx, tt * 32 * 896 + nt * 64); });
    } else {
      sfor<0, 4>([&](auto i) { ld2u<(i >> 1) * 1024 + (i & 1) * 8>(x0[i], x.vl16, x.rs, OFF_X + 2 * SZ_X + (tt * 28 + 2 * nt) * KB); });
    }
    await(x, FL_O(L, x.c, th), 8);
    u32x4 b[8][2];
    const unsigned s0 = OFF_O + L * SZ_O + (tt * 32 + 8 * w) * KB, s1 = OFF_O + L * SZ_O + ((tt0 + ((w >> 1) ^ 1)) * 32 + 8 * w) * KB;
    auto ld = [&](auto i) { ld4<(i & 3) * 1024>(b[i][0], x.vl16, x.rs, s0 + (i >> 2) * 4096); ld4<(i & 3) * 1024>(b[i][1], x.vl16, x.rs, s1 + (i >> 2) * 4096); };
    constexpr int WN = OPWN;
    sfor<0, WN>(ld);
    f32x16 acc[4];
    sfor<0, 8>([&](auto i) {
      constexpr int last = (i + WN - 1 < 7 ? i + WN - 1 : 7);
      wv<2 * (last - i) + 1>();
      m32a<A_WO + 8 * i, i == 0>(acc[0], b[i][0]); m32a<A_WO + 8 * i + 4, i == 0>(acc[1], b[i][0]);
      wv<2 * (last - i)>();
      m32a<A_WO + 8 * i, i == 0>(acc[2], b[i][1]); m32a<A_WO + 8 * i + 4, i == 0>(acc[3], b[i][1]);
      if constexpr (i + WN < 8) ld(std::integral_constant<int, i + WN>{});
    });
    nop<11>();
    sfor<0, 4>([&](auto k) { pin(acc[k]); pin(x0[k]); });
    reduce_put(x, acc); reduce_get(x, acc[0]);
    pin(acc[0]);
    const u32x4 x2[2] = {u32x4{x0[0][0], x0[0][1], x0[1][0], x0[1][1]}, u32x4{x0[2][0], x0[2][1], x0[3][0], x0[3][1]}};
    res_epilogue<false>(x, acc[0], x2, OFF_X + (2 * L + 1) * SZ_X, OFF_ST + (2 * L + 1) * SZ_ST, tt, nt);
    wv<0>();
    signal(x, FL_X1(L, x.c), 1);
  }
#pragma unroll
  for (int j = 0; j < 12; ++j)
    *(f32x4*)(lds + (112 + 8 * (j >> 1) + 2 * w + (j & 1)) * KB + x.vl16) = f32x4{g2[j >> 2][4 * (j & 3)], g2[j >> 2][4 * (j & 3) + 1], g2[j >> 2][4 * (j & 3) + 2], g2[j >> 2][4 * (j & 3) + 3]};
}

constexpr int DWN = 7;
constexpr bool dn_half(int o) { return o == 4 || o == 5 || o == 14 || o == 15; }
struct DnSrc { unsigned s0, s1, l0, l1; };
DI DnSrc dn_src(const Ctx& x, int L) {
  const int tt0 = 4 * x.c + 2 * ((x.m / 7) & 1), tt = tt0 + (x.w >> 1), tto = tt0 + ((x.w >> 1) ^ 1);
  return DnSrc{OFF_H + L * SZ_H + (tt * 112 + 28 * x.w) * KB, OFF_H + L * SZ_H + (tto * 112 + 28 * x.w) * KB,
               OFF_HL + ((L * 64 + tt) * 16 + 4 * x.w) * KB, OFF_HL + ((L * 64 + tto) * 16 + 4 * x.w) * KB};
}
template <int O> DI void dn_ld(Ctx& x, const DnSrc& d, u32x4 (&b)[DWN][2]) {
  constexpr int i = dn_ks(O);
  if constexpr (dn_half(O)) {
    constexpr int imm = (O >= 14 ? 2048 : 0) + (O & 1) * 512;
    u32x2 lo, hi, lo1, hi1;
    ld2u<imm>(lo, x.l * 8, x.rs, d.l0); ld2u<imm + 1024>(hi, x.l * 8, x.rs, d.l0);
    ld2u<imm>(lo1, x.l * 8, x.rs, d.l1); ld2u<imm + 1024>(hi1, x.l * 8, x.rs, d.l1);
    b[O % DWN][0] = u32x4{lo[0], lo[1], hi[0], hi[1]}; b[O % DWN][1] = u32x4{lo1[0], lo1[1], hi1[0], hi1[1]};
  } else {
    ld4<(i & 3) * 1024>(b[O % DWN][0], x.vl16, x.rs, d.s0 + (i >> 2) * 4096); ld4<(i & 3) * 1024>(b[O % DWN][1], x.vl16, x.rs, d.s1 + (i >> 2) * 4096);
  }
}

DI void gu_tile(const f32x4 (&acc)[2][2], const u32x4 (&cs)[2], const float (&mean)[2], const float (&r2)[2], u32x2 (&hp)[2]) {
  const f32x4 cg = __builtin_bit_cast(f32x4, cs[0]), cu = __builtin_bit_cast(f32x4, cs[1]);
#pragma unroll
  for (int u = 0; u < 2; ++u) {
    const f32x2 nm = {-mean[u], -mean[u]}, rr = {r2[u], r2[u]};
#pragma unroll
    for (int e = 0; e < 2; ++e) {
      const f32x2 g = __builtin_elementwise_fma(nm, f32x2{cg[2 * e], cg[2 * e + 1]}, f32x2{acc[0][u][2 * e], acc[0][u][2 * e + 1]});
      const f32x2 v = __builtin_elementwise_fma(nm, f32x2{cu[2 * e], cu[2 * e + 1]}, f32x2{acc[1][u][2 * e], acc[1][u][2 * e + 1]});
      const f32x2 y = g * rr * v;
      hp[u][e] = cvtpk(y[0], y[1]);
    }
  }
}

DI void phase_gu(Ctx& x, const int L, u32x4 (&b)[DWN][2]) {
  const int w = x.w, l = x.l, g = l >> 4, c16 = l & 15, m = x.m, tt = 4 * x.c + w;
  if (L == 0 && wave0(x)) { unsigned v; ldflag(v, x.rs, OFF_FLAG + FL_W2); wv<0>(); recheck(x, v, FL_W2, 8); }
  await(x, FL_X1(L, x.c), 14);
  constexpr int NXV = 11;
  u32x4 xf[NXV][2]; f32x2 sp[2][4];
  const unsigned sx = OFF_X + (2 * L + 1) * SZ_X + tt * 28 * KB, st = OFF_ST + (2 * L + 1) * SZ_ST + tt * 32 * 8;
  auto ldx = [&](auto k, auto u) {
    if constexpr (k < NXV) ld4<(k & 1) * 2048 + 256 * u>(xf[k][u], x.vg16, x.rs, sx + (k >> 1) * 4096);
    else if constexpr (k < 14) lda<A_GX + 8 * (k - NXV) + 4 * u, (k & 1) * 2048 + 256 * u>(x.vg16, x.rs, sx + (k >> 1) * 4096);
    else { constexpr int i = k - 14; ld2<128 * u>(sp[u][i], (((i < 3 || g < 2) ? g + 4 * i : g) * 2048 + c16) * 8, x.rs, st); }
  };
  using I0 = std::integral_constant<int, 0>; using I1 = std::integral_constant<int, 1>;
  constexpr int XA = XAHEAD;
  sfor<0, XA>([&](auto k) { ldx(k, I0{}); ldx(k, I1{}); });
  const unsigned a0 = x.vl16, a1 = x.vl16 + 65536, a2 = x.vl16 + 131072;
  constexpr int DP = 6;
  u32x4 wf[8];
  auto rd = [&](auto ii) {
    constexpr int i = ii, slot = i >= 160 ? i - 160 : i;
    if constexpr (slot < 64) dsr<slot * 1024>(wf[i & 7], a0); else if constexpr (slot < 128) dsr<(slot - 64) * 1024>(wf[i & 7], a1); else dsr<(slot - 128) * 1024>(wf[i & 7], a2);
  };
  auto mm = [&](f32x4& c, const u32x4& a, auto k, auto u) {
    if constexpr (k < NXV) m16<k == 0>(c, a, xf[k][u]); else m16a<false, A_GX + 8 * (k - NXV) + 4 * u>(c, a);
  };
  const Two wd = base_wd(x);
  f32x4 aa[5][2][2], ch0, ch1;
  float mean[2], r2[2];
  sfor<0, DP>(rd);
  sfor<0, 140>([&](auto i) {
    constexpr int KS = i / 10, s = i % 10, t = s >> 1, mat = i & 1;
    using K = std::integral_constant<int, KS>;
    constexpr auto side = [](int k) { return (k >= 0 && k <= 3) || (k >= 5 && k <= 12); };
    constexpr auto all = [](int k) { return (k + XA < 14 ? 2 : 0) + ((k >= 0 && k <= 3) || (k >= 5 && k <= 9) ? 2 : 0) + (k == 5 ? 1 : 0) + (k >= 10 ? 3 : 0); };
    if constexpr (s == 0) {
      constexpr int cnt = [&] { int n = side(KS - XA) ? 1 : 0; for (int k = KS - XA + 1; k < KS; ++k) n += all(k); return n; }();
      wv<cnt>();
      if constexpr (KS == 5) asm volatile("s_barrier" ::: "memory");
    }
    if constexpr ((i & 1) == 0) { rd(std::integral_constant<int, i + DP>{}); rd(std::integral_constant<int, i + DP + 1>{}); wl<DP>(); }
    mm(aa[t][mat][0], wf[i & 7], K{}, I0{});
    if constexpr (s == 1 && KS + XA < 14) ldx(std::integral_constant<int, KS + XA>{}, I0{});
    if constexpr (s == 6 && KS + XA < 14) ldx(std::integral_constant<int, KS + XA>{}, I1{});
    if constexpr (KS <= 3 && s == 3) ldx(std::integral_constant<int, 14 + KS>{}, I0{});
    if constexpr (KS <= 3 && s == 8) ldx(std::integral_constant<int, 14 + KS>{}, I1{});
    if constexpr (KS == 5 && s == 3) asm volatile("buffer_load_dwordx4 %0, %2, %3, %4 offen sc1\n\tbuffer_load_dwordx4 %1, %2, %3, %4 offen offset:1024 sc1" : "=&v"(ch0), "=&v"(ch1) : "v"(x.vl16), "s"(x.rs), "s"(OFF_CSGU + m * 2048));
    if constexpr (KS == 11 && s == 2) { pin(ch0); pin(ch1); *(f32x4*)(lds + 40 * KB + x.vl16) = ch0 + ch1; }
    if constexpr (KS >= 5 && KS <= 9 && (s == 3 || s == 8) && !(KS == 5 && s == 3)) pf_wgu3<2 * (KS - 5) + (s == 8) - 1>(x);
    if constexpr (KS >= 10 && (s == 3 || s == 5 || s == 8) && 3 * (KS - 10) + (s == 3 ? 0 : (s == 5 ? 1 : 2)) < 10) {
      constexpr int f = 46 + 3 * (KS - 10) + (s == 3 ? 0 : (s == 5 ? 1 : 2));
      pf_wd<f, f + 1>(x, wd);
    }
    if constexpr ((KS == 10 || KS == 12) && s == 2) {
      constexpr int u = KS == 12;
      sfor<0, 4>([&](auto e) { pin(sp[u][e]); });
      float sm = sp[u][0][0] + sp[u][1][0] + sp[u][2][0], q = sp[u][0][1] + sp[u][1][1] + sp[u][2][1];
      if (g < 2) { sm += sp[u][3][0]; q += sp[u][3][1]; }
      sm = sum32(sum16(sm)); q = sum32(sum16(q));
      float rs; row_stats(sm, q, mean[u], rs); r2[u] = rs * rs;
      pin(mean[u]); pin(r2[u]);
    }
    mm(aa[t][mat][1], wf[i & 7], K{}, I1{}); SB();
  });
  u32x4 cs[7][2];
  const unsigned csa = 40 * KB + g * 16;
  sfor<0, 10>([&](auto i) { dsr<64 * i>(cs[i >> 1][i & 1], csa); });
  wv<10>();
  asm volatile("s_barrier" ::: "memory");
  const unsigned H = OFF_H + L * SZ_H + tt * 112 * KB;
  const int gp = (m & 1) ? 7 * m + 1 : 7 * m;
  auto pair = [&](const f32x4 (&t0)[2][2], const f32x4 (&t1)[2][2], auto lt) {
    u32x2 h0[2], h1[2];
    gu_tile(t0, cs[lt], mean, r2, h0); gu_tile(t1, cs[lt + 1], mean, r2, h1);
    st4<0>(u32x4{h0[0][0], h0[0][1], h1[0][0], h1[0][1]}, x.vg16, x.rs, H + (gp + lt - 1) * 1024);
    st4<256>(u32x4{h0[1][0], h0[1][1], h1[1][0], h1[1][1]}, x.vg16, x.rs, H + (gp + lt - 1) * 1024);
  };
  {
    wl<0>();
    sfor<0, 20>([&](auto i) { pin(aa[i >> 2][(i >> 1) & 1][i & 1]); });
    sfor<0, 10>([&](auto i) { pin(cs[i >> 1][i & 1]); });
    u32x2 hl[2];
    gu_tile(aa[0], cs[0], mean, r2, hl);
    const unsigned sl = OFF_HL + ((L * 64 + tt) * 16 + m) * KB;
    st2<0>(hl[0], x.vg16 >> 1, x.rs, sl); st2<128>(hl[1], x.vg16 >> 1, x.rs, sl);
    pair(aa[1], aa[2], std::integral_constant<int, 1>{});
    pair(aa[3], aa[4], std::integral_constant<int, 3>{});
  }
  SB();
  sfor<10, 14>([&](auto i) { dsr<64 * i>(cs[i >> 1][i & 1], csa); });
  f32x4 ab[2][2][2];
  unsigned fa;
  sfor<140, 196>([&](auto i) {
    constexpr int j = i - 140, KS = j / 4, t = (j % 4) >> 1, mat = i & 1;
    using K = std::integral_constant<int, KS>;
    if constexpr (j == 18) {
      if (L == 0) wv<6>(); else wv<0>();
      asm volatile("s_barrier" ::: "memory");
      count(x, FL_H(L, x.c, 0), 1);
      SB();
    }
    if constexpr ((i & 1) == 0) { if constexpr (i + DP < 196) { rd(std::integral_constant<int, i + DP>{}); rd(std::integral_constant<int, i + DP + 1>{}); wl<DP>(); } else wl<194 - i>(); }
    mm(ab[t][mat][0], wf[i & 7], K{}, I0{});
    if constexpr (j % 3 == 1 && j < 42) { if (L == 0) pf_wq<j / 3>(x); SB(); }
    if constexpr (j == 44) ldflag(fa, x.rs, OFF_FLAG + FL_H(L, x.c, 0));
    mm(ab[t][mat][1], wf[i & 7], K{}, I1{}); SB();
  });
  wv<0>();
  recheck(x, fa, FL_H(L, x.c, 0), 16);
  { const DnSrc d = dn_src(x, L); sfor<0, 3>([&](auto o) { dn_ld<o>(x, d, b); }); }
  sfor<0, 8>([&](auto i) { pin(ab[i >> 2][(i >> 1) & 1][i & 1]); });
  sfor<10, 14>([&](auto i) { pin(cs[i >> 1][i & 1]); });
  pair(ab[0], ab[1], std::integral_constant<int, 5>{});
}

DI void phase_down(Ctx& x, const int L, char* out, u32x4 (&b)[DWN][2]) {
  const int w = x.w;
  if (x.m >= 14) {
    wv<0>();
    sfor<0, 3>([&](auto o) { pin(b[o][0]); pin(b[o][1]); });
    signal(x, FL_H(L, x.c, 1), 1);
    return;
  }
  const int th = x.m / 7, ct = x.m % 7, tt = 4 * x.c + 2 * th + (w >> 1), nt = 2 * ct + (w & 1);
  constexpr int WN = DWN;
  u32x4 xr[2];
  const DnSrc d = dn_src(x, L);
  auto ld = [&](auto o) { dn_ld<o>(x, d, b); };
  sfor<3, WN>(ld);
  f32x16 acc[4];
  unsigned fb;
  sfor<0, 25>([&](auto i) {
    constexpr int last = i + WN - 1 < 27 ? i + WN - 1 : 27, ex = i <= 2 ? 2 : ((i >= 8 && i <= 13) ? 1 : ((i >= 21) ? 2 : 0));
    constexpr int later = [&] { int n = 0; for (int o = i + 1; o <= last; ++o) n += dn_half(o) ? 4 : 2; return n; }();
    wv<later + (dn_half(i) ? 2 : 1) + ex>();
    if constexpr (i == 3) signal(x, FL_H(L, x.c, 1), 1);
    m32a<A_WD + 8 * i, i == 0>(acc[0], b[i % WN][0]); m32a<A_WD + 8 * i + 4, i == 0>(acc[1], b[i % WN][0]);
    wv<later + ex>();
    m32a<A_WD + 8 * i, i == 0>(acc[2], b[i % WN][1]); m32a<A_WD + 8 * i + 4, i == 0>(acc[3], b[i % WN][1]);
    if constexpr (i == 7) ldflag(fb, x.rs, OFF_FLAG + FL_H(L, x.c, 1));
    if constexpr (i + WN == 20) { wv<12>(); recheck(x, fb, FL_H(L, x.c, 1), 16); }
    if constexpr (i + WN < 28) ld(std::integral_constant<int, i + WN>{});
    if constexpr (i + WN == 27) {
      const unsigned sr = OFF_X + (2 * L + 1) * SZ_X + (tt * 28 + 2 * nt) * KB;
      ld4<0>(xr[0], x.vl16, x.rs, sr); ld4<1024>(xr[1], x.vl16, x.rs, sr);
    }
  });
  wv<2>();
  sfor<1, 5>([&](auto t) {
    constexpr int k = t & 3;
    m32a<A_WD + 8 * 25 + 4 * (k & 1), false>(acc[k], b[25 % WN][k >> 1]);
    m32a<A_WD + 8 * 26 + 4 * (k & 1), false>(acc[k], b[26 % WN][k >> 1]);
    if constexpr (t > 1) { nop<(t == 2 ? 9 : 7)>(); red_put1<t - 1, 0>(x, acc[t - 1]); red_put1<t - 1, 1>(x, acc[t - 1]); }
    m32a<A_WD + 8 * 27 + 4 * (k & 1), false>(acc[k], b[27 % WN][k >> 1]);
    if constexpr (t > 1) { red_put1<t - 1, 2>(x, acc[t - 1]); red_put1<t - 1, 3>(x, acc[t - 1]); }
  });
  if (L == 0 && (x.m & 7) < 2) {
    ldl<4>(x.vl16, x.rs, OFF_WQ + ((16 + (x.m & 1)) * 28 + 7 * w) * KB, 7 * w * KB);
    ldl<3>(x.vl16, x.rs, OFF_WQ + ((16 + (x.m & 1)) * 28 + 7 * w + 4) * KB, (7 * w + 4) * KB);
  }
  nop<9>();
  pin(acc[0]);
  reduce_get(x, acc[0]);
  wv<0>();
  pin(xr[0]); pin(xr[1]);
  if (L > 0) {
    res_epilogue<true>(x, acc[0], xr, 0, 0, tt, nt, out);
  } else {
    res_epilogue<false>(x, acc[0], xr, OFF_X + 2 * SZ_X, OFF_ST + 2 * SZ_ST, tt, nt);
    wv<0>();
    signal(x, FL_X(x.c, th), 1);
  }
}

__global__ __launch_bounds__(256, 1) void k_main(Args) {
  typedef unsigned long long u64x8 __attribute__((ext_vector_type(8)));
  typedef unsigned long long u64x2 __attribute__((ext_vector_type(2)));
  u64x8 pin8; u64x2 pws;
  asm volatile("s_load_dwordx16 s[8:23], %4, 0x0\n\ts_load_dwordx4 s[24:27], %4, 0x40\n\t"
               "s_getpc_b64 s[4:5]\n.Lk_begin:\n\t"
               "s_mov_b32 s6, .Lk_end-.Lk_begin\n\ts_mov_b32 s7, 0x20000\n\ts_and_b32 s5, s5, 0xffff\n\t"
               "v_lshlrev_b32 v1, 4, %2\n\t"
               "s_bfe_u32 s28, %3, 0x20003\n\ts_lshl_b32 s28, s28, 12\n\t"
               "buffer_load_dwordx4 a[112:115], v1, s[4:7], s28 offen\n\t"
               "s_bfe_u32 s28, %3, 0x30004\n\ts_lshl_b32 s28, s28, 13\n\t"
               "buffer_load_dwordx4 a[112:115], v1, s[4:7], s28 offen\n\t"
               "s_add_u32 s28, s28, 4096\n\t"
               "buffer_load_dwordx4 a[112:115], v1, s[4:7], s28 offen\n\t"
               "s_waitcnt lgkmcnt(0)"
               : "={s[8:23]}"(pin8), "={s[24:27]}"(pws) : "v"(threadIdx.x), "{s2}"(blockIdx.x), "{s[0:1]}"(__builtin_amdgcn_kernarg_segment_ptr())
               : "s4", "s5", "s6", "s7", "s28", "v1", "scc", ACLOB);
  const Args a = {(const char*)pin8[0], (const char*)pin8[1], (const char*)pin8[2], (const char*)pin8[3], (const char*)pin8[4], (const char*)pin8[5],
                  (const char*)pin8[6], (const char*)pin8[7], (char*)pws[0], (char*)pws[1]};
  Ctx x;
  x.rs = mkrs(a.ws, WS_USED);
  x.w = __builtin_amdgcn_readfirstlane(threadIdx.x >> 6);
  asm volatile("s_nop 4" : "+s"(x.w));
  { const int b = blockIdx.x; x.c = 2 * (b & 7) + (b >> 7); x.m = (b >> 3) & 15; }
  f32x16 kf[3];
  { Ctx y = x; fresh(y); phase_pre(y, a, kf); }
  for (int L = 0;; ++L) {
    f32x16 g2[3];
    int Lo = L; asm volatile("" : "+s"(Lo));
    { Ctx y = x; fresh(y); phase_att(y, Lo, kf, g2); }
    { Ctx y = x; fresh(y); phase_oproj(y, Lo, a, g2); }
    { Ctx y = x; u32x4 b[DWN][2]; fresh(y); phase_gu(y, Lo, b); fresh(y); phase_down(y, Lo, a.out, b); }
    if (L == 1) break;
    { Ctx y = x; fresh(y); phase_front(y, kf); }
  }
  fresh(x);
  if (blockIdx.x == 0 && wave0(x)) {
    get_tag(x);
    if (x.l == 0) st2<0>(u32x2{x.tlo, x.thi}, 0, x.rs, OFF_TAG);
  }
  asm volatile(".Lk_end:");
}

extern "C" void kernel_launch(void* const* d_in, const int* in_sizes, int n_in, void* d_out, int out_size, void* d_ws, size_t ws_size,
                              hipStream_t stream) {
  Args a;
  a.x = (const char*)d_in[0]; a.wq = (const char*)d_in[1]; a.wk = (const char*)d_in[2]; a.wv = (const char*)d_in[3];
  a.wo = (const char*)d_in[4]; a.wg = (const char*)d_in[6]; a.wu = (const char*)d_in[7]; a.wd = (const char*)d_in[8];
  a.ws = (char*)d_ws; a.out = (char*)d_out;
  (void)hipFuncSetAttribute((const void*)k_main, hipFuncAttributeMaxDynamicSharedMemorySize, 160 * 1024);
  k_main<<<256, 256, 160 * 1024, stream>>>(a);
}
```

```cpp
#include <hip/hip_runtime.h>
#include <stdint.h>
#include <type_traits>

typedef float    f32x16 __attribute__((ext_vector_type(16)));
typedef float    f32x4  __attribute__((ext_vector_type(4)));
typedef float    f32x2  __attribute__((ext_vector_type(2)));
typedef unsigned u32x4  __attribute__((ext_vector_type(4)));
typedef unsigned u32x2  __attribute__((ext_vector_type(2)));
typedef __bf16   bf16x2 __attribute__((ext_vector_type(2)));
typedef __amdgpu_buffer_rsrc_t rsrc_t;
#define DI __device__ __forceinline__
#define GAS __attribute__((address_space(1)))
#define SB() __builtin_amdgcn_sched_barrier(0)

constexpr int S = 2048, D = 448, F = 1792;
constexpr unsigned KB = 1024;
constexpr unsigned OFF_FLAG = 0;
constexpr unsigned OFF_CSQ  = 64 * KB;
constexpr unsigned OFF_CSGU = OFF_CSQ + 4 * KB;
constexpr unsigned OFF_SLOT = OFF_CSGU + 32 * KB;
constexpr unsigned OFF_TAG  = OFF_SLOT + 32 * KB;
constexpr unsigned OFF_WQ   = 160 * KB;
constexpr unsigned OFF_WO   = OFF_WQ + 18 * 28 * KB;
constexpr unsigned OFF_WGU  = OFF_WO + 14 * 32 * KB;
constexpr unsigned OFF_WD   = OFF_WGU + 16 * 196 * KB;
constexpr unsigned SZ_X = 64 * 28 * KB, SZ_ST = 14 * 2048 * 8, SZ_O = 64 * 32 * KB, SZ_H = 64 * 112 * KB, SZ_KV = 64 * 4 * KB;
constexpr unsigned OFF_X    = OFF_WD + 14 * 112 * KB;
constexpr unsigned OFF_ST   = OFF_X + 4 * SZ_X;
constexpr unsigned OFF_O    = OFF_ST + 4 * SZ_ST;
constexpr unsigned OFF_H    = OFF_O + 2 * SZ_O;
constexpr unsigned OFF_HL   = OFF_H + 2 * SZ_H;
constexpr unsigned OFF_KV   = OFF_HL + 2 * 1024 * KB;
constexpr unsigned OFF_TS   = OFF_KV + 2 * SZ_KV;
constexpr unsigned WS_USED  = OFF_TS + 520 * KB;
DI constexpr unsigned FL_X(int c, int th)         { return (c * 16 + th) * 256u; }
DI constexpr unsigned FL_O(int L, int c, int th)  { return (c * 16 + 2 + L * 2 + th) * 256u; }
DI constexpr unsigned FL_X1(int L, int c)         { return (c * 16 + 6 + L) * 256u; }
DI constexpr unsigned FL_H(int L, int c, int p)   { return (c * 16 + 8 + L * 2 + p) * 256u; }
DI constexpr unsigned FL_W(int c)                 { return (c * 16 + 12) * 256u; }
constexpr unsigned FL_W2 = 13 * 256u;

#define XAHEAD 3
#define OPWN 5
constexpr float QSCALE = 0.18898223650461362f * 1.4426950408889634f;
constexpr float M_LIMIT = 48.0f;

template <int I, int N, class Fn> DI void sfor(Fn&& f) { if constexpr (I < N) { f(std::integral_constant<int, I>{}); sfor<I + 1, N>(f); } }

DI unsigned cvtpk(float lo, float hi) { f32x2 v = {lo, hi}; bf16x2 b = __builtin_convertvector(v, bf16x2); return __builtin_bit_cast(unsigned, b); }
DI float bflo(unsigned u) { return __uint_as_float(u << 16); }
DI float bfhi(unsigned u) { return __uint_as_float(u & 0xffff0000u); }
DI float dot2(unsigned a, unsigned b, float c) { return __builtin_amdgcn_fdot2_f32_bf16(__builtin_bit_cast(bf16x2, a), __builtin_bit_cast(bf16x2, b), c, false); }
DI float sum32(float v) { auto r = __builtin_amdgcn_permlane32_swap(__float_as_uint(v), __float_as_uint(v), false, false); return __uint_as_float(r[0]) + __uint_as_float(r[1]); }
DI float sum16(float v) { auto r = __builtin_amdgcn_permlane16_swap(__float_as_uint(v), __float_as_uint(v), false, false); return __uint_as_float(r[0]) + __uint_as_float(r[1]); }
DI float max32(float v) { auto r = __builtin_amdgcn_permlane32_swap(__float_as_uint(v), __float_as_uint(v), false, false); return fmaxf(__uint_as_float(r[0]), __uint_as_float(r[1])); }

DI unsigned umax16(unsigned v) {
  v = max(v, (unsigned)__builtin_amdgcn_update_dpp(0, (int)v, 0x128, 0xf, 0xf, false));
  v = max(v, (unsigned)__builtin_amdgcn_update_dpp(0, (int)v, 0x124, 0xf, 0xf, false));
  v = max(v, (unsigned)__builtin_amdgcn_update_dpp(0, (int)v, 0x122, 0xf, 0xf, false));
  v = max(v, (unsigned)__builtin_amdgcn_update_dpp(0, (int)v, 0x121, 0xf, 0xf, false));
  return v;
}
DI unsigned umax64(unsigned v) {
  v = umax16(v);
  auto a = __builtin_amdgcn_permlane16_swap(v, v, false, false); v = max(a[0], a[1]);
  auto b = __builtin_amdgcn_permlane32_swap(v, v, false, false); return max(b[0], b[1]);
}

typedef short s16x4 __attribute__((ext_vector_type(4)));
DI u32x2 trread(const char* p) { return __builtin_bit_cast(u32x2, __builtin_amdgcn_ds_read_tr16_b64_v4i16((__attribute__((address_space(3))) s16x4*)p)); }
DI u32x4 trfrag(const char* lo, const char* hi) { const u32x2 a = trread(lo), b = trread(hi); return u32x4{a[0], a[1], b[0], b[1]}; }
DI void row_stats(float s, float q, float& mean, float& rstd) {
  mean = s * (1.0f / D);
  const float var = fmaxf(q * (1.0f / D) - mean * mean, 0.f);
  rstd = __builtin_amdgcn_rsqf(var + 1e-5f);
}
DI float fragsum(const u32x4& f, float s) {
#pragma unroll
  for (int d = 0; d < 4; ++d) s = dot2(f[d], 0x3f803f80u, s);
  return s;
}
struct Args { const char *x, *wq, *wk, *wv, *wo, *wg, *wu, *wd; char* ws; char* out; };

#define ACLOB "a0","a1","a2","a3","a4","a5","a6","a7","a8","a9","a10","a11","a12","a13","a14","a15","a16","a17","a18","a19","a20","a21","a22","a23","a24","a25","a26","a27","a28","a29","a30","a31","a32","a33","a34","a35","a36","a37","a38","a39","a40","a41","a42","a43","a44","a45","a46","a47","a48","a49","a50","a51","a52","a53","a54","a55","a56","a57","a58","a59","a60","a61","a62","a63","a64","a65","a66","a67","a68","a69","a70","a71","a72","a73","a74","a75","a76","a77","a78","a79","a80","a81","a82","a83","a84","a85","a86","a87","a88","a89","a90","a91","a92","a93","a94","a95","a96","a97","a98","a99","a100","a101","a102","a103","a104","a105","a106","a107","a108","a109","a110","a111","a112","a113","a114","a115","a116","a117","a118","a119","a120","a121","a122","a123","a124","a125","a126","a127","a128","a129","a130","a131","a132","a133","a134","a135","a136","a137","a138","a139","a140","a141","a142","a143","a144","a145","a146","a147","a148","a149","a150","a151","a152","a153","a154","a155","a156","a157","a158","a159","a160","a161","a162","a163","a164","a165","a166","a167","a168","a169","a170","a171","a172","a173","a174","a175","a176","a177","a178","a179","a180","a181","a182","a183","a184","a185","a186","a187","a188","a189","a190","a191","a192","a193","a194","a195","a196","a197","a198","a199","a200","a201","a202","a203","a204","a205","a206","a207","a208","a209","a210","a211","a212","a213","a214","a215","a216","a217","a218","a219","a220","a221","a222","a223","a224","a225","a226","a227","a228","a229","a230","a231","a232","a233","a234","a235","a236","a237","a238","a239","a240","a241","a242","a243","a244","a245","a246","a247","a248","a249","a250","a251","a252","a253","a254","a255"

template <int IMM> DI void ld4(u32x4& d, unsigned vo, rsrc_t rs, unsigned so) {
  asm volatile("buffer_load_dwordx4 %0, %1, %2, %3 offen offset:%c4 sc1" : "=v"(d) : "v"(vo), "s"(rs), "s"(so), "i"(IMM));
}
template <int IMM> DI void ld2(f32x2& d, unsigned vo, rsrc_t rs, unsigned so) {
  asm volatile("buffer_load_dwordx2 %0, %1, %2, %3 offen offset:%c4 sc1" : "=v"(d) : "v"(vo), "s"(rs), "s"(so), "i"(IMM));
}
template <int IMM> DI void ld4i(u32x4& d, unsigned vo, rsrc_t rs, unsigned so) {
  asm volatile("buffer_load_dwordx4 %0, %1, %2, %3 offen offset:%c4" : "=v"(d) : "v"(vo), "s"(rs), "s"(so), "i"(IMM));
}
template <int IMM> DI void ld2i(u32x2& d, unsigned vo, rsrc_t rs, unsigned so) {
  asm volatile("buffer_load_dwordx2 %0, %1, %2, %3 offen offset:%c4" : "=v"(d) : "v"(vo), "s"(rs), "s"(so), "i"(IMM));
}
template <int IMM> DI void ld2u(u32x2& d, unsigned vo, rsrc_t rs, unsigned so) {
  asm volatile("buffer_load_dwordx2 %0, %1, %2, %3 offen offset:%c4 sc1" : "=v"(d) : "v"(vo), "s"(rs), "s"(so), "i"(IMM));
}
template <int IMM> DI void ld4f(f32x4& d, unsigned vo, rsrc_t rs, unsigned so) {
  asm volatile("buffer_load_dwordx4 %0, %1, %2, %3 offen offset:%c4 sc1" : "=v"(d) : "v"(vo), "s"(rs), "s"(so), "i"(IMM));
}
DI void ld1(float& d, unsigned vo, rsrc_t rs, unsigned so) {
  asm volatile("buffer_load_dword %0, %1, %2, %3 offen sc1" : "=v"(d) : "v"(vo), "s"(rs), "s"(so));
}
template <int AR, int IMM> DI void lda(unsigned vo, rsrc_t rs, unsigned so) {
  asm volatile("buffer_load_dwordx4 a[%c3:%c4], %0, %1, %2 offen offset:%c5 sc1" :: "v"(vo), "s"(rs), "s"(so), "i"(AR), "i"(AR + 3), "i"(IMM) : ACLOB);
}
template <int N> DI void ldl(unsigned vo, rsrc_t rs, unsigned so, unsigned lds) {
  asm volatile("s_mov_b32 m0, %3\n\ts_nop 0\n\tbuffer_load_dwordx4 %0, %1, %2 offen sc1 lds" :: "v"(vo), "s"(rs), "s"(so), "s"(lds) : "memory");
  if constexpr (N > 1) asm volatile("buffer_load_dwordx4 %0, %1, %2 offen offset:1024 sc1 lds" :: "v"(vo), "s"(rs), "s"(so) : "memory");
  if constexpr (N > 2) asm volatile("buffer_load_dwordx4 %0, %1, %2 offen offset:2048 sc1 lds" :: "v"(vo), "s"(rs), "s"(so) : "memory");
  if constexpr (N > 3) asm volatile("buffer_load_dwordx4 %0, %1, %2 offen offset:3072 sc1 lds" :: "v"(vo), "s"(rs), "s"(so) : "memory");
}
template <int IMM> DI void st4(const u32x4& d, unsigned vo, rsrc_t rs, unsigned so) {
  asm volatile("buffer_store_dwordx4 %0, %1, %2, %3 offen offset:%c4 sc1\n\ts_nop 1" :: "v"(d), "v"(vo), "s"(rs), "s"(so), "i"(IMM) : "memory");
}
template <int IMM> DI void st4p(const u32x4& d, unsigned vo, rsrc_t rs, unsigned so) {
  asm volatile("buffer_store_dwordx4 %0, %1, %2, %3 offen offset:%c4\n\ts_nop 1" :: "v"(d), "v"(vo), "s"(rs), "s"(so), "i"(IMM) : "memory");
}
template <int IMM> DI void st2(const u32x2& d, unsigned vo, rsrc_t rs, unsigned so) {
  asm volatile("buffer_store_dwordx2 %0, %1, %2, %3 offen offset:%c4 sc1" :: "v"(d), "v"(vo), "s"(rs), "s"(so), "i"(IMM) : "memory");
}
DI void st1(float d, unsigned vo, rsrc_t rs, unsigned so) {
  asm volatile("buffer_store_dword %0, %1, %2, %3 offen sc1" :: "v"(d), "v"(vo), "s"(rs), "s"(so) : "memory");
}
template <int N> DI void wv() { asm volatile("s_waitcnt vmcnt(%c0)" :: "i"(N) : "memory"); SB(); }
template <int N> DI void wl() { asm volatile("s_waitcnt lgkmcnt(%c0)" :: "i"(N) : "memory"); SB(); }
template <class T> DI void pin(T& x) { asm volatile("" : "+v"(x)); }
DI void bar() { asm volatile("s_waitcnt lgkmcnt(0)\n\ts_barrier" ::: "memory"); }
template <int N> DI void nop() { asm volatile("s_nop %c0" :: "i"(N)); SB(); }

template <int OFF> DI void trd(u32x2& r, unsigned a) { asm volatile("ds_read_b64_tr_b16 %0, %1 offset:%c2" : "=v"(r) : "v"(a), "i"(OFF)); }
template <int OFF> DI void dsr(u32x4& r, unsigned a) { asm volatile("ds_read_b128 %0, %1 offset:%c2" : "=v"(r) : "v"(a), "i"(OFF)); }

DI void m32(f32x16& c, const u32x4& a, const u32x4& b)  { asm volatile("v_mfma_f32_32x32x16_bf16 %0, %1, %2, %0" : "+v"(c) : "v"(a), "v"(b)); }
DI void m32z(f32x16& c, const u32x4& a, const u32x4& b) { asm volatile("v_mfma_f32_32x32x16_bf16 %0, %1, %2, 0" : "=&v"(c) : "v"(a), "v"(b)); }
template <int AR, bool Z> DI void m32a(f32x16& c, const u32x4& b) {
  if constexpr (Z) asm volatile("v_mfma_f32_32x32x16_bf16 %0, a[%c2:%c3], %1, 0" : "=&v"(c) : "v"(b), "i"(AR), "i"(AR + 3));
  else             asm volatile("v_mfma_f32_32x32x16_bf16 %0, a[%c2:%c3], %1, %0" : "+v"(c) : "v"(b), "i"(AR), "i"(AR + 3));
}
template <int AR, bool Z> DI void m32b(f32x16& c, const u32x4& a) {
  if constexpr (Z) asm volatile("v_mfma_f32_32x32x16_bf16 %0, %1, a[%c2:%c3], 0" : "=&v"(c) : "v"(a), "i"(AR), "i"(AR + 3));
  else             asm volatile("v_mfma_f32_32x32x16_bf16 %0, %1, a[%c2:%c3], %0" : "+v"(c) : "v"(a), "i"(AR), "i"(AR + 3));
}
template <bool Z> DI void m16(f32x4& c, const u32x4& a, const u32x4& b) {
  if constexpr (Z) asm volatile("v_mfma_f32_16x16x32_bf16 %0, %1, %2, 0" : "=&v"(c) : "v"(a), "v"(b));
  else             asm volatile("v_mfma_f32_16x16x32_bf16 %0, %1, %2, %0" : "+v"(c) : "v"(a), "v"(b));
}

template <bool Z, int AR> DI void m16a(f32x4& c, const u32x4& a) {
  if constexpr (Z) asm volatile("v_mfma_f32_16x16x32_bf16 %0, %1, a[%c2:%c3], 0" : "=&v"(c) : "v"(a), "i"(AR), "i"(AR + 3));
  else             asm volatile("v_mfma_f32_16x16x32_bf16 %0, %1, a[%c2:%c3], %0" : "+v"(c) : "v"(a), "i"(AR), "i"(AR + 3));
}
DI void ldflag(unsigned& v, rsrc_t rs, unsigned so) { asm volatile("buffer_load_dword %0, off, %1, %2 sc1" : "=v"(v) : "s"(rs), "s"(so)); }

struct Ctx {
  rsrc_t rs;
  unsigned tlo, thi;
  unsigned vl16;
  unsigned vg16;
  int w, l, c, m;
};
DI void fresh(Ctx& x) {
  asm volatile("" : "+s"(x.w), "+s"(x.c), "+s"(x.m));
  asm volatile("v_mbcnt_lo_u32_b32 %0, -1, 0\n\tv_mbcnt_hi_u32_b32 %0, -1, %0" : "=v"(x.l));
  x.vl16 = x.l * 16;
  const int g = x.l >> 4;
  x.vg16 = ((g >> 1) * 64 + (g & 1) * 32 + (x.l & 15)) * 16;
}
extern __shared__ __attribute__((aligned(16))) char lds[];
constexpr unsigned SCR = 112 * KB;

DI void pstamp(Ctx&) {}
DI void fstamp(Ctx&) {}
DI void stamp(Ctx&) {}
DI void stamp_flush(Ctx&) {}

DI void get_tag(Ctx& x) {
  unsigned lo, hi;
  asm volatile("v_accvgpr_read_b32 v255, a252\n\ts_nop 1\n\tv_readfirstlane_b32 %0, v255\n\tv_accvgpr_read_b32 v255, a253\n\ts_nop 1\n\tv_readfirstlane_b32 %1, v255\n\ts_nop 4"
               : "=s"(lo), "=s"(hi) :: "v255");
  const unsigned long long e = ((unsigned long long)hi << 32) | lo;
  unsigned long long n = e * 0x9E3779B97F4A7C15ull + 0x632BE59BD9B4E019ull;
  if (((n ^ e) >> 16) == 0) n += 0x10000;
  x.tlo = (unsigned)n; x.thi = (unsigned)(n >> 32);
}
DI bool wave0(const Ctx& x) { int w = x.w; asm volatile("" : "+s"(w)); return w == 0; }
DI void count(Ctx& x, unsigned fl, int n) {
  if (wave0(x)) {
    const unsigned long long mask = (1ull << n) - 1;
    unsigned long long keep;
    asm volatile("s_mov_b64 %0, exec\n\ts_mov_b64 exec, %5\n\tbuffer_atomic_add %1, %2, %3, %4 offen\n\ts_mov_b64 exec, %0"
                 : "=&s"(keep) : "v"(1u), "v"(x.l * 256), "s"(x.rs), "s"(OFF_FLAG + fl), "s"(mask) : "memory");
  }
}
DI void signal(Ctx& x, unsigned fl, int n) { bar(); count(x, fl, n); }
DI void await(Ctx& x, unsigned fl, unsigned target) {
  if (wave0(x)) {
#pragma nounroll
    for (unsigned spin = 0; spin < (1u << 18); ++spin) {
      unsigned v;
      asm volatile("buffer_load_dword %0, off, %1, %2 sc1\n\ts_waitcnt vmcnt(0)" : "=v"(v) : "s"(x.rs), "s"(OFF_FLAG + fl) : "memory");
      if ((unsigned)__builtin_amdgcn_readfirstlane(v) >= target) break;
      __builtin_amdgcn_s_sleep(1);
    }
  }
  bar();
}

DI void recheck(Ctx& x, unsigned v, unsigned fl, unsigned target) {
  pin(v);
#pragma nounroll
  for (unsigned spin = 0; (unsigned)__builtin_amdgcn_readfirstlane(v) < target && spin < (1u << 18); ++spin) {
    __builtin_amdgcn_s_sleep(2);
    asm volatile("buffer_load_dword %0, off, %1, %2 sc1\n\ts_waitcnt vmcnt(0)" : "=v"(v) : "s"(x.rs), "s"(OFF_FLAG + fl) : "memory");
  }
  SB();
}

DI void reduce_put(Ctx& x, const f32x16 (&acc)[4]) {
  sfor<1, 4>([&](auto k) {
    f32x4* p = (f32x4*)(lds + SCR + ((x.w ^ k) * 3 + k - 1) * 4096 + x.vl16);
#pragma unroll
    for (int q = 0; q < 4; ++q) p[q * 64] = f32x4{acc[k][4 * q], acc[k][4 * q + 1], acc[k][4 * q + 2], acc[k][4 * q + 3]};
  });
}
template <int K, int Q> DI void red_put1(Ctx& x, const f32x16& t) {
  asm volatile("ds_write_b128 %0, %1 offset:%c2" :: "v"(SCR + ((x.w ^ K) * 3 + K - 1) * 4096 + x.vl16),
               "v"(__builtin_shufflevector(t, t, 4 * Q, 4 * Q + 1, 4 * Q + 2, 4 * Q + 3)), "i"(Q * 1024) : "memory");
}
DI void reduce_get(Ctx& x, f32x16& own) {
  bar();
  const f32x4* p = (const f32x4*)(lds + SCR + x.w * 3 * 4096 + x.vl16);
#pragma unroll
  for (int k = 0; k < 3; ++k)
#pragma unroll
    for (int q = 0; q < 4; ++q) {
      f32x4 v = p[k * 256 + q * 64];
#pragma unroll
      for (int i = 0; i < 4; ++i) own[4 * q + i] += v[i];
    }
}

constexpr int A_WO = 184, A_WD = 0, A_GX = 224;
constexpr int dn_ks(int o) { return o < 20 ? 2 * ((o >> 1) % 5 + ((o >> 1) % 5 >= 2) + 7 * (o / 10)) + (o & 1) : 2 * (((o - 20) >> 1) * 4 - ((o - 20) >> 2) + 2) + (o & 1); }
static_assert(dn_ks(0) == 0 && dn_ks(4) == 6 && dn_ks(9) == 11 && dn_ks(10) == 14 && dn_ks(19) == 25 && dn_ks(20) == 4 && dn_ks(22) == 12 && dn_ks(24) == 18 && dn_ks(27) == 27, "");
struct Two { unsigned s0, s1; };
constexpr int SL_WQ = 56;
template <int J> DI void pf_wq(Ctx& x) {
  ldl<1>(x.vl16, x.rs, OFF_WQ + ((x.m & 7) * 56 + 14 * x.w + J) * KB, (SL_WQ + 14 * x.w + J) * KB);
}
DI Two base_wo(const Ctx& x) { const int ct = x.m % 7;
  return Two{OFF_WO + ((2 * ct + (x.w & 1)) * 32 + 8 * x.w) * KB, OFF_WO + ((2 * ct + ((x.w & 1) ^ 1)) * 32 + 8 * x.w) * KB}; }
DI void pf_wo(Ctx& x) {
  const Two b = base_wo(x);
  sfor<0, 8>([&](auto i) {
    lda<A_WO + 8 * i, (i & 3) * 1024>(x.vl16, x.rs, b.s0 + (i >> 2) * 4096);
    lda<A_WO + 8 * i + 4, (i & 3) * 1024>(x.vl16, x.rs, b.s1 + (i >> 2) * 4096);
  });
}
DI Two base_wd(const Ctx& x) { const int ct = x.m % 7;
  return Two{OFF_WD + ((2 * ct + (x.w & 1)) * 112 + 28 * x.w) * KB, OFF_WD + ((2 * ct + ((x.w & 1) ^ 1)) * 112 + 28 * x.w) * KB}; }
template <int I0, int I1> DI void pf_wd(Ctx& x, const Two& b) {
  sfor<I0, I1>([&](auto f) {
    constexpr int i = f >> 1;
    lda<A_WD + 4 * f, (i & 3) * 1024>(x.vl16, x.rs, ((f & 1) ? b.s1 : b.s0) + (i >> 2) * 4096);
  });
}
DI void pf_wgu1(Ctx& x) {
  for (int t = 0; t < 14; ++t) ldl<2>(x.vl16, x.rs, OFF_WGU + (x.m * 196 + 8 * t + 2 * x.w) * KB, (8 * t + 2 * x.w) * KB);
}
template <int J> DI void pf_wgu3(Ctx& x) {
  ldl<1>(x.vl16, x.rs, OFF_WGU + (x.m * 196 + 160 + 9 * x.w + J) * KB, (9 * x.w + J) * KB);
}

constexpr unsigned S_QP = SCR, S_KP = SCR + 16384, S_QF = SCR + 24576, S_ST = SCR + 32768, S_CS = SCR + 33792, S_KM = SCR + 36864, S_TR = SCR + 40960;
DI unsigned kv_finish(Ctx& x, const float (&raw)[16], const f32x4 (&cs)[4], float mean, float rstd, bool isv, unsigned so, int slot) {
  const int h = x.l >> 5, r = x.l & 31;
  u32x4 y[2];
#pragma unroll
  for (int d = 0; d < 8; ++d)
    y[d >> 2][d & 3] = cvtpk((raw[2 * d] - mean * cs[d >> 1][2 * (d & 1)]) * rstd, (raw[2 * d + 1] - mean * cs[d >> 1][2 * (d & 1) + 1]) * rstd);
  if (h) { y[1][2] = 0; y[1][3] = 0; }
  unsigned nb = 0;
  if (!isv) {
    float n2 = 0.f;
#pragma unroll
    for (int d = 0; d < 8; ++d) n2 = dot2(y[d >> 2][d & 3], y[d >> 2][d & 3], n2);
    n2 = sum32(n2);
    nb = umax64(n2 < 3.0e38f ? __float_as_uint(n2) : 0x7f800000u);
    nb = cvtpk(1.01f * __builtin_amdgcn_sqrtf(__uint_as_float(nb)), 0.f);
    if (h) y[1][2] = 0x3f80u;
  } else {
    if (h) y[1][2] = 0x3f80u;
    char* im = lds + S_TR + slot * 2048;
#pragma unroll
    for (int a = 0; a < 4; ++a) *(u32x2*)(im + r * 64 + 16 * a + 8 * h) = u32x2{y[a >> 1][2 * (a & 1)], y[a >> 1][2 * (a & 1) + 1]};
    asm volatile("s_waitcnt lgkmcnt(0)" ::: "memory");
    const char* tb = im + (4 * h + ((x.l >> 2) & 3)) * 64 + ((x.l >> 4) & 1) * 32 + (x.l & 3) * 8;
    y[0] = trfrag(tb, tb + 512); y[1] = trfrag(tb + 1024, tb + 1536);
  }
  st4<0>(y[0], x.vl16, x.rs, so); st4<1024>(y[1], x.vl16, x.rs, so);
  return nb;
}
DI void kv_publish(Ctx& x, int L, int p, unsigned km) {
  get_tag(x);
  st2<0>(u32x2{(x.tlo & 0xffff0000u) | km, x.thi}, x.l < 16 ? x.l * 1024 : ~0u, x.rs, OFF_SLOT + (L * 2048 + p) * 8);
}
DI void q_finish(Ctx& x, const f32x16& qa, const f32x4 (&cq)[4], float mean, float rstd) {
  const int h = x.l >> 5;
  u32x4 qf[2];
  const f32x4* p = (const f32x4*)(lds + S_QP + x.w * 4096 + x.vl16);
  const float sc = rstd * QSCALE;
#pragma unroll
  for (int q = 0; q < 4; ++q) {
    const f32x4 o = p[q * 64];
    float v[4];
#pragma unroll
    for (int i = 0; i < 4; ++i) v[i] = (qa[4 * q + i] + o[i] - mean * cq[q][i]) * sc;
    qf[q >> 1][2 * (q & 1)] = cvtpk(v[0], v[1]); qf[q >> 1][2 * (q & 1) + 1] = cvtpk(v[2], v[3]);
  }
  if (h) { qf[1][2] = 0; qf[1][3] = 0; }
  float qn2 = 0.f, qn3 = 0.f;
#pragma unroll
  for (int d = 0; d < 4; ++d) { qn2 = dot2(qf[0][d], qf[0][d], qn2); qn3 = dot2(qf[1][d], qf[1][d], qn3); }
  qn2 = sum32(qn2 + qn3);
  if (h) qf[1][2] = cvtpk(1.01f * __builtin_amdgcn_sqrtf(qn2), 0.f);
  ((u32x4*)(lds + S_QF + x.w * 2048 + x.vl16))[0] = qf[0];
  ((u32x4*)(lds + S_QF + x.w * 2048 + x.vl16))[64] = qf[1];
}
DI void put_tile(char* p, const f32x16& t) {
#pragma unroll
  for (int q = 0; q < 4; ++q) ((f32x4*)p)[q * 64] = f32x4{t[4 * q], t[4 * q + 1], t[4 * q + 2], t[4 * q + 3]};
}

constexpr unsigned IMG_Q = 0, IMG_T = 56 * KB;
DI rsrc_t mkrs(const char* p, unsigned n) { return __builtin_amdgcn_make_buffer_rsrc((char*)p, 0, n, 0x00020000); }

DI void ask_slots(Ctx& x, int L) {
  if (wave0(x)) asm volatile("buffer_load_dwordx4 a[248:251], %0, %1, %2 offen sc1" :: "v"(x.vl16), "s"(x.rs), "s"(OFF_SLOT + (L * 16 + x.c) * 1024) : ACLOB);
}
DI void kv_wait(Ctx& x, int L) {
  wv<0>();
  if (wave0(x)) {
    get_tag(x);
    unsigned s0, s1, s2, s3;
#pragma nounroll
    for (unsigned spin = 0; spin < (1u << 18); ++spin) {
      asm volatile("v_accvgpr_read_b32 %0, a248\n\tv_accvgpr_read_b32 %1, a249\n\tv_accvgpr_read_b32 %2, a250\n\tv_accvgpr_read_b32 %3, a251" : "=v"(s0), "=v"(s1), "=v"(s2), "=v"(s3));
      if (__all(s1 == x.thi && s3 == x.thi && (((s0 ^ x.tlo) | (s2 ^ x.tlo)) >> 16) == 0)) break;
      __builtin_amdgcn_s_sleep(2);
      ask_slots(x, L); wv<0>();
    }
    const unsigned km = umax64(max(s0 << 16, s2 << 16));
    if (x.l == 0) ((unsigned*)(lds + S_KM))[4] = km;
  }
  bar();
}
DI void ask_kv(Ctx& x, int L, f32x16 (&kf)[3]) {
  const unsigned kvs = OFF_KV + L * SZ_KV + 16 * x.w * 4096;
  asm volatile(".irp i,0,1,2,3\n\tbuffer_load_dwordx4 v[128+4*\\i:131+4*\\i], %3, %4, %5 offen offset:1024*\\i sc1\n\t.endr\n\t"
               ".irp i,0,1,2,3\n\tbuffer_load_dwordx4 v[144+4*\\i:147+4*\\i], %3, %4, %6 offen offset:1024*\\i sc1\n\t.endr\n\t"
               ".irp i,0,1,2,3\n\tbuffer_load_dwordx4 v[160+4*\\i:163+4*\\i], %3, %4, %7 offen offset:1024*\\i sc1\n\t.endr"
               : "={v[128:143]}"(kf[0]), "={v[144:159]}"(kf[1]), "={v[160:175]}"(kf[2]) : "{v204}"(x.vl16), "s"(x.rs), "s"(kvs), "s"(kvs + 4096), "s"(kvs + 8192));
}

DI void phase_pre(Ctx& x, const Args& a, f32x16 (&kf)[3]) {
  const int w = x.w, l = x.l, h = l >> 5, r = l & 31;
  const int th = x.m >> 3, hp = x.m & 7, tl = w & 1, kh = w >> 1, tt = 4 * x.c + 2 * th + tl, wi = 2 * x.c + th;
  const bool kvp = hp < 2;
  { unsigned long long pc; unsigned len;
    asm volatile("s_getpc_b64 %0\n.Lk_begin:\n\ts_mov_b32 %1, .Lk_end-.Lk_begin" : "=s"(pc), "=s"(len));
    const rsrc_t rc = mkrs((const char*)pc, len);
    asm volatile("buffer_load_dwordx4 a[112:115], %0, %1, %2 offen\n\tbuffer_load_dwordx4 a[112:115], %0, %1, %2 offen offset:1024"
                 :: "v"(x.vl16), "s"(rc), "s"((unsigned)(hp * 8 + 2 * w) * KB) : ACLOB); }
  if (x.m == 2) asm volatile("buffer_load_dword a116, %0, %1, 0 offen sc1" :: "v"(l < 16 ? OFF_FLAG + (x.c * 16 + l) * 256 : OFF_SLOT + ((l >> 3) & 1) * 16384 + x.c * 1024 + (l & 7) * 128), "s"(x.rs) : ACLOB);
  u32x4 xf[14];
  { const rsrc_t rx = mkrs(a.x, S * D * 2);
    sfor<0, 14>([&](auto i) { ld4i<32 * i>(xf[i], r * 896 + h * 16, rx, tt * 32 * 896 + kh * 448); }); }
  asm volatile("buffer_load_dwordx2 a[252:253], off, %0, %1 sc1" :: "s"(x.rs), "s"(OFF_TAG) : ACLOB);
  pstamp(x);
  const int task = wi * 6 + hp - 2;
  const rsrc_t rq = mkrs(a.wq, D * D * 2);
  auto ask_q = [&](auto i) {
    ldl<1>((8 * w + (l >> 3)) * 896 + (l & 7) * 16, rq, hp * 112 + i * 32 * 896, IMG_Q + (4 * i + w) * KB);
  };
  auto ask_t = [&](const char* tp, unsigned tn, unsigned ts, unsigned tb) {
    const rsrc_t rt = mkrs(tp, tn);
    const unsigned vo = (8 * w + (l >> 3)) * ts + (l & 7) * 16;
    sfor<0, 7>([&](auto i) { ldl<1>(vo, rt, tb + i * 32 * ts, IMG_T + (4 * i + w) * KB); });
  };
  const int tco = ((l >> 4) & 1) * 32 + (l & 3) * 8;
  const int sro = 4 * h + ((l >> 2) & 3);
  if (kvp) ask_t(hp ? a.wv : a.wk, D * 56, 128, 0);
  else if (task < 112) ask_t(task < 56 ? a.wg : a.wu, D * F * 2, F * 2, (task & 1) * 224 * F * 2 + ((task % 56) >> 1) * 128);
  else if (task < 168) ask_t(a.wd, D * F * 2, 896, ((task - 112) / 7) * 224 * 896 + ((task - 112) % 7) * 128);
  else ask_t(a.wo, task < 182 ? D * D * 2 : 0, 896, ((task - 168) / 7) * 224 * 896 + ((task - 168) % 7) * 128);
  wv<8>();
  {
    float s = 0.f, q = 0.f, s1 = 0.f, q1 = 0.f;
    sfor<0, 14>([&](auto i) {
      if (!kvp) ask_q(i);
      pin(xf[i]);
      s = dot2(xf[i][0], 0x3f803f80u, s); q = dot2(xf[i][0], xf[i][0], q); s1 = dot2(xf[i][1], 0x3f803f80u, s1); q1 = dot2(xf[i][1], xf[i][1], q1);
      s = dot2(xf[i][2], 0x3f803f80u, s); q = dot2(xf[i][2], xf[i][2], q); s1 = dot2(xf[i][3], 0x3f803f80u, s1); q1 = dot2(xf[i][3], xf[i][3], q1);
      pin(s); pin(q); pin(s1); pin(q1);
    });
    s = sum32(s + s1); q = sum32(q + q1);
    if (h == 0) ((f32x2*)(lds + S_ST))[w * 32 + r] = f32x2{s, q};
  }
  if (kvp) wv<0>(); else wv<14>();
  bar();
  stamp(x);
  if (!kvp) {
    if (task < 112) {
      const int g = l >> 4, mat = task >= 56, gt = 4 * ((task % 56) >> 1) + w, m = gt / 7, j = gt % 7, lt = (m & 1) ? j : (j == 6 ? 0 : j + 1);
      const char* tb = lds + IMG_T + (16 * (g >> 1) + 4 * (g & 1) + ((l >> 2) & 3)) * 128 + 32 * w + (l & 3) * 8;
      const unsigned st = lt < 5 ? 10 * KB : 4 * KB, so = OFF_WGU + (m * 196 + (lt < 5 ? lt * 2 + mat : 140 + (lt - 5) * 2 + mat)) * KB + (task & 1) * 7 * st;
      u32x4 f[7];
      sfor<0, 7>([&](auto i) { f[i] = trfrag(tb + i * 4096, tb + i * 4096 + 1024); });
      float cs = 0.f;
      sfor<0, 7>([&](auto i) { st4<0>(f[i], x.vl16, x.rs, so + i * st); cs = fragsum(f[i], cs); });
      cs = sum32(sum16(cs));
      st1(cs, l < 16 ? l * 4 : ~0u, x.rs, OFF_CSGU + m * 2048 + (task & 1) * 1024 + (lt * 2 + mat) * 64);
    } else if (task < 168) {
      const int nt = 2 * ((task - 112) % 7) + (w & 1), rb = (task - 112) / 7;
      const char* tb = lds + IMG_T + sro * 128 + 64 * (w & 1) + tco;
      const unsigned so = OFF_WD + (nt * 112 + 28 * (rb >> 1)) * KB, sa = so + (rb & 1) * 10 * KB, sb = so + (20 + (rb & 1) * 4) * KB;
      u32x4 f[7];
      if (w < 2) {
        sfor<0, 7>([&](auto i) { const char* p = tb + (32 * (i >> 1) + 8 * (i & 1)) * 128; f[i] = trfrag(p, p + 2048); });
        sfor<0, 7>([&](auto i) { st4<0>(f[i], x.vl16, x.rs, i < 4 ? sa + i * KB : (i < 6 ? sb + (i - 4) * KB : sa + 4 * KB)); });
      } else {
        sfor<0, 7>([&](auto i) { const char* p = tb + (32 * ((i + 7) >> 1) + 8 * ((i + 7) & 1)) * 128; f[i] = trfrag(p, p + 2048); });
        sfor<0, 7>([&](auto i) { st4<0>(f[i], x.vl16, x.rs, i < 5 ? sa + (5 + i) * KB : sb + (i - 3) * KB); });
      }
      st1(0.f, ~0u, x.rs, 0);
    } else {
      const int nt = 2 * ((task - 168) % 7) + (w & 1);
      const char* tb = lds + IMG_T + (112 * (w >> 1) + sro) * 128 + 64 * (w & 1) + tco;
      const unsigned vo = task < 182 ? x.vl16 : ~0u - 4096;
      u32x4 f[8];
      sfor<0, 8>([&](auto i) { const char* p = tb + (28 * (i >> 1) + 16 * (i & 1)) * 128; f[i] = trfrag(p, p + 1024); if ((i & 1) && h) { f[i][2] = 0; f[i][3] = 0; } });
      sfor<0, 8>([&](auto i) { st4<(i & 3) * 1024>(f[i], vo, x.rs, OFF_WO + (nt * 32 + 16 * ((task - 168) / 7) + 8 * (w >> 1) + (i & ~3)) * KB); });
    }
  }
  pstamp(x);
  if (!kvp) { wv<8>(); bar(); }
  stamp(x);
  float mean, rstd;
  { const f32x2 p0 = ((const f32x2*)(lds + S_ST))[tl * 32 + r], p1 = ((const f32x2*)(lds + S_ST))[(tl + 2) * 32 + r];
    row_stats(p0[0] + p1[0], p0[1] + p1[1], mean, rstd); }
  float* csw = (float*)(lds + S_CS);
  const int tro = 8 * h + ((l >> 2) & 3);
  u32x4 ones = {0x3f803f80u, 0x3f803f80u, 0x3f803f80u, 0x3f803f80u};
  pin(ones);
  if (kvp) {
    f32x16 kv, cs;
    const unsigned tb = IMG_T + (224 * kh + tro) * 56 + tco;
    u32x2 f[14][2];
    auto rd = [&](auto i) { trd<i * 896>(f[i][0], tb); trd<i * 896 + 224>(f[i][1], tb); };
    sfor<0, 7>(rd);
    sfor<0, 14>([&](auto i) {
      if constexpr (i + 7 < 14) { wl<12>(); rd(std::integral_constant<int, i + 7>{}); } else wl<2 * (13 - i)>();
      pin(f[i][0]); pin(f[i][1]);
      u32x4 g = {f[i][0][0], f[i][0][1], f[i][1][0], f[i][1][1]};
      pin(g); nop<0>();
      if constexpr (i == 0) { m32z(kv, g, xf[i]); m32z(cs, g, ones); } else { m32(kv, g, xf[i]); m32(cs, g, ones); }
    });
    nop<11>(); pin(kv); pin(cs);
    if (r == 0) sfor<0, 4>([&](auto q) { *(f32x4*)(csw + (w * 3 + 2) * 32 + 8 * q + 4 * h) = f32x4{cs[4 * q], cs[4 * q + 1], cs[4 * q + 2], cs[4 * q + 3]}; });
    pstamp(x);
    if (kh) put_tile(lds + S_KP + tl * 4096 + x.vl16, kv);
    bar();
    if (!kh) {
      if (x.m == 0 && w == 0) st1(0.f, l < 16 ? l * 256 : ~0u, x.rs, OFF_FLAG + x.c * 4096);
      float raw[16]; f32x4 c4[4];
      sfor<0, 4>([&](auto q) {
        const f32x4 o = ((const f32x4*)(lds + S_KP + tl * 4096 + x.vl16))[q * 64];
        c4[q] = *(const f32x4*)(csw + (w * 3 + 2) * 32 + 8 * q + 4 * h) + *(const f32x4*)(csw + ((w + 2) * 3 + 2) * 32 + 8 * q + 4 * h);
#pragma unroll
        for (int i = 0; i < 4; ++i) raw[4 * q + i] = kv[4 * q + i] + o[i];
      });
      const unsigned km = kv_finish(x, raw, c4, mean, rstd, hp == 1, OFF_KV + (tt * 4 + 2 * hp) * KB, tl);
      pstamp(x);
      wv<0>();
      kv_publish(x, 0, 2 * tt + hp, km);
      if (tt == 0 && r == 0) sfor<0, 4>([&](auto q) { st4<32 * q>(__builtin_bit_cast(u32x4, c4[q]), h * 16, x.rs, OFF_CSQ + (16 + hp) * 128); });
    } else {
      sfor<0, 28>([&](auto i) { ldl<1>((16 * tl + 8 * (i & 1) + (l >> 3)) * 896 + (l & 7) * 16, rq, hp * 112 + (i >> 1) * 32 * 896, IMG_Q + (4 * (i >> 1) + 2 * tl + (i & 1)) * KB); });
      pstamp(x);
      wv<0>();
    }
    bar();
    pstamp(x);
  } else { pstamp(x); pstamp(x); pstamp(x); }
  f32x16 qa;
  {
    f32x16 q1, q2, cs;
    const unsigned t1 = IMG_Q + (224 * kh + tro) * 128 + tco + 56 * tl, t2 = IMG_Q + (224 * kh + tro) * 128 + tco + 56 * (tl ^ 1);
    u32x2 f[14][4];
    auto rd = [&](auto i) { trd<i * 2048>(f[i][0], t1); trd<i * 2048 + 512>(f[i][1], t1); trd<i * 2048>(f[i][2], t2); trd<i * 2048 + 512>(f[i][3], t2); };
    wl<0>();
    sfor<0, 3>(rd);
    sfor<0, 14>([&](auto i) {
      if constexpr (i + 3 < 14) { wl<8>(); rd(std::integral_constant<int, i + 3>{}); } else wl<4 * (13 - i)>();
      sfor<0, 4>([&](auto e) { pin(f[i][e]); });
      u32x4 g1 = {f[i][0][0], f[i][0][1], f[i][1][0], f[i][1][1]}, g2 = {f[i][2][0], f[i][2][1], f[i][3][0], f[i][3][1]};
      pin(g1); pin(g2); nop<0>();
      if constexpr (i == 0) { m32z(q1, g1, xf[i]); m32z(q2, g2, xf[i]); m32z(cs, g1, ones); } else { m32(q1, g1, xf[i]); m32(q2, g2, xf[i]); m32(cs, g1, ones); }
      if constexpr (i == 10) { ask_slots(x, 0); SB(); }
    });
    nop<11>(); pin(q1); pin(q2); pin(cs);
    pstamp(x);
    if (r == 0) sfor<0, 4>([&](auto q) { *(f32x4*)(csw + (w * 3) * 32 + 8 * q + 4 * h) = f32x4{cs[4 * q], cs[4 * q + 1], cs[4 * q + 2], cs[4 * q + 3]}; });
    if (kh == tl) { put_tile(lds + S_QP + (w ^ 2) * 4096 + x.vl16, q2); qa = q1; } else { put_tile(lds + S_QP + (w ^ 2) * 4096 + x.vl16, q1); qa = q2; }
  }
  if (th == 0 && x.c < 14) {
    const int f = 4 * x.c + w, hd = f >= 28, ks = f - 28 * hd;
    const char* tb = lds + IMG_Q + (16 * ks + sro) * 128 + tco + 56 * hd;
    st4<0>(trfrag(tb, tb + 1024), x.vl16, x.rs, OFF_WQ + (hp * 56 + f) * KB);
  }
  if (kvp && th == 1 && x.c < 7) {
    const char* tb = lds + IMG_T + (16 * (4 * x.c + w) + sro) * 56 + tco;
    st4<0>(trfrag(tb, tb + 448), x.vl16, x.rs, OFF_WQ + ((16 + hp) * 28 + 4 * x.c + w) * KB);
  }
  pstamp(x);
  kv_wait(x, 0);
  count(x, FL_W(x.c), 1);
  ask_kv(x, 0, kf);
  stamp(x);
  const bool own = x.c == hp && th == 0;
  {
    f32x4 cq[4];
    sfor<0, 4>([&](auto q) {
      cq[q] = *(const f32x4*)(csw + (kh * 3) * 32 + 8 * q + 4 * h) + *(const f32x4*)(csw + ((kh + 2) * 3) * 32 + 8 * q + 4 * h);
      if (own && tl == 0 && r == 0) st4<32 * q>(__builtin_bit_cast(u32x4, cq[q]), h * 16, x.rs, OFF_CSQ + (2 * hp + kh) * 128);
    });
    q_finish(x, qa, cq, mean, rstd);
  }
  pstamp(x);
}

DI void phase_front(Ctx& x, f32x16 (&kf)[3]) {
  const int w = x.w, l = x.l, h = l >> 5, r = l & 31;
  const int th = x.m >> 3, hp = x.m & 7, tl = w & 1, kh = w >> 1, tt = 4 * x.c + 2 * th + tl;
  const unsigned KV = OFF_KV + SZ_KV;
  {
    f32x4 cq[4], ck[4];
    sfor<0, 4>([&](auto q) { ld4f<32 * q>(cq[q], h * 16, x.rs, OFF_CSQ + (2 * hp + kh) * 128); ld4f<32 * q>(ck[q], h * 16, x.rs, OFF_CSQ + (16 + (hp & 1)) * 128); });
    wv<0>();
    await(x, FL_X(x.c, th), 7);
    stamp(x);
    u32x4 xf[14]; f32x2 sp[7];
    {
      const unsigned so = OFF_X + 2 * SZ_X + (tt * 28 + 14 * kh) * KB;
      sfor<0, 7>([&](auto i) { ld4<(i & 3) * 1024>(xf[i], x.vl16, x.rs, so + (i >> 2) * 4096); });
      sfor<0, 7>([&](auto i) { ld2<0>(sp[i], (h * 7 * 2048 + r) * 8, x.rs, OFF_ST + 2 * SZ_ST + (i * 2048 + tt * 32) * 8); });
      sfor<7, 14>([&](auto i) { ld4<(i & 3) * 1024>(xf[i], x.vl16, x.rs, so + (i >> 2) * 4096); });
    }
    float mean, rstd;
    auto stats = [&]() {
      sfor<0, 7>([&](auto i) { pin(sp[i]); });
      sfor<0, 4>([&](auto i) { pin(cq[i]); pin(ck[i]); });
      float s = 0.f, q = 0.f;
#pragma unroll
      for (int i = 0; i < 7; ++i) { s += sp[i][0]; q += sp[i][1]; }
      row_stats(sum32(s), sum32(q), mean, rstd);
      pin(mean); pin(rstd); SB();
    };
    f32x16 qa, qb;
    u32x4 wa[14], wb[14];
    const unsigned pa = x.vl16 + (SL_WQ + 28 * kh + 14 * kh) * KB, pb = x.vl16 + (SL_WQ + 28 * (kh ^ 1) + 14 * kh) * KB;
    if (hp < 2) {
      f32x16 kv;
      sfor<0, 14>([&](auto i) { dsr<i * 1024>(wa[i], x.vl16 + 14 * kh * KB); });
      sfor<0, 14>([&](auto i) {
        wv<(i < 7 ? 20 : 13) - i>(); wl<13 - i>();
        if constexpr (i == 0) m32z(kv, wa[i], xf[i]); else m32(kv, wa[i], xf[i]);
        if constexpr (i == 7) stats();
      });
      nop<11>(); pin(kv);
      if (kh) put_tile(lds + S_KP + tl * 4096 + x.vl16, kv);
      bar();
      unsigned km = 0;
      if (!kh) {
        float raw[16];
        sfor<0, 4>([&](auto q) {
          const f32x4 o = ((const f32x4*)(lds + S_KP + tl * 4096 + x.vl16))[q * 64];
#pragma unroll
          for (int i = 0; i < 4; ++i) raw[4 * q + i] = kv[4 * q + i] + o[i];
        });
        km = kv_finish(x, raw, ck, mean, rstd, hp == 1, KV + (tt * 4 + 2 * hp) * KB, tl);
      }
      wl<0>();
      sfor<0, 7>([&](auto i) { dsr<i * 1024>(wb[i], pb); dsr<i * 1024>(wa[i], pa); });
      sfor<0, 14>([&](auto i) {
        if constexpr (i < 7) wl<12>(); else wl<2 * (13 - i)>();
        if constexpr (i == 0) { m32z(qb, wb[i], xf[i]); m32z(qa, wa[i], xf[i]); } else { m32(qb, wb[i], xf[i]); m32(qa, wa[i], xf[i]); }
        if constexpr (i < 7) { dsr<(i + 7) * 1024>(wb[i + 7], pb); dsr<(i + 7) * 1024>(wa[i + 7], pa); }
        if constexpr (i == 8) { if (!kh) { wv<0>(); kv_publish(x, 1, 2 * tt + hp, km); } SB(); }
      });
    } else {
      sfor<0, 7>([&](auto i) { dsr<i * 1024>(wb[i], pb); dsr<i * 1024>(wa[i], pa); });
      sfor<0, 14>([&](auto i) {
        wv<(i < 7 ? 20 : 13) - i>();
        if constexpr (i < 7) wl<12>(); else wl<2 * (13 - i)>();
        if constexpr (i == 0) { m32z(qb, wb[i], xf[i]); m32z(qa, wa[i], xf[i]); } else { m32(qb, wb[i], xf[i]); m32(qa, wa[i], xf[i]); }
        if constexpr (i < 7) { dsr<(i + 7) * 1024>(wb[i + 7], pb); dsr<(i + 7) * 1024>(wa[i + 7], pa); }
        if constexpr (i == 7) stats();
      });
    }
    nop<11>();
    pin(qa); pin(qb);
    put_tile(lds + S_QP + (w ^ 2) * 4096 + x.vl16, qb);
    bar();
    q_finish(x, qa, cq, mean, rstd);
    stamp(x);
    ask_slots(x, 1);
    kv_wait(x, 1);
    ask_kv(x, 1, kf);
    stamp(x);
  }
}

DI void phase_att(Ctx& x, const int L, f32x16 (&kf)[3], f32x16 (&g2)[3]) {
  const int w = x.w, l = x.l, h = l >> 5, r = l & 31;
  const int th = x.m >> 3, hp = x.m & 7, tl = w & 1, kh = w >> 1, tt = 4 * x.c + 2 * th + tl;
  const unsigned kvs = OFF_KV + L * SZ_KV + 16 * w * 4096;
  stamp(x);
  if (L == 0) bar();
  stamp(x);
  unsigned kma = S_KM + 16; asm volatile("" : "+s"(kma));
  const float kmx = *(const float*)(lds + kma);
  u32x4 q[4][2];
#pragma unroll
  for (int k = 0; k < 4; ++k) {
    q[k][0] = ((const u32x4*)(lds + S_QF + (w ^ k) * 2048 + x.vl16))[0];
    q[k][1] = ((const u32x4*)(lds + S_QF + (w ^ k) * 2048 + x.vl16))[64];
  }
  bool safe = true;
  {
#pragma unroll
    for (int k = 0; k < 4; ++k) {
      const float mrow = bflo(q[k][1][2]) * kmx + 0.01f;
      safe = safe && (!h || mrow <= M_LIMIT);
      if (h) q[k][1][2] = cvtpk(-mrow, 0.f);
    }
  }
  const bool fast = __all(safe);
  stamp(x);
  f32x16 own;
  if (fast) {
    unsigned so_kv = kvs + 3 * 4096, so_w = OFF_WGU + (x.m * 196 + 2 * w) * KB, so_l = 2 * w * KB, spin = 1u << 16, fvo = (l & 15) * 4096;
    Two ab = base_wo(x);
    const Two wd = base_wd(x);
    asm volatile(
      ".macro GRP s, pa, pb\n\t"
      "v_exp_f32 v200, v[\\s]\n\tv_exp_f32 v201, v[\\s+1]\n\tv_cvt_pk_bf16_f32 v[\\pa], v202, v203\n\t"
      "v_exp_f32 v202, v[\\s+2]\n\tv_exp_f32 v203, v[\\s+3]\n\tv_cvt_pk_bf16_f32 v[\\pb], v200, v201\n\t"
      ".endm\n\t"
      ".macro MEM kind, i, ld\n\t"
      ".if \\kind == 1\n\t"
      "buffer_load_dwordx4 v[128+16*\\ld+4*\\i:128+16*\\ld+4*\\i+3], %[vo], %[rs], %[skv] offen offset:1024*\\i sc1\n\t"
      ".elseif \\kind == 5\n\t"
      "ds_write_b128 v205, v[4*\\i:4*\\i+3] offset:1024*\\i\n\t"
      ".elseif \\kind == 6\n\t"
      "ds_write_b128 v205, v[4:7] offset:1024\n\t"
      ".elseif \\kind == 7\n\t"
      "ds_write_b128 v205, v[12:15] offset:3072\n\t"
      ".elseif \\kind == 2\n\t"
      "buffer_load_dword v206, v205, %[rs], 0 offen offset:3072 sc1\n\t"
      ".elseif \\kind == 3\n\t"
      ".if att_n < 16 || att_n >= 56\n\t"
      ".if att_n < 102\n\t"
      ".if att_n < 16\n\t.set att_r, 184+4*att_n\n\t.else\n\t.set att_r, 4*(att_n-56)\n\t.endif\n\t"
      ".if att_n & 1\n\t"
      "buffer_load_dwordx4 a[att_r:att_r+3], %[vo], %[rs], %[ab1] offen offset:1024*((att_n>>1)&3) sc1\n\t"
      ".else\n\t"
      "buffer_load_dwordx4 a[att_r:att_r+3], %[vo], %[rs], %[ab0] offen offset:1024*((att_n>>1)&3) sc1\n\t"
      ".endif\n\t"
      ".if att_n == 15\n\t"
      "s_mov_b32 %[ab0], %[wd0]\n\ts_mov_b32 %[ab1], %[wd1]\n\t"
      ".elseif (att_n & 7) == 7\n\t"
      "s_add_u32 %[ab0], %[ab0], 4096\n\ts_add_u32 %[ab1], %[ab1], 4096\n\t"
      ".endif\n\t"
      ".endif\n\t"
      ".elseif att_n < 44\n\t"
      ".if att_n & 1\n\t"
      "buffer_load_dwordx4 %[vo], %[rs], %[sw] offen offset:1024 sc1 lds\n\t"
      "s_add_u32 %[sw], %[sw], 8192\n\ts_add_u32 m0, m0, 8192\n\t"
      ".else\n\t"
      "buffer_load_dwordx4 %[vo], %[rs], %[sw] offen sc1 lds\n\t"
      ".endif\n\t"
      ".else\n\t"
      "buffer_load_dwordx4 v[208+4*(att_n-44):208+4*(att_n-44)+3], %[vo], %[rs], %[sw] offen offset:1024*(att_n&1) sc1\n\t"
      ".if att_n & 1\n\ts_add_u32 %[sw], %[sw], 8192\n\t.endif\n\t"
      ".endif\n\t"
      ".set att_n, att_n+1\n\t"
      ".endif\n\t"
      ".endm\n\t"
      ".macro STEP sc, sn, k, nk, pk, tk, nt, pt, ld, m0k, m0i, m1k, m2k, m2i, m3k\n\t"
      "v_mfma_f32_32x32x16_bf16 v[\\sn:\\sn+15], v[128+16*\\nt:128+16*\\nt+3], v[64+8*\\nk:64+8*\\nk+3], 0\n\t"
      "MEM \\m0k, \\m0i, \\ld\n\t"
      "GRP \\sc, 199, 192\n\t"
      "v_mfma_f32_32x32x16_bf16 v[\\sn:\\sn+15], v[132+16*\\nt:132+16*\\nt+3], v[68+8*\\nk:68+8*\\nk+3], v[\\sn:\\sn+15]\n\t"
      "MEM \\m1k, 0, 0\n\t"
      "GRP \\sc+4, 193, 194\n\t"
      "v_mfma_f32_32x32x16_bf16 v[16*\\pk:16*\\pk+15], v[140+16*\\pt:140+16*\\pt+3], v[196:199], v[16*\\pk:16*\\pk+15]\n\t"
      "MEM \\m2k, \\m2i, \\ld\n\t"
      "GRP \\sc+8, 195, 196\n\t"
      "v_mfma_f32_32x32x16_bf16 v[16*\\k:16*\\k+15], v[136+16*\\tk:136+16*\\tk+3], v[192:195], v[16*\\k:16*\\k+15]\n\t"
      "MEM \\m3k, 0, 0\n\t"
      "GRP \\sc+12, 197, 198\n\t"
      ".endm\n\t"
      ".macro TILE tk, nx, pv, on, wait, p, fl=0\n\t"
      "STEP 96, 112, 0, 1, 3, \\tk, \\tk, \\pv, \\pv, \\p, 0, \\p, \\p, 0, \\fl\n\t"
      "STEP 112, 96, 1, 2, 0, \\tk, \\tk, \\tk, \\pv, \\on, 0, \\p, \\on, 1, 0\n\t"
      "STEP 96, 112, 2, 3, 1, \\tk, \\tk, \\tk, \\pv, \\on, 2, \\p, \\on, 3, 0\n\t"
      ".if \\on\n\ts_add_u32 %[skv], %[skv], 4096\n\t.endif\n\t"
      "s_waitcnt vmcnt(\\wait)\n\t"
      "STEP 112, 96, 3, 0, 2, \\tk, \\nx, \\tk, \\pv, \\p, 0, \\p, \\p, 0, 0\n\t"
      ".endm\n\t"
      ".set att_n, 0\n\t"
      "s_mov_b32 m0, %[sl]\n\t"
      "v_mov_b32 v196, 0\n\tv_mov_b32 v197, 0\n\tv_mov_b32 v198, 0\n\tv_mov_b32 v199, 0\n\tv_mov_b32 v202, 0\n\tv_mov_b32 v203, 0\n\t"
      "v_mov_b32 v188, 0\n\tv_mov_b32 v189, 0\n\tv_mov_b32 v190, 0\n\tv_mov_b32 v191, 0\n\t"
      "v_mfma_f32_32x32x16_bf16 v[0:15], v[196:199], v[196:199], 0\n\t"
      "v_mfma_f32_32x32x16_bf16 v[16:31], v[196:199], v[196:199], 0\n\t"
      "v_mfma_f32_32x32x16_bf16 v[32:47], v[196:199], v[196:199], 0\n\t"
      "v_mfma_f32_32x32x16_bf16 v[48:63], v[196:199], v[196:199], 0\n\t"
      "s_waitcnt vmcnt(8)\n\t"
      "v_mfma_f32_32x32x16_bf16 v[96:111], v[128:131], v[64:67], 0\n\t"
      "v_mfma_f32_32x32x16_bf16 v[96:111], v[132:135], v[68:71], v[96:111]\n\t"
      "s_nop 11\n\t"
      "TILE 0, 1, 3, 1, 8, 0\n\t"
      "TILE 1, 2, 0, 1, 8, 0\n\tTILE 2, 3, 1, 1, 9, 0, 2\n\t"
      "s_waitcnt vmcnt(4)\n\t"
      "1:\n\t"
      "v_cmp_gt_u32 vcc, 16, v206\n\t"
      "s_and_b64 vcc, exec, vcc\n\t"
      "s_cbranch_vccz 2f\n\t"
      "s_sleep 24\n\t"
      "buffer_load_dword v206, v205, %[rs], 0 offen offset:3072 sc1\n\t"
      "s_sub_u32 %[spin], %[spin], 1\n\t"
      "s_waitcnt vmcnt(0)\n\t"
      "s_cbranch_scc0 1b\n\t"
      "2:\n\t"
      "s_barrier\n\t"
      "TILE 3, 0, 2, 1, 14, 3\n\t"
      "TILE 0, 1, 3, 1, 21, 3\n\tTILE 1, 2, 0, 1, 24, 3\n\tTILE 2, 3, 1, 1, 24, 3\n\tTILE 3, 0, 2, 1, 24, 3\n\t"
      "TILE 0, 1, 3, 1, 24, 3\n\tTILE 1, 2, 0, 1, 24, 3\n\tTILE 2, 3, 1, 1, 24, 3\n\tTILE 3, 0, 2, 1, 24, 3\n\t"
      "TILE 0, 1, 3, 1, 24, 3\n\tTILE 1, 2, 0, 0, 20, 3\n\tTILE 2, 3, 1, 0, 16, 3\n\t"
      "v_add_u32 v205, %[d1], v204\n\tv_add_u32 v206, %[d2], v204\n\t"
      "STEP 96, 112, 0, 1, 3, 3, 3, 2, 0, 3, 0, 3, 3, 0, 3\n\t"
      "STEP 112, 96, 1, 2, 0, 3, 3, 3, 0, 3, 0, 3, 0, 0, 0\n\t"
      "STEP 96, 112, 2, 3, 1, 3, 3, 3, 0, 5, 0, 6, 5, 2, 7\n\t"
      "ds_write_b128 v206, v[16:19]\n\t"
      "GRP 112, 199, 192\n\t"
      "ds_write_b128 v206, v[20:23] offset:1024\n\t"
      "GRP 116, 193, 194\n\t"
      "v_mfma_f32_32x32x16_bf16 v[32:47], v[188:191], v[196:199], v[32:47]\n\t"
      "ds_write_b128 v206, v[24:27] offset:2048\n\t"
      "GRP 120, 195, 196\n\t"
      "v_mfma_f32_32x32x16_bf16 v[48:63], v[184:187], v[192:195], v[48:63]\n\t"
      "ds_write_b128 v206, v[28:31] offset:3072\n\t"
      "GRP 124, 197, 198\n\t"
      "v_add_u32 v200, %[d3], v204\n\t"
      "v_cvt_pk_bf16_f32 v199, v202, v203\n\t"
      "s_nop 1\n\t"
      "v_mfma_f32_32x32x16_bf16 v[48:63], v[188:191], v[196:199], v[48:63]\n\t"
      "ds_write_b128 v200, v[32:35]\n\tds_write_b128 v200, v[36:39] offset:1024\n\tds_write_b128 v200, v[40:43] offset:2048\n\tds_write_b128 v200, v[44:47] offset:3072\n\t"
      "s_nop 7\n\t"
      ".purgem GRP\n\t.purgem MEM\n\t.purgem STEP\n\t.purgem TILE"
      : "={v[48:63]}"(own), "+{v[128:143]}"(kf[0]), "+{v[144:159]}"(kf[1]), "+{v[160:175]}"(kf[2]),
        "={v[208:223]}"(g2[0]), "={v[224:239]}"(g2[1]), "={v[240:255]}"(g2[2]),
        [skv] "+s"(so_kv), [sw] "+s"(so_w), [ab0] "+s"(ab.s0), [ab1] "+s"(ab.s1), [spin] "+s"(spin), "+{v205}"(fvo)
      : "{v[64:67]}"(q[1][0]), "{v[68:71]}"(q[1][1]), "{v[72:75]}"(q[2][0]), "{v[76:79]}"(q[2][1]),
        "{v[80:83]}"(q[3][0]), "{v[84:87]}"(q[3][1]), "{v[88:91]}"(q[0][0]), "{v[92:95]}"(q[0][1]),
        [vo] "{v204}"(x.vl16), [d1] "s"(SCR + (w ^ 1) * 3 * 4096), [d2] "s"(SCR + ((w ^ 2) * 3 + 1) * 4096), [d3] "s"(SCR + ((w ^ 3) * 3 + 2) * 4096), [rs] "s"(x.rs), [sl] "s"(so_l), [wd0] "s"(wd.s0), [wd1] "s"(wd.s1)
      : "memory", "scc", "vcc",
        "v0","v1","v2","v3","v4","v5","v6","v7","v8","v9","v10","v11","v12","v13","v14","v15","v16","v17","v18","v19","v20","v21","v22","v23",
        "v24","v25","v26","v27","v28","v29","v30","v31","v32","v33","v34","v35","v36","v37","v38","v39","v40","v41","v42","v43","v44","v45","v46","v47",
        "v96","v97","v98","v99","v100","v101","v102","v103","v104","v105","v106","v107","v108","v109","v110","v111",
        "v112","v113","v114","v115","v116","v117","v118","v119","v120","v121","v122","v123","v124","v125","v126","v127",
        "v176","v177","v178","v179","v180","v181","v182","v183","v184","v185","v186","v187","v188","v189","v190","v191",
        "v192","v193","v194","v195","v196","v197","v198","v199","v200","v201","v202","v203","v206", ACLOB);
  } else {
    f32x16 o[4];
    wv<0>();
    pin(kf[0]); pin(kf[1]); pin(kf[2]);
    float mx[4];
#pragma unroll
    for (int k = 0; k < 4; ++k) {
      mx[k] = -3.0e38f; if (h) q[k][1][2] = 0; pin(q[k][1]);
#pragma unroll
      for (int i = 0; i < 16; ++i) o[k][i] = 0.f;
    }
#pragma nounroll
    for (unsigned spin = 0; spin < (1u << 16); ++spin) {
      float v;
      ld1(v, (l & 15) * 4096, x.rs, OFF_FLAG + FL_W(0)); wv<0>(); pin(v);
      if (__all(__float_as_uint(v) >= 16u)) break;
      __builtin_amdgcn_s_sleep(24);
    }
    pf_wo(x);
    pf_wgu1(x);
    sfor<0, 12>([&](auto j) {
      u32x4 t;
      ld4<1024 * (j & 1)>(t, x.vl16, x.rs, OFF_WGU + (x.m * 196 + 112 + 8 * (j >> 1) + 2 * w) * KB); wv<0>(); pin(t);
#pragma unroll
      for (int i = 0; i < 4; ++i) g2[j >> 2][4 * (j & 3) + i] = __uint_as_float(t[i]);
    });
    pf_wd<0, 46>(x, base_wd(x));
    for (int tk = 0; tk < 16; ++tk) {
      u32x4 kf[4];
      sfor<0, 4>([&](auto i) { ld4<i * 1024>(kf[i], x.vl16, x.rs, kvs + tk * 4096); });
      wv<0>();
      sfor<0, 4>([&](auto k) {
        f32x16 s;
        nop<1>();
        m32z(s, kf[0], q[k][0]); m32(s, kf[1], q[k][1]);
        nop<11>(); pin(s);
        float tm = s[0];
#pragma unroll
        for (int i = 1; i < 16; ++i) tm = fmaxf(tm, s[i]);
        tm = max32(tm);
        const float mn = fmaxf(mx[k], tm), al = __builtin_amdgcn_exp2f(mx[k] - mn);
        mx[k] = mn;
#pragma unroll
        for (int i = 0; i < 16; ++i) o[k][i] *= al;
        u32x4 p0, p1;
#pragma unroll
        for (int d = 0; d < 4; ++d) {
          p0[d] = cvtpk(__builtin_amdgcn_exp2f(s[2 * d] - mn), __builtin_amdgcn_exp2f(s[2 * d + 1] - mn));
          p1[d] = cvtpk(__builtin_amdgcn_exp2f(s[8 + 2 * d] - mn), __builtin_amdgcn_exp2f(s[9 + 2 * d] - mn));
        }
        pin(p0); pin(p1); pin(o[k]);
        nop<1>();
        m32(o[k], kf[2], p0); m32(o[k], kf[3], p1);
        nop<11>(); pin(o[k]);
      });
    }
    bar();
    float* mm = (float*)(lds + SCR);
#pragma unroll
    for (int k = 0; k < 4; ++k) if (h == 0) mm[((w ^ k) * 4 + w) * 32 + r] = mx[k];
    bar();
#pragma unroll
    for (int k = 0; k < 4; ++k) {
      const float* p = mm + (w ^ k) * 128 + r;
      const float g = fmaxf(fmaxf(p[0], p[32]), fmaxf(p[64], p[96])), f = __builtin_amdgcn_exp2f(mx[k] - g);
#pragma unroll
      for (int i = 0; i < 16; ++i) o[k][i] *= f;
    }
    bar();
    reduce_put(x, o);
    own = o[0];
  }
  stamp(x);
  reduce_get(x, own);
  pin(own); fstamp(x);
  {
    auto lr = __builtin_amdgcn_permlane32_swap(__float_as_uint(own[12]), __float_as_uint(own[12]), false, false);
    const float inv = __builtin_amdgcn_rcpf(__uint_as_float(lr[1]));
    u32x4 a, b;
#pragma unroll
    for (int d = 0; d < 4; ++d) { a[d] = cvtpk(own[2 * d] * inv, own[2 * d + 1] * inv); b[d] = cvtpk(own[8 + 2 * d] * inv, own[9 + 2 * d] * inv); }
    if (h) { b[2] = 0; b[3] = 0; }
    const unsigned so = OFF_O + L * SZ_O + (tt * 32 + 2 * (2 * hp + kh)) * KB;
    st4<0>(a, x.vl16, x.rs, so); st4<1024>(b, x.vl16, x.rs, so);
  }
  fstamp(x);
  wv<0>();
  fstamp(x);
  signal(x, FL_O(L, x.c, th), 1);
  if (L == 0 && th == 0) {
    if (x.c == hp) count(x, FL_W2, 1);
  }
}

template <bool FINAL> DI void res_epilogue(Ctx& x, const f32x16& acc, const u32x4 (&xr)[2], unsigned Xo, unsigned STo, int tt, int nt, char* out = nullptr) {
  const int h = x.l >> 5, r = x.l & 31;
  u32x4 y[2];
#pragma unroll
  for (int d = 0; d < 8; ++d) y[d >> 2][d & 3] = cvtpk(acc[2 * d] + bflo(xr[d >> 2][d & 3]), acc[2 * d + 1] + bfhi(xr[d >> 2][d & 3]));
  if constexpr (FINAL) {
#pragma unroll
    for (int s = 0; s < 2; ++s) {
      auto a = __builtin_amdgcn_permlane32_swap(y[s][0], y[s][2], false, false);
      auto b = __builtin_amdgcn_permlane32_swap(y[s][1], y[s][3], false, false);
      y[s] = u32x4{a[0], b[0], a[1], b[1]};
    }
    const unsigned vo = r * (D * 2) + h * 16, so = tt * 32 * D * 2 + nt * 64;
    const rsrc_t ro = mkrs(out, S * D * 2);
    st4p<0>(y[0], vo, ro, so); st4p<32>(y[1], vo, ro, so);
  } else {
    const unsigned so = Xo + (tt * 28 + 2 * nt) * KB;
    st4<0>(y[0], x.vl16, x.rs, so); st4<1024>(y[1], x.vl16, x.rs, so);
    float s = 0.f, q = 0.f, s1 = 0.f, q1 = 0.f;
#pragma unroll
    for (int d = 0; d < 4; ++d) {
      s = dot2(y[0][d], 0x3f803f80u, s); q = dot2(y[0][d], y[0][d], q); s1 = dot2(y[1][d], 0x3f803f80u, s1); q1 = dot2(y[1][d], y[1][d], q1);
    }
    s = sum32(s + s1); q = sum32(q + q1);
    if (h == 0) st2<0>(u32x2{__float_as_uint(s), __float_as_uint(q)}, r * 8, x.rs, STo + (nt * 2048 + tt * 32) * 8);
  }
}

DI void phase_oproj(Ctx& x, const int L, const Args& a, const f32x16 (&g2)[3]) {
  const int w = x.w;
  if (x.m < 14) {
    const int th = x.m / 7, ct = x.m % 7, tt0 = 4 * x.c + 2 * th, tt = tt0 + (w >> 1), nt = 2 * ct + (w & 1);
    u32x2 x0[4];
    if (L == 0) {
      const rsrc_t rx = mkrs(a.x, S * D * 2);
      sfor<0, 4>([&](auto i) { ld2i<16 * i>(x0[i], (x.l & 31) * 896 + (x.l >> 5) * 8, rx, tt * 32 * 896 + nt * 64); });
    } else {
      sfor<0, 4>([&](auto i) { ld2u<(i >> 1) * 1024 + (i & 1) * 8>(x0[i], x.vl16, x.rs, OFF_X + 2 * SZ_X + (tt * 28 + 2 * nt) * KB); });
    }
    await(x, FL_O(L, x.c, th), 8);
    stamp(x);
    u32x4 b[8][2];
    const unsigned s0 = OFF_O + L * SZ_O + (tt * 32 + 8 * w) * KB, s1 = OFF_O + L * SZ_O + ((tt0 + ((w >> 1) ^ 1)) * 32 + 8 * w) * KB;
    auto ld = [&](auto i) { ld4<(i & 3) * 1024>(b[i][0], x.vl16, x.rs, s0 + (i >> 2) * 4096); ld4<(i & 3) * 1024>(b[i][1], x.vl16, x.rs, s1 + (i >> 2) * 4096); };
    constexpr int WN = OPWN;
    sfor<0, WN>(ld);
    f32x16 acc[4];
    sfor<0, 8>([&](auto i) {
      constexpr int last = (i + WN - 1 < 7 ? i + WN - 1 : 7);
      wv<2 * (last - i) + 1>();
      m32a<A_WO + 8 * i, i == 0>(acc[0], b[i][0]); m32a<A_WO + 8 * i + 4, i == 0>(acc[1], b[i][0]);
      wv<2 * (last - i)>();
      m32a<A_WO + 8 * i, i == 0>(acc[2], b[i][1]); m32a<A_WO + 8 * i + 4, i == 0>(acc[3], b[i][1]);
      if constexpr (i + WN < 8) ld(std::integral_constant<int, i + WN>{});
    });
    nop<11>();
    sfor<0, 4>([&](auto k) { pin(acc[k]); pin(x0[k]); });
    stamp(x);
    reduce_put(x, acc); reduce_get(x, acc[0]);
    pin(acc[0]); fstamp(x);
    const u32x4 x2[2] = {u32x4{x0[0][0], x0[0][1], x0[1][0], x0[1][1]}, u32x4{x0[2][0], x0[2][1], x0[3][0], x0[3][1]}};
    res_epilogue<false>(x, acc[0], x2, OFF_X + (2 * L + 1) * SZ_X, OFF_ST + (2 * L + 1) * SZ_ST, tt, nt);
    fstamp(x);
    wv<0>();
    fstamp(x);
    signal(x, FL_X1(L, x.c), 1);
    fstamp(x);
  } else {
    stamp(x); stamp(x); fstamp(x); fstamp(x); fstamp(x); fstamp(x);
  }
#pragma unroll
  for (int j = 0; j < 12; ++j)
    *(f32x4*)(lds + (112 + 8 * (j >> 1) + 2 * w + (j & 1)) * KB + x.vl16) = f32x4{g2[j >> 2][4 * (j & 3)], g2[j >> 2][4 * (j & 3) + 1], g2[j >> 2][4 * (j & 3) + 2], g2[j >> 2][4 * (j & 3) + 3]};
}

constexpr int DWN = 7;
constexpr bool dn_half(int o) { return o == 4 || o == 5 || o == 14 || o == 15; }
struct DnSrc { unsigned s0, s1, l0, l1; };
DI DnSrc dn_src(const Ctx& x, int L) {
  const int tt0 = 4 * x.c + 2 * ((x.m / 7) & 1), tt = tt0 + (x.w >> 1), tto = tt0 + ((x.w >> 1) ^ 1);
  return DnSrc{OFF_H + L * SZ_H + (tt * 112 + 28 * x.w) * KB, OFF_H + L * SZ_H + (tto * 112 + 28 * x.w) * KB,
               OFF_HL + ((L * 64 + tt) * 16 + 4 * x.w) * KB, OFF_HL + ((L * 64 + tto) * 16 + 4 * x.w) * KB};
}
template <int O> DI void dn_ld(Ctx& x, const DnSrc& d, u32x4 (&b)[DWN][2]) {
  constexpr int i = dn_ks(O);
  if constexpr (dn_half(O)) {
    constexpr int imm = (O >= 14 ? 2048 : 0) + (O & 1) * 512;
    u32x2 lo, hi, lo1, hi1;
    ld2u<imm>(lo, x.l * 8, x.rs, d.l0); ld2u<imm + 1024>(hi, x.l * 8, x.rs, d.l0);
    ld2u<imm>(lo1, x.l * 8, x.rs, d.l1); ld2u<imm + 1024>(hi1, x.l * 8, x.rs, d.l1);
    b[O % DWN][0] = u32x4{lo[0], lo[1], hi[0], hi[1]}; b[O % DWN][1] = u32x4{lo1[0], lo1[1], hi1[0], hi1[1]};
  } else {
    ld4<(i & 3) * 1024>(b[O % DWN][0], x.vl16, x.rs, d.s0 + (i >> 2) * 4096); ld4<(i & 3) * 1024>(b[O % DWN][1], x.vl16, x.rs, d.s1 + (i >> 2) * 4096);
  }
}

DI void gu_tile(const f32x4 (&acc)[2][2], const u32x4 (&cs)[2], const float (&mean)[2], const float (&r2)[2], u32x2 (&hp)[2]) {
  const f32x4 cg = __builtin_bit_cast(f32x4, cs[0]), cu = __builtin_bit_cast(f32x4, cs[1]);
#pragma unroll
  for (int u = 0; u < 2; ++u) {
    const f32x2 nm = {-mean[u], -mean[u]}, rr = {r2[u], r2[u]};
#pragma unroll
    for (int e = 0; e < 2; ++e) {
      const f32x2 g = __builtin_elementwise_fma(nm, f32x2{cg[2 * e], cg[2 * e + 1]}, f32x2{acc[0][u][2 * e], acc[0][u][2 * e + 1]});
      const f32x2 v = __builtin_elementwise_fma(nm, f32x2{cu[2 * e], cu[2 * e + 1]}, f32x2{acc[1][u][2 * e], acc[1][u][2 * e + 1]});
      const f32x2 y = g * rr * v;
      hp[u][e] = cvtpk(y[0], y[1]);
    }
  }
}

DI void phase_gu(Ctx& x, const int L, u32x4 (&b)[DWN][2]) {
  const int w = x.w, l = x.l, g = l >> 4, c16 = l & 15, m = x.m, tt = 4 * x.c + w;
  if (L == 0 && wave0(x)) { unsigned v; ldflag(v, x.rs, OFF_FLAG + FL_W2); wv<0>(); recheck(x, v, FL_W2, 8); }
  await(x, FL_X1(L, x.c), 14);
  stamp(x);
  constexpr int NXV = 11;
  u32x4 xf[NXV][2]; f32x2 sp[2][4];
  const unsigned sx = OFF_X + (2 * L + 1) * SZ_X + tt * 28 * KB, st = OFF_ST + (2 * L + 1) * SZ_ST + tt * 32 * 8;
  auto ldx = [&](auto k, auto u) {
    if constexpr (k < NXV) ld4<(k & 1) * 2048 + 256 * u>(xf[k][u], x.vg16, x.rs, sx + (k >> 1) * 4096);
    else if constexpr (k < 14) lda<A_GX + 8 * (k - NXV) + 4 * u, (k & 1) * 2048 + 256 * u>(x.vg16, x.rs, sx + (k >> 1) * 4096);
    else { constexpr int i = k - 14; ld2<128 * u>(sp[u][i], (((i < 3 || g < 2) ? g + 4 * i : g) * 2048 + c16) * 8, x.rs, st); }
  };
  using I0 = std::integral_constant<int, 0>; using I1 = std::integral_constant<int, 1>;
  constexpr int XA = XAHEAD;
  sfor<0, XA>([&](auto k) { ldx(k, I0{}); ldx(k, I1{}); });
  const unsigned a0 = x.vl16, a1 = x.vl16 + 65536, a2 = x.vl16 + 131072;
  constexpr int DP = 6;
  u32x4 wf[8];
  auto rd = [&](auto ii) {
    constexpr int i = ii, slot = i >= 160 ? i - 160 : i;
    if constexpr (slot < 64) dsr<slot * 1024>(wf[i & 7], a0); else if constexpr (slot < 128) dsr<(slot - 64) * 1024>(wf[i & 7], a1); else dsr<(slot - 128) * 1024>(wf[i & 7], a2);
  };
  auto mm = [&](f32x4& c, const u32x4& a, auto k, auto u) {
    if constexpr (k < NXV) m16<k == 0>(c, a, xf[k][u]); else m16a<false, A_GX + 8 * (k - NXV) + 4 * u>(c, a);
  };
  const Two wd = base_wd(x);
  f32x4 aa[5][2][2], ch0, ch1;
  float mean[2], r2[2];
  sfor<0, DP>(rd);
  sfor<0, 140>([&](auto i) {
    constexpr int KS = i / 10, s = i % 10, t = s >> 1, mat = i & 1;
    using K = std::integral_constant<int, KS>;
    constexpr auto side = [](int k) { return (k >= 0 && k <= 3) || (k >= 5 && k <= 12); };
    constexpr auto all = [](int k) { return (k + XA < 14 ? 2 : 0) + ((k >= 0 && k <= 3) || (k >= 5 && k <= 9) ? 2 : 0) + (k == 5 ? 1 : 0) + (k >= 10 ? 3 : 0); };
    if constexpr (s == 0) {
      constexpr int cnt = [&] { int n = side(KS - XA) ? 1 : 0; for (int k = KS - XA + 1; k < KS; ++k) n += all(k); return n; }();
      wv<cnt>();
      if constexpr (KS == 5) asm volatile("s_barrier" ::: "memory");
    }
    if constexpr ((i & 1) == 0) { rd(std::integral_constant<int, i + DP>{}); rd(std::integral_constant<int, i + DP + 1>{}); wl<DP>(); }
    mm(aa[t][mat][0], wf[i & 7], K{}, I0{});
    if constexpr (s == 1 && KS + XA < 14) ldx(std::integral_constant<int, KS + XA>{}, I0{});
    if constexpr (s == 6 && KS + XA < 14) ldx(std::integral_constant<int, KS + XA>{}, I1{});
    if constexpr (KS <= 3 && s == 3) ldx(std::integral_constant<int, 14 + KS>{}, I0{});
    if constexpr (KS <= 3 && s == 8) ldx(std::integral_constant<int, 14 + KS>{}, I1{});
    if constexpr (KS == 5 && s == 3) asm volatile("buffer_load_dwordx4 %0, %2, %3, %4 offen sc1\n\tbuffer_load_dwordx4 %1, %2, %3, %4 offen offset:1024 sc1" : "=&v"(ch0), "=&v"(ch1) : "v"(x.vl16), "s"(x.rs), "s"(OFF_CSGU + m * 2048));
    if constexpr (KS == 11 && s == 2) { pin(ch0); pin(ch1); *(f32x4*)(lds + 40 * KB + x.vl16) = ch0 + ch1; }
    if constexpr (KS >= 5 && KS <= 9 && (s == 3 || s == 8) && !(KS == 5 && s == 3)) pf_wgu3<2 * (KS - 5) + (s == 8) - 1>(x);
    if constexpr (KS >= 10 && (s == 3 || s == 5 || s == 8) && 3 * (KS - 10) + (s == 3 ? 0 : (s == 5 ? 1 : 2)) < 10) {
      constexpr int f = 46 + 3 * (KS - 10) + (s == 3 ? 0 : (s == 5 ? 1 : 2));
      pf_wd<f, f + 1>(x, wd);
    }
    if constexpr ((KS == 10 || KS == 12) && s == 2) {
      constexpr int u = KS == 12;
      sfor<0, 4>([&](auto e) { pin(sp[u][e]); });
      float sm = sp[u][0][0] + sp[u][1][0] + sp[u][2][0], q = sp[u][0][1] + sp[u][1][1] + sp[u][2][1];
      if (g < 2) { sm += sp[u][3][0]; q += sp[u][3][1]; }
      sm = sum32(sum16(sm)); q = sum32(sum16(q));
      float rs; row_stats(sm, q, mean[u], rs); r2[u] = rs * rs;
      pin(mean[u]); pin(r2[u]);
    }
    mm(aa[t][mat][1], wf[i & 7], K{}, I1{}); SB();
  });
  fstamp(x);
  u32x4 cs[7][2];
  const unsigned csa = 40 * KB + g * 16;
  sfor<0, 10>([&](auto i) { dsr<64 * i>(cs[i >> 1][i & 1], csa); });
  wv<10>();
  asm volatile("s_barrier" ::: "memory");
  stamp(x);
  const unsigned H = OFF_H + L * SZ_H + tt * 112 * KB;
  const int gp = (m & 1) ? 7 * m + 1 : 7 * m;
  auto pair = [&](const f32x4 (&t0)[2][2], const f32x4 (&t1)[2][2], auto lt) {
    u32x2 h0[2], h1[2];
    gu_tile(t0, cs[lt], mean, r2, h0); gu_tile(t1, cs[lt + 1], mean, r2, h1);
    st4<0>(u32x4{h0[0][0], h0[0][1], h1[0][0], h1[0][1]}, x.vg16, x.rs, H + (gp + lt - 1) * 1024);
    st4<256>(u32x4{h0[1][0], h0[1][1], h1[1][0], h1[1][1]}, x.vg16, x.rs, H + (gp + lt - 1) * 1024);
  };
  {
    wl<0>();
    sfor<0, 20>([&](auto i) { pin(aa[i >> 2][(i >> 1) & 1][i & 1]); });
    sfor<0, 10>([&](auto i) { pin(cs[i >> 1][i & 1]); });
    u32x2 hl[2];
    gu_tile(aa[0], cs[0], mean, r2, hl);
    const unsigned sl = OFF_HL + ((L * 64 + tt) * 16 + m) * KB;
    st2<0>(hl[0], x.vg16 >> 1, x.rs, sl); st2<128>(hl[1], x.vg16 >> 1, x.rs, sl);
    pair(aa[1], aa[2], std::integral_constant<int, 1>{});
    pair(aa[3], aa[4], std::integral_constant<int, 3>{});
  }
  SB();
  sfor<10, 14>([&](auto i) { dsr<64 * i>(cs[i >> 1][i & 1], csa); });
  fstamp(x);
  f32x4 ab[2][2][2];
  unsigned fa;
  sfor<140, 196>([&](auto i) {
    constexpr int j = i - 140, KS = j / 4, t = (j % 4) >> 1, mat = i & 1;
    using K = std::integral_constant<int, KS>;
    if constexpr (j == 18) {
      fstamp(x);
      if (L == 0) wv<6>(); else wv<0>();
      asm volatile("s_barrier" ::: "memory");
      count(x, FL_H(L, x.c, 0), 1);
      SB();
      fstamp(x);
    }
    if constexpr ((i & 1) == 0) { if constexpr (i + DP < 196) { rd(std::integral_constant<int, i + DP>{}); rd(std::integral_constant<int, i + DP + 1>{}); wl<DP>(); } else wl<194 - i>(); }
    mm(ab[t][mat][0], wf[i & 7], K{}, I0{});
    if constexpr (j % 3 == 1 && j < 42) { if (L == 0) pf_wq<j / 3>(x); SB(); }
    if constexpr (j == 44) ldflag(fa, x.rs, OFF_FLAG + FL_H(L, x.c, 0));
    mm(ab[t][mat][1], wf[i & 7], K{}, I1{}); SB();
  });
  wv<0>();
  recheck(x, fa, FL_H(L, x.c, 0), 16);
  { const DnSrc d = dn_src(x, L); sfor<0, 3>([&](auto o) { dn_ld<o>(x, d, b); }); }
  stamp(x);
  sfor<0, 8>([&](auto i) { pin(ab[i >> 2][(i >> 1) & 1][i & 1]); });
  sfor<10, 14>([&](auto i) { pin(cs[i >> 1][i & 1]); });
  pair(ab[0], ab[1], std::integral_constant<int, 5>{});
}

DI void phase_down(Ctx& x, const int L, char* out, u32x4 (&b)[DWN][2]) {
  const int w = x.w;
  if (x.m >= 14) {
    wv<0>();
    sfor<0, 3>([&](auto o) { pin(b[o][0]); pin(b[o][1]); });
    signal(x, FL_H(L, x.c, 1), 1);
    stamp(x); fstamp(x); fstamp(x); stamp(x);
    return;
  }
  const int th = x.m / 7, ct = x.m % 7, tt = 4 * x.c + 2 * th + (w >> 1), nt = 2 * ct + (w & 1);
  stamp(x);
  constexpr int WN = DWN;
  u32x4 xr[2];
  const DnSrc d = dn_src(x, L);
  auto ld = [&](auto o) { dn_ld<o>(x, d, b); };
  sfor<3, WN>(ld);
  f32x16 acc[4];
  unsigned fb;
  sfor<0, 25>([&](auto i) {
    constexpr int last = i + WN - 1 < 27 ? i + WN - 1 : 27, ex = i <= 2 ? 2 : ((i >= 8 && i <= 13) ? 1 : ((i >= 21) ? 2 : 0));
    constexpr int later = [&] { int n = 0; for (int o = i + 1; o <= last; ++o) n += dn_half(o) ? 4 : 2; return n; }();
    wv<later + (dn_half(i) ? 2 : 1) + ex>();
    if constexpr (i == 3) signal(x, FL_H(L, x.c, 1), 1);
    m32a<A_WD + 8 * i, i == 0>(acc[0], b[i % WN][0]); m32a<A_WD + 8 * i + 4, i == 0>(acc[1], b[i % WN][0]);
    wv<later + ex>();
    m32a<A_WD + 8 * i, i == 0>(acc[2], b[i % WN][1]); m32a<A_WD + 8 * i + 4, i == 0>(acc[3], b[i % WN][1]);
    if constexpr (i == 7) ldflag(fb, x.rs, OFF_FLAG + FL_H(L, x.c, 1));
    if constexpr (i + WN == 20) { fstamp(x); wv<12>(); recheck(x, fb, FL_H(L, x.c, 1), 16); fstamp(x); }
    if constexpr (i + WN < 28) ld(std::integral_constant<int, i + WN>{});
    if constexpr (i + WN == 27) {
      const unsigned sr = OFF_X + (2 * L + 1) * SZ_X + (tt * 28 + 2 * nt) * KB;
      ld4<0>(xr[0], x.vl16, x.rs, sr); ld4<1024>(xr[1], x.vl16, x.rs, sr);
    }
  });
  wv<2>();
  sfor<1, 5>([&](auto t) {
    constexpr int k = t & 3;
    m32a<A_WD + 8 * 25 + 4 * (k & 1), false>(acc[k], b[25 % WN][k >> 1]);
    m32a<A_WD + 8 * 26 + 4 * (k & 1), false>(acc[k], b[26 % WN][k >> 1]);
    if constexpr (t > 1) { nop<(t == 2 ? 9 : 7)>(); red_put1<t - 1, 0>(x, acc[t - 1]); red_put1<t - 1, 1>(x, acc[t - 1]); }
    m32a<A_WD + 8 * 27 + 4 * (k & 1), false>(acc[k], b[27 % WN][k >> 1]);
    if constexpr (t > 1) { red_put1<t - 1, 2>(x, acc[t - 1]); red_put1<t - 1, 3>(x, acc[t - 1]); }
  });
  if (L == 0 && (x.m & 7) < 2) {
    ldl<4>(x.vl16, x.rs, OFF_WQ + ((16 + (x.m & 1)) * 28 + 7 * w) * KB, 7 * w * KB);
    ldl<3>(x.vl16, x.rs, OFF_WQ + ((16 + (x.m & 1)) * 28 + 7 * w + 4) * KB, (7 * w + 4) * KB);
  }
  nop<9>();
  pin(acc[0]);
  stamp(x);
  reduce_get(x, acc[0]);
  wv<0>();
  pin(xr[0]); pin(xr[1]);
  if (L > 0) {
    res_epilogue<true>(x, acc[0], xr, 0, 0, tt, nt, out);
  } else {
    res_epilogue<false>(x, acc[0], xr, OFF_X + 2 * SZ_X, OFF_ST + 2 * SZ_ST, tt, nt);
    wv<0>();
    signal(x, FL_X(x.c, th), 1);
  }
}

#define STAMP_SYNC
__global__ __launch_bounds__(256, 1) void k_main(Args a) {
  Ctx x;
  x.rs = mkrs(a.ws, WS_USED);
  x.w = __builtin_amdgcn_readfirstlane(threadIdx.x >> 6);
  asm volatile("s_nop 4" : "+s"(x.w));
  { const int b = blockIdx.x, i = b >> 4; x.c = 2 * (b & 7) + ((b >> 3) & 1); x.m = i < 4 ? (i & 1) + 8 * (i >> 1) : (i < 10 ? i - 2 : i); }
  f32x16 kf[3];
  { Ctx y = x; fresh(y); stamp(y); phase_pre(y, a, kf); STAMP_SYNC }
  for (int L = 0;; ++L) {
    f32x16 g2[3];
    int Lo = L; asm volatile("" : "+s"(Lo));
    { Ctx y = x; fresh(y); phase_att(y, Lo, kf, g2); stamp(y); STAMP_SYNC }
    { Ctx y = x; fresh(y); phase_oproj(y, Lo, a, g2); stamp(y); STAMP_SYNC }
    { Ctx y = x; u32x4 b[DWN][2]; fresh(y); phase_gu(y, Lo, b); stamp(y); fresh(y); phase_down(y, Lo, a.out, b); stamp(y); STAMP_SYNC }
    if (L == 1) break;
    { Ctx y = x; fresh(y); phase_front(y, kf); STAMP_SYNC }
  }
  fresh(x);
  if (blockIdx.x == 0 && wave0(x)) {
    get_tag(x);
    if (x.l == 0) st2<0>(u32x2{x.tlo, x.thi}, 0, x.rs, OFF_TAG);
  }
  stamp_flush(x);
  asm volatile(".Lk_end:");
}

extern "C" void kernel_launch(void* const* d_in, const int* in_sizes, int n_in, void* d_out, int out_size, void* d_ws, size_t ws_size,
                              hipStream_t stream) {
  Args a;
  a.x = (const char*)d_in[0]; a.wq = (const char*)d_in[1]; a.wk = (const char*)d_in[2]; a.wv = (const char*)d_in[3];
  a.wo = (const char*)d_in[4]; a.wg = (const char*)d_in[6]; a.wu = (const char*)d_in[7]; a.wd = (const char*)d_in[8];
  a.ws = (char*)d_ws; a.out = (char*)d_out;
  (void)hipFuncSetAttribute((const void*)k_main, hipFuncAttributeMaxDynamicSharedMemorySize, 160 * 1024);
  k_main<<<256, 256, 160 * 1024, stream>>>(a);
}
```

```cpp
#include <hip/hip_runtime.h>
#include <stdint.h>
#include <type_traits>

typedef float    f32x16 __attribute__((ext_vector_type(16)));
typedef float    f32x4  __attribute__((ext_vector_type(4)));
typedef float    f32x2  __attribute__((ext_vector_type(2)));
typedef unsigned u32x4  __attribute__((ext_vector_type(4)));
typedef unsigned u32x2  __attribute__((ext_vector_type(2)));
typedef __bf16   bf16x2 __attribute__((ext_vector_type(2)));
typedef __amdgpu_buffer_rsrc_t rsrc_t;
#define DI __device__ __forceinline__
#define GAS __attribute__((address_space(1)))
#define SB() __builtin_amdgcn_sched_barrier(0)

constexpr int S = 2048, D = 448, F = 1792;
constexpr unsigned KB = 1024;
constexpr unsigned OFF_FLAG = 0;
constexpr unsigned OFF_CSQ  = 64 * KB;
constexpr unsigned OFF_CSGU = OFF_CSQ + 4 * KB;
constexpr unsigned OFF_SLOT = OFF_CSGU + 32 * KB;
constexpr unsigned OFF_TAG  = OFF_SLOT + 32 * KB;
constexpr unsigned OFF_WQ   = 160 * KB;
constexpr unsigned OFF_WO   = OFF_WQ + 18 * 28 * KB;
constexpr unsigned OFF_WGU  = OFF_WO + 14 * 32 * KB;
constexpr unsigned OFF_WD   = OFF_WGU + 16 * 196 * KB;
constexpr unsigned SZ_X = 64 * 28 * KB, SZ_ST = 14 * 2048 * 8, SZ_O = 64 * 32 * KB, SZ_H = 64 * 112 * KB, SZ_KV = 64 * 4 * KB;
constexpr unsigned OFF_X    = OFF_WD + 14 * 112 * KB;
constexpr unsigned OFF_ST   = OFF_X + 4 * SZ_X;
constexpr unsigned OFF_O    = OFF_ST + 4 * SZ_ST;
constexpr unsigned OFF_H    = OFF_O + 2 * SZ_O;
constexpr unsigned OFF_HL   = OFF_H + 2 * SZ_H;
constexpr unsigned OFF_KV   = OFF_HL + 2 * 1024 * KB;
constexpr unsigned OFF_TS   = OFF_KV + 2 * SZ_KV;
constexpr unsigned WS_USED  = OFF_TS + 520 * KB;
DI constexpr unsigned FL_X(int c, int th)         { return (c * 16 + th) * 256u; }
DI constexpr unsigned FL_O(int L, int c, int th)  { return (c * 16 + 2 + L * 2 + th) * 256u; }
DI constexpr unsigned FL_X1(int L, int c)         { return (c * 16 + 6 + L) * 256u; }
DI constexpr unsigned FL_H(int L, int c, int p)   { return (c * 16 + 8 + L * 2 + p) * 256u; }
DI constexpr unsigned FL_W(int c)                 { return (c * 16 + 12) * 256u; }
constexpr unsigned FL_W2 = 13 * 256u;

#define XAHEAD 3
#define OPWN 5
constexpr float QSCALE = 0.18898223650461362f * 1.4426950408889634f;
constexpr float M_LIMIT = 48.0f;

template <int I, int N, class Fn> DI void sfor(Fn&& f) { if constexpr (I < N) { f(std::integral_constant<int, I>{}); sfor<I + 1, N>(f); } }

DI unsigned cvtpk(float lo, float hi) { f32x2 v = {lo, hi}; bf16x2 b = __builtin_convertvector(v, bf16x2); return __builtin_bit_cast(unsigned, b); }
DI float bflo(unsigned u) { return __uint_as_float(u << 16); }
DI float bfhi(unsigned u) { return __uint_as_float(u & 0xffff0000u); }
DI float dot2(unsigned a, unsigned b, float c) { return __builtin_amdgcn_fdot2_f32_bf16(__builtin_bit_cast(bf16x2, a), __builtin_bit_cast(bf16x2, b), c, false); }
DI float sum32(float v) { auto r = __builtin_amdgcn_permlane32_swap(__float_as_uint(v), __float_as_uint(v), false, false); return __uint_as_float(r[0]) + __uint_as_float(r[1]); }
DI float sum16(float v) { auto r = __builtin_amdgcn_permlane16_swap(__float_as_uint(v), __float_as_uint(v), false, false); return __uint_as_float(r[0]) + __uint_as_float(r[1]); }
DI float max32(float v) { auto r = __builtin_amdgcn_permlane32_swap(__float_as_uint(v), __float_as_uint(v), false, false); return fmaxf(__uint_as_float(r[0]), __uint_as_float(r[1])); }

DI unsigned umax16(unsigned v) {
  v = max(v, (unsigned)__builtin_amdgcn_update_dpp(0, (int)v, 0x128, 0xf, 0xf, false));
  v = max(v, (unsigned)__builtin_amdgcn_update_dpp(0, (int)v, 0x124, 0xf, 0xf, false));
  v = max(v, (unsigned)__builtin_amdgcn_update_dpp(0, (int)v, 0x122, 0xf, 0xf, false));
  v = max(v, (unsigned)__builtin_amdgcn_update_dpp(0, (int)v, 0x121, 0xf, 0xf, false));
  return v;
}
DI unsigned umax64(unsigned v) {
  v = umax16(v);
  auto a = __builtin_amdgcn_permlane16_swap(v, v, false, false); v = max(a[0], a[1]);
  auto b = __builtin_amdgcn_permlane32_swap(v, v, false, false); return max(b[0], b[1]);
}

typedef short s16x4 __attribute__((ext_vector_type(4)));
DI u32x2 trread(const char* p) { return __builtin_bit_cast(u32x2, __builtin_amdgcn_ds_read_tr16_b64_v4i16((__attribute__((address_space(3))) s16x4*)p)); }
DI u32x4 trfrag(const char* lo, const char* hi) { const u32x2 a = trread(lo), b = trread(hi); return u32x4{a[0], a[1], b[0], b[1]}; }
DI void row_stats(float s, float q, float& mean, float& rstd) {
  mean = s * (1.0f / D);
  const float var = fmaxf(q * (1.0f / D) - mean * mean, 0.f);
  rstd = __builtin_amdgcn_rsqf(var + 1e-5f);
}
DI float fragsum(const u32x4& f, float s) {
#pragma unroll
  for (int d = 0; d < 4; ++d) s = dot2(f[d], 0x3f803f80u, s);
  return s;
}
struct Args { const char *x, *wq, *wk, *wv, *wo, *wg, *wu, *wd; char* ws; char* out; };

#define ACLOB "a0","a1","a2","a3","a4","a5","a6","a7","a8","a9","a10","a11","a12","a13","a14","a15","a16","a17","a18","a19","a20","a21","a22","a23","a24","a25","a26","a27","a28","a29","a30","a31","a32","a33","a34","a35","a36","a37","a38","a39","a40","a41","a42","a43","a44","a45","a46","a47","a48","a49","a50","a51","a52","a53","a54","a55","a56","a57","a58","a59","a60","a61","a62","a63","a64","a65","a66","a67","a68","a69","a70","a71","a72","a73","a74","a75","a76","a77","a78","a79","a80","a81","a82","a83","a84","a85","a86","a87","a88","a89","a90","a91","a92","a93","a94","a95","a96","a97","a98","a99","a100","a101","a102","a103","a104","a105","a106","a107","a108","a109","a110","a111","a112","a113","a114","a115","a116","a117","a118","a119","a120","a121","a122","a123","a124","a125","a126","a127","a128","a129","a130","a131","a132","a133","a134","a135","a136","a137","a138","a139","a140","a141","a142","a143","a144","a145","a146","a147","a148","a149","a150","a151","a152","a153","a154","a155","a156","a157","a158","a159","a160","a161","a162","a163","a164","a165","a166","a167","a168","a169","a170","a171","a172","a173","a174","a175","a176","a177","a178","a179","a180","a181","a182","a183","a184","a185","a186","a187","a188","a189","a190","a191","a192","a193","a194","a195","a196","a197","a198","a199","a200","a201","a202","a203","a204","a205","a206","a207","a208","a209","a210","a211","a212","a213","a214","a215","a216","a217","a218","a219","a220","a221","a222","a223","a224","a225","a226","a227","a228","a229","a230","a231","a232","a233","a234","a235","a236","a237","a238","a239","a240","a241","a242","a243","a244","a245","a246","a247","a248","a249","a250","a251","a252","a253","a254","a255"

template <int IMM> DI void ld4(u32x4& d, unsigned vo, rsrc_t rs, unsigned so) {
  asm volatile("buffer_load_dwordx4 %0, %1, %2, %3 offen offset:%c4 sc1" : "=v"(d) : "v"(vo), "s"(rs), "s"(so), "i"(IMM));
}
template <int IMM> DI void ld2(f32x2& d, unsigned vo, rsrc_t rs, unsigned so) {
  asm volatile("buffer_load_dwordx2 %0, %1, %2, %3 offen offset:%c4 sc1" : "=v"(d) : "v"(vo), "s"(rs), "s"(so), "i"(IMM));
}
template <int IMM> DI void ld4i(u32x4& d, unsigned vo, rsrc_t rs, unsigned so) {
  asm volatile("buffer_load_dwordx4 %0, %1, %2, %3 offen offset:%c4" : "=v"(d) : "v"(vo), "s"(rs), "s"(so), "i"(IMM));
}
template <int IMM> DI void ld2i(u32x2& d, unsigned vo, rsrc_t rs, unsigned so) {
  asm volatile("buffer_load_dwordx2 %0, %1, %2, %3 offen offset:%c4" : "=v"(d) : "v"(vo), "s"(rs), "s"(so), "i"(IMM));
}
template <int IMM> DI void ld2u(u32x2& d, unsigned vo, rsrc_t rs, unsigned so) {
  asm volatile("buffer_load_dwordx2 %0, %1, %2, %3 offen offset:%c4 sc1" : "=v"(d) : "v"(vo), "s"(rs), "s"(so), "i"(IMM));
}
template <int IMM> DI void ld4f(f32x4& d, unsigned vo, rsrc_t rs, unsigned so) {
  asm volatile("buffer_load_dwordx4 %0, %1, %2, %3 offen offset:%c4 sc1" : "=v"(d) : "v"(vo), "s"(rs), "s"(so), "i"(IMM));
}
DI void ld1(float& d, unsigned vo, rsrc_t rs, unsigned so) {
  asm volatile("buffer_load_dword %0, %1, %2, %3 offen sc1" : "=v"(d) : "v"(vo), "s"(rs), "s"(so));
}
template <int AR, int IMM> DI void lda(unsigned vo, rsrc_t rs, unsigned so) {
  asm volatile("buffer_load_dwordx4 a[%c3:%c4], %0, %1, %2 offen offset:%c5 sc1" :: "v"(vo), "s"(rs), "s"(so), "i"(AR), "i"(AR + 3), "i"(IMM) : ACLOB);
}
template <int N> DI void ldl(unsigned vo, rsrc_t rs, unsigned so, unsigned lds) {
  asm volatile("s_mov_b32 m0, %3\n\ts_nop 0\n\tbuffer_load_dwordx4 %0, %1, %2 offen sc1 lds" :: "v"(vo), "s"(rs), "s"(so), "s"(lds) : "memory");
  if constexpr (N > 1) asm volatile("buffer_load_dwordx4 %0, %1, %2 offen offset:1024 sc1 lds" :: "v"(vo), "s"(rs), "s"(so) : "memory");
  if constexpr (N > 2) asm volatile("buffer_load_dwordx4 %0, %1, %2 offen offset:2048 sc1 lds" :: "v"(vo), "s"(rs), "s"(so) : "memory");
  if constexpr (N > 3) asm volatile("buffer_load_dwordx4 %0, %1, %2 offen offset:3072 sc1 lds" :: "v"(vo), "s"(rs), "s"(so) : "memory");
}
template <int IMM> DI void st4(const u32x4& d, unsigned vo, rsrc_t rs, unsigned so) {
  asm volatile("buffer_store_dwordx4 %0, %1, %2, %3 offen offset:%c4 sc1\n\ts_nop 1" :: "v"(d), "v"(vo), "s"(rs), "s"(so), "i"(IMM) : "memory");
}
template <int IMM> DI void st4p(const u32x4& d, unsigned vo, rsrc_t rs, unsigned so) {
  asm volatile("buffer_store_dwordx4 %0, %1, %2, %3 offen offset:%c4\n\ts_nop 1" :: "v"(d), "v"(vo), "s"(rs), "s"(so), "i"(IMM) : "memory");
}
template <int IMM> DI void st2(const u32x2& d, unsigned vo, rsrc_t rs, unsigned so) {
  asm volatile("buffer_store_dwordx2 %0, %1, %2, %3 offen offset:%c4 sc1" :: "v"(d), "v"(vo), "s"(rs), "s"(so), "i"(IMM) : "memory");
}
DI void st1(float d, unsigned vo, rsrc_t rs, unsigned so) {
  asm volatile("buffer_store_dword %0, %1, %2, %3 offen sc1" :: "v"(d), "v"(vo), "s"(rs), "s"(so) : "memory");
}
template <int N> DI void wv() { asm volatile("s_waitcnt vmcnt(%c0)" :: "i"(N) : "memory"); SB(); }
template <int N> DI void wl() { asm volatile("s_waitcnt lgkmcnt(%c0)" :: "i"(N) : "memory"); SB(); }
template <class T> DI void pin(T& x) { asm volatile("" : "+v"(x)); }
DI void bar() { asm volatile("s_waitcnt lgkmcnt(0)\n\ts_barrier" ::: "memory"); }
template <int N> DI void nop() { asm volatile("s_nop %c0" :: "i"(N)); SB(); }

template <int OFF> DI void trd(u32x2& r, unsigned a) { asm volatile("ds_read_b64_tr_b16 %0, %1 offset:%c2" : "=v"(r) : "v"(a), "i"(OFF)); }
template <int OFF> DI void dsr(u32x4& r, unsigned a) { asm volatile("ds_read_b128 %0, %1 offset:%c2" : "=v"(r) : "v"(a), "i"(OFF)); }

DI void m32(f32x16& c, const u32x4& a, const u32x4& b)  { asm volatile("v_mfma_f32_32x32x16_bf16 %0, %1, %2, %0" : "+v"(c) : "v"(a), "v"(b)); }
DI void m32z(f32x16& c, const u32x4& a, const u32x4& b) { asm volatile("v_mfma_f32_32x32x16_bf16 %0, %1, %2, 0" : "=&v"(c) : "v"(a), "v"(b)); }
template <int AR, bool Z> DI void m32a(f32x16& c, const u32x4& b) {
  if constexpr (Z) asm volatile("v_mfma_f32_32x32x16_bf16 %0, a[%c2:%c3], %1, 0" : "=&v"(c) : "v"(b), "i"(AR), "i"(AR + 3));
  else             asm volatile("v_mfma_f32_32x32x16_bf16 %0, a[%c2:%c3], %1, %0" : "+v"(c) : "v"(b), "i"(AR), "i"(AR + 3));
}
template <int AR, bool Z> DI void m32b(f32x16& c, const u32x4& a) {
  if constexpr (Z) asm volatile("v_mfma_f32_32x32x16_bf16 %0, %1, a[%c2:%c3], 0" : "=&v"(c) : "v"(a), "i"(AR), "i"(AR + 3));
  else             asm volatile("v_mfma_f32_32x32x16_bf16 %0, %1, a[%c2:%c3], %0" : "+v"(c) : "v"(a), "i"(AR), "i"(AR + 3));
}
template <bool Z> DI void m16(f32x4& c, const u32x4& a, const u32x4& b) {
  if constexpr (Z) asm volatile("v_mfma_f32_16x16x32_bf16 %0, %1, %2, 0" : "=&v"(c) : "v"(a), "v"(b));
  else             asm volatile("v_mfma_f32_16x16x32_bf16 %0, %1, %2, %0" : "+v"(c) : "v"(a), "v"(b));
}

template <bool Z, int AR> DI void m16a(f32x4& c, const u32x4& a) {
  if constexpr (Z) asm volatile("v_mfma_f32_16x16x32_bf16 %0, %1, a[%c2:%c3], 0" : "=&v"(c) : "v"(a), "i"(AR), "i"(AR + 3));
  else             asm volatile("v_mfma_f32_16x16x32_bf16 %0, %1, a[%c2:%c3], %0" : "+v"(c) : "v"(a), "i"(AR), "i"(AR + 3));
}
DI void ldflag(unsigned& v, rsrc_t rs, unsigned so) { asm volatile("buffer_load_dword %0, off, %1, %2 sc1" : "=v"(v) : "s"(rs), "s"(so)); }

struct Ctx {
  rsrc_t rs;
  unsigned tlo, thi;
  unsigned vl16;
  unsigned vg16;
  int w, l, c, m;
};
DI void fresh(Ctx& x) {
  asm volatile("" : "+s"(x.w), "+s"(x.c), "+s"(x.m));
  asm volatile("v_mbcnt_lo_u32_b32 %0, -1, 0\n\tv_mbcnt_hi_u32_b32 %0, -1, %0" : "=v"(x.l));
  x.vl16 = x.l * 16;
  const int g = x.l >> 4;
  x.vg16 = ((g >> 1) * 64 + (g & 1) * 32 + (x.l & 15)) * 16;
}
extern __shared__ __attribute__((aligned(16))) char lds[];
constexpr unsigned SCR = 112 * KB;

DI void pstamp(Ctx&) {}
DI void fstamp(Ctx&) {}
DI void stamp(Ctx&) {}
DI void stamp_flush(Ctx&) {}

DI void get_tag(Ctx& x) {
  unsigned lo, hi;
  asm volatile("v_accvgpr_read_b32 v255, a252\n\ts_nop 1\n\tv_readfirstlane_b32 %0, v255\n\tv_accvgpr_read_b32 v255, a253\n\ts_nop 1\n\tv_readfirstlane_b32 %1, v255\n\ts_nop 4"
               : "=s"(lo), "=s"(hi) :: "v255");
  const unsigned long long e = ((unsigned long long)hi << 32) | lo;
  unsigned long long n = e * 0x9E3779B97F4A7C15ull + 0x632BE59BD9B4E019ull;
  if (((n ^ e) >> 16) == 0) n += 0x10000;
  x.tlo = (unsigned)n; x.thi = (unsigned)(n >> 32);
}
DI bool wave0(const Ctx& x) { int w = x.w; asm volatile("" : "+s"(w)); return w == 0; }
DI void count(Ctx& x, unsigned fl, int n) {
  if (wave0(x)) {
    const unsigned long long mask = (1ull << n) - 1;
    unsigned long long keep;
    asm volatile("s_mov_b64 %0, exec\n\ts_mov_b64 exec, %5\n\tbuffer_atomic_add %1, %2, %3, %4 offen\n\ts_mov_b64 exec, %0"
                 : "=&s"(keep) : "v"(1u), "v"(x.l * 256), "s"(x.rs), "s"(OFF_FLAG + fl), "s"(mask) : "memory");
  }
}
DI void signal(Ctx& x, unsigned fl, int n) { bar(); count(x, fl, n); }
DI void await(Ctx& x, unsigned fl, unsigned target) {
  if (wave0(x)) {
#pragma nounroll
    for (unsigned spin = 0; spin < (1u << 18); ++spin) {
      unsigned v;
      asm volatile("buffer_load_dword %0, off, %1, %2 sc1\n\ts_waitcnt vmcnt(0)" : "=v"(v) : "s"(x.rs), "s"(OFF_FLAG + fl) : "memory");
      if ((unsigned)__builtin_amdgcn_readfirstlane(v) >= target) break;
      __builtin_amdgcn_s_sleep(1);
    }
  }
  bar();
}

DI void recheck(Ctx& x, unsigned v, unsigned fl, unsigned target) {
  pin(v);
#pragma nounroll
  for (unsigned spin = 0; (unsigned)__builtin_amdgcn_readfirstlane(v) < target && spin < (1u << 18); ++spin) {
    __builtin_amdgcn_s_sleep(2);
    asm volatile("buffer_load_dword %0, off, %1, %2 sc1\n\ts_waitcnt vmcnt(0)" : "=v"(v) : "s"(x.rs), "s"(OFF_FLAG + fl) : "memory");
  }
  SB();
}

DI void reduce_put(Ctx& x, const f32x16 (&acc)[4]) {
  sfor<1, 4>([&](auto k) {
    f32x4* p = (f32x4*)(lds + SCR + ((x.w ^ k) * 3 + k - 1) * 4096 + x.vl16);
#pragma unroll
    for (int q = 0; q < 4; ++q) p[q * 64] = f32x4{acc[k][4 * q], acc[k][4 * q + 1], acc[k][4 * q + 2], acc[k][4 * q + 3]};
  });
}
template <int K, int Q> DI void red_put1(Ctx& x, const f32x16& t) {
  asm volatile("ds_write_b128 %0, %1 offset:%c2" :: "v"(SCR + ((x.w ^ K) * 3 + K - 1) * 4096 + x.vl16),
               "v"(__builtin_shufflevector(t, t, 4 * Q, 4 * Q + 1, 4 * Q + 2, 4 * Q + 3)), "i"(Q * 1024) : "memory");
}
DI void reduce_get(Ctx& x, f32x16& own) {
  bar();
  const f32x4* p = (const f32x4*)(lds + SCR + x.w * 3 * 4096 + x.vl16);
#pragma unroll
  for (int k = 0; k < 3; ++k)
#pragma unroll
    for (int q = 0; q < 4; ++q) {
      f32x4 v = p[k * 256 + q * 64];
#pragma unroll
      for (int i = 0; i < 4; ++i) own[4 * q + i] += v[i];
    }
}

constexpr int A_WO = 184, A_WD = 0, A_GX = 224;
constexpr int dn_ks(int o) { return o < 20 ? 2 * ((o >> 1) % 5 + ((o >> 1) % 5 >= 2) + 7 * (o / 10)) + (o & 1) : 2 * (((o - 20) >> 1) * 4 - ((o - 20) >> 2) + 2) + (o & 1); }
static_assert(dn_ks(0) == 0 && dn_ks(4) == 6 && dn_ks(9) == 11 && dn_ks(10) == 14 && dn_ks(19) == 25 && dn_ks(20) == 4 && dn_ks(22) == 12 && dn_ks(24) == 18 && dn_ks(27) == 27, "");
struct Two { unsigned s0, s1; };
constexpr int SL_WQ = 56;
template <int J> DI void pf_wq(Ctx& x) {
  ldl<1>(x.vl16, x.rs, OFF_WQ + ((x.m & 7) * 56 + 14 * x.w + J) * KB, (SL_WQ + 14 * x.w + J) * KB);
}
DI Two base_wo(const Ctx& x) { const int ct = x.m % 7;
  return Two{OFF_WO + ((2 * ct + (x.w & 1)) * 32 + 8 * x.w) * KB, OFF_WO + ((2 * ct + ((x.w & 1) ^ 1)) * 32 + 8 * x.w) * KB}; }
DI void pf_wo(Ctx& x) {
  const Two b = base_wo(x);
  sfor<0, 8>([&](auto i) {
    lda<A_WO + 8 * i, (i & 3) * 1024>(x.vl16, x.rs, b.s0 + (i >> 2) * 4096);
    lda<A_WO + 8 * i + 4, (i & 3) * 1024>(x.vl16, x.rs, b.s1 + (i >> 2) * 4096);
  });
}
DI Two base_wd(const Ctx& x) { const int ct = x.m % 7;
  return Two{OFF_WD + ((2 * ct + (x.w & 1)) * 112 + 28 * x.w) * KB, OFF_WD + ((2 * ct + ((x.w & 1) ^ 1)) * 112 + 28 * x.w) * KB}; }
template <int I0, int I1> DI void pf_wd(Ctx& x, const Two& b) {
  sfor<I0, I1>([&](auto f) {
    constexpr int i = f >> 1;
    lda<A_WD + 4 * f, (i & 3) * 1024>(x.vl16, x.rs, ((f & 1) ? b.s1 : b.s0) + (i >> 2) * 4096);
  });
}
DI void pf_wgu1(Ctx& x) {
  for (int t = 0; t < 14; ++t) ldl<2>(x.vl16, x.rs, OFF_WGU + (x.m * 196 + 8 * t + 2 * x.w) * KB, (8 * t + 2 * x.w) * KB);
}
template <int J> DI void pf_wgu3(Ctx& x) {
  ldl<1>(x.vl16, x.rs, OFF_WGU + (x.m * 196 + 160 + 9 * x.w + J) * KB, (9 * x.w + J) * KB);
}

constexpr unsigned S_QP = SCR, S_KP = SCR + 16384, S_QF = SCR + 24576, S_ST = SCR + 32768, S_CS = SCR + 33792, S_KM = SCR + 36864, S_TR = SCR + 40960;
DI unsigned kv_finish(Ctx& x, const float (&raw)[16], const f32x4 (&cs)[4], float mean, float rstd, bool isv, unsigned so, int slot) {
  const int h = x.l >> 5, r = x.l & 31;
  u32x4 y[2];
#pragma unroll
  for (int d = 0; d < 8; ++d)
    y[d >> 2][d & 3] = cvtpk((raw[2 * d] - mean * cs[d >> 1][2 * (d & 1)]) * rstd, (raw[2 * d + 1] - mean * cs[d >> 1][2 * (d & 1) + 1]) * rstd);
  if (h) { y[1][2] = 0; y[1][3] = 0; }
  unsigned nb = 0;
  if (!isv) {
    float n2 = 0.f;
#pragma unroll
    for (int d = 0; d < 8; ++d) n2 = dot2(y[d >> 2][d & 3], y[d >> 2][d & 3], n2);
    n2 = sum32(n2);
    nb = umax64(n2 < 3.0e38f ? __float_as_uint(n2) : 0x7f800000u);
    nb = cvtpk(1.01f * __builtin_amdgcn_sqrtf(__uint_as_float(nb)), 0.f);
    if (h) y[1][2] = 0x3f80u;
  } else {
    if (h) y[1][2] = 0x3f80u;
    char* im = lds + S_TR + slot * 2048;
#pragma unroll
    for (int a = 0; a < 4; ++a) *(u32x2*)(im + r * 64 + 16 * a + 8 * h) = u32x2{y[a >> 1][2 * (a & 1)], y[a >> 1][2 * (a & 1) + 1]};
    asm volatile("s_waitcnt lgkmcnt(0)" ::: "memory");
    const char* tb = im + (4 * h + ((x.l >> 2) & 3)) * 64 + ((x.l >> 4) & 1) * 32 + (x.l & 3) * 8;
    y[0] = trfrag(tb, tb + 512); y[1] = trfrag(tb + 1024, tb + 1536);
  }
  st4<0>(y[0], x.vl16, x.rs, so); st4<1024>(y[1], x.vl16, x.rs, so);
  return nb;
}
DI void kv_publish(Ctx& x, int L, int p, unsigned km) {
  get_tag(x);
  st2<0>(u32x2{(x.tlo & 0xffff0000u) | km, x.thi}, x.l < 16 ? x.l * 1024 : ~0u, x.rs, OFF_SLOT + (L * 2048 + p) * 8);
}
DI void q_finish(Ctx& x, const f32x16& qa, const f32x4 (&cq)[4], float mean, float rstd) {
  const int h = x.l >> 5;
  u32x4 qf[2];
  const f32x4* p = (const f32x4*)(lds + S_QP + x.w * 4096 + x.vl16);
  const float sc = rstd * QSCALE;
#pragma unroll
  for (int q = 0; q < 4; ++q) {
    const f32x4 o = p[q * 64];
    float v[4];
#pragma unroll
    for (int i = 0; i < 4; ++i) v[i] = (qa[4 * q + i] + o[i] - mean * cq[q][i]) * sc;
    qf[q >> 1][2 * (q & 1)] = cvtpk(v[0], v[1]); qf[q >> 1][2 * (q & 1) + 1] = cvtpk(v[2], v[3]);
  }
  if (h) { qf[1][2] = 0; qf[1][3] = 0; }
  float qn2 = 0.f, qn3 = 0.f;
#pragma unroll
  for (int d = 0; d < 4; ++d) { qn2 = dot2(qf[0][d], qf[0][d], qn2); qn3 = dot2(qf[1][d], qf[1][d], qn3); }
  qn2 = sum32(qn2 + qn3);
  if (h) qf[1][2] = cvtpk(1.01f * __builtin_amdgcn_sqrtf(qn2), 0.f);
  ((u32x4*)(lds + S_QF + x.w * 2048 + x.vl16))[0] = qf[0];
  ((u32x4*)(lds + S_QF + x.w * 2048 + x.vl16))[64] = qf[1];
}
DI void put_tile(char* p, const f32x16& t) {
#pragma unroll
  for (int q = 0; q < 4; ++q) ((f32x4*)p)[q * 64] = f32x4{t[4 * q], t[4 * q + 1], t[4 * q + 2], t[4 * q + 3]};
}

constexpr unsigned IMG_Q = 0, IMG_T = 56 * KB;
DI rsrc_t mkrs(const char* p, unsigned n) { return __builtin_amdgcn_make_buffer_rsrc((char*)p, 0, n, 0x00020000); }

DI void ask_slots(Ctx& x, int L) {
  if (wave0(x)) asm volatile("buffer_load_dwordx4 a[248:251], %0, %1, %2 offen sc1" :: "v"(x.vl16), "s"(x.rs), "s"(OFF_SLOT + (L * 16 + x.c) * 1024) : ACLOB);
}
DI void kv_wait(Ctx& x, int L) {
  wv<0>();
  if (wave0(x)) {
    get_tag(x);
    unsigned s0, s1, s2, s3;
#pragma nounroll
    for (unsigned spin = 0; spin < (1u << 18); ++spin) {
      asm volatile("v_accvgpr_read_b32 %0, a248\n\tv_accvgpr_read_b32 %1, a249\n\tv_accvgpr_read_b32 %2, a250\n\tv_accvgpr_read_b32 %3, a251" : "=v"(s0), "=v"(s1), "=v"(s2), "=v"(s3));
      if (__all(s1 == x.thi && s3 == x.thi && (((s0 ^ x.tlo) | (s2 ^ x.tlo)) >> 16) == 0)) break;
      __builtin_amdgcn_s_sleep(2);
      ask_slots(x, L); wv<0>();
    }
    const unsigned km = umax64(max(s0 << 16, s2 << 16));
    if (x.l == 0) ((unsigned*)(lds + S_KM))[4] = km;
  }
  bar();
}
DI void ask_kv(Ctx& x, int L, f32x16 (&kf)[3]) {
  const unsigned kvs = OFF_KV + L * SZ_KV + 16 * x.w * 4096;
  asm volatile(".irp i,0,1,2,3\n\tbuffer_load_dwordx4 v[128+4*\\i:131+4*\\i], %3, %4, %5 offen offset:1024*\\i sc1\n\t.endr\n\t"
               ".irp i,0,1,2,3\n\tbuffer_load_dwordx4 v[144+4*\\i:147+4*\\i], %3, %4, %6 offen offset:1024*\\i sc1\n\t.endr\n\t"
               ".irp i,0,1,2,3\n\tbuffer_load_dwordx4 v[160+4*\\i:163+4*\\i], %3, %4, %7 offen offset:1024*\\i sc1\n\t.endr"
               : "={v[128:143]}"(kf[0]), "={v[144:159]}"(kf[1]), "={v[160:175]}"(kf[2]) : "{v204}"(x.vl16), "s"(x.rs), "s"(kvs), "s"(kvs + 4096), "s"(kvs + 8192));
}

DI void phase_pre(Ctx& x, const Args& a, f32x16 (&kf)[3]) {
  const int w = x.w, l = x.l, h = l >> 5, r = l & 31;
  const int th = x.m >> 3, hp = x.m & 7, tl = w & 1, kh = w >> 1, tt = 4 * x.c + 2 * th + tl, wi = 2 * x.c + th;
  const bool kvp = hp < 2;
  if (!kvp) __builtin_amdgcn_s_sleep(48);
  u32x4 xf[14];
  { const rsrc_t rx = mkrs(a.x, S * D * 2);
    sfor<0, 14>([&](auto i) { ld4i<32 * i>(xf[i], r * 896 + h * 16, rx, tt * 32 * 896 + kh * 448); }); }
  if (x.m == 2) asm volatile("buffer_load_dword a116, %0, %1, 0 offen sc1" :: "v"(l < 16 ? OFF_FLAG + (x.c * 16 + l) * 256 : OFF_SLOT + ((l >> 3) & 1) * 16384 + x.c * 1024 + (l & 7) * 128), "s"(x.rs) : ACLOB);
  asm volatile("buffer_load_dwordx2 a[252:253], off, %0, %1 sc1" :: "s"(x.rs), "s"(OFF_TAG) : ACLOB);
  pstamp(x);
  const int task = wi * 6 + hp - 2;
  const rsrc_t rq = mkrs(a.wq, D * D * 2);
  auto ask_q = [&](auto i) {
    ldl<1>((8 * w + (l >> 3)) * 896 + (l & 7) * 16, rq, hp * 112 + i * 32 * 896, IMG_Q + (4 * i + w) * KB);
  };
  auto ask_t = [&](const char* tp, unsigned tn, unsigned ts, unsigned tb) {
    const rsrc_t rt = mkrs(tp, tn);
    const unsigned vo = (8 * w + (l >> 3)) * ts + (l & 7) * 16;
    sfor<0, 7>([&](auto i) { ldl<1>(vo, rt, tb + i * 32 * ts, IMG_T + (4 * i + w) * KB); });
  };
  const int tco = ((l >> 4) & 1) * 32 + (l & 3) * 8;
  const int sro = 4 * h + ((l >> 2) & 3);
  if (kvp) ask_t(hp ? a.wv : a.wk, D * 56, 128, 0);
  else if (task < 112) ask_t(task < 56 ? a.wg : a.wu, D * F * 2, F * 2, (task & 1) * 224 * F * 2 + ((task % 56) >> 1) * 128);
  else if (task < 168) ask_t(a.wd, D * F * 2, 896, ((task - 112) / 7) * 224 * 896 + ((task - 112) % 7) * 128);
  else ask_t(a.wo, task < 182 ? D * D * 2 : 0, 896, ((task - 168) / 7) * 224 * 896 + ((task - 168) % 7) * 128);
  wv<8>();
  {
    float s = 0.f, q = 0.f, s1 = 0.f, q1 = 0.f;
    sfor<0, 14>([&](auto i) {
      if (!kvp) ask_q(i);
      pin(xf[i]);
      s = dot2(xf[i][0], 0x3f803f80u, s); q = dot2(xf[i][0], xf[i][0], q); s1 = dot2(xf[i][1], 0x3f803f80u, s1); q1 = dot2(xf[i][1], xf[i][1], q1);
      s = dot2(xf[i][2], 0x3f803f80u, s); q = dot2(xf[i][2], xf[i][2], q); s1 = dot2(xf[i][3], 0x3f803f80u, s1); q1 = dot2(xf[i][3], xf[i][3], q1);
      pin(s); pin(q); pin(s1); pin(q1);
    });
    s = sum32(s + s1); q = sum32(q + q1);
    if (h == 0) ((f32x2*)(lds + S_ST))[w * 32 + r] = f32x2{s, q};
  }
  if (kvp) wv<0>(); else wv<14>();
  bar();
  stamp(x);
  if (!kvp) {
    if (task < 112) {
      const int g = l >> 4, mat = task >= 56, gt = 4 * ((task % 56) >> 1) + w, m = gt / 7, j = gt % 7, lt = (m & 1) ? j : (j == 6 ? 0 : j + 1);
      const char* tb = lds + IMG_T + (16 * (g >> 1) + 4 * (g & 1) + ((l >> 2) & 3)) * 128 + 32 * w + (l & 3) * 8;
      const unsigned st = lt < 5 ? 10 * KB : 4 * KB, so = OFF_WGU + (m * 196 + (lt < 5 ? lt * 2 + mat : 140 + (lt - 5) * 2 + mat)) * KB + (task & 1) * 7 * st;
      u32x4 f[7];
      sfor<0, 7>([&](auto i) { f[i] = trfrag(tb + i * 4096, tb + i * 4096 + 1024); });
      float cs = 0.f;
      sfor<0, 7>([&](auto i) { st4<0>(f[i], x.vl16, x.rs, so + i * st); cs = fragsum(f[i], cs); });
      cs = sum32(sum16(cs));
      st1(cs, l < 16 ? l * 4 : ~0u, x.rs, OFF_CSGU + m * 2048 + (task & 1) * 1024 + (lt * 2 + mat) * 64);
    } else if (task < 168) {
      const int nt = 2 * ((task - 112) % 7) + (w & 1), rb = (task - 112) / 7;
      const char* tb = lds + IMG_T + sro * 128 + 64 * (w & 1) + tco;
      const unsigned so = OFF_WD + (nt * 112 + 28 * (rb >> 1)) * KB, sa = so + (rb & 1) * 10 * KB, sb = so + (20 + (rb & 1) * 4) * KB;
      u32x4 f[7];
      if (w < 2) {
        sfor<0, 7>([&](auto i) { const char* p = tb + (32 * (i >> 1) + 8 * (i & 1)) * 128; f[i] = trfrag(p, p + 2048); });
        sfor<0, 7>([&](auto i) { st4<0>(f[i], x.vl16, x.rs, i < 4 ? sa + i * KB : (i < 6 ? sb + (i - 4) * KB : sa + 4 * KB)); });
      } else {
        sfor<0, 7>([&](auto i) { const char* p = tb + (32 * ((i + 7) >> 1) + 8 * ((i + 7) & 1)) * 128; f[i] = trfrag(p, p + 2048); });
        sfor<0, 7>([&](auto i) { st4<0>(f[i], x.vl16, x.rs, i < 5 ? sa + (5 + i) * KB : sb + (i - 3) * KB); });
      }
      st1(0.f, ~0u, x.rs, 0);
    } else {
      const int nt = 2 * ((task - 168) % 7) + (w & 1);
      const char* tb = lds + IMG_T + (112 * (w >> 1) + sro) * 128 + 64 * (w & 1) + tco;
      const unsigned vo = task < 182 ? x.vl16 : ~0u - 4096;
      u32x4 f[8];
      sfor<0, 8>([&](auto i) { const char* p = tb + (28 * (i >> 1) + 16 * (i & 1)) * 128; f[i] = trfrag(p, p + 1024); if ((i & 1) && h) { f[i][2] = 0; f[i][3] = 0; } });
      sfor<0, 8>([&](auto i) { st4<(i & 3) * 1024>(f[i], vo, x.rs, OFF_WO + (nt * 32 + 16 * ((task - 168) / 7) + 8 * (w >> 1) + (i & ~3)) * KB); });
    }
  }
  pstamp(x);
  if (!kvp) { wv<8>(); bar(); }
  stamp(x);
  float mean, rstd;
  { const f32x2 p0 = ((const f32x2*)(lds + S_ST))[tl * 32 + r], p1 = ((const f32x2*)(lds + S_ST))[(tl + 2) * 32 + r];
    row_stats(p0[0] + p1[0], p0[1] + p1[1], mean, rstd); }
  float* csw = (float*)(lds + S_CS);
  const int tro = 8 * h + ((l >> 2) & 3);
  u32x4 ones = {0x3f803f80u, 0x3f803f80u, 0x3f803f80u, 0x3f803f80u};
  pin(ones);
  if (kvp) {
    f32x16 kv, cs;
    const unsigned tb = IMG_T + (224 * kh + tro) * 56 + tco;
    u32x2 f[14][2];
    auto rd = [&](auto i) { trd<i * 896>(f[i][0], tb); trd<i * 896 + 224>(f[i][1], tb); };
    sfor<0, 7>(rd);
    sfor<0, 14>([&](auto i) {
      if constexpr (i + 7 < 14) { wl<12>(); rd(std::integral_constant<int, i + 7>{}); } else wl<2 * (13 - i)>();
      pin(f[i][0]); pin(f[i][1]);
      u32x4 g = {f[i][0][0], f[i][0][1], f[i][1][0], f[i][1][1]};
      pin(g); nop<0>();
      if constexpr (i == 0) { m32z(kv, g, xf[i]); m32z(cs, g, ones); } else { m32(kv, g, xf[i]); m32(cs, g, ones); }
    });
    nop<11>(); pin(kv); pin(cs);
    if (r == 0) sfor<0, 4>([&](auto q) { *(f32x4*)(csw + (w * 3 + 2) * 32 + 8 * q + 4 * h) = f32x4{cs[4 * q], cs[4 * q + 1], cs[4 * q + 2], cs[4 * q + 3]}; });
    pstamp(x);
    if (kh) put_tile(lds + S_KP + tl * 4096 + x.vl16, kv);
    bar();
    if (!kh) {
      if (x.m == 0 && w == 0) st1(0.f, l < 16 ? l * 256 : ~0u, x.rs, OFF_FLAG + x.c * 4096);
      float raw[16]; f32x4 c4[4];
      sfor<0, 4>([&](auto q) {
        const f32x4 o = ((const f32x4*)(lds + S_KP + tl * 4096 + x.vl16))[q * 64];
        c4[q] = *(const f32x4*)(csw + (w * 3 + 2) * 32 + 8 * q + 4 * h) + *(const f32x4*)(csw + ((w + 2) * 3 + 2) * 32 + 8 * q + 4 * h);
#pragma unroll
        for (int i = 0; i < 4; ++i) raw[4 * q + i] = kv[4 * q + i] + o[i];
      });
      const unsigned km = kv_finish(x, raw, c4, mean, rstd, hp == 1, OFF_KV + (tt * 4 + 2 * hp) * KB, tl);
      pstamp(x);
      wv<0>();
      kv_publish(x, 0, 2 * tt + hp, km);
      if (tt == 0 && r == 0) sfor<0, 4>([&](auto q) { st4<32 * q>(__builtin_bit_cast(u32x4, c4[q]), h * 16, x.rs, OFF_CSQ + (16 + hp) * 128); });
    } else {
      sfor<0, 28>([&](auto i) { ldl<1>((16 * tl + 8 * (i & 1) + (l >> 3)) * 896 + (l & 7) * 16, rq, hp * 112 + (i >> 1) * 32 * 896, IMG_Q + (4 * (i >> 1) + 2 * tl + (i & 1)) * KB); });
      pstamp(x);
      wv<0>();
    }
    bar();
    pstamp(x);
  } else { pstamp(x); pstamp(x); pstamp(x); }
  f32x16 qa;
  {
    f32x16 q1, q2, cs;
    const unsigned t1 = IMG_Q + (224 * kh + tro) * 128 + tco + 56 * tl, t2 = IMG_Q + (224 * kh + tro) * 128 + tco + 56 * (tl ^ 1);
    u32x2 f[14][4];
    auto rd = [&](auto i) { trd<i * 2048>(f[i][0], t1); trd<i * 2048 + 512>(f[i][1], t1); trd<i * 2048>(f[i][2], t2); trd<i * 2048 + 512>(f[i][3], t2); };
    wl<0>();
    sfor<0, 3>(rd);
    sfor<0, 14>([&](auto i) {
      if constexpr (i + 3 < 14) { wl<8>(); rd(std::integral_constant<int, i + 3>{}); } else wl<4 * (13 - i)>();
      sfor<0, 4>([&](auto e) { pin(f[i][e]); });
      u32x4 g1 = {f[i][0][0], f[i][0][1], f[i][1][0], f[i][1][1]}, g2 = {f[i][2][0], f[i][2][1], f[i][3][0], f[i][3][1]};
      pin(g1); pin(g2); nop<0>();
      if constexpr (i == 0) { m32z(q1, g1, xf[i]); m32z(q2, g2, xf[i]); m32z(cs, g1, ones); } else { m32(q1, g1, xf[i]); m32(q2, g2, xf[i]); m32(cs, g1, ones); }
      if constexpr (i == 10) { ask_slots(x, 0); SB(); }
    });
    nop<11>(); pin(q1); pin(q2); pin(cs);
    pstamp(x);
    if (r == 0) sfor<0, 4>([&](auto q) { *(f32x4*)(csw + (w * 3) * 32 + 8 * q + 4 * h) = f32x4{cs[4 * q], cs[4 * q + 1], cs[4 * q + 2], cs[4 * q + 3]}; });
    if (kh == tl) { put_tile(lds + S_QP + (w ^ 2) * 4096 + x.vl16, q2); qa = q1; } else { put_tile(lds + S_QP + (w ^ 2) * 4096 + x.vl16, q1); qa = q2; }
  }
  if (th == 0 && x.c < 14) {
    const int f = 4 * x.c + w, hd = f >= 28, ks = f - 28 * hd;
    const char* tb = lds + IMG_Q + (16 * ks + sro) * 128 + tco + 56 * hd;
    st4<0>(trfrag(tb, tb + 1024), x.vl16, x.rs, OFF_WQ + (hp * 56 + f) * KB);
  }
  if (kvp && th == 1 && x.c < 7) {
    const char* tb = lds + IMG_T + (16 * (4 * x.c + w) + sro) * 56 + tco;
    st4<0>(trfrag(tb, tb + 448), x.vl16, x.rs, OFF_WQ + ((16 + hp) * 28 + 4 * x.c + w) * KB);
  }
  pstamp(x);
  kv_wait(x, 0);
  count(x, FL_W(x.c), 1);
  ask_kv(x, 0, kf);
  stamp(x);
  const bool own = x.c == hp && th == 0;
  {
    f32x4 cq[4];
    sfor<0, 4>([&](auto q) {
      cq[q] = *(const f32x4*)(csw + (kh * 3) * 32 + 8 * q + 4 * h) + *(const f32x4*)(csw + ((kh + 2) * 3) * 32 + 8 * q + 4 * h);
      if (own && tl == 0 && r == 0) st4<32 * q>(__builtin_bit_cast(u32x4, cq[q]), h * 16, x.rs, OFF_CSQ + (2 * hp + kh) * 128);
    });
    q_finish(x, qa, cq, mean, rstd);
  }
  pstamp(x);
}

DI void phase_front(Ctx& x, f32x16 (&kf)[3]) {
  const int w = x.w, l = x.l, h = l >> 5, r = l & 31;
  const int th = x.m >> 3, hp = x.m & 7, tl = w & 1, kh = w >> 1, tt = 4 * x.c + 2 * th + tl;
  const unsigned KV = OFF_KV + SZ_KV;
  {
    f32x4 cq[4], ck[4];
    sfor<0, 4>([&](auto q) { ld4f<32 * q>(cq[q], h * 16, x.rs, OFF_CSQ + (2 * hp + kh) * 128); ld4f<32 * q>(ck[q], h * 16, x.rs, OFF_CSQ + (16 + (hp & 1)) * 128); });
    wv<0>();
    await(x, FL_X(x.c, th), 7);
    stamp(x);
    u32x4 xf[14]; f32x2 sp[7];
    {
      const unsigned so = OFF_X + 2 * SZ_X + (tt * 28 + 14 * kh) * KB;
      sfor<0, 7>([&](auto i) { ld4<(i & 3) * 1024>(xf[i], x.vl16, x.rs, so + (i >> 2) * 4096); });
      sfor<0, 7>([&](auto i) { ld2<0>(sp[i], (h * 7 * 2048 + r) * 8, x.rs, OFF_ST + 2 * SZ_ST + (i * 2048 + tt * 32) * 8); });
      sfor<7, 14>([&](auto i) { ld4<(i & 3) * 1024>(xf[i], x.vl16, x.rs, so + (i >> 2) * 4096); });
    }
    float mean, rstd;
    auto stats = [&]() {
      sfor<0, 7>([&](auto i) { pin(sp[i]); });
      sfor<0, 4>([&](auto i) { pin(cq[i]); pin(ck[i]); });
      float s = 0.f, q = 0.f;
#pragma unroll
      for (int i = 0; i < 7; ++i) { s += sp[i][0]; q += sp[i][1]; }
      row_stats(sum32(s), sum32(q), mean, rstd);
      pin(mean); pin(rstd); SB();
    };
    f32x16 qa, qb;
    u32x4 wa[14], wb[14];
    const unsigned pa = x.vl16 + (SL_WQ + 28 * kh + 14 * kh) * KB, pb = x.vl16 + (SL_WQ + 28 * (kh ^ 1) + 14 * kh) * KB;
    if (hp < 2) {
      f32x16 kv;
      sfor<0, 14>([&](auto i) { dsr<i * 1024>(wa[i], x.vl16 + 14 * kh * KB); });
      sfor<0, 14>([&](auto i) {
        wv<(i < 7 ? 20 : 13) - i>(); wl<13 - i>();
        if constexpr (i == 0) m32z(kv, wa[i], xf[i]); else m32(kv, wa[i], xf[i]);
        if constexpr (i == 7) stats();
      });
      nop<11>(); pin(kv);
      if (kh) put_tile(lds + S_KP + tl * 4096 + x.vl16, kv);
      bar();
      unsigned km = 0;
      if (!kh) {
        float raw[16];
        sfor<0, 4>([&](auto q) {
          const f32x4 o = ((const f32x4*)(lds + S_KP + tl * 4096 + x.vl16))[q * 64];
#pragma unroll
          for (int i = 0; i < 4; ++i) raw[4 * q + i] = kv[4 * q + i] + o[i];
        });
        km = kv_finish(x, raw, ck, mean, rstd, hp == 1, KV + (tt * 4 + 2 * hp) * KB, tl);
      }
      wl<0>();
      sfor<0, 7>([&](auto i) { dsr<i * 1024>(wb[i], pb); dsr<i * 1024>(wa[i], pa); });
      sfor<0, 14>([&](auto i) {
        if constexpr (i < 7) wl<12>(); else wl<2 * (13 - i)>();
        if constexpr (i == 0) { m32z(qb, wb[i], xf[i]); m32z(qa, wa[i], xf[i]); } else { m32(qb, wb[i], xf[i]); m32(qa, wa[i], xf[i]); }
        if constexpr (i < 7) { dsr<(i + 7) * 1024>(wb[i + 7], pb); dsr<(i + 7) * 1024>(wa[i + 7], pa); }
        if constexpr (i == 8) { if (!kh) { wv<0>(); kv_publish(x, 1, 2 * tt + hp, km); } SB(); }
      });
    } else {
      sfor<0, 7>([&](auto i) { dsr<i * 1024>(wb[i], pb); dsr<i * 1024>(wa[i], pa); });
      sfor<0, 14>([&](auto i) {
        wv<(i < 7 ? 20 : 13) - i>();
        if constexpr (i < 7) wl<12>(); else wl<2 * (13 - i)>();
        if constexpr (i == 0) { m32z(qb, wb[i], xf[i]); m32z(qa, wa[i], xf[i]); } else { m32(qb, wb[i], xf[i]); m32(qa, wa[i], xf[i]); }
        if constexpr (i < 7) { dsr<(i + 7) * 1024>(wb[i + 7], pb); dsr<(i + 7) * 1024>(wa[i + 7], pa); }
        if constexpr (i == 7) stats();
      });
    }
    nop<11>();
    pin(qa); pin(qb);
    put_tile(lds + S_QP + (w ^ 2) * 4096 + x.vl16, qb);
    bar();
    q_finish(x, qa, cq, mean, rstd);
    stamp(x);
    ask_slots(x, 1);
    kv_wait(x, 1);
    ask_kv(x, 1, kf);
    stamp(x);
  }
}

DI void phase_att(Ctx& x, const int L, f32x16 (&kf)[3], f32x16 (&g2)[3]) {
  const int w = x.w, l = x.l, h = l >> 5, r = l & 31;
  const int th = x.m >> 3, hp = x.m & 7, tl = w & 1, kh = w >> 1, tt = 4 * x.c + 2 * th + tl;
  const unsigned kvs = OFF_KV + L * SZ_KV + 16 * w * 4096;
  stamp(x);
  if (L == 0) bar();
  stamp(x);
  unsigned kma = S_KM + 16; asm volatile("" : "+s"(kma));
  const float kmx = *(const float*)(lds + kma);
  u32x4 q[4][2];
#pragma unroll
  for (int k = 0; k < 4; ++k) {
    q[k][0] = ((const u32x4*)(lds + S_QF + (w ^ k) * 2048 + x.vl16))[0];
    q[k][1] = ((const u32x4*)(lds + S_QF + (w ^ k) * 2048 + x.vl16))[64];
  }
  bool safe = true;
  {
#pragma unroll
    for (int k = 0; k < 4; ++k) {
      const float mrow = bflo(q[k][1][2]) * kmx + 0.01f;
      safe = safe && (!h || mrow <= M_LIMIT);
      if (h) q[k][1][2] = cvtpk(-mrow, 0.f);
    }
  }
  const bool fast = __all(safe);
  stamp(x);
  f32x16 own;
  if (fast) {
    unsigned so_kv = kvs + 3 * 4096, so_w = OFF_WGU + (x.m * 196 + 2 * w) * KB, so_l = 2 * w * KB, spin = 1u << 16, fvo = (l & 15) * 4096;
    Two ab = base_wo(x);
    const Two wd = base_wd(x);
    asm volatile(
      ".macro GRP s, pa, pb\n\t"
      "v_exp_f32 v200, v[\\s]\n\tv_exp_f32 v201, v[\\s+1]\n\tv_cvt_pk_bf16_f32 v[\\pa], v202, v203\n\t"
      "v_exp_f32 v202, v[\\s+2]\n\tv_exp_f32 v203, v[\\s+3]\n\tv_cvt_pk_bf16_f32 v[\\pb], v200, v201\n\t"
      ".endm\n\t"
      ".macro MEM kind, i, ld\n\t"
      ".if \\kind == 1\n\t"
      "buffer_load_dwordx4 v[128+16*\\ld+4*\\i:128+16*\\ld+4*\\i+3], %[vo], %[rs], %[skv] offen offset:1024*\\i sc1\n\t"
      ".elseif \\kind == 5\n\t"
      "ds_write_b128 v205, v[4*\\i:4*\\i+3] offset:1024*\\i\n\t"
      ".elseif \\kind == 6\n\t"
      "ds_write_b128 v205, v[4:7] offset:1024\n\t"
      ".elseif \\kind == 7\n\t"
      "ds_write_b128 v205, v[12:15] offset:3072\n\t"
      ".elseif \\kind == 2\n\t"
      "buffer_load_dword v206, v205, %[rs], 0 offen offset:3072 sc1\n\t"
      ".elseif \\kind == 3\n\t"
      ".if att_n < 16 || att_n >= 56\n\t"
      ".if att_n < 102\n\t"
      ".if att_n < 16\n\t.set att_r, 184+4*att_n\n\t.else\n\t.set att_r, 4*(att_n-56)\n\t.endif\n\t"
      ".if att_n & 1\n\t"
      "buffer_load_dwordx4 a[att_r:att_r+3], %[vo], %[rs], %[ab1] offen offset:1024*((att_n>>1)&3) sc1\n\t"
      ".else\n\t"
      "buffer_load_dwordx4 a[att_r:att_r+3], %[vo], %[rs], %[ab0] offen offset:1024*((att_n>>1)&3) sc1\n\t"
      ".endif\n\t"
      ".if att_n == 15\n\t"
      "s_mov_b32 %[ab0], %[wd0]\n\ts_mov_b32 %[ab1], %[wd1]\n\t"
      ".elseif (att_n & 7) == 7\n\t"
      "s_add_u32 %[ab0], %[ab0], 4096\n\ts_add_u32 %[ab1], %[ab1], 4096\n\t"
      ".endif\n\t"
      ".endif\n\t"
      ".elseif att_n < 44\n\t"
      ".if att_n & 1\n\t"
      "buffer_load_dwordx4 %[vo], %[rs], %[sw] offen offset:1024 sc1 lds\n\t"
      "s_add_u32 %[sw], %[sw], 8192\n\ts_add_u32 m0, m0, 8192\n\t"
      ".else\n\t"
      "buffer_load_dwordx4 %[vo], %[rs], %[sw] offen sc1 lds\n\t"
      ".endif\n\t"
      ".else\n\t"
      "buffer_load_dwordx4 v[208+4*(att_n-44):208+4*(att_n-44)+3], %[vo], %[rs], %[sw] offen offset:1024*(att_n&1) sc1\n\t"
      ".if att_n & 1\n\ts_add_u32 %[sw], %[sw], 8192\n\t.endif\n\t"
      ".endif\n\t"
      ".set att_n, att_n+1\n\t"
      ".endif\n\t"
      ".endm\n\t"
      ".macro STEP sc, sn, k, nk, pk, tk, nt, pt, ld, m0k, m0i, m1k, m2k, m2i, m3k\n\t"
      "v_mfma_f32_32x32x16_bf16 v[\\sn:\\sn+15], v[128+16*\\nt:128+16*\\nt+3], v[64+8*\\nk:64+8*\\nk+3], 0\n\t"
      "MEM \\m0k, \\m0i, \\ld\n\t"
      "GRP \\sc, 199, 192\n\t"
      "v_mfma_f32_32x32x16_bf16 v[\\sn:\\sn+15], v[132+16*\\nt:132+16*\\nt+3], v[68+8*\\nk:68+8*\\nk+3], v[\\sn:\\sn+15]\n\t"
      "MEM \\m1k, 0, 0\n\t"
      "GRP \\sc+4, 193, 194\n\t"
      "v_mfma_f32_32x32x16_bf16 v[16*\\pk:16*\\pk+15], v[140+16*\\pt:140+16*\\pt+3], v[196:199], v[16*\\pk:16*\\pk+15]\n\t"
      "MEM \\m2k, \\m2i, \\ld\n\t"
      "GRP \\sc+8, 195, 196\n\t"
      "v_mfma_f32_32x32x16_bf16 v[16*\\k:16*\\k+15], v[136+16*\\tk:136+16*\\tk+3], v[192:195], v[16*\\k:16*\\k+15]\n\t"
      "MEM \\m3k, 0, 0\n\t"
      "GRP \\sc+12, 197, 198\n\t"
      ".endm\n\t"
      ".macro TILE tk, nx, pv, on, wait, p, fl=0\n\t"
      "STEP 96, 112, 0, 1, 3, \\tk, \\tk, \\pv, \\pv, \\p, 0, \\p, \\p, 0, \\fl\n\t"
      "STEP 112, 96, 1, 2, 0, \\tk, \\tk, \\tk, \\pv, \\on, 0, \\p, \\on, 1, 0\n\t"
      "STEP 96, 112, 2, 3, 1, \\tk, \\tk, \\tk, \\pv, \\on, 2, \\p, \\on, 3, 0\n\t"
      ".if \\on\n\ts_add_u32 %[skv], %[skv], 4096\n\t.endif\n\t"
      "s_waitcnt vmcnt(\\wait)\n\t"
      "STEP 112, 96, 3, 0, 2, \\tk, \\nx, \\tk, \\pv, \\p, 0, \\p, \\p, 0, 0\n\t"
      ".endm\n\t"
      ".set att_n, 0\n\t"
      "s_mov_b32 m0, %[sl]\n\t"
      "v_mov_b32 v196, 0\n\tv_mov_b32 v197, 0\n\tv_mov_b32 v198, 0\n\tv_mov_b32 v199, 0\n\tv_mov_b32 v202, 0\n\tv_mov_b32 v203, 0\n\t"
      "v_mov_b32 v188, 0\n\tv_mov_b32 v189, 0\n\tv_mov_b32 v190, 0\n\tv_mov_b32 v191, 0\n\t"
      "v_mfma_f32_32x32x16_bf16 v[0:15], v[196:199], v[196:199], 0\n\t"
      "v_mfma_f32_32x32x16_bf16 v[16:31], v[196:199], v[196:199], 0\n\t"
      "v_mfma_f32_32x32x16_bf16 v[32:47], v[196:199], v[196:199], 0\n\t"
      "v_mfma_f32_32x32x16_bf16 v[48:63], v[196:199], v[196:199], 0\n\t"
      "s_waitcnt vmcnt(8)\n\t"
      "v_mfma_f32_32x32x16_bf16 v[96:111], v[128:131], v[64:67], 0\n\t"
      "v_mfma_f32_32x32x16_bf16 v[96:111], v[132:135], v[68:71], v[96:111]\n\t"
      "s_nop 11\n\t"
      "TILE 0, 1, 3, 1, 8, 0\n\t"
      "TILE 1, 2, 0, 1, 8, 0\n\tTILE 2, 3, 1, 1, 9, 0, 2\n\t"
      "s_waitcnt vmcnt(4)\n\t"
      "1:\n\t"
      "v_cmp_gt_u32 vcc, 16, v206\n\t"
      "s_and_b64 vcc, exec, vcc\n\t"
      "s_cbranch_vccz 2f\n\t"
      "s_sleep 24\n\t"
      "buffer_load_dword v206, v205, %[rs], 0 offen offset:3072 sc1\n\t"
      "s_sub_u32 %[spin], %[spin], 1\n\t"
      "s_waitcnt vmcnt(0)\n\t"
      "s_cbranch_scc0 1b\n\t"
      "2:\n\t"
      "s_barrier\n\t"
      "TILE 3, 0, 2, 1, 14, 3\n\t"
      "TILE 0, 1, 3, 1, 21, 3\n\tTILE 1, 2, 0, 1, 24, 3\n\tTILE 2, 3, 1, 1, 24, 3\n\tTILE 3, 0, 2, 1, 24, 3\n\t"
      "TILE 0, 1, 3, 1, 24, 3\n\tTILE 1, 2, 0, 1, 24, 3\n\tTILE 2, 3, 1, 1, 24, 3\n\tTILE 3, 0, 2, 1, 24, 3\n\t"
      "TILE 0, 1, 3, 1, 24, 3\n\tTILE 1, 2, 0, 0, 20, 3\n\tTILE 2, 3, 1, 0, 16, 3\n\t"
      "v_add_u32 v205, %[d1], v204\n\tv_add_u32 v206, %[d2], v204\n\t"
      "STEP 96, 112, 0, 1, 3, 3, 3, 2, 0, 3, 0, 3, 3, 0, 3\n\t"
      "STEP 112, 96, 1, 2, 0, 3, 3, 3, 0, 3, 0, 3, 0, 0, 0\n\t"
      "STEP 96, 112, 2, 3, 1, 3, 3, 3, 0, 5, 0, 6, 5, 2, 7\n\t"
      "ds_write_b128 v206, v[16:19]\n\t"
      "GRP 112, 199, 192\n\t"
      "ds_write_b128 v206, v[20:23] offset:1024\n\t"
      "GRP 116, 193, 194\n\t"
      "v_mfma_f32_32x32x16_bf16 v[32:47], v[188:191], v[196:199], v[32:47]\n\t"
      "ds_write_b128 v206, v[24:27] offset:2048\n\t"
      "GRP 120, 195, 196\n\t"
      "v_mfma_f32_32x32x16_bf16 v[48:63], v[184:187], v[192:195], v[48:63]\n\t"
      "ds_write_b128 v206, v[28:31] offset:3072\n\t"
      "GRP 124, 197, 198\n\t"
      "v_add_u32 v200, %[d3], v204\n\t"
      "v_cvt_pk_bf16_f32 v199, v202, v203\n\t"
      "s_nop 1\n\t"
      "v_mfma_f32_32x32x16_bf16 v[48:63], v[188:191], v[196:199], v[48:63]\n\t"
      "ds_write_b128 v200, v[32:35]\n\tds_write_b128 v200, v[36:39] offset:1024\n\tds_write_b128 v200, v[40:43] offset:2048\n\tds_write_b128 v200, v[44:47] offset:3072\n\t"
      "s_nop 7\n\t"
      ".purgem GRP\n\t.purgem MEM\n\t.purgem STEP\n\t.purgem TILE"
      : "={v[48:63]}"(own), "+{v[128:143]}"(kf[0]), "+{v[144:159]}"(kf[1]), "+{v[160:175]}"(kf[2]),
        "={v[208:223]}"(g2[0]), "={v[224:239]}"(g2[1]), "={v[240:255]}"(g2[2]),
        [skv] "+s"(so_kv), [sw] "+s"(so_w), [ab0] "+s"(ab.s0), [ab1] "+s"(ab.s1), [spin] "+s"(spin), "+{v205}"(fvo)
      : "{v[64:67]}"(q[1][0]), "{v[68:71]}"(q[1][1]), "{v[72:75]}"(q[2][0]), "{v[76:79]}"(q[2][1]),
        "{v[80:83]}"(q[3][0]), "{v[84:87]}"(q[3][1]), "{v[88:91]}"(q[0][0]), "{v[92:95]}"(q[0][1]),
        [vo] "{v204}"(x.vl16), [d1] "s"(SCR + (w ^ 1) * 3 * 4096), [d2] "s"(SCR + ((w ^ 2) * 3 + 1) * 4096), [d3] "s"(SCR + ((w ^ 3) * 3 + 2) * 4096), [rs] "s"(x.rs), [sl] "s"(so_l), [wd0] "s"(wd.s0), [wd1] "s"(wd.s1)
      : "memory", "scc", "vcc",
        "v0","v1","v2","v3","v4","v5","v6","v7","v8","v9","v10","v11","v12","v13","v14","v15","v16","v17","v18","v19","v20","v21","v22","v23",
        "v24","v25","v26","v27","v28","v29","v30","v31","v32","v33","v34","v35","v36","v37","v38","v39","v40","v41","v42","v43","v44","v45","v46","v47",
        "v96","v97","v98","v99","v100","v101","v102","v103","v104","v105","v106","v107","v108","v109","v110","v111",
        "v112","v113","v114","v115","v116","v117","v118","v119","v120","v121","v122","v123","v124","v125","v126","v127",
        "v176","v177","v178","v179","v180","v181","v182","v183","v184","v185","v186","v187","v188","v189","v190","v191",
        "v192","v193","v194","v195","v196","v197","v198","v199","v200","v201","v202","v203","v206", ACLOB);
  } else {
    f32x16 o[4];
    wv<0>();
    pin(kf[0]); pin(kf[1]); pin(kf[2]);
    float mx[4];
#pragma unroll
    for (int k = 0; k < 4; ++k) {
      mx[k] = -3.0e38f; if (h) q[k][1][2] = 0; pin(q[k][1]);
#pragma unroll
      for (int i = 0; i < 16; ++i) o[k][i] = 0.f;
    }
#pragma nounroll
    for (unsigned spin = 0; spin < (1u << 16); ++spin) {
      float v;
      ld1(v, (l & 15) * 4096, x.rs, OFF_FLAG + FL_W(0)); wv<0>(); pin(v);
      if (__all(__float_as_uint(v) >= 16u)) break;
      __builtin_amdgcn_s_sleep(24);
    }
    pf_wo(x);
    pf_wgu1(x);
    sfor<0, 12>([&](auto j) {
      u32x4 t;
      ld4<1024 * (j & 1)>(t, x.vl16, x.rs, OFF_WGU + (x.m * 196 + 112 + 8 * (j >> 1) + 2 * w) * KB); wv<0>(); pin(t);
#pragma unroll
      for (int i = 0; i < 4; ++i) g2[j >> 2][4 * (j & 3) + i] = __uint_as_float(t[i]);
    });
    pf_wd<0, 46>(x, base_wd(x));
    for (int tk = 0; tk < 16; ++tk) {
      u32x4 kf[4];
      sfor<0, 4>([&](auto i) { ld4<i * 1024>(kf[i], x.vl16, x.rs, kvs + tk * 4096); });
      wv<0>();
      sfor<0, 4>([&](auto k) {
        f32x16 s;
        nop<1>();
        m32z(s, kf[0], q[k][0]); m32(s, kf[1], q[k][1]);
        nop<11>(); pin(s);
        float tm = s[0];
#pragma unroll
        for (int i = 1; i < 16; ++i) tm = fmaxf(tm, s[i]);
        tm = max32(tm);
        const float mn = fmaxf(mx[k], tm), al = __builtin_amdgcn_exp2f(mx[k] - mn);
        mx[k] = mn;
#pragma unroll
        for (int i = 0; i < 16; ++i) o[k][i] *= al;
        u32x4 p0, p1;
#pragma unroll
        for (int d = 0; d < 4; ++d) {
          p0[d] = cvtpk(__builtin_amdgcn_exp2f(s[2 * d] - mn), __builtin_amdgcn_exp2f(s[2 * d + 1] - mn));
          p1[d] = cvtpk(__builtin_amdgcn_exp2f(s[8 + 2 * d] - mn), __builtin_amdgcn_exp2f(s[9 + 2 * d] - mn));
        }
        pin(p0); pin(p1); pin(o[k]);
        nop<1>();
        m32(o[k], kf[2], p0); m32(o[k], kf[3], p1);
        nop<11>(); pin(o[k]);
      });
    }
    bar();
    float* mm = (float*)(lds + SCR);
#pragma unroll
    for (int k = 0; k < 4; ++k) if (h == 0) mm[((w ^ k) * 4 + w) * 32 + r] = mx[k];
    bar();
#pragma unroll
    for (int k = 0; k < 4; ++k) {
      const float* p = mm + (w ^ k) * 128 + r;
      const float g = fmaxf(fmaxf(p[0], p[32]), fmaxf(p[64], p[96])), f = __builtin_amdgcn_exp2f(mx[k] - g);
#pragma unroll
      for (int i = 0; i < 16; ++i) o[k][i] *= f;
    }
    bar();
    reduce_put(x, o);
    own = o[0];
  }
  stamp(x);
  reduce_get(x, own);
  pin(own); fstamp(x);
  {
    auto lr = __builtin_amdgcn_permlane32_swap(__float_as_uint(own[12]), __float_as_uint(own[12]), false, false);
    const float inv = __builtin_amdgcn_rcpf(__uint_as_float(lr[1]));
    u32x4 a, b;
#pragma unroll
    for (int d = 0; d < 4; ++d) { a[d] = cvtpk(own[2 * d] * inv, own[2 * d + 1] * inv); b[d] = cvtpk(own[8 + 2 * d] * inv, own[9 + 2 * d] * inv); }
    if (h) { b[2] = 0; b[3] = 0; }
    const unsigned so = OFF_O + L * SZ_O + (tt * 32 + 2 * (2 * hp + kh)) * KB;
    st4<0>(a, x.vl16, x.rs, so); st4<1024>(b, x.vl16, x.rs, so);
  }
  fstamp(x);
  wv<0>();
  fstamp(x);
  signal(x, FL_O(L, x.c, th), 1);
  if (L == 0 && th == 0) {
    if (x.c == hp) count(x, FL_W2, 1);
  }
}

template <bool FINAL> DI void res_epilogue(Ctx& x, const f32x16& acc, const u32x4 (&xr)[2], unsigned Xo, unsigned STo, int tt, int nt, char* out = nullptr) {
  const int h = x.l >> 5, r = x.l & 31;
  u32x4 y[2];
#pragma unroll
  for (int d = 0; d < 8; ++d) y[d >> 2][d & 3] = cvtpk(acc[2 * d] + bflo(xr[d >> 2][d & 3]), acc[2 * d + 1] + bfhi(xr[d >> 2][d & 3]));
  if constexpr (FINAL) {
#pragma unroll
    for (int s = 0; s < 2; ++s) {
      auto a = __builtin_amdgcn_permlane32_swap(y[s][0], y[s][2], false, false);
      auto b = __builtin_amdgcn_permlane32_swap(y[s][1], y[s][3], false, false);
      y[s] = u32x4{a[0], b[0], a[1], b[1]};
    }
    const unsigned vo = r * (D * 2) + h * 16, so = tt * 32 * D * 2 + nt * 64;
    const rsrc_t ro = mkrs(out, S * D * 2);
    st4p<0>(y[0], vo, ro, so); st4p<32>(y[1], vo, ro, so);
  } else {
    const unsigned so = Xo + (tt * 28 + 2 * nt) * KB;
    st4<0>(y[0], x.vl16, x.rs, so); st4<1024>(y[1], x.vl16, x.rs, so);
    float s = 0.f, q = 0.f, s1 = 0.f, q1 = 0.f;
#pragma unroll
    for (int d = 0; d < 4; ++d) {
      s = dot2(y[0][d], 0x3f803f80u, s); q = dot2(y[0][d], y[0][d], q); s1 = dot2(y[1][d], 0x3f803f80u, s1); q1 = dot2(y[1][d], y[1][d], q1);
    }
    s = sum32(s + s1); q = sum32(q + q1);
    if (h == 0) st2<0>(u32x2{__float_as_uint(s), __float_as_uint(q)}, r * 8, x.rs, STo + (nt * 2048 + tt * 32) * 8);
  }
}

DI void phase_oproj(Ctx& x, const int L, const Args& a, const f32x16 (&g2)[3]) {
  const int w = x.w;
  if (x.m < 14) {
    const int th = x.m / 7, ct = x.m % 7, tt0 = 4 * x.c + 2 * th, tt = tt0 + (w >> 1), nt = 2 * ct + (w & 1);
    u32x2 x0[4];
    if (L == 0) {
      const rsrc_t rx = mkrs(a.x, S * D * 2);
      sfor<0, 4>([&](auto i) { ld2i<16 * i>(x0[i], (x.l & 31) * 896 + (x.l >> 5) * 8, rx, tt * 32 * 896 + nt * 64); });
    } else {
      sfor<0, 4>([&](auto i) { ld2u<(i >> 1) * 1024 + (i & 1) * 8>(x0[i], x.vl16, x.rs, OFF_X + 2 * SZ_X + (tt * 28 + 2 * nt) * KB); });
    }
    await(x, FL_O(L, x.c, th), 8);
    stamp(x);
    u32x4 b[8][2];
    const unsigned s0 = OFF_O + L * SZ_O + (tt * 32 + 8 * w) * KB, s1 = OFF_O + L * SZ_O + ((tt0 + ((w >> 1) ^ 1)) * 32 + 8 * w) * KB;
    auto ld = [&](auto i) { ld4<(i & 3) * 1024>(b[i][0], x.vl16, x.rs, s0 + (i >> 2) * 4096); ld4<(i & 3) * 1024>(b[i][1], x.vl16, x.rs, s1 + (i >> 2) * 4096); };
    constexpr int WN = OPWN;
    sfor<0, WN>(ld);
    f32x16 acc[4];
    sfor<0, 8>([&](auto i) {
      constexpr int last = (i + WN - 1 < 7 ? i + WN - 1 : 7);
      wv<2 * (last - i) + 1>();
      m32a<A_WO + 8 * i, i == 0>(acc[0], b[i][0]); m32a<A_WO + 8 * i + 4, i == 0>(acc[1], b[i][0]);
      wv<2 * (last - i)>();
      m32a<A_WO + 8 * i, i == 0>(acc[2], b[i][1]); m32a<A_WO + 8 * i + 4, i == 0>(acc[3], b[i][1]);
      if constexpr (i + WN < 8) ld(std::integral_constant<int, i + WN>{});
    });
    nop<11>();
    sfor<0, 4>([&](auto k) { pin(acc[k]); pin(x0[k]); });
    stamp(x);
    reduce_put(x, acc); reduce_get(x, acc[0]);
    pin(acc[0]); fstamp(x);
    const u32x4 x2[2] = {u32x4{x0[0][0], x0[0][1], x0[1][0], x0[1][1]}, u32x4{x0[2][0], x0[2][1], x0[3][0], x0[3][1]}};
    res_epilogue<false>(x, acc[0], x2, OFF_X + (2 * L + 1) * SZ_X, OFF_ST + (2 * L + 1) * SZ_ST, tt, nt);
    fstamp(x);
    wv<0>();
    fstamp(x);
    signal(x, FL_X1(L, x.c), 1);
    fstamp(x);
  } else {
    stamp(x); stamp(x); fstamp(x); fstamp(x); fstamp(x); fstamp(x);
  }
#pragma unroll
  for (int j = 0; j < 12; ++j)
    *(f32x4*)(lds + (112 + 8 * (j >> 1) + 2 * w + (j & 1)) * KB + x.vl16) = f32x4{g2[j >> 2][4 * (j & 3)], g2[j >> 2][4 * (j & 3) + 1], g2[j >> 2][4 * (j & 3) + 2], g2[j >> 2][4 * (j & 3) + 3]};
}

constexpr int DWN = 7;
constexpr bool dn_half(int o) { return o == 4 || o == 5 || o == 14 || o == 15; }
struct DnSrc { unsigned s0, s1, l0, l1; };
DI DnSrc dn_src(const Ctx& x, int L) {
  const int tt0 = 4 * x.c + 2 * ((x.m / 7) & 1), tt = tt0 + (x.w >> 1), tto = tt0 + ((x.w >> 1) ^ 1);
  return DnSrc{OFF_H + L * SZ_H + (tt * 112 + 28 * x.w) * KB, OFF_H + L * SZ_H + (tto * 112 + 28 * x.w) * KB,
               OFF_HL + ((L * 64 + tt) * 16 + 4 * x.w) * KB, OFF_HL + ((L * 64 + tto) * 16 + 4 * x.w) * KB};
}
template <int O> DI void dn_ld(Ctx& x, const DnSrc& d, u32x4 (&b)[DWN][2]) {
  constexpr int i = dn_ks(O);
  if constexpr (dn_half(O)) {
    constexpr int imm = (O >= 14 ? 2048 : 0) + (O & 1) * 512;
    u32x2 lo, hi, lo1, hi1;
    ld2u<imm>(lo, x.l * 8, x.rs, d.l0); ld2u<imm + 1024>(hi, x.l * 8, x.rs, d.l0);
    ld2u<imm>(lo1, x.l * 8, x.rs, d.l1); ld2u<imm + 1024>(hi1, x.l * 8, x.rs, d.l1);
    b[O % DWN][0] = u32x4{lo[0], lo[1], hi[0], hi[1]}; b[O % DWN][1] = u32x4{lo1[0], lo1[1], hi1[0], hi1[1]};
  } else {
    ld4<(i & 3) * 1024>(b[O % DWN][0], x.vl16, x.rs, d.s0 + (i >> 2) * 4096); ld4<(i & 3) * 1024>(b[O % DWN][1], x.vl16, x.rs, d.s1 + (i >> 2) * 4096);
  }
}

DI void gu_tile(const f32x4 (&acc)[2][2], const u32x4 (&cs)[2], const float (&mean)[2], const float (&r2)[2], u32x2 (&hp)[2]) {
  const f32x4 cg = __builtin_bit_cast(f32x4, cs[0]), cu = __builtin_bit_cast(f32x4, cs[1]);
#pragma unroll
  for (int u = 0; u < 2; ++u) {
    const f32x2 nm = {-mean[u], -mean[u]}, rr = {r2[u], r2[u]};
#pragma unroll
    for (int e = 0; e < 2; ++e) {
      const f32x2 g = __builtin_elementwise_fma(nm, f32x2{cg[2 * e], cg[2 * e + 1]}, f32x2{acc[0][u][2 * e], acc[0][u][2 * e + 1]});
      const f32x2 v = __builtin_elementwise_fma(nm, f32x2{cu[2 * e], cu[2 * e + 1]}, f32x2{acc[1][u][2 * e], acc[1][u][2 * e + 1]});
      const f32x2 y = g * rr * v;
      hp[u][e] = cvtpk(y[0], y[1]);
    }
  }
}

DI void phase_gu(Ctx& x, const int L, u32x4 (&b)[DWN][2]) {
  const int w = x.w, l = x.l, g = l >> 4, c16 = l & 15, m = x.m, tt = 4 * x.c + w;
  if (L == 0 && wave0(x)) { unsigned v; ldflag(v, x.rs, OFF_FLAG + FL_W2); wv<0>(); recheck(x, v, FL_W2, 8); }
  await(x, FL_X1(L, x.c), 14);
  stamp(x);
  constexpr int NXV = 11;
  u32x4 xf[NXV][2]; f32x2 sp[2][4];
  const unsigned sx = OFF_X + (2 * L + 1) * SZ_X + tt * 28 * KB, st = OFF_ST + (2 * L + 1) * SZ_ST + tt * 32 * 8;
  auto ldx = [&](auto k, auto u) {
    if constexpr (k < NXV) ld4<(k & 1) * 2048 + 256 * u>(xf[k][u], x.vg16, x.rs, sx + (k >> 1) * 4096);
    else if constexpr (k < 14) lda<A_GX + 8 * (k - NXV) + 4 * u, (k & 1) * 2048 + 256 * u>(x.vg16, x.rs, sx + (k >> 1) * 4096);
    else { constexpr int i = k - 14; ld2<128 * u>(sp[u][i], (((i < 3 || g < 2) ? g + 4 * i : g) * 2048 + c16) * 8, x.rs, st); }
  };
  using I0 = std::integral_constant<int, 0>; using I1 = std::integral_constant<int, 1>;
  constexpr int XA = XAHEAD;
  sfor<0, XA>([&](auto k) { ldx(k, I0{}); ldx(k, I1{}); });
  const unsigned a0 = x.vl16, a1 = x.vl16 + 65536, a2 = x.vl16 + 131072;
  constexpr int DP = 6;
  u32x4 wf[8];
  auto rd = [&](auto ii) {
    constexpr int i = ii, slot = i >= 160 ? i - 160 : i;
    if constexpr (slot < 64) dsr<slot * 1024>(wf[i & 7], a0); else if constexpr (slot < 128) dsr<(slot - 64) * 1024>(wf[i & 7], a1); else dsr<(slot - 128) * 1024>(wf[i & 7], a2);
  };
  auto mm = [&](f32x4& c, const u32x4& a, auto k, auto u) {
    if constexpr (k < NXV) m16<k == 0>(c, a, xf[k][u]); else m16a<false, A_GX + 8 * (k - NXV) + 4 * u>(c, a);
  };
  const Two wd = base_wd(x);
  f32x4 aa[5][2][2], ch0, ch1;
  float mean[2], r2[2];
  sfor<0, DP>(rd);
  sfor<0, 140>([&](auto i) {
    constexpr int KS = i / 10, s = i % 10, t = s >> 1, mat = i & 1;
    using K = std::integral_constant<int, KS>;
    constexpr auto side = [](int k) { return (k >= 0 && k <= 3) || (k >= 5 && k <= 12); };
    constexpr auto all = [](int k) { return (k + XA < 14 ? 2 : 0) + ((k >= 0 && k <= 3) || (k >= 5 && k <= 9) ? 2 : 0) + (k == 5 ? 1 : 0) + (k >= 10 ? 3 : 0); };
    if constexpr (s == 0) {
      constexpr int cnt = [&] { int n = side(KS - XA) ? 1 : 0; for (int k = KS - XA + 1; k < KS; ++k) n += all(k); return n; }();
      wv<cnt>();
      if constexpr (KS == 5) asm volatile("s_barrier" ::: "memory");
    }
    if constexpr ((i & 1) == 0) { rd(std::integral_constant<int, i + DP>{}); rd(std::integral_constant<int, i + DP + 1>{}); wl<DP>(); }
    mm(aa[t][mat][0], wf[i & 7], K{}, I0{});
    if constexpr (s == 1 && KS + XA < 14) ldx(std::integral_constant<int, KS + XA>{}, I0{});
    if constexpr (s == 6 && KS + XA < 14) ldx(std::integral_constant<int, KS + XA>{}, I1{});
    if constexpr (KS <= 3 && s == 3) ldx(std::integral_constant<int, 14 + KS>{}, I0{});
    if constexpr (KS <= 3 && s == 8) ldx(std::integral_constant<int, 14 + KS>{}, I1{});
    if constexpr (KS == 5 && s == 3) asm volatile("buffer_load_dwordx4 %0, %2, %3, %4 offen sc1\n\tbuffer_load_dwordx4 %1, %2, %3, %4 offen offset:1024 sc1" : "=&v"(ch0), "=&v"(ch1) : "v"(x.vl16), "s"(x.rs), "s"(OFF_CSGU + m * 2048));
    if constexpr (KS == 11 && s == 2) { pin(ch0); pin(ch1); *(f32x4*)(lds + 40 * KB + x.vl16) = ch0 + ch1; }
    if constexpr (KS >= 5 && KS <= 9 && (s == 3 || s == 8) && !(KS == 5 && s == 3)) pf_wgu3<2 * (KS - 5) + (s == 8) - 1>(x);
    if constexpr (KS >= 10 && (s == 3 || s == 5 || s == 8) && 3 * (KS - 10) + (s == 3 ? 0 : (s == 5 ? 1 : 2)) < 10) {
      constexpr int f = 46 + 3 * (KS - 10) + (s == 3 ? 0 : (s == 5 ? 1 : 2));
      pf_wd<f, f + 1>(x, wd);
    }
    if constexpr ((KS == 10 || KS == 12) && s == 2) {
      constexpr int u = KS == 12;
      sfor<0, 4>([&](auto e) { pin(sp[u][e]); });
      float sm = sp[u][0][0] + sp[u][1][0] + sp[u][2][0], q = sp[u][0][1] + sp[u][1][1] + sp[u][2][1];
      if (g < 2) { sm += sp[u][3][0]; q += sp[u][3][1]; }
      sm = sum32(sum16(sm)); q = sum32(sum16(q));
      float rs; row_stats(sm, q, mean[u], rs); r2[u] = rs * rs;
      pin(mean[u]); pin(r2[u]);
    }
    mm(aa[t][mat][1], wf[i & 7], K{}, I1{}); SB();
  });
  fstamp(x);
  u32x4 cs[7][2];
  const unsigned csa = 40 * KB + g * 16;
  sfor<0, 10>([&](auto i) { dsr<64 * i>(cs[i >> 1][i & 1], csa); });
  wv<10>();
  asm volatile("s_barrier" ::: "memory");
  stamp(x);
  const unsigned H = OFF_H + L * SZ_H + tt * 112 * KB;
  const int gp = (m & 1) ? 7 * m + 1 : 7 * m;
  auto pair = [&](const f32x4 (&t0)[2][2], const f32x4 (&t1)[2][2], auto lt) {
    u32x2 h0[2], h1[2];
    gu_tile(t0, cs[lt], mean, r2, h0); gu_tile(t1, cs[lt + 1], mean, r2, h1);
    st4<0>(u32x4{h0[0][0], h0[0][1], h1[0][0], h1[0][1]}, x.vg16, x.rs, H + (gp + lt - 1) * 1024);
    st4<256>(u32x4{h0[1][0], h0[1][1], h1[1][0], h1[1][1]}, x.vg16, x.rs, H + (gp + lt - 1) * 1024);
  };
  {
    wl<0>();
    sfor<0, 20>([&](auto i) { pin(aa[i >> 2][(i >> 1) & 1][i & 1]); });
    sfor<0, 10>([&](auto i) { pin(cs[i >> 1][i & 1]); });
    u32x2 hl[2];
    gu_tile(aa[0], cs[0], mean, r2, hl);
    const unsigned sl = OFF_HL + ((L * 64 + tt) * 16 + m) * KB;
    st2<0>(hl[0], x.vg16 >> 1, x.rs, sl); st2<128>(hl[1], x.vg16 >> 1, x.rs, sl);
    pair(aa[1], aa[2], std::integral_constant<int, 1>{});
    pair(aa[3], aa[4], std::integral_constant<int, 3>{});
  }
  SB();
  sfor<10, 14>([&](auto i) { dsr<64 * i>(cs[i >> 1][i & 1], csa); });
  fstamp(x);
  f32x4 ab[2][2][2];
  unsigned fa;
  sfor<140, 196>([&](auto i) {
    constexpr int j = i - 140, KS = j / 4, t = (j % 4) >> 1, mat = i & 1;
    using K = std::integral_constant<int, KS>;
    if constexpr (j == 18) {
      fstamp(x);
      if (L == 0) wv<6>(); else wv<0>();
      asm volatile("s_barrier" ::: "memory");
      count(x, FL_H(L, x.c, 0), 1);
      SB();
      fstamp(x);
    }
    if constexpr ((i & 1) == 0) { if constexpr (i + DP < 196) { rd(std::integral_constant<int, i + DP>{}); rd(std::integral_constant<int, i + DP + 1>{}); wl<DP>(); } else wl<194 - i>(); }
    mm(ab[t][mat][0], wf[i & 7], K{}, I0{});
    if constexpr (j % 3 == 1 && j < 42) { if (L == 0) pf_wq<j / 3>(x); SB(); }
    if constexpr (j == 44) ldflag(fa, x.rs, OFF_FLAG + FL_H(L, x.c, 0));
    mm(ab[t][mat][1], wf[i & 7], K{}, I1{}); SB();
  });
  wv<0>();
  recheck(x, fa, FL_H(L, x.c, 0), 16);
  { const DnSrc d = dn_src(x, L); sfor<0, 3>([&](auto o) { dn_ld<o>(x, d, b); }); }
  stamp(x);
  sfor<0, 8>([&](auto i) { pin(ab[i >> 2][(i >> 1) & 1][i & 1]); });
  sfor<10, 14>([&](auto i) { pin(cs[i >> 1][i & 1]); });
  pair(ab[0], ab[1], std::integral_constant<int, 5>{});
}

DI void phase_down(Ctx& x, const int L, char* out, u32x4 (&b)[DWN][2]) {
  const int w = x.w;
  if (x.m >= 14) {
    wv<0>();
    sfor<0, 3>([&](auto o) { pin(b[o][0]); pin(b[o][1]); });
    signal(x, FL_H(L, x.c, 1), 1);
    stamp(x); fstamp(x); fstamp(x); stamp(x);
    return;
  }
  const int th = x.m / 7, ct = x.m % 7, tt = 4 * x.c + 2 * th + (w >> 1), nt = 2 * ct + (w & 1);
  stamp(x);
  constexpr int WN = DWN;
  u32x4 xr[2];
  const DnSrc d = dn_src(x, L);
  auto ld = [&](auto o) { dn_ld<o>(x, d, b); };
  sfor<3, WN>(ld);
  f32x16 acc[4];
  unsigned fb;
  sfor<0, 25>([&](auto i) {
    constexpr int last = i + WN - 1 < 27 ? i + WN - 1 : 27, ex = i <= 2 ? 2 : ((i >= 8 && i <= 13) ? 1 : ((i >= 21) ? 2 : 0));
    constexpr int later = [&] { int n = 0; for (int o = i + 1; o <= last; ++o) n += dn_half(o) ? 4 : 2; return n; }();
    wv<later + (dn_half(i) ? 2 : 1) + ex>();
    if constexpr (i == 3) signal(x, FL_H(L, x.c, 1), 1);
    m32a<A_WD + 8 * i, i == 0>(acc[0], b[i % WN][0]); m32a<A_WD + 8 * i + 4, i == 0>(acc[1], b[i % WN][0]);
    wv<later + ex>();
    m32a<A_WD + 8 * i, i == 0>(acc[2], b[i % WN][1]); m32a<A_WD + 8 * i + 4, i == 0>(acc[3], b[i % WN][1]);
    if constexpr (i == 7) ldflag(fb, x.rs, OFF_FLAG + FL_H(L, x.c, 1));
    if constexpr (i + WN == 20) { fstamp(x); wv<12>(); recheck(x, fb, FL_H(L, x.c, 1), 16); fstamp(x); }
    if constexpr (i + WN < 28) ld(std::integral_constant<int, i + WN>{});
    if constexpr (i + WN == 27) {
      const unsigned sr = OFF_X + (2 * L + 1) * SZ_X + (tt * 28 + 2 * nt) * KB;
      ld4<0>(xr[0], x.vl16, x.rs, sr); ld4<1024>(xr[1], x.vl16, x.rs, sr);
    }
  });
  wv<2>();
  sfor<1, 5>([&](auto t) {
    constexpr int k = t & 3;
    m32a<A_WD + 8 * 25 + 4 * (k & 1), false>(acc[k], b[25 % WN][k >> 1]);
    m32a<A_WD + 8 * 26 + 4 * (k & 1), false>(acc[k], b[26 % WN][k >> 1]);
    if constexpr (t > 1) { nop<(t == 2 ? 9 : 7)>(); red_put1<t - 1, 0>(x, acc[t - 1]); red_put1<t - 1, 1>(x, acc[t - 1]); }
    m32a<A_WD + 8 * 27 + 4 * (k & 1), false>(acc[k], b[27 % WN][k >> 1]);
    if constexpr (t > 1) { red_put1<t - 1, 2>(x, acc[t - 1]); red_put1<t - 1, 3>(x, acc[t - 1]); }
  });
  if (L == 0 && (x.m & 7) < 2) {
    ldl<4>(x.vl16, x.rs, OFF_WQ + ((16 + (x.m & 1)) * 28 + 7 * w) * KB, 7 * w * KB);
    ldl<3>(x.vl16, x.rs, OFF_WQ + ((16 + (x.m & 1)) * 28 + 7 * w + 4) * KB, (7 * w + 4) * KB);
  }
  nop<9>();
  pin(acc[0]);
  stamp(x);
  reduce_get(x, acc[0]);
  wv<0>();
  pin(xr[0]); pin(xr[1]);
  if (L > 0) {
    res_epilogue<true>(x, acc[0], xr, 0, 0, tt, nt, out);
  } else {
    res_epilogue<false>(x, acc[0], xr, OFF_X + 2 * SZ_X, OFF_ST + 2 * SZ_ST, tt, nt);
    wv<0>();
    signal(x, FL_X(x.c, th), 1);
  }
}

#define STAMP_SYNC
__global__ __launch_bounds__(256, 1) void k_main(Args) {
  typedef unsigned long long u64x8 __attribute__((ext_vector_type(8)));
  typedef unsigned long long u64x2 __attribute__((ext_vector_type(2)));
  u64x8 pin8; u64x2 pws;
  asm volatile("s_load_dwordx16 s[8:23], %4, 0x0\n\ts_load_dwordx4 s[24:27], %4, 0x40\n\t"
               "s_getpc_b64 s[4:5]\n.Lk_begin:\n\t"
               "s_mov_b32 s6, .Lk_end-.Lk_begin\n\ts_mov_b32 s7, 0x20000\n\ts_and_b32 s5, s5, 0xffff\n\t"
               "v_lshlrev_b32 v1, 4, %2\n\t"
               "s_bfe_u32 s28, %3, 0x20003\n\ts_lshl_b32 s28, s28, 12\n\t"
               "buffer_load_dwordx4 a[112:115], v1, s[4:7], s28 offen\n\t"
               "s_bfe_u32 s28, %3, 0x30004\n\ts_lshl_b32 s28, s28, 13\n\t"
               "buffer_load_dwordx4 a[112:115], v1, s[4:7], s28 offen\n\t"
               "s_add_u32 s28, s28, 4096\n\t"
               "buffer_load_dwordx4 a[112:115], v1, s[4:7], s28 offen\n\t"
               "s_waitcnt lgkmcnt(0)"
               : "={s[8:23]}"(pin8), "={s[24:27]}"(pws) : "v"(threadIdx.x), "{s2}"(blockIdx.x), "{s[0:1]}"(__builtin_amdgcn_kernarg_segment_ptr())
               : "s4", "s5", "s6", "s7", "s28", "v1", "scc", ACLOB);
  const Args a = {(const char*)pin8[0], (const char*)pin8[1], (const char*)pin8[2], (const char*)pin8[3], (const char*)pin8[4], (const char*)pin8[5],
                  (const char*)pin8[6], (const char*)pin8[7], (char*)pws[0], (char*)pws[1]};
  Ctx x;
  x.rs = mkrs(a.ws, WS_USED);
  x.w = __builtin_amdgcn_readfirstlane(threadIdx.x >> 6);
  asm volatile("s_nop 4" : "+s"(x.w));
  { const int b = blockIdx.x; x.c = 2 * (b & 7) + (b >> 7); x.m = (b >> 3) & 15; }
  f32x16 kf[3];
  { Ctx y = x; fresh(y); stamp(y); phase_pre(y, a, kf); STAMP_SYNC }
  for (int L = 0;; ++L) {
    f32x16 g2[3];
    int Lo = L; asm volatile("" : "+s"(Lo));
    { Ctx y = x; fresh(y); phase_att(y, Lo, kf, g2); stamp(y); STAMP_SYNC }
    { Ctx y = x; fresh(y); phase_oproj(y, Lo, a, g2); stamp(y); STAMP_SYNC }
    { Ctx y = x; u32x4 b[DWN][2]; fresh(y); phase_gu(y, Lo, b); stamp(y); fresh(y); phase_down(y, Lo, a.out, b); stamp(y); STAMP_SYNC }
    if (L == 1) break;
    { Ctx y = x; fresh(y); phase_front(y, kf); STAMP_SYNC }
  }
  fresh(x);
  if (blockIdx.x == 0 && wave0(x)) {
    get_tag(x);
    if (x.l == 0) st2<0>(u32x2{x.tlo, x.thi}, 0, x.rs, OFF_TAG);
  }
  stamp_flush(x);
  asm volatile(".Lk_end:");
}

extern "C" void kernel_launch(void* const* d_in, const int* in_sizes, int n_in, void* d_out, int out_size, void* d_ws, size_t ws_size,
                              hipStream_t stream) {
  Args a;
  a.x = (const char*)d_in[0]; a.wq = (const char*)d_in[1]; a.wk = (const char*)d_in[2]; a.wv = (const char*)d_in[3];
  a.wo = (const char*)d_in[4]; a.wg = (const char*)d_in[6]; a.wu = (const char*)d_in[7]; a.wd = (const char*)d_in[8];
  a.ws = (char*)d_ws; a.out = (char*)d_out;
  (void)hipFuncSetAttribute((const void*)k_main, hipFuncAttributeMaxDynamicSharedMemorySize, 160 * 1024);
  k_main<<<256, 256, 160 * 1024, stream>>>(a);
}
```

```cpp
#include <hip/hip_runtime.h>
#include <stdint.h>
#include <type_traits>

typedef float    f32x16 __attribute__((ext_vector_type(16)));
typedef float    f32x4  __attribute__((ext_vector_type(4)));
typedef float    f32x2  __attribute__((ext_vector_type(2)));
typedef unsigned u32x4  __attribute__((ext_vector_type(4)));
typedef unsigned u32x2  __attribute__((ext_vector_type(2)));
typedef __bf16   bf16x2 __attribute__((ext_vector_type(2)));
typedef __amdgpu_buffer_rsrc_t rsrc_t;
#define DI __device__ __forceinline__
#define GAS __attribute__((address_space(1)))
#define SB() __builtin_amdgcn_sched_barrier(0)

constexpr int S = 2048, D = 448, F = 1792;
constexpr unsigned KB = 1024;
constexpr unsigned OFF_FLAG = 0;
constexpr unsigned OFF_CSQ  = 64 * KB;
constexpr unsigned OFF_CSGU = OFF_CSQ + 4 * KB;
constexpr unsigned OFF_SLOT = OFF_CSGU + 32 * KB;
constexpr unsigned OFF_TAG  = OFF_SLOT + 32 * KB;
constexpr unsigned OFF_WQ   = 160 * KB;
constexpr unsigned OFF_WO   = OFF_WQ + 18 * 28 * KB;
constexpr unsigned OFF_WGU  = OFF_WO + 14 * 32 * KB;
constexpr unsigned OFF_WD   = OFF_WGU + 16 * 196 * KB;
constexpr unsigned SZ_X = 64 * 28 * KB, SZ_ST = 14 * 2048 * 8, SZ_O = 64 * 32 * KB, SZ_H = 64 * 112 * KB, SZ_KV = 64 * 4 * KB;
constexpr unsigned OFF_X    = OFF_WD + 14 * 112 * KB;
constexpr unsigned OFF_ST   = OFF_X + 4 * SZ_X;
constexpr unsigned OFF_O    = OFF_ST + 4 * SZ_ST;
constexpr unsigned OFF_H    = OFF_O + 2 * SZ_O;
constexpr unsigned OFF_HL   = OFF_H + 2 * SZ_H;
constexpr unsigned OFF_KV   = OFF_HL + 2 * 1024 * KB;
constexpr unsigned OFF_TS   = OFF_KV + 2 * SZ_KV;
constexpr unsigned WS_USED  = OFF_TS + 520 * KB;
DI constexpr unsigned FL_X(int c, int th)         { return (c * 16 + th) * 256u; }
DI constexpr unsigned FL_O(int L, int c, int th)  { return (c * 16 + 2 + L * 2 + th) * 256u; }
DI constexpr unsigned FL_X1(int L, int c)         { return (c * 16 + 6 + L) * 256u; }
DI constexpr unsigned FL_H(int L, int c, int p)   { return (c * 16 + 8 + L * 2 + p) * 256u; }
DI constexpr unsigned FL_W(int c)                 { return (c * 16 + 12) * 256u; }
constexpr unsigned FL_W2 = 13 * 256u;

#define XAHEAD 3
#define OPWN 5
constexpr float QSCALE = 0.18898223650461362f * 1.4426950408889634f;
constexpr float M_LIMIT = 48.0f;

template <int I, int N, class Fn> DI void sfor(Fn&& f) { if constexpr (I < N) { f(std::integral_constant<int, I>{}); sfor<I + 1, N>(f); } }

DI unsigned cvtpk(float lo, float hi) { f32x2 v = {lo, hi}; bf16x2 b = __builtin_convertvector(v, bf16x2); return __builtin_bit_cast(unsigned, b); }
DI float bflo(unsigned u) { return __uint_as_float(u << 16); }
DI float bfhi(unsigned u) { return __uint_as_float(u & 0xffff0000u); }
DI float dot2(unsigned a, unsigned b, float c) { return __builtin_amdgcn_fdot2_f32_bf16(__builtin_bit_cast(bf16x2, a), __builtin_bit_cast(bf16x2, b), c, false); }
DI float sum32(float v) { auto r = __builtin_amdgcn_permlane32_swap(__float_as_uint(v), __float_as_uint(v), false, false); return __uint_as_float(r[0]) + __uint_as_float(r[1]); }
DI float sum16(float v) { auto r = __builtin_amdgcn_permlane16_swap(__float_as_uint(v), __float_as_uint(v), false, false); return __uint_as_float(r[0]) + __uint_as_float(r[1]); }
DI float max32(float v) { auto r = __builtin_amdgcn_permlane32_swap(__float_as_uint(v), __float_as_uint(v), false, false); return fmaxf(__uint_as_float(r[0]), __uint_as_float(r[1])); }

DI unsigned umax16(unsigned v) {
  v = max(v, (unsigned)__builtin_amdgcn_update_dpp(0, (int)v, 0x128, 0xf, 0xf, false));
  v = max(v, (unsigned)__builtin_amdgcn_update_dpp(0, (int)v, 0x124, 0xf, 0xf, false));
  v = max(v, (unsigned)__builtin_amdgcn_update_dpp(0, (int)v, 0x122, 0xf, 0xf, false));
  v = max(v, (unsigned)__builtin_amdgcn_update_dpp(0, (int)v, 0x121, 0xf, 0xf, false));
  return v;
}
DI unsigned umax64(unsigned v) {
  v = umax16(v);
  auto a = __builtin_amdgcn_permlane16_swap(v, v, false, false); v = max(a[0], a[1]);
  auto b = __builtin_amdgcn_permlane32_swap(v, v, false, false); return max(b[0], b[1]);
}

typedef short s16x4 __attribute__((ext_vector_type(4)));
DI u32x2 trread(const char* p) { return __builtin_bit_cast(u32x2, __builtin_amdgcn_ds_read_tr16_b64_v4i16((__attribute__((address_space(3))) s16x4*)p)); }
DI u32x4 trfrag(const char* lo, const char* hi) { const u32x2 a = trread(lo), b = trread(hi); return u32x4{a[0], a[1], b[0], b[1]}; }
DI void row_stats(float s, float q, float& mean, float& rstd) {
  mean = s * (1.0f / D);
  const float var = fmaxf(q * (1.0f / D) - mean * mean, 0.f);
  rstd = __builtin_amdgcn_rsqf(var + 1e-5f);
}
DI float fragsum(const u32x4& f, float s) {
#pragma unroll
  for (int d = 0; d < 4; ++d) s = dot2(f[d], 0x3f803f80u, s);
  return s;
}
struct Args { const char *x, *wq, *wk, *wv, *wo, *wg, *wu, *wd; char* ws; char* out; };

#define ACLOB "a0","a1","a2","a3","a4","a5","a6","a7","a8","a9","a10","a11","a12","a13","a14","a15","a16","a17","a18","a19","a20","a21","a22","a23","a24","a25","a26","a27","a28","a29","a30","a31","a32","a33","a34","a35","a36","a37","a38","a39","a40","a41","a42","a43","a44","a45","a46","a47","a48","a49","a50","a51","a52","a53","a54","a55","a56","a57","a58","a59","a60","a61","a62","a63","a64","a65","a66","a67","a68","a69","a70","a71","a72","a73","a74","a75","a76","a77","a78","a79","a80","a81","a82","a83","a84","a85","a86","a87","a88","a89","a90","a91","a92","a93","a94","a95","a96","a97","a98","a99","a100","a101","a102","a103","a104","a105","a106","a107","a108","a109","a110","a111","a112","a113","a114","a115","a116","a117","a118","a119","a120","a121","a122","a123","a124","a125","a126","a127","a128","a129","a130","a131","a132","a133","a134","a135","a136","a137","a138","a139","a140","a141","a142","a143","a144","a145","a146","a147","a148","a149","a150","a151","a152","a153","a154","a155","a156","a157","a158","a159","a160","a161","a162","a163","a164","a165","a166","a167","a168","a169","a170","a171","a172","a173","a174","a175","a176","a177","a178","a179","a180","a181","a182","a183","a184","a185","a186","a187","a188","a189","a190","a191","a192","a193","a194","a195","a196","a197","a198","a199","a200","a201","a202","a203","a204","a205","a206","a207","a208","a209","a210","a211","a212","a213","a214","a215","a216","a217","a218","a219","a220","a221","a222","a223","a224","a225","a226","a227","a228","a229","a230","a231","a232","a233","a234","a235","a236","a237","a238","a239","a240","a241","a242","a243","a244","a245","a246","a247","a248","a249","a250","a251","a252","a253","a254","a255"

template <int IMM> DI void ld4(u32x4& d, unsigned vo, rsrc_t rs, unsigned so) {
  asm volatile("buffer_load_dwordx4 %0, %1, %2, %3 offen offset:%c4 sc1" : "=v"(d) : "v"(vo), "s"(rs), "s"(so), "i"(IMM));
}
template <int IMM> DI void ld2(f32x2& d, unsigned vo, rsrc_t rs, unsigned so) {
  asm volatile("buffer_load_dwordx2 %0, %1, %2, %3 offen offset:%c4 sc1" : "=v"(d) : "v"(vo), "s"(rs), "s"(so), "i"(IMM));
}
template <int IMM> DI void ld4i(u32x4& d, unsigned vo, rsrc_t rs, unsigned so) {
  asm volatile("buffer_load_dwordx4 %0, %1, %2, %3 offen offset:%c4" : "=v"(d) : "v"(vo), "s"(rs), "s"(so), "i"(IMM));
}
template <int IMM> DI void ld2i(u32x2& d, unsigned vo, rsrc_t rs, unsigned so) {
  asm volatile("buffer_load_dwordx2 %0, %1, %2, %3 offen offset:%c4" : "=v"(d) : "v"(vo), "s"(rs), "s"(so), "i"(IMM));
}
template <int IMM> DI void ld2u(u32x2& d, unsigned vo, rsrc_t rs, unsigned so) {
  asm volatile("buffer_load_dwordx2 %0, %1, %2, %3 offen offset:%c4 sc1" : "=v"(d) : "v"(vo), "s"(rs), "s"(so), "i"(IMM));
}
template <int IMM> DI void ld4f(f32x4& d, unsigned vo, rsrc_t rs, unsigned so) {
  asm volatile("buffer_load_dwordx4 %0, %1, %2, %3 offen offset:%c4 sc1" : "=v"(d) : "v"(vo), "s"(rs), "s"(so), "i"(IMM));
}
DI void ld1(float& d, unsigned vo, rsrc_t rs, unsigned so) {
  asm volatile("buffer_load_dword %0, %1, %2, %3 offen sc1" : "=v"(d) : "v"(vo), "s"(rs), "s"(so));
}
template <int AR, int IMM> DI void lda(unsigned vo, rsrc_t rs, unsigned so) {
  asm volatile("buffer_load_dwordx4 a[%c3:%c4], %0, %1, %2 offen offset:%c5 sc1" :: "v"(vo), "s"(rs), "s"(so), "i"(AR), "i"(AR + 3), "i"(IMM) : ACLOB);
}
template <int N> DI void ldl(unsigned vo, rsrc_t rs, unsigned so, unsigned lds) {
  asm volatile("s_mov_b32 m0, %3\n\ts_nop 0\n\tbuffer_load_dwordx4 %0, %1, %2 offen sc1 lds" :: "v"(vo), "s"(rs), "s"(so), "s"(lds) : "memory");
  if constexpr (N > 1) asm volatile("buffer_load_dwordx4 %0, %1, %2 offen offset:1024 sc1 lds" :: "v"(vo), "s"(rs), "s"(so) : "memory");
  if constexpr (N > 2) asm volatile("buffer_load_dwordx4 %0, %1, %2 offen offset:2048 sc1 lds" :: "v"(vo), "s"(rs), "s"(so) : "memory");
  if constexpr (N > 3) asm volatile("buffer_load_dwordx4 %0, %1, %2 offen offset:3072 sc1 lds" :: "v"(vo), "s"(rs), "s"(so) : "memory");
}
template <int IMM> DI void st4(const u32x4& d, unsigned vo, rsrc_t rs, unsigned so) {
  asm volatile("buffer_store_dwordx4 %0, %1, %2, %3 offen offset:%c4 sc1\n\ts_nop 1" :: "v"(d), "v"(vo), "s"(rs), "s"(so), "i"(IMM) : "memory");
}
template <int IMM> DI void st4p(const u32x4& d, unsigned vo, rsrc_t rs, unsigned so) {
  asm volatile("buffer_store_dwordx4 %0, %1, %2, %3 offen offset:%c4\n\ts_nop 1" :: "v"(d), "v"(vo), "s"(rs), "s"(so), "i"(IMM) : "memory");
}
template <int IMM> DI void st2(const u32x2& d, unsigned vo, rsrc_t rs, unsigned so) {
  asm volatile("buffer_store_dwordx2 %0, %1, %2, %3 offen offset:%c4 sc1" :: "v"(d), "v"(vo), "s"(rs), "s"(so), "i"(IMM) : "memory");
}
DI void st1(float d, unsigned vo, rsrc_t rs, unsigned so) {
  asm volatile("buffer_store_dword %0, %1, %2, %3 offen sc1" :: "v"(d), "v"(vo), "s"(rs), "s"(so) : "memory");
}
template <int N> DI void wv() { asm volatile("s_waitcnt vmcnt(%c0)" :: "i"(N) : "memory"); SB(); }
template <int N> DI void wl() { asm volatile("s_waitcnt lgkmcnt(%c0)" :: "i"(N) : "memory"); SB(); }
template <class T> DI void pin(T& x) { asm volatile("" : "+v"(x)); }
DI void bar() { asm volatile("s_waitcnt lgkmcnt(0)\n\ts_barrier" ::: "memory"); }
template <int N> DI void nop() { asm volatile("s_nop %c0" :: "i"(N)); SB(); }

template <int OFF> DI void trd(u32x2& r, unsigned a) { asm volatile("ds_read_b64_tr_b16 %0, %1 offset:%c2" : "=v"(r) : "v"(a), "i"(OFF)); }
template <int OFF> DI void dsr(u32x4& r, unsigned a) { asm volatile("ds_read_b128 %0, %1 offset:%c2" : "=v"(r) : "v"(a), "i"(OFF)); }

DI void m32(f32x16& c, const u32x4& a, const u32x4& b)  { asm volatile("v_mfma_f32_32x32x16_bf16 %0, %1, %2, %0" : "+v"(c) : "v"(a), "v"(b)); }
DI void m32z(f32x16& c, const u32x4& a, const u32x4& b) { asm volatile("v_mfma_f32_32x32x16_bf16 %0, %1, %2, 0" : "=&v"(c) : "v"(a), "v"(b)); }
template <int AR, bool Z> DI void m32a(f32x16& c, const u32x4& b) {
  if constexpr (Z) asm volatile("v_mfma_f32_32x32x16_bf16 %0, a[%c2:%c3], %1, 0" : "=&v"(c) : "v"(b), "i"(AR), "i"(AR + 3));
  else             asm volatile("v_mfma_f32_32x32x16_bf16 %0, a[%c2:%c3], %1, %0" : "+v"(c) : "v"(b), "i"(AR), "i"(AR + 3));
}
template <int AR, bool Z> DI void m32b(f32x16& c, const u32x4& a) {
  if constexpr (Z) asm volatile("v_mfma_f32_32x32x16_bf16 %0, %1, a[%c2:%c3], 0" : "=&v"(c) : "v"(a), "i"(AR), "i"(AR + 3));
  else             asm volatile("v_mfma_f32_32x32x16_bf16 %0, %1, a[%c2:%c3], %0" : "+v"(c) : "v"(a), "i"(AR), "i"(AR + 3));
}
template <bool Z> DI void m16(f32x4& c, const u32x4& a, const u32x4& b) {
  if constexpr (Z) asm volatile("v_mfma_f32_16x16x32_bf16 %0, %1, %2, 0" : "=&v"(c) : "v"(a), "v"(b));
  else             asm volatile("v_mfma_f32_16x16x32_bf16 %0, %1, %2, %0" : "+v"(c) : "v"(a), "v"(b));
}

template <bool Z, int AR> DI void m16a(f32x4& c, const u32x4& a) {
  if constexpr (Z) asm volatile("v_mfma_f32_16x16x32_bf16 %0, %1, a[%c2:%c3], 0" : "=&v"(c) : "v"(a), "i"(AR), "i"(AR + 3));
  else             asm volatile("v_mfma_f32_16x16x32_bf16 %0, %1, a[%c2:%c3], %0" : "+v"(c) : "v"(a), "i"(AR), "i"(AR + 3));
}
DI void ldflag(unsigned& v, rsrc_t rs, unsigned so) { asm volatile("buffer_load_dword %0, off, %1, %2 sc1" : "=v"(v) : "s"(rs), "s"(so)); }

struct Ctx {
  rsrc_t rs;
  unsigned tlo, thi;
  unsigned vl16;
  unsigned vg16;
  int w, l, c, m;
};
DI void fresh(Ctx& x) {
  asm volatile("" : "+s"(x.w), "+s"(x.c), "+s"(x.m));
  asm volatile("v_mbcnt_lo_u32_b32 %0, -1, 0\n\tv_mbcnt_hi_u32_b32 %0, -1, %0" : "=v"(x.l));
  x.vl16 = x.l * 16;
  const int g = x.l >> 4;
  x.vg16 = ((g >> 1) * 64 + (g & 1) * 32 + (x.l & 15)) * 16;
}
extern __shared__ __attribute__((aligned(16))) char lds[];
constexpr unsigned SCR = 112 * KB;

DI void pstamp(Ctx&) {}
DI void fstamp(Ctx&) {}
DI void stamp(Ctx&) {}
DI void stamp_flush(Ctx&) {}

DI void get_tag(Ctx& x) {
  unsigned lo, hi;
  asm volatile("v_accvgpr_read_b32 v255, a252\n\ts_nop 1\n\tv_readfirstlane_b32 %0, v255\n\tv_accvgpr_read_b32 v255, a253\n\ts_nop 1\n\tv_readfirstlane_b32 %1, v255\n\ts_nop 4"
               : "=s"(lo), "=s"(hi) :: "v255");
  const unsigned long long e = ((unsigned long long)hi << 32) | lo;
  unsigned long long n = e * 0x9E3779B97F4A7C15ull + 0x632BE59BD9B4E019ull;
  if (((n ^ e) >> 16) == 0) n += 0x10000;
  x.tlo = (unsigned)n; x.thi = (unsigned)(n >> 32);
}
DI bool wave0(const Ctx& x) { int w = x.w; asm volatile("" : "+s"(w)); return w == 0; }
DI void count(Ctx& x, unsigned fl, int n) {
  if (wave0(x)) {
    const unsigned long long mask = (1ull << n) - 1;
    unsigned long long keep;
    asm volatile("s_mov_b64 %0, exec\n\ts_mov_b64 exec, %5\n\tbuffer_atomic_add %1, %2, %3, %4 offen\n\ts_mov_b64 exec, %0"
                 : "=&s"(keep) : "v"(1u), "v"(x.l * 256), "s"(x.rs), "s"(OFF_FLAG + fl), "s"(mask) : "memory");
  }
}
DI void signal(Ctx& x, unsigned fl, int n) { bar(); count(x, fl, n); }
DI void await(Ctx& x, unsigned fl, unsigned target) {
  if (wave0(x)) {
#pragma nounroll
    for (unsigned spin = 0; spin < (1u << 18); ++spin) {
      unsigned v;
      asm volatile("buffer_load_dword %0, off, %1, %2 sc1\n\ts_waitcnt vmcnt(0)" : "=v"(v) : "s"(x.rs), "s"(OFF_FLAG + fl) : "memory");
      if ((unsigned)__builtin_amdgcn_readfirstlane(v) >= target) break;
      __builtin_amdgcn_s_sleep(1);
    }
  }
  bar();
}

DI void recheck(Ctx& x, unsigned v, unsigned fl, unsigned target) {
  pin(v);
#pragma nounroll
  for (unsigned spin = 0; (unsigned)__builtin_amdgcn_readfirstlane(v) < target && spin < (1u << 18); ++spin) {
    __builtin_amdgcn_s_sleep(2);
    asm volatile("buffer_load_dword %0, off, %1, %2 sc1\n\ts_waitcnt vmcnt(0)" : "=v"(v) : "s"(x.rs), "s"(OFF_FLAG + fl) : "memory");
  }
  SB();
}

DI void reduce_put(Ctx& x, const f32x16 (&acc)[4]) {
  sfor<1, 4>([&](auto k) {
    f32x4* p = (f32x4*)(lds + SCR + ((x.w ^ k) * 3 + k - 1) * 4096 + x.vl16);
#pragma unroll
    for (int q = 0; q < 4; ++q) p[q * 64] = f32x4{acc[k][4 * q], acc[k][4 * q + 1], acc[k][4 * q + 2], acc[k][4 * q + 3]};
  });
}
template <int K, int Q> DI void red_put1(Ctx& x, const f32x16& t) {
  asm volatile("ds_write_b128 %0, %1 offset:%c2" :: "v"(SCR + ((x.w ^ K) * 3 + K - 1) * 4096 + x.vl16),
               "v"(__builtin_shufflevector(t, t, 4 * Q, 4 * Q + 1, 4 * Q + 2, 4 * Q + 3)), "i"(Q * 1024) : "memory");
}
DI void reduce_get(Ctx& x, f32x16& own) {
  bar();
  const f32x4* p = (const f32x4*)(lds + SCR + x.w * 3 * 4096 + x.vl16);
#pragma unroll
  for (int k = 0; k < 3; ++k)
#pragma unroll
    for (int q = 0; q < 4; ++q) {
      f32x4 v = p[k * 256 + q * 64];
#pragma unroll
      for (int i = 0; i < 4; ++i) own[4 * q + i] += v[i];
    }
}

constexpr int A_WO = 184, A_WD = 0, A_GX = 224;
constexpr int dn_ks(int o) { return o < 20 ? 2 * ((o >> 1) % 5 + ((o >> 1) % 5 >= 2) + 7 * (o / 10)) + (o & 1) : 2 * (((o - 20) >> 1) * 4 - ((o - 20) >> 2) + 2) + (o & 1); }
static_assert(dn_ks(0) == 0 && dn_ks(4) == 6 && dn_ks(9) == 11 && dn_ks(10) == 14 && dn_ks(19) == 25 && dn_ks(20) == 4 && dn_ks(22) == 12 && dn_ks(24) == 18 && dn_ks(27) == 27, "");
struct Two { unsigned s0, s1; };
constexpr int SL_WQ = 56;
template <int J> DI void pf_wq(Ctx& x) {
  ldl<1>(x.vl16, x.rs, OFF_WQ + ((x.m & 7) * 56 + 14 * x.w + J) * KB, (SL_WQ + 14 * x.w + J) * KB);
}
DI Two base_wo(const Ctx& x) { const int ct = x.m % 7;
  return Two{OFF_WO + ((2 * ct + (x.w & 1)) * 32 + 8 * x.w) * KB, OFF_WO + ((2 * ct + ((x.w & 1) ^ 1)) * 32 + 8 * x.w) * KB}; }
DI void pf_wo(Ctx& x) {
  const Two b = base_wo(x);
  sfor<0, 8>([&](auto i) {
    lda<A_WO + 8 * i, (i & 3) * 1024>(x.vl16, x.rs, b.s0 + (i >> 2) * 4096);
    lda<A_WO + 8 * i + 4, (i & 3) * 1024>(x.vl16, x.rs, b.s1 + (i >> 2) * 4096);
  });
}
DI Two base_wd(const Ctx& x) { const int ct = x.m % 7;
  return Two{OFF_WD + ((2 * ct + (x.w & 1)) * 112 + 28 * x.w) * KB, OFF_WD + ((2 * ct + ((x.w & 1) ^ 1)) * 112 + 28 * x.w) * KB}; }
template <int I0, int I1> DI void pf_wd(Ctx& x, const Two& b) {
  sfor<I0, I1>([&](auto f) {
    constexpr int i = f >> 1;
    lda<A_WD + 4 * f, (i & 3) * 1024>(x.vl16, x.rs, ((f & 1) ? b.s1 : b.s0) + (i >> 2) * 4096);
  });
}
DI void pf_wgu1(Ctx& x) {
  for (int t = 0; t < 14; ++t) ldl<2>(x.vl16, x.rs, OFF_WGU + (x.m * 196 + 8 * t + 2 * x.w) * KB, (8 * t + 2 * x.w) * KB);
}
template <int J> DI void pf_wgu3(Ctx& x) {
  ldl<1>(x.vl16, x.rs, OFF_WGU + (x.m * 196 + 160 + 9 * x.w + J) * KB, (9 * x.w + J) * KB);
}

constexpr unsigned S_QP = SCR, S_KP = SCR + 16384, S_QF = SCR + 24576, S_ST = SCR + 32768, S_CS = SCR + 33792, S_KM = SCR + 36864, S_TR = SCR + 40960;
DI unsigned kv_finish(Ctx& x, const float (&raw)[16], const f32x4 (&cs)[4], float mean, float rstd, bool isv, unsigned so, int slot) {
  const int h = x.l >> 5, r = x.l & 31;
  u32x4 y[2];
#pragma unroll
  for (int d = 0; d < 8; ++d)
    y[d >> 2][d & 3] = cvtpk((raw[2 * d] - mean * cs[d >> 1][2 * (d & 1)]) * rstd, (raw[2 * d + 1] - mean * cs[d >> 1][2 * (d & 1) + 1]) * rstd);
  if (h) { y[1][2] = 0; y[1][3] = 0; }
  unsigned nb = 0;
  if (!isv) {
    float n2 = 0.f;
#pragma unroll
    for (int d = 0; d < 8; ++d) n2 = dot2(y[d >> 2][d & 3], y[d >> 2][d & 3], n2);
    n2 = sum32(n2);
    nb = umax64(n2 < 3.0e38f ? __float_as_uint(n2) : 0x7f800000u);
    nb = cvtpk(1.01f * __builtin_amdgcn_sqrtf(__uint_as_float(nb)), 0.f);
    if (h) y[1][2] = 0x3f80u;
  } else {
    if (h) y[1][2] = 0x3f80u;
    char* im = lds + S_TR + slot * 2048;
#pragma unroll
    for (int a = 0; a < 4; ++a) *(u32x2*)(im + r * 64 + 16 * a + 8 * h) = u32x2{y[a >> 1][2 * (a & 1)], y[a >> 1][2 * (a & 1) + 1]};
    asm volatile("s_waitcnt lgkmcnt(0)" ::: "memory");
    const char* tb = im + (4 * h + ((x.l >> 2) & 3)) * 64 + ((x.l >> 4) & 1) * 32 + (x.l & 3) * 8;
    y[0] = trfrag(tb, tb + 512); y[1] = trfrag(tb + 1024, tb + 1536);
  }
  st4<0>(y[0], x.vl16, x.rs, so); st4<1024>(y[1], x.vl16, x.rs, so);
  return nb;
}
DI void kv_publish(Ctx& x, int L, int p, unsigned km) {
  get_tag(x);
  st2<0>(u32x2{(x.tlo & 0xffff0000u) | km, x.thi}, x.l < 16 ? x.l * 1024 : ~0u, x.rs, OFF_SLOT + (L * 2048 + p) * 8);
}
DI void q_finish(Ctx& x, const f32x16& qa, const f32x4 (&cq)[4], float mean, float rstd) {
  const int h = x.l >> 5;
  u32x4 qf[2];
  const f32x4* p = (const f32x4*)(lds + S_QP + x.w * 4096 + x.vl16);
  const float sc = rstd * QSCALE;
#pragma unroll
  for (int q = 0; q < 4; ++q) {
    const f32x4 o = p[q * 64];
    float v[4];
#pragma unroll
    for (int i = 0; i < 4; ++i) v[i] = (qa[4 * q + i] + o[i] - mean * cq[q][i]) * sc;
    qf[q >> 1][2 * (q & 1)] = cvtpk(v[0], v[1]); qf[q >> 1][2 * (q & 1) + 1] = cvtpk(v[2], v[3]);
  }
  if (h) { qf[1][2] = 0; qf[1][3] = 0; }
  float qn2 = 0.f, qn3 = 0.f;
#pragma unroll
  for (int d = 0; d < 4; ++d) { qn2 = dot2(qf[0][d], qf[0][d], qn2); qn3 = dot2(qf[1][d], qf[1][d], qn3); }
  qn2 = sum32(qn2 + qn3);
  if (h) qf[1][2] = cvtpk(1.01f * __builtin_amdgcn_sqrtf(qn2), 0.f);
  ((u32x4*)(lds + S_QF + x.w * 2048 + x.vl16))[0] = qf[0];
  ((u32x4*)(lds + S_QF + x.w * 2048 + x.vl16))[64] = qf[1];
}
DI void put_tile(char* p, const f32x16& t) {
#pragma unroll
  for (int q = 0; q < 4; ++q) ((f32x4*)p)[q * 64] = f32x4{t[4 * q], t[4 * q + 1], t[4 * q + 2], t[4 * q + 3]};
}

constexpr unsigned IMG_Q = 0, IMG_T = 64 * KB;
DI rsrc_t mkrs(const char* p, unsigned n) { return __builtin_amdgcn_make_buffer_rsrc((char*)p, 0, n, 0x00020000); }

DI void ask_slots(Ctx& x, int L) {
  if (wave0(x)) asm volatile("buffer_load_dwordx4 a[248:251], %0, %1, %2 offen sc1" :: "v"(x.vl16), "s"(x.rs), "s"(OFF_SLOT + (L * 16 + x.c) * 1024) : ACLOB);
}
DI void kv_wait(Ctx& x, int L) {
  wv<0>();
  if (wave0(x)) {
    get_tag(x);
    unsigned s0, s1, s2, s3;
#pragma nounroll
    for (unsigned spin = 0; spin < (1u << 18); ++spin) {
      asm volatile("v_accvgpr_read_b32 %0, a248\n\tv_accvgpr_read_b32 %1, a249\n\tv_accvgpr_read_b32 %2, a250\n\tv_accvgpr_read_b32 %3, a251" : "=v"(s0), "=v"(s1), "=v"(s2), "=v"(s3));
      if (__all(s1 == x.thi && s3 == x.thi && (((s0 ^ x.tlo) | (s2 ^ x.tlo)) >> 16) == 0)) break;
      __builtin_amdgcn_s_sleep(2);
      ask_slots(x, L); wv<0>();
    }
    const unsigned km = umax64(max(s0 << 16, s2 << 16));
    if (x.l == 0) ((unsigned*)(lds + S_KM))[4] = km;
  }
  bar();
}
DI void ask_kv(Ctx& x, int L, f32x16 (&kf)[3]) {
  const unsigned kvs = OFF_KV + L * SZ_KV + 16 * x.w * 4096;
  asm volatile(".irp i,0,1,2,3\n\tbuffer_load_dwordx4 v[128+4*\\i:131+4*\\i], %3, %4, %5 offen offset:1024*\\i sc1\n\t.endr\n\t"
               ".irp i,0,1,2,3\n\tbuffer_load_dwordx4 v[144+4*\\i:147+4*\\i], %3, %4, %6 offen offset:1024*\\i sc1\n\t.endr\n\t"
               ".irp i,0,1,2,3\n\tbuffer_load_dwordx4 v[160+4*\\i:163+4*\\i], %3, %4, %7 offen offset:1024*\\i sc1\n\t.endr"
               : "={v[128:143]}"(kf[0]), "={v[144:159]}"(kf[1]), "={v[160:175]}"(kf[2]) : "{v204}"(x.vl16), "s"(x.rs), "s"(kvs), "s"(kvs + 4096), "s"(kvs + 8192));
}

DI void phase_pre(Ctx& x, const Args& a, f32x16 (&kf)[3]) {
  const int w = x.w, l = x.l, h = l >> 5, r = l & 31;
  const int th = x.m >> 3, hp = x.m & 7, tl = w & 1, kh = w >> 1, tt = 4 * x.c + 2 * th + tl, wi = 2 * x.c + th;
  const bool kvp = hp < 2;
  u32x4 xf[14];
  { const rsrc_t rx = mkrs(a.x, S * D * 2);
    if (kvp) {
      sfor<0, 16>([&](auto i) {
        const int c = r ^ ((2 * i + h) & 15);
        ldl<1>(c < 28 ? h * 896 + c * 16 : 0x80000000u, rx, (tt * 32 + 2 * i) * 896 + kh * 448, (16 * w + i) * KB);
      });
    } else {
      sfor<0, 14>([&](auto i) { ld4i<32 * i>(xf[i], r * 896 + h * 16, rx, tt * 32 * 896 + kh * 448); });
    } }
  if (x.m == 2) asm volatile("buffer_load_dword a116, %0, %1, 0 offen sc1" :: "v"(l < 16 ? OFF_FLAG + (x.c * 16 + l) * 256 : OFF_SLOT + ((l >> 3) & 1) * 16384 + x.c * 1024 + (l & 7) * 128), "s"(x.rs) : ACLOB);
  asm volatile("buffer_load_dwordx2 a[252:253], off, %0, %1 sc1" :: "s"(x.rs), "s"(OFF_TAG) : ACLOB);
  pstamp(x);
  const int task = wi * 6 + hp - 2;
  const rsrc_t rq = mkrs(a.wq, D * D * 2);
  auto ask_q = [&](auto i) {
    ldl<1>((8 * w + (l >> 3)) * 896 + (l & 7) * 16, rq, hp * 112 + i * 32 * 896, IMG_Q + (4 * i + w) * KB);
  };
  auto ask_t = [&](const char* tp, unsigned tn, unsigned ts, unsigned tb) {
    const rsrc_t rt = mkrs(tp, tn);
    const unsigned vo = (8 * w + (l >> 3)) * ts + (l & 7) * 16;
    sfor<0, 7>([&](auto i) { ldl<1>(vo, rt, tb + i * 32 * ts, IMG_T + (4 * i + w) * KB); });
  };
  const int tco = ((l >> 4) & 1) * 32 + (l & 3) * 8;
  const int sro = 4 * h + ((l >> 2) & 3);
  if (kvp) ask_t(hp ? a.wv : a.wk, D * 56, 128, 0);
  else if (task < 112) ask_t(task < 56 ? a.wg : a.wu, D * F * 2, F * 2, (task & 1) * 224 * F * 2 + ((task % 56) >> 1) * 128);
  else if (task < 168) ask_t(a.wd, D * F * 2, 896, ((task - 112) / 7) * 224 * 896 + ((task - 112) % 7) * 128);
  else ask_t(a.wo, task < 182 ? D * D * 2 : 0, 896, ((task - 168) / 7) * 224 * 896 + ((task - 168) % 7) * 128);
  wv<8>();
  if (kvp) {
    sfor<0, 14>([&](auto i) { dsr<0>(xf[i], w * 16 * KB + r * 512 + (((2 * i + h) ^ (r & 15)) << 4)); });
    wl<0>();
  }
  {
    float s = 0.f, q = 0.f, s1 = 0.f, q1 = 0.f;
    sfor<0, 14>([&](auto i) {
      if (!kvp) ask_q(i);
      pin(xf[i]);
      s = dot2(xf[i][0], 0x3f803f80u, s); q = dot2(xf[i][0], xf[i][0], q); s1 = dot2(xf[i][1], 0x3f803f80u, s1); q1 = dot2(xf[i][1], xf[i][1], q1);
      s = dot2(xf[i][2], 0x3f803f80u, s); q = dot2(xf[i][2], xf[i][2], q); s1 = dot2(xf[i][3], 0x3f803f80u, s1); q1 = dot2(xf[i][3], xf[i][3], q1);
      pin(s); pin(q); pin(s1); pin(q1);
    });
    s = sum32(s + s1); q = sum32(q + q1);
    if (h == 0) ((f32x2*)(lds + S_ST))[w * 32 + r] = f32x2{s, q};
  }
  if (kvp) wv<0>(); else wv<14>();
  bar();
  stamp(x);
  if (!kvp) {
    if (task < 112) {
      const int g = l >> 4, mat = task >= 56, gt = 4 * ((task % 56) >> 1) + w, m = gt / 7, j = gt % 7, lt = (m & 1) ? j : (j == 6 ? 0 : j + 1);
      const char* tb = lds + IMG_T + (16 * (g >> 1) + 4 * (g & 1) + ((l >> 2) & 3)) * 128 + 32 * w + (l & 3) * 8;
      const unsigned st = lt < 5 ? 10 * KB : 4 * KB, so = OFF_WGU + (m * 196 + (lt < 5 ? lt * 2 + mat : 140 + (lt - 5) * 2 + mat)) * KB + (task & 1) * 7 * st;
      u32x4 f[7];
      sfor<0, 7>([&](auto i) { f[i] = trfrag(tb + i * 4096, tb + i * 4096 + 1024); });
      float cs = 0.f;
      sfor<0, 7>([&](auto i) { st4<0>(f[i], x.vl16, x.rs, so + i * st); cs = fragsum(f[i], cs); });
      cs = sum32(sum16(cs));
      st1(cs, l < 16 ? l * 4 : ~0u, x.rs, OFF_CSGU + m * 2048 + (task & 1) * 1024 + (lt * 2 + mat) * 64);
    } else if (task < 168) {
      const int nt = 2 * ((task - 112) % 7) + (w & 1), rb = (task - 112) / 7;
      const char* tb = lds + IMG_T + sro * 128 + 64 * (w & 1) + tco;
      const unsigned so = OFF_WD + (nt * 112 + 28 * (rb >> 1)) * KB, sa = so + (rb & 1) * 10 * KB, sb = so + (20 + (rb & 1) * 4) * KB;
      u32x4 f[7];
      if (w < 2) {
        sfor<0, 7>([&](auto i) { const char* p = tb + (32 * (i >> 1) + 8 * (i & 1)) * 128; f[i] = trfrag(p, p + 2048); });
        sfor<0, 7>([&](auto i) { st4<0>(f[i], x.vl16, x.rs, i < 4 ? sa + i * KB : (i < 6 ? sb + (i - 4) * KB : sa + 4 * KB)); });
      } else {
        sfor<0, 7>([&](auto i) { const char* p = tb + (32 * ((i + 7) >> 1) + 8 * ((i + 7) & 1)) * 128; f[i] = trfrag(p, p + 2048); });
        sfor<0, 7>([&](auto i) { st4<0>(f[i], x.vl16, x.rs, i < 5 ? sa + (5 + i) * KB : sb + (i - 3) * KB); });
      }
      st1(0.f, ~0u, x.rs, 0);
    } else {
      const int nt = 2 * ((task - 168) % 7) + (w & 1);
      const char* tb = lds + IMG_T + (112 * (w >> 1) + sro) * 128 + 64 * (w & 1) + tco;
      const unsigned vo = task < 182 ? x.vl16 : ~0u - 4096;
      u32x4 f[8];
      sfor<0, 8>([&](auto i) { const char* p = tb + (28 * (i >> 1) + 16 * (i & 1)) * 128; f[i] = trfrag(p, p + 1024); if ((i & 1) && h) { f[i][2] = 0; f[i][3] = 0; } });
      sfor<0, 8>([&](auto i) { st4<(i & 3) * 1024>(f[i], vo, x.rs, OFF_WO + (nt * 32 + 16 * ((task - 168) / 7) + 8 * (w >> 1) + (i & ~3)) * KB); });
    }
  }
  pstamp(x);
  if (!kvp) { wv<8>(); bar(); }
  stamp(x);
  float mean, rstd;
  { const f32x2 p0 = ((const f32x2*)(lds + S_ST))[tl * 32 + r], p1 = ((const f32x2*)(lds + S_ST))[(tl + 2) * 32 + r];
    row_stats(p0[0] + p1[0], p0[1] + p1[1], mean, rstd); }
  float* csw = (float*)(lds + S_CS);
  const int tro = 8 * h + ((l >> 2) & 3);
  u32x4 ones = {0x3f803f80u, 0x3f803f80u, 0x3f803f80u, 0x3f803f80u};
  pin(ones);
  if (kvp) {
    f32x16 kv, cs;
    const unsigned tb = IMG_T + (224 * kh + tro) * 56 + tco;
    u32x2 f[14][2];
    auto rd = [&](auto i) { trd<i * 896>(f[i][0], tb); trd<i * 896 + 224>(f[i][1], tb); };
    sfor<0, 7>(rd);
    sfor<0, 14>([&](auto i) {
      if constexpr (i + 7 < 14) { wl<12>(); rd(std::integral_constant<int, i + 7>{}); } else wl<2 * (13 - i)>();
      pin(f[i][0]); pin(f[i][1]);
      u32x4 g = {f[i][0][0], f[i][0][1], f[i][1][0], f[i][1][1]};
      pin(g); nop<0>();
      if constexpr (i == 0) { m32z(kv, g, xf[i]); m32z(cs, g, ones); } else { m32(kv, g, xf[i]); m32(cs, g, ones); }
    });
    nop<11>(); pin(kv); pin(cs);
    if (r == 0) sfor<0, 4>([&](auto q) { *(f32x4*)(csw + (w * 3 + 2) * 32 + 8 * q + 4 * h) = f32x4{cs[4 * q], cs[4 * q + 1], cs[4 * q + 2], cs[4 * q + 3]}; });
    pstamp(x);
    if (kh) put_tile(lds + S_KP + tl * 4096 + x.vl16, kv);
    bar();
    if (!kh) {
      if (x.m == 0 && w == 0) st1(0.f, l < 16 ? l * 256 : ~0u, x.rs, OFF_FLAG + x.c * 4096);
      float raw[16]; f32x4 c4[4];
      sfor<0, 4>([&](auto q) {
        const f32x4 o = ((const f32x4*)(lds + S_KP + tl * 4096 + x.vl16))[q * 64];
        c4[q] = *(const f32x4*)(csw + (w * 3 + 2) * 32 + 8 * q + 4 * h) + *(const f32x4*)(csw + ((w + 2) * 3 + 2) * 32 + 8 * q + 4 * h);
#pragma unroll
        for (int i = 0; i < 4; ++i) raw[4 * q + i] = kv[4 * q + i] + o[i];
      });
      const unsigned km = kv_finish(x, raw, c4, mean, rstd, hp == 1, OFF_KV + (tt * 4 + 2 * hp) * KB, tl);
      pstamp(x);
      wv<0>();
      kv_publish(x, 0, 2 * tt + hp, km);
      if (tt == 0 && r == 0) sfor<0, 4>([&](auto q) { st4<32 * q>(__builtin_bit_cast(u32x4, c4[q]), h * 16, x.rs, OFF_CSQ + (16 + hp) * 128); });
    } else {
      sfor<0, 28>([&](auto i) { ldl<1>((16 * tl + 8 * (i & 1) + (l >> 3)) * 896 + (l & 7) * 16, rq, hp * 112 + (i >> 1) * 32 * 896, IMG_Q + (4 * (i >> 1) + 2 * tl + (i & 1)) * KB); });
      pstamp(x);
      wv<0>();
    }
    bar();
    pstamp(x);
  } else { pstamp(x); pstamp(x); pstamp(x); }
  f32x16 qa;
  {
    f32x16 q1, q2, cs;
    const unsigned t1 = IMG_Q + (224 * kh + tro) * 128 + tco + 56 * tl, t2 = IMG_Q + (224 * kh + tro) * 128 + tco + 56 * (tl ^ 1);
    u32x2 f[14][4];
    auto rd = [&](auto i) { trd<i * 2048>(f[i][0], t1); trd<i * 2048 + 512>(f[i][1], t1); trd<i * 2048>(f[i][2], t2); trd<i * 2048 + 512>(f[i][3], t2); };
    wl<0>();
    sfor<0, 3>(rd);
    sfor<0, 14>([&](auto i) {
      if constexpr (i + 3 < 14) { wl<8>(); rd(std::integral_constant<int, i + 3>{}); } else wl<4 * (13 - i)>();
      sfor<0, 4>([&](auto e) { pin(f[i][e]); });
      u32x4 g1 = {f[i][0][0], f[i][0][1], f[i][1][0], f[i][1][1]}, g2 = {f[i][2][0], f[i][2][1], f[i][3][0], f[i][3][1]};
      pin(g1); pin(g2); nop<0>();
      if constexpr (i == 0) { m32z(q1, g1, xf[i]); m32z(q2, g2, xf[i]); m32z(cs, g1, ones); } else { m32(q1, g1, xf[i]); m32(q2, g2, xf[i]); m32(cs, g1, ones); }
      if constexpr (i == 10) { ask_slots(x, 0); SB(); }
    });
    nop<11>(); pin(q1); pin(q2); pin(cs);
    pstamp(x);
    if (r == 0) sfor<0, 4>([&](auto q) { *(f32x4*)(csw + (w * 3) * 32 + 8 * q + 4 * h) = f32x4{cs[4 * q], cs[4 * q + 1], cs[4 * q + 2], cs[4 * q + 3]}; });
    if (kh == tl) { put_tile(lds + S_QP + (w ^ 2) * 4096 + x.vl16, q2); qa = q1; } else { put_tile(lds + S_QP + (w ^ 2) * 4096 + x.vl16, q1); qa = q2; }
  }
  if (th == 0 && x.c < 14) {
    const int f = 4 * x.c + w, hd = f >= 28, ks = f - 28 * hd;
    const char* tb = lds + IMG_Q + (16 * ks + sro) * 128 + tco + 56 * hd;
    st4<0>(trfrag(tb, tb + 1024), x.vl16, x.rs, OFF_WQ + (hp * 56 + f) * KB);
  }
  if (kvp && th == 1 && x.c < 7) {
    const char* tb = lds + IMG_T + (16 * (4 * x.c + w) + sro) * 56 + tco;
    st4<0>(trfrag(tb, tb + 448), x.vl16, x.rs, OFF_WQ + ((16 + hp) * 28 + 4 * x.c + w) * KB);
  }
  pstamp(x);
  kv_wait(x, 0);
  count(x, FL_W(x.c), 1);
  ask_kv(x, 0, kf);
  stamp(x);
  const bool own = x.c == hp && th == 0;
  {
    f32x4 cq[4];
    sfor<0, 4>([&](auto q) {
      cq[q] = *(const f32x4*)(csw + (kh * 3) * 32 + 8 * q + 4 * h) + *(const f32x4*)(csw + ((kh + 2) * 3) * 32 + 8 * q + 4 * h);
      if (own && tl == 0 && r == 0) st4<32 * q>(__builtin_bit_cast(u32x4, cq[q]), h * 16, x.rs, OFF_CSQ + (2 * hp + kh) * 128);
    });
    q_finish(x, qa, cq, mean, rstd);
  }
  pstamp(x);
}

DI void phase_front(Ctx& x, f32x16 (&kf)[3]) {
  const int w = x.w, l = x.l, h = l >> 5, r = l & 31;
  const int th = x.m >> 3, hp = x.m & 7, tl = w & 1, kh = w >> 1, tt = 4 * x.c + 2 * th + tl;
  const unsigned KV = OFF_KV + SZ_KV;
  {
    f32x4 cq[4], ck[4];
    sfor<0, 4>([&](auto q) { ld4f<32 * q>(cq[q], h * 16, x.rs, OFF_CSQ + (2 * hp + kh) * 128); ld4f<32 * q>(ck[q], h * 16, x.rs, OFF_CSQ + (16 + (hp & 1)) * 128); });
    wv<0>();
    await(x, FL_X(x.c, th), 7);
    stamp(x);
    u32x4 xf[14]; f32x2 sp[7];
    {
      const unsigned so = OFF_X + 2 * SZ_X + (tt * 28 + 14 * kh) * KB;
      sfor<0, 7>([&](auto i) { ld4<(i & 3) * 1024>(xf[i], x.vl16, x.rs, so + (i >> 2) * 4096); });
      sfor<0, 7>([&](auto i) { ld2<0>(sp[i], (h * 7 * 2048 + r) * 8, x.rs, OFF_ST + 2 * SZ_ST + (i * 2048 + tt * 32) * 8); });
      sfor<7, 14>([&](auto i) { ld4<(i & 3) * 1024>(xf[i], x.vl16, x.rs, so + (i >> 2) * 4096); });
    }
    float mean, rstd;
    auto stats = [&]() {
      sfor<0, 7>([&](auto i) { pin(sp[i]); });
      sfor<0, 4>([&](auto i) { pin(cq[i]); pin(ck[i]); });
      float s = 0.f, q = 0.f;
#pragma unroll
      for (int i = 0; i < 7; ++i) { s += sp[i][0]; q += sp[i][1]; }
      row_stats(sum32(s), sum32(q), mean, rstd);
      pin(mean); pin(rstd); SB();
    };
    f32x16 qa, qb;
    u32x4 wa[14], wb[14];
    const unsigned pa = x.vl16 + (SL_WQ + 28 * kh + 14 * kh) * KB, pb = x.vl16 + (SL_WQ + 28 * (kh ^ 1) + 14 * kh) * KB;
    if (hp < 2) {
      f32x16 kv;
      sfor<0, 14>([&](auto i) { dsr<i * 1024>(wa[i], x.vl16 + 14 * kh * KB); });
      sfor<0, 14>([&](auto i) {
        wv<(i < 7 ? 20 : 13) - i>(); wl<13 - i>();
        if constexpr (i == 0) m32z(kv, wa[i], xf[i]); else m32(kv, wa[i], xf[i]);
        if constexpr (i == 7) stats();
      });
      nop<11>(); pin(kv);
      if (kh) put_tile(lds + S_KP + tl * 4096 + x.vl16, kv);
      bar();
      unsigned km = 0;
      if (!kh) {
        float raw[16];
        sfor<0, 4>([&](auto q) {
          const f32x4 o = ((const f32x4*)(lds + S_KP + tl * 4096 + x.vl16))[q * 64];
#pragma unroll
          for (int i = 0; i < 4; ++i) raw[4 * q + i] = kv[4 * q + i] + o[i];
        });
        km = kv_finish(x, raw, ck, mean, rstd, hp == 1, KV + (tt * 4 + 2 * hp) * KB, tl);
      }
      wl<0>();
      sfor<0, 7>([&](auto i) { dsr<i * 1024>(wb[i], pb); dsr<i * 1024>(wa[i], pa); });
      sfor<0, 14>([&](auto i) {
        if constexpr (i < 7) wl<12>(); else wl<2 * (13 - i)>();
        if constexpr (i == 0) { m32z(qb, wb[i], xf[i]); m32z(qa, wa[i], xf[i]); } else { m32(qb, wb[i], xf[i]); m32(qa, wa[i], xf[i]); }
        if constexpr (i < 7) { dsr<(i + 7) * 1024>(wb[i + 7], pb); dsr<(i + 7) * 1024>(wa[i + 7], pa); }
        if constexpr (i == 8) { if (!kh) { wv<0>(); kv_publish(x, 1, 2 * tt + hp, km); } SB(); }
      });
    } else {
      sfor<0, 7>([&](auto i) { dsr<i * 1024>(wb[i], pb); dsr<i * 1024>(wa[i], pa); });
      sfor<0, 14>([&](auto i) {
        wv<(i < 7 ? 20 : 13) - i>();
        if constexpr (i < 7) wl<12>(); else wl<2 * (13 - i)>();
        if constexpr (i == 0) { m32z(qb, wb[i], xf[i]); m32z(qa, wa[i], xf[i]); } else { m32(qb, wb[i], xf[i]); m32(qa, wa[i], xf[i]); }
        if constexpr (i < 7) { dsr<(i + 7) * 1024>(wb[i + 7], pb); dsr<(i + 7) * 1024>(wa[i + 7], pa); }
        if constexpr (i == 7) stats();
      });
    }
    nop<11>();
    pin(qa); pin(qb);
    put_tile(lds + S_QP + (w ^ 2) * 4096 + x.vl16, qb);
    bar();
    q_finish(x, qa, cq, mean, rstd);
    stamp(x);
    ask_slots(x, 1);
    kv_wait(x, 1);
    ask_kv(x, 1, kf);
    stamp(x);
  }
}

DI void phase_att(Ctx& x, const int L, f32x16 (&kf)[3], f32x16 (&g2)[3]) {
  const int w = x.w, l = x.l, h = l >> 5, r = l & 31;
  const int th = x.m >> 3, hp = x.m & 7, tl = w & 1, kh = w >> 1, tt = 4 * x.c + 2 * th + tl;
  const unsigned kvs = OFF_KV + L * SZ_KV + 16 * w * 4096;
  stamp(x);
  if (L == 0) bar();
  stamp(x);
  unsigned kma = S_KM + 16; asm volatile("" : "+s"(kma));
  const float kmx = *(const float*)(lds + kma);
  u32x4 q[4][2];
#pragma unroll
  for (int k = 0; k < 4; ++k) {
    q[k][0] = ((const u32x4*)(lds + S_QF + (w ^ k) * 2048 + x.vl16))[0];
    q[k][1] = ((const u32x4*)(lds + S_QF + (w ^ k) * 2048 + x.vl16))[64];
  }
  bool safe = true;
  {
#pragma unroll
    for (int k = 0; k < 4; ++k) {
      const float mrow = bflo(q[k][1][2]) * kmx + 0.01f;
      safe = safe && (!h || mrow <= M_LIMIT);
      if (h) q[k][1][2] = cvtpk(-mrow, 0.f);
    }
  }
  const bool fast = __all(safe);
  stamp(x);
  f32x16 own;
  if (fast) {
    unsigned so_kv = kvs + 3 * 4096, so_w = OFF_WGU + (x.m * 196 + 2 * w) * KB, so_l = 2 * w * KB, spin = 1u << 16, fvo = (l & 15) * 4096;
    Two ab = base_wo(x);
    const Two wd = base_wd(x);
    asm volatile(
      ".macro GRP s, pa, pb\n\t"
      "v_exp_f32 v200, v[\\s]\n\tv_exp_f32 v201, v[\\s+1]\n\tv_cvt_pk_bf16_f32 v[\\pa], v202, v203\n\t"
      "v_exp_f32 v202, v[\\s+2]\n\tv_exp_f32 v203, v[\\s+3]\n\tv_cvt_pk_bf16_f32 v[\\pb], v200, v201\n\t"
      ".endm\n\t"
      ".macro MEM kind, i, ld\n\t"
      ".if \\kind == 1\n\t"
      "buffer_load_dwordx4 v[128+16*\\ld+4*\\i:128+16*\\ld+4*\\i+3], %[vo], %[rs], %[skv] offen offset:1024*\\i sc1\n\t"
      ".elseif \\kind == 5\n\t"
      "ds_write_b128 v205, v[4*\\i:4*\\i+3] offset:1024*\\i\n\t"
      ".elseif \\kind == 6\n\t"
      "ds_write_b128 v205, v[4:7] offset:1024\n\t"
      ".elseif \\kind == 7\n\t"
      "ds_write_b128 v205, v[12:15] offset:3072\n\t"
      ".elseif \\kind == 2\n\t"
      "buffer_load_dword v206, v205, %[rs], 0 offen offset:3072 sc1\n\t"
      ".elseif \\kind == 3\n\t"
      ".if att_n < 16 || att_n >= 56\n\t"
      ".if att_n < 102\n\t"
      ".if att_n < 16\n\t.set att_r, 184+4*att_n\n\t.else\n\t.set att_r, 4*(att_n-56)\n\t.endif\n\t"
      ".if att_n & 1\n\t"
      "buffer_load_dwordx4 a[att_r:att_r+3], %[vo], %[rs], %[ab1] offen offset:1024*((att_n>>1)&3) sc1\n\t"
      ".else\n\t"
      "buffer_load_dwordx4 a[att_r:att_r+3], %[vo], %[rs], %[ab0] offen offset:1024*((att_n>>1)&3) sc1\n\t"
      ".endif\n\t"
      ".if att_n == 15\n\t"
      "s_mov_b32 %[ab0], %[wd0]\n\ts_mov_b32 %[ab1], %[wd1]\n\t"
      ".elseif (att_n & 7) == 7\n\t"
      "s_add_u32 %[ab0], %[ab0], 4096\n\ts_add_u32 %[ab1], %[ab1], 4096\n\t"
      ".endif\n\t"
      ".endif\n\t"
      ".elseif att_n < 44\n\t"
      ".if att_n & 1\n\t"
      "buffer_load_dwordx4 %[vo], %[rs], %[sw] offen offset:1024 sc1 lds\n\t"
      "s_add_u32 %[sw], %[sw], 8192\n\ts_add_u32 m0, m0, 8192\n\t"
      ".else\n\t"
      "buffer_load_dwordx4 %[vo], %[rs], %[sw] offen sc1 lds\n\t"
      ".endif\n\t"
      ".else\n\t"
      "buffer_load_dwordx4 v[208+4*(att_n-44):208+4*(att_n-44)+3], %[vo], %[rs], %[sw] offen offset:1024*(att_n&1) sc1\n\t"
      ".if att_n & 1\n\ts_add_u32 %[sw], %[sw], 8192\n\t.endif\n\t"
      ".endif\n\t"
      ".set att_n, att_n+1\n\t"
      ".endif\n\t"
      ".endm\n\t"
      ".macro STEP sc, sn, k, nk, pk, tk, nt, pt, ld, m0k, m0i, m1k, m2k, m2i, m3k\n\t"
      "v_mfma_f32_32x32x16_bf16 v[\\sn:\\sn+15], v[128+16*\\nt:128+16*\\nt+3], v[64+8*\\nk:64+8*\\nk+3], 0\n\t"
      "MEM \\m0k, \\m0i, \\ld\n\t"
      "GRP \\sc, 199, 192\n\t"
      "v_mfma_f32_32x32x16_bf16 v[\\sn:\\sn+15], v[132+16*\\nt:132+16*\\nt+3], v[68+8*\\nk:68+8*\\nk+3], v[\\sn:\\sn+15]\n\t"
      "MEM \\m1k, 0, 0\n\t"
      "GRP \\sc+4, 193, 194\n\t"
      "v_mfma_f32_32x32x16_bf16 v[16*\\pk:16*\\pk+15], v[140+16*\\pt:140+16*\\pt+3], v[196:199], v[16*\\pk:16*\\pk+15]\n\t"
      "MEM \\m2k, \\m2i, \\ld\n\t"
      "GRP \\sc+8, 195, 196\n\t"
      "v_mfma_f32_32x32x16_bf16 v[16*\\k:16*\\k+15], v[136+16*\\tk:136+16*\\tk+3], v[192:195], v[16*\\k:16*\\k+15]\n\t"
      "MEM \\m3k, 0, 0\n\t"
      "GRP \\sc+12, 197, 198\n\t"
      ".endm\n\t"
      ".macro TILE tk, nx, pv, on, wait, p, fl=0\n\t"
      "STEP 96, 112, 0, 1, 3, \\tk, \\tk, \\pv, \\pv, \\p, 0, \\p, \\p, 0, \\fl\n\t"
      "STEP 112, 96, 1, 2, 0, \\tk, \\tk, \\tk, \\pv, \\on, 0, \\p, \\on, 1, 0\n\t"
      "STEP 96, 112, 2, 3, 1, \\tk, \\tk, \\tk, \\pv, \\on, 2, \\p, \\on, 3, 0\n\t"
      ".if \\on\n\ts_add_u32 %[skv], %[skv], 4096\n\t.endif\n\t"
      "s_waitcnt vmcnt(\\wait)\n\t"
      "STEP 112, 96, 3, 0, 2, \\tk, \\nx, \\tk, \\pv, \\p, 0, \\p, \\p, 0, 0\n\t"
      ".endm\n\t"
      ".set att_n, 0\n\t"
      "s_mov_b32 m0, %[sl]\n\t"
      "v_mov_b32 v196, 0\n\tv_mov_b32 v197, 0\n\tv_mov_b32 v198, 0\n\tv_mov_b32 v199, 0\n\tv_mov_b32 v202, 0\n\tv_mov_b32 v203, 0\n\t"
      "v_mov_b32 v188, 0\n\tv_mov_b32 v189, 0\n\tv_mov_b32 v190, 0\n\tv_mov_b32 v191, 0\n\t"
      "v_mfma_f32_32x32x16_bf16 v[0:15], v[196:199], v[196:199], 0\n\t"
      "v_mfma_f32_32x32x16_bf16 v[16:31], v[196:199], v[196:199], 0\n\t"
      "v_mfma_f32_32x32x16_bf16 v[32:47], v[196:199], v[196:199], 0\n\t"
      "v_mfma_f32_32x32x16_bf16 v[48:63], v[196:199], v[196:199], 0\n\t"
      "s_waitcnt vmcnt(8)\n\t"
      "v_mfma_f32_32x32x16_bf16 v[96:111], v[128:131], v[64:67], 0\n\t"
      "v_mfma_f32_32x32x16_bf16 v[96:111], v[132:135], v[68:71], v[96:111]\n\t"
      "s_nop 11\n\t"
      "TILE 0, 1, 3, 1, 8, 0\n\t"
      "TILE 1, 2, 0, 1, 8, 0\n\tTILE 2, 3, 1, 1, 9, 0, 2\n\t"
      "s_waitcnt vmcnt(4)\n\t"
      "1:\n\t"
      "v_cmp_gt_u32 vcc, 16, v206\n\t"
      "s_and_b64 vcc, exec, vcc\n\t"
      "s_cbranch_vccz 2f\n\t"
      "s_sleep 24\n\t"
      "buffer_load_dword v206, v205, %[rs], 0 offen offset:3072 sc1\n\t"
      "s_sub_u32 %[spin], %[spin], 1\n\t"
      "s_waitcnt vmcnt(0)\n\t"
      "s_cbranch_scc0 1b\n\t"
      "2:\n\t"
      "s_barrier\n\t"
      "TILE 3, 0, 2, 1, 14, 3\n\t"
      "TILE 0, 1, 3, 1, 21, 3\n\tTILE 1, 2, 0, 1, 24, 3\n\tTILE 2, 3, 1, 1, 24, 3\n\tTILE 3, 0, 2, 1, 24, 3\n\t"
      "TILE 0, 1, 3, 1, 24, 3\n\tTILE 1, 2, 0, 1, 24, 3\n\tTILE 2, 3, 1, 1, 24, 3\n\tTILE 3, 0, 2, 1, 24, 3\n\t"
      "TILE 0, 1, 3, 1, 24, 3\n\tTILE 1, 2, 0, 0, 20, 3\n\tTILE 2, 3, 1, 0, 16, 3\n\t"
      "v_add_u32 v205, %[d1], v204\n\tv_add_u32 v206, %[d2], v204\n\t"
      "STEP 96, 112, 0, 1, 3, 3, 3, 2, 0, 3, 0, 3, 3, 0, 3\n\t"
      "STEP 112, 96, 1, 2, 0, 3, 3, 3, 0, 3, 0, 3, 0, 0, 0\n\t"
      "STEP 96, 112, 2, 3, 1, 3, 3, 3, 0, 5, 0, 6, 5, 2, 7\n\t"
      "ds_write_b128 v206, v[16:19]\n\t"
      "GRP 112, 199, 192\n\t"
      "ds_write_b128 v206, v[20:23] offset:1024\n\t"
      "GRP 116, 193, 194\n\t"
      "v_mfma_f32_32x32x16_bf16 v[32:47], v[188:191], v[196:199], v[32:47]\n\t"
      "ds_write_b128 v206, v[24:27] offset:2048\n\t"
      "GRP 120, 195, 196\n\t"
      "v_mfma_f32_32x32x16_bf16 v[48:63], v[184:187], v[192:195], v[48:63]\n\t"
      "ds_write_b128 v206, v[28:31] offset:3072\n\t"
      "GRP 124, 197, 198\n\t"
      "v_add_u32 v200, %[d3], v204\n\t"
      "v_cvt_pk_bf16_f32 v199, v202, v203\n\t"
      "s_nop 1\n\t"
      "v_mfma_f32_32x32x16_bf16 v[48:63], v[188:191], v[196:199], v[48:63]\n\t"
      "ds_write_b128 v200, v[32:35]\n\tds_write_b128 v200, v[36:39] offset:1024\n\tds_write_b128 v200, v[40:43] offset:2048\n\tds_write_b128 v200, v[44:47] offset:3072\n\t"
      "s_nop 7\n\t"
      ".purgem GRP\n\t.purgem MEM\n\t.purgem STEP\n\t.purgem TILE"
      : "={v[48:63]}"(own), "+{v[128:143]}"(kf[0]), "+{v[144:159]}"(kf[1]), "+{v[160:175]}"(kf[2]),
        "={v[208:223]}"(g2[0]), "={v[224:239]}"(g2[1]), "={v[240:255]}"(g2[2]),
        [skv] "+s"(so_kv), [sw] "+s"(so_w), [ab0] "+s"(ab.s0), [ab1] "+s"(ab.s1), [spin] "+s"(spin), "+{v205}"(fvo)
      : "{v[64:67]}"(q[1][0]), "{v[68:71]}"(q[1][1]), "{v[72:75]}"(q[2][0]), "{v[76:79]}"(q[2][1]),
        "{v[80:83]}"(q[3][0]), "{v[84:87]}"(q[3][1]), "{v[88:91]}"(q[0][0]), "{v[92:95]}"(q[0][1]),
        [vo] "{v204}"(x.vl16), [d1] "s"(SCR + (w ^ 1) * 3 * 4096), [d2] "s"(SCR + ((w ^ 2) * 3 + 1) * 4096), [d3] "s"(SCR + ((w ^ 3) * 3 + 2) * 4096), [rs] "s"(x.rs), [sl] "s"(so_l), [wd0] "s"(wd.s0), [wd1] "s"(wd.s1)
      : "memory", "scc", "vcc",
        "v0","v1","v2","v3","v4","v5","v6","v7","v8","v9","v10","v11","v12","v13","v14","v15","v16","v17","v18","v19","v20","v21","v22","v23",
        "v24","v25","v26","v27","v28","v29","v30","v31","v32","v33","v34","v35","v36","v37","v38","v39","v40","v41","v42","v43","v44","v45","v46","v47",
        "v96","v97","v98","v99","v100","v101","v102","v103","v104","v105","v106","v107","v108","v109","v110","v111",
        "v112","v113","v114","v115","v116","v117","v118","v119","v120","v121","v122","v123","v124","v125","v126","v127",
        "v176","v177","v178","v179","v180","v181","v182","v183","v184","v185","v186","v187","v188","v189","v190","v191",
        "v192","v193","v194","v195","v196","v197","v198","v199","v200","v201","v202","v203","v206", ACLOB);
  } else {
    f32x16 o[4];
    wv<0>();
    pin(kf[0]); pin(kf[1]); pin(kf[2]);
    float mx[4];
#pragma unroll
    for (int k = 0; k < 4; ++k) {
      mx[k] = -3.0e38f; if (h) q[k][1][2] = 0; pin(q[k][1]);
#pragma unroll
      for (int i = 0; i < 16; ++i) o[k][i] = 0.f;
    }
#pragma nounroll
    for (unsigned spin = 0; spin < (1u << 16); ++spin) {
      float v;
      ld1(v, (l & 15) * 4096, x.rs, OFF_FLAG + FL_W(0)); wv<0>(); pin(v);
      if (__all(__float_as_uint(v) >= 16u)) break;
      __builtin_amdgcn_s_sleep(24);
    }
    pf_wo(x);
    pf_wgu1(x);
    sfor<0, 12>([&](auto j) {
      u32x4 t;
      ld4<1024 * (j & 1)>(t, x.vl16, x.rs, OFF_WGU + (x.m * 196 + 112 + 8 * (j >> 1) + 2 * w) * KB); wv<0>(); pin(t);
#pragma unroll
      for (int i = 0; i < 4; ++i) g2[j >> 2][4 * (j & 3) + i] = __uint_as_float(t[i]);
    });
    pf_wd<0, 46>(x, base_wd(x));
    for (int tk = 0; tk < 16; ++tk) {
      u32x4 kf[4];
      sfor<0, 4>([&](auto i) { ld4<i * 1024>(kf[i], x.vl16, x.rs, kvs + tk * 4096); });
      wv<0>();
      sfor<0, 4>([&](auto k) {
        f32x16 s;
        nop<1>();
        m32z(s, kf[0], q[k][0]); m32(s, kf[1], q[k][1]);
        nop<11>(); pin(s);
        float tm = s[0];
#pragma unroll
        for (int i = 1; i < 16; ++i) tm = fmaxf(tm, s[i]);
        tm = max32(tm);
        const float mn = fmaxf(mx[k], tm), al = __builtin_amdgcn_exp2f(mx[k] - mn);
        mx[k] = mn;
#pragma unroll
        for (int i = 0; i < 16; ++i) o[k][i] *= al;
        u32x4 p0, p1;
#pragma unroll
        for (int d = 0; d < 4; ++d) {
          p0[d] = cvtpk(__builtin_amdgcn_exp2f(s[2 * d] - mn), __builtin_amdgcn_exp2f(s[2 * d + 1] - mn));
          p1[d] = cvtpk(__builtin_amdgcn_exp2f(s[8 + 2 * d] - mn), __builtin_amdgcn_exp2f(s[9 + 2 * d] - mn));
        }
        pin(p0); pin(p1); pin(o[k]);
        nop<1>();
        m32(o[k], kf[2], p0); m32(o[k], kf[3], p1);
        nop<11>(); pin(o[k]);
      });
    }
    bar();
    float* mm = (float*)(lds + SCR);
#pragma unroll
    for (int k = 0; k < 4; ++k) if (h == 0) mm[((w ^ k) * 4 + w) * 32 + r] = mx[k];
    bar();
#pragma unroll
    for (int k = 0; k < 4; ++k) {
      const float* p = mm + (w ^ k) * 128 + r;
      const float g = fmaxf(fmaxf(p[0], p[32]), fmaxf(p[64], p[96])), f = __builtin_amdgcn_exp2f(mx[k] - g);
#pragma unroll
      for (int i = 0; i < 16; ++i) o[k][i] *= f;
    }
    bar();
    reduce_put(x, o);
    own = o[0];
  }
  stamp(x);
  reduce_get(x, own);
  pin(own); fstamp(x);
  {
    auto lr = __builtin_amdgcn_permlane32_swap(__float_as_uint(own[12]), __float_as_uint(own[12]), false, false);
    const float inv = __builtin_amdgcn_rcpf(__uint_as_float(lr[1]));
    u32x4 a, b;
#pragma unroll
    for (int d = 0; d < 4; ++d) { a[d] = cvtpk(own[2 * d] * inv, own[2 * d + 1] * inv); b[d] = cvtpk(own[8 + 2 * d] * inv, own[9 + 2 * d] * inv); }
    if (h) { b[2] = 0; b[3] = 0; }
    const unsigned so = OFF_O + L * SZ_O + (tt * 32 + 2 * (2 * hp + kh)) * KB;
    st4<0>(a, x.vl16, x.rs, so); st4<1024>(b, x.vl16, x.rs, so);
  }
  fstamp(x);
  wv<0>();
  fstamp(x);
  signal(x, FL_O(L, x.c, th), 1);
  if (L == 0 && th == 0) {
    if (x.c == hp) count(x, FL_W2, 1);
  }
}

template <bool FINAL> DI void res_epilogue(Ctx& x, const f32x16& acc, const u32x4 (&xr)[2], unsigned Xo, unsigned STo, int tt, int nt, char* out = nullptr) {
  const int h = x.l >> 5, r = x.l & 31;
  u32x4 y[2];
#pragma unroll
  for (int d = 0; d < 8; ++d) y[d >> 2][d & 3] = cvtpk(acc[2 * d] + bflo(xr[d >> 2][d & 3]), acc[2 * d + 1] + bfhi(xr[d >> 2][d & 3]));
  if constexpr (FINAL) {
#pragma unroll
    for (int s = 0; s < 2; ++s) {
      auto a = __builtin_amdgcn_permlane32_swap(y[s][0], y[s][2], false, false);
      auto b = __builtin_amdgcn_permlane32_swap(y[s][1], y[s][3], false, false);
      y[s] = u32x4{a[0], b[0], a[1], b[1]};
    }
    const unsigned vo = r * (D * 2) + h * 16, so = tt * 32 * D * 2 + nt * 64;
    const rsrc_t ro = mkrs(out, S * D * 2);
    st4p<0>(y[0], vo, ro, so); st4p<32>(y[1], vo, ro, so);
  } else {
    const unsigned so = Xo + (tt * 28 + 2 * nt) * KB;
    st4<0>(y[0], x.vl16, x.rs, so); st4<1024>(y[1], x.vl16, x.rs, so);
    float s = 0.f, q = 0.f, s1 = 0.f, q1 = 0.f;
#pragma unroll
    for (int d = 0; d < 4; ++d) {
      s = dot2(y[0][d], 0x3f803f80u, s); q = dot2(y[0][d], y[0][d], q); s1 = dot2(y[1][d], 0x3f803f80u, s1); q1 = dot2(y[1][d], y[1][d], q1);
    }
    s = sum32(s + s1); q = sum32(q + q1);
    if (h == 0) st2<0>(u32x2{__float_as_uint(s), __float_as_uint(q)}, r * 8, x.rs, STo + (nt * 2048 + tt * 32) * 8);
  }
}

DI void phase_oproj(Ctx& x, const int L, const Args& a, const f32x16 (&g2)[3]) {
  const int w = x.w;
  if (x.m < 14) {
    const int th = x.m / 7, ct = x.m % 7, tt0 = 4 * x.c + 2 * th, tt = tt0 + (w >> 1), nt = 2 * ct + (w & 1);
    u32x2 x0[4];
    if (L == 0) {
      const rsrc_t rx = mkrs(a.x, S * D * 2);
      sfor<0, 4>([&](auto i) { ld2i<16 * i>(x0[i], (x.l & 31) * 896 + (x.l >> 5) * 8, rx, tt * 32 * 896 + nt * 64); });
    } else {
      sfor<0, 4>([&](auto i) { ld2u<(i >> 1) * 1024 + (i & 1) * 8>(x0[i], x.vl16, x.rs, OFF_X + 2 * SZ_X + (tt * 28 + 2 * nt) * KB); });
    }
    await(x, FL_O(L, x.c, th), 8);
    stamp(x);
    u32x4 b[8][2];
    const unsigned s0 = OFF_O + L * SZ_O + (tt * 32 + 8 * w) * KB, s1 = OFF_O + L * SZ_O + ((tt0 + ((w >> 1) ^ 1)) * 32 + 8 * w) * KB;
    auto ld = [&](auto i) { ld4<(i & 3) * 1024>(b[i][0], x.vl16, x.rs, s0 + (i >> 2) * 4096); ld4<(i & 3) * 1024>(b[i][1], x.vl16, x.rs, s1 + (i >> 2) * 4096); };
    constexpr int WN = OPWN;
    sfor<0, WN>(ld);
    f32x16 acc[4];
    sfor<0, 8>([&](auto i) {
      constexpr int last = (i + WN - 1 < 7 ? i + WN - 1 : 7);
      wv<2 * (last - i) + 1>();
      m32a<A_WO + 8 * i, i == 0>(acc[0], b[i][0]); m32a<A_WO + 8 * i + 4, i == 0>(acc[1], b[i][0]);
      wv<2 * (last - i)>();
      m32a<A_WO + 8 * i, i == 0>(acc[2], b[i][1]); m32a<A_WO + 8 * i + 4, i == 0>(acc[3], b[i][1]);
      if constexpr (i + WN < 8) ld(std::integral_constant<int, i + WN>{});
    });
    nop<11>();
    sfor<0, 4>([&](auto k) { pin(acc[k]); pin(x0[k]); });
    stamp(x);
    reduce_put(x, acc); reduce_get(x, acc[0]);
    pin(acc[0]); fstamp(x);
    const u32x4 x2[2] = {u32x4{x0[0][0], x0[0][1], x0[1][0], x0[1][1]}, u32x4{x0[2][0], x0[2][1], x0[3][0], x0[3][1]}};
    res_epilogue<false>(x, acc[0], x2, OFF_X + (2 * L + 1) * SZ_X, OFF_ST + (2 * L + 1) * SZ_ST, tt, nt);
    fstamp(x);
    wv<0>();
    fstamp(x);
    signal(x, FL_X1(L, x.c), 1);
    fstamp(x);
  } else {
    stamp(x); stamp(x); fstamp(x); fstamp(x); fstamp(x); fstamp(x);
  }
#pragma unroll
  for (int j = 0; j < 12; ++j)
    *(f32x4*)(lds + (112 + 8 * (j >> 1) + 2 * w + (j & 1)) * KB + x.vl16) = f32x4{g2[j >> 2][4 * (j & 3)], g2[j >> 2][4 * (j & 3) + 1], g2[j >> 2][4 * (j & 3) + 2], g2[j >> 2][4 * (j & 3) + 3]};
}

constexpr int DWN = 7;
constexpr bool dn_half(int o) { return o == 4 || o == 5 || o == 14 || o == 15; }
struct DnSrc { unsigned s0, s1, l0, l1; };
DI DnSrc dn_src(const Ctx& x, int L) {
  const int tt0 = 4 * x.c + 2 * ((x.m / 7) & 1), tt = tt0 + (x.w >> 1), tto = tt0 + ((x.w >> 1) ^ 1);
  return DnSrc{OFF_H + L * SZ_H + (tt * 112 + 28 * x.w) * KB, OFF_H + L * SZ_H + (tto * 112 + 28 * x.w) * KB,
               OFF_HL + ((L * 64 + tt) * 16 + 4 * x.w) * KB, OFF_HL + ((L * 64 + tto) * 16 + 4 * x.w) * KB};
}
template <int O> DI void dn_ld(Ctx& x, const DnSrc& d, u32x4 (&b)[DWN][2]) {
  constexpr int i = dn_ks(O);
  if constexpr (dn_half(O)) {
    constexpr int imm = (O >= 14 ? 2048 : 0) + (O & 1) * 512;
    u32x2 lo, hi, lo1, hi1;
    ld2u<imm>(lo, x.l * 8, x.rs, d.l0); ld2u<imm + 1024>(hi, x.l * 8, x.rs, d.l0);
    ld2u<imm>(lo1, x.l * 8, x.rs, d.l1); ld2u<imm + 1024>(hi1, x.l * 8, x.rs, d.l1);
    b[O % DWN][0] = u32x4{lo[0], lo[1], hi[0], hi[1]}; b[O % DWN][1] = u32x4{lo1[0], lo1[1], hi1[0], hi1[1]};
  } else {
    ld4<(i & 3) * 1024>(b[O % DWN][0], x.vl16, x.rs, d.s0 + (i >> 2) * 4096); ld4<(i & 3) * 1024>(b[O % DWN][1], x.vl16, x.rs, d.s1 + (i >> 2) * 4096);
  }
}

DI void gu_tile(const f32x4 (&acc)[2][2], const u32x4 (&cs)[2], const float (&mean)[2], const float (&r2)[2], u32x2 (&hp)[2]) {
  const f32x4 cg = __builtin_bit_cast(f32x4, cs[0]), cu = __builtin_bit_cast(f32x4, cs[1]);
#pragma unroll
  for (int u = 0; u < 2; ++u) {
    const f32x2 nm = {-mean[u], -mean[u]}, rr = {r2[u], r2[u]};
#pragma unroll
    for (int e = 0; e < 2; ++e) {
      const f32x2 g = __builtin_elementwise_fma(nm, f32x2{cg[2 * e], cg[2 * e + 1]}, f32x2{acc[0][u][2 * e], acc[0][u][2 * e + 1]});
      const f32x2 v = __builtin_elementwise_fma(nm, f32x2{cu[2 * e], cu[2 * e + 1]}, f32x2{acc[1][u][2 * e], acc[1][u][2 * e + 1]});
      const f32x2 y = g * rr * v;
      hp[u][e] = cvtpk(y[0], y[1]);
    }
  }
}

DI void phase_gu(Ctx& x, const int L, u32x4 (&b)[DWN][2]) {
  const int w = x.w, l = x.l, g = l >> 4, c16 = l & 15, m = x.m, tt = 4 * x.c + w;
  if (L == 0 && wave0(x)) { unsigned v; ldflag(v, x.rs, OFF_FLAG + FL_W2); wv<0>(); recheck(x, v, FL_W2, 8); }
  await(x, FL_X1(L, x.c), 14);
  stamp(x);
  constexpr int NXV = 11;
  u32x4 xf[NXV][2]; f32x2 sp[2][4];
  const unsigned sx = OFF_X + (2 * L + 1) * SZ_X + tt * 28 * KB, st = OFF_ST + (2 * L + 1) * SZ_ST + tt * 32 * 8;
  auto ldx = [&](auto k, auto u) {
    if constexpr (k < NXV) ld4<(k & 1) * 2048 + 256 * u>(xf[k][u], x.vg16, x.rs, sx + (k >> 1) * 4096);
    else if constexpr (k < 14) lda<A_GX + 8 * (k - NXV) + 4 * u, (k & 1) * 2048 + 256 * u>(x.vg16, x.rs, sx + (k >> 1) * 4096);
    else { constexpr int i = k - 14; ld2<128 * u>(sp[u][i], (((i < 3 || g < 2) ? g + 4 * i : g) * 2048 + c16) * 8, x.rs, st); }
  };
  using I0 = std::integral_constant<int, 0>; using I1 = std::integral_constant<int, 1>;
  constexpr int XA = XAHEAD;
  sfor<0, XA>([&](auto k) { ldx(k, I0{}); ldx(k, I1{}); });
  const unsigned a0 = x.vl16, a1 = x.vl16 + 65536, a2 = x.vl16 + 131072;
  constexpr int DP = 6;
  u32x4 wf[8];
  auto rd = [&](auto ii) {
    constexpr int i = ii, slot = i >= 160 ? i - 160 : i;
    if constexpr (slot < 64) dsr<slot * 1024>(wf[i & 7], a0); else if constexpr (slot < 128) dsr<(slot - 64) * 1024>(wf[i & 7], a1); else dsr<(slot - 128) * 1024>(wf[i & 7], a2);
  };
  auto mm = [&](f32x4& c, const u32x4& a, auto k, auto u) {
    if constexpr (k < NXV) m16<k == 0>(c, a, xf[k][u]); else m16a<false, A_GX + 8 * (k - NXV) + 4 * u>(c, a);
  };
  const Two wd = base_wd(x);
  f32x4 aa[5][2][2], ch0, ch1;
  float mean[2], r2[2];
  sfor<0, DP>(rd);
  sfor<0, 140>([&](auto i) {
    constexpr int KS = i / 10, s = i % 10, t = s >> 1, mat = i & 1;
    using K = std::integral_constant<int, KS>;
    constexpr auto side = [](int k) { return (k >= 0 && k <= 3) || (k >= 5 && k <= 12); };
    constexpr auto all = [](int k) { return (k + XA < 14 ? 2 : 0) + ((k >= 0 && k <= 3) || (k >= 5 && k <= 9) ? 2 : 0) + (k == 5 ? 1 : 0) + (k >= 10 ? 3 : 0); };
    if constexpr (s == 0) {
      constexpr int cnt = [&] { int n = side(KS - XA) ? 1 : 0; for (int k = KS - XA + 1; k < KS; ++k) n += all(k); return n; }();
      wv<cnt>();
      if constexpr (KS == 5) asm volatile("s_barrier" ::: "memory");
    }
    if constexpr ((i & 1) == 0) { rd(std::integral_constant<int, i + DP>{}); rd(std::integral_constant<int, i + DP + 1>{}); wl<DP>(); }
    mm(aa[t][mat][0], wf[i & 7], K{}, I0{});
    if constexpr (s == 1 && KS + XA < 14) ldx(std::integral_constant<int, KS + XA>{}, I0{});
    if constexpr (s == 6 && KS + XA < 14) ldx(std::integral_constant<int, KS + XA>{}, I1{});
    if constexpr (KS <= 3 && s == 3) ldx(std::integral_constant<int, 14 + KS>{}, I0{});
    if constexpr (KS <= 3 && s == 8) ldx(std::integral_constant<int, 14 + KS>{}, I1{});
    if constexpr (KS == 5 && s == 3) asm volatile("buffer_load_dwordx4 %0, %2, %3, %4 offen sc1\n\tbuffer_load_dwordx4 %1, %2, %3, %4 offen offset:1024 sc1" : "=&v"(ch0), "=&v"(ch1) : "v"(x.vl16), "s"(x.rs), "s"(OFF_CSGU + m * 2048));
    if constexpr (KS == 11 && s == 2) { pin(ch0); pin(ch1); *(f32x4*)(lds + 40 * KB + x.vl16) = ch0 + ch1; }
    if constexpr (KS >= 5 && KS <= 9 && (s == 3 || s == 8) && !(KS == 5 && s == 3)) pf_wgu3<2 * (KS - 5) + (s == 8) - 1>(x);
    if constexpr (KS >= 10 && (s == 3 || s == 5 || s == 8) && 3 * (KS - 10) + (s == 3 ? 0 : (s == 5 ? 1 : 2)) < 10) {
      constexpr int f = 46 + 3 * (KS - 10) + (s == 3 ? 0 : (s == 5 ? 1 : 2));
      pf_wd<f, f + 1>(x, wd);
    }
    if constexpr ((KS == 10 || KS == 12) && s == 2) {
      constexpr int u = KS == 12;
      sfor<0, 4>([&](auto e) { pin(sp[u][e]); });
      float sm = sp[u][0][0] + sp[u][1][0] + sp[u][2][0], q = sp[u][0][1] + sp[u][1][1] + sp[u][2][1];
      if (g < 2) { sm += sp[u][3][0]; q += sp[u][3][1]; }
      sm = sum32(sum16(sm)); q = sum32(sum16(q));
      float rs; row_stats(sm, q, mean[u], rs); r2[u] = rs * rs;
      pin(mean[u]); pin(r2[u]);
    }
    mm(aa[t][mat][1], wf[i & 7], K{}, I1{}); SB();
  });
  fstamp(x);
  u32x4 cs[7][2];
  const unsigned csa = 40 * KB + g * 16;
  sfor<0, 10>([&](auto i) { dsr<64 * i>(cs[i >> 1][i & 1], csa); });
  wv<10>();
  asm volatile("s_barrier" ::: "memory");
  stamp(x);
  const unsigned H = OFF_H + L * SZ_H + tt * 112 * KB;
  const int gp = (m & 1) ? 7 * m + 1 : 7 * m;
  auto pair = [&](const f32x4 (&t0)[2][2], const f32x4 (&t1)[2][2], auto lt) {
    u32x2 h0[2], h1[2];
    gu_tile(t0, cs[lt], mean, r2, h0); gu_tile(t1, cs[lt + 1], mean, r2, h1);
    st4<0>(u32x4{h0[0][0], h0[0][1], h1[0][0], h1[0][1]}, x.vg16, x.rs, H + (gp + lt - 1) * 1024);
    st4<256>(u32x4{h0[1][0], h0[1][1], h1[1][0], h1[1][1]}, x.vg16, x.rs, H + (gp + lt - 1) * 1024);
  };
  {
    wl<0>();
    sfor<0, 20>([&](auto i) { pin(aa[i >> 2][(i >> 1) & 1][i & 1]); });
    sfor<0, 10>([&](auto i) { pin(cs[i >> 1][i & 1]); });
    u32x2 hl[2];
    gu_tile(aa[0], cs[0], mean, r2, hl);
    const unsigned sl = OFF_HL + ((L * 64 + tt) * 16 + m) * KB;
    st2<0>(hl[0], x.vg16 >> 1, x.rs, sl); st2<128>(hl[1], x.vg16 >> 1, x.rs, sl);
    pair(aa[1], aa[2], std::integral_constant<int, 1>{});
    pair(aa[3], aa[4], std::integral_constant<int, 3>{});
  }
  SB();
  sfor<10, 14>([&](auto i) { dsr<64 * i>(cs[i >> 1][i & 1], csa); });
  fstamp(x);
  f32x4 ab[2][2][2];
  unsigned fa;
  sfor<140, 196>([&](auto i) {
    constexpr int j = i - 140, KS = j / 4, t = (j % 4) >> 1, mat = i & 1;
    using K = std::integral_constant<int, KS>;
    if constexpr (j == 18) {
      fstamp(x);
      if (L == 0) wv<6>(); else wv<0>();
      asm volatile("s_barrier" ::: "memory");
      count(x, FL_H(L, x.c, 0), 1);
      SB();
      fstamp(x);
    }
    if constexpr ((i & 1) == 0) { if constexpr (i + DP < 196) { rd(std::integral_constant<int, i + DP>{}); rd(std::integral_constant<int, i + DP + 1>{}); wl<DP>(); } else wl<194 - i>(); }
    mm(ab[t][mat][0], wf[i & 7], K{}, I0{});
    if constexpr (j % 3 == 1 && j < 42) { if (L == 0) pf_wq<j / 3>(x); SB(); }
    if constexpr (j == 44) ldflag(fa, x.rs, OFF_FLAG + FL_H(L, x.c, 0));
    mm(ab[t][mat][1], wf[i & 7], K{}, I1{}); SB();
  });
  wv<0>();
  recheck(x, fa, FL_H(L, x.c, 0), 16);
  { const DnSrc d = dn_src(x, L); sfor<0, 3>([&](auto o) { dn_ld<o>(x, d, b); }); }
  stamp(x);
  sfor<0, 8>([&](auto i) { pin(ab[i >> 2][(i >> 1) & 1][i & 1]); });
  sfor<10, 14>([&](auto i) { pin(cs[i >> 1][i & 1]); });
  pair(ab[0], ab[1], std::integral_constant<int, 5>{});
}

DI void phase_down(Ctx& x, const int L, char* out, u32x4 (&b)[DWN][2]) {
  const int w = x.w;
  if (x.m >= 14) {
    wv<0>();
    sfor<0, 3>([&](auto o) { pin(b[o][0]); pin(b[o][1]); });
    signal(x, FL_H(L, x.c, 1), 1);
    stamp(x); fstamp(x); fstamp(x); stamp(x);
    return;
  }
  const int th = x.m / 7, ct = x.m % 7, tt = 4 * x.c + 2 * th + (w >> 1), nt = 2 * ct + (w & 1);
  stamp(x);
  constexpr int WN = DWN;
  u32x4 xr[2];
  const DnSrc d = dn_src(x, L);
  auto ld = [&](auto o) { dn_ld<o>(x, d, b); };
  sfor<3, WN>(ld);
  f32x16 acc[4];
  unsigned fb;
  sfor<0, 25>([&](auto i) {
    constexpr int last = i + WN - 1 < 27 ? i + WN - 1 : 27, ex = i <= 2 ? 2 : ((i >= 8 && i <= 13) ? 1 : ((i >= 21) ? 2 : 0));
    constexpr int later = [&] { int n = 0; for (int o = i + 1; o <= last; ++o) n += dn_half(o) ? 4 : 2; return n; }();
    wv<later + (dn_half(i) ? 2 : 1) + ex>();
    if constexpr (i == 3) signal(x, FL_H(L, x.c, 1), 1);
    m32a<A_WD + 8 * i, i == 0>(acc[0], b[i % WN][0]); m32a<A_WD + 8 * i + 4, i == 0>(acc[1], b[i % WN][0]);
    wv<later + ex>();
    m32a<A_WD + 8 * i, i == 0>(acc[2], b[i % WN][1]); m32a<A_WD + 8 * i + 4, i == 0>(acc[3], b[i % WN][1]);
    if constexpr (i == 7) ldflag(fb, x.rs, OFF_FLAG + FL_H(L, x.c, 1));
    if constexpr (i + WN == 20) { fstamp(x); wv<12>(); recheck(x, fb, FL_H(L, x.c, 1), 16); fstamp(x); }
    if constexpr (i + WN < 28) ld(std::integral_constant<int, i + WN>{});
    if constexpr (i + WN == 27) {
      const unsigned sr = OFF_X + (2 * L + 1) * SZ_X + (tt * 28 + 2 * nt) * KB;
      ld4<0>(xr[0], x.vl16, x.rs, sr); ld4<1024>(xr[1], x.vl16, x.rs, sr);
    }
  });
  wv<2>();
  sfor<1, 5>([&](auto t) {
    constexpr int k = t & 3;
    m32a<A_WD + 8 * 25 + 4 * (k & 1), false>(acc[k], b[25 % WN][k >> 1]);
    m32a<A_WD + 8 * 26 + 4 * (k & 1), false>(acc[k], b[26 % WN][k >> 1]);
    if constexpr (t > 1) { nop<(t == 2 ? 9 : 7)>(); red_put1<t - 1, 0>(x, acc[t - 1]); red_put1<t - 1, 1>(x, acc[t - 1]); }
    m32a<A_WD + 8 * 27 + 4 * (k & 1), false>(acc[k], b[27 % WN][k >> 1]);
    if constexpr (t > 1) { red_put1<t - 1, 2>(x, acc[t - 1]); red_put1<t - 1, 3>(x, acc[t - 1]); }
  });
  if (L == 0 && (x.m & 7) < 2) {
    ldl<4>(x.vl16, x.rs, OFF_WQ + ((16 + (x.m & 1)) * 28 + 7 * w) * KB, 7 * w * KB);
    ldl<3>(x.vl16, x.rs, OFF_WQ + ((16 + (x.m & 1)) * 28 + 7 * w + 4) * KB, (7 * w + 4) * KB);
  }
  nop<9>();
  pin(acc[0]);
  stamp(x);
  reduce_get(x, acc[0]);
  wv<0>();
  pin(xr[0]); pin(xr[1]);
  if (L > 0) {
    res_epilogue<true>(x, acc[0], xr, 0, 0, tt, nt, out);
  } else {
    res_epilogue<false>(x, acc[0], xr, OFF_X + 2 * SZ_X, OFF_ST + 2 * SZ_ST, tt, nt);
    wv<0>();
    signal(x, FL_X(x.c, th), 1);
  }
}

#define STAMP_SYNC
__global__ __launch_bounds__(256, 1) void k_main(Args) {
  typedef unsigned long long u64x8 __attribute__((ext_vector_type(8)));
  typedef unsigned long long u64x2 __attribute__((ext_vector_type(2)));
  u64x8 pin8; u64x2 pws;
  asm volatile("s_load_dwordx16 s[8:23], %4, 0x0\n\ts_load_dwordx4 s[24:27], %4, 0x40\n\t"
               "s_getpc_b64 s[4:5]\n.Lk_begin:\n\t"
               "s_mov_b32 s6, .Lk_end-.Lk_begin\n\ts_mov_b32 s7, 0x20000\n\ts_and_b32 s5, s5, 0xffff\n\t"
               "v_lshlrev_b32 v1, 4, %2\n\t"
               "s_bfe_u32 s28, %3, 0x20003\n\ts_lshl_b32 s28, s28, 12\n\t"
               "buffer_load_dwordx4 a[112:115], v1, s[4:7], s28 offen\n\t"
               "s_bfe_u32 s28, %3, 0x30004\n\ts_lshl_b32 s28, s28, 13\n\t"
               "buffer_load_dwordx4 a[112:115], v1, s[4:7], s28 offen\n\t"
               "s_add_u32 s28, s28, 4096\n\t"
               "buffer_load_dwordx4 a[112:115], v1, s[4:7], s28 offen\n\t"
               "s_waitcnt lgkmcnt(0)"
               : "={s[8:23]}"(pin8), "={s[24:27]}"(pws) : "v"(threadIdx.x), "{s2}"(blockIdx.x), "{s[0:1]}"(__builtin_amdgcn_kernarg_segment_ptr())
               : "s4", "s5", "s6", "s7", "s28", "v1", "scc", ACLOB);
  const Args a = {(const char*)pin8[0], (const char*)pin8[1], (const char*)pin8[2], (const char*)pin8[3], (const char*)pin8[4], (const char*)pin8[5],
                  (const char*)pin8[6], (const char*)pin8[7], (char*)pws[0], (char*)pws[1]};
  Ctx x;
  x.rs = mkrs(a.ws, WS_USED);
  x.w = __builtin_amdgcn_readfirstlane(threadIdx.x >> 6);
  asm volatile("s_nop 4" : "+s"(x.w));
  { const int b = blockIdx.x; x.c = 2 * (b & 7) + (b >> 7); x.m = (b >> 3) & 15; }
  f32x16 kf[3];
  { Ctx y = x; fresh(y); stamp(y); phase_pre(y, a, kf); STAMP_SYNC }
  for (int L = 0;; ++L) {
    f32x16 g2[3];
    int Lo = L; asm volatile("" : "+s"(Lo));
    { Ctx y = x; fresh(y); phase_att(y, Lo, kf, g2); stamp(y); STAMP_SYNC }
    { Ctx y = x; fresh(y); phase_oproj(y, Lo, a, g2); stamp(y); STAMP_SYNC }
    { Ctx y = x; u32x4 b[DWN][2]; fresh(y); phase_gu(y, Lo, b); stamp(y); fresh(y); phase_down(y, Lo, a.out, b); stamp(y); STAMP_SYNC }
    if (L == 1) break;
    { Ctx y = x; fresh(y); phase_front(y, kf); STAMP_SYNC }
  }
  fresh(x);
  if (blockIdx.x == 0 && wave0(x)) {
    get_tag(x);
    if (x.l == 0) st2<0>(u32x2{x.tlo, x.thi}, 0, x.rs, OFF_TAG);
  }
  stamp_flush(x);
  asm volatile(".Lk_end:");
}

extern "C" void kernel_launch(void* const* d_in, const int* in_sizes, int n_in, void* d_out, int out_size, void* d_ws, size_t ws_size,
                              hipStream_t stream) {
  Args a;
  a.x = (const char*)d_in[0]; a.wq = (const char*)d_in[1]; a.wk = (const char*)d_in[2]; a.wv = (const char*)d_in[3];
  a.wo = (const char*)d_in[4]; a.wg = (const char*)d_in[6]; a.wu = (const char*)d_in[7]; a.wd = (const char*)d_in[8];
  a.ws = (char*)d_ws; a.out = (char*)d_out;
  (void)hipFuncSetAttribute((const void*)k_main, hipFuncAttributeMaxDynamicSharedMemorySize, 160 * 1024);
  k_main<<<256, 256, 160 * 1024, stream>>>(a);
}
```

```cpp
#define TA 0
#define TB 1
#include <hip/hip_runtime.h>
#include <stdint.h>
#include <type_traits>

typedef float    f32x16 __attribute__((ext_vector_type(16)));
typedef float    f32x4  __attribute__((ext_vector_type(4)));
typedef float    f32x2  __attribute__((ext_vector_type(2)));
typedef unsigned u32x4  __attribute__((ext_vector_type(4)));
typedef unsigned u32x2  __attribute__((ext_vector_type(2)));
typedef __bf16   bf16x2 __attribute__((ext_vector_type(2)));
typedef __amdgpu_buffer_rsrc_t rsrc_t;
#define DI __device__ __forceinline__
#define GAS __attribute__((address_space(1)))
#define SB() __builtin_amdgcn_sched_barrier(0)

constexpr int S = 2048, D = 448, F = 1792;
constexpr unsigned KB = 1024;
constexpr unsigned OFF_FLAG = 0;
constexpr unsigned OFF_CSQ  = 64 * KB;
constexpr unsigned OFF_CSGU = OFF_CSQ + 4 * KB;
constexpr unsigned OFF_SLOT = OFF_CSGU + 32 * KB;
constexpr unsigned OFF_TAG  = OFF_SLOT + 32 * KB;
constexpr unsigned OFF_WQ   = 160 * KB;
constexpr unsigned OFF_WO   = OFF_WQ + 18 * 28 * KB;
constexpr unsigned OFF_WGU  = OFF_WO + 14 * 32 * KB;
constexpr unsigned OFF_WD   = OFF_WGU + 16 * 196 * KB;
constexpr unsigned SZ_X = 64 * 28 * KB, SZ_ST = 14 * 2048 * 8, SZ_O = 64 * 32 * KB, SZ_H = 64 * 112 * KB, SZ_KV = 64 * 4 * KB;
constexpr unsigned OFF_X    = OFF_WD + 14 * 112 * KB;
constexpr unsigned OFF_ST   = OFF_X + 4 * SZ_X;
constexpr unsigned OFF_O    = OFF_ST + 4 * SZ_ST;
constexpr unsigned OFF_H    = OFF_O + 2 * SZ_O;
constexpr unsigned OFF_HL   = OFF_H + 2 * SZ_H;
constexpr unsigned OFF_KV   = OFF_HL + 2 * 1024 * KB;
constexpr unsigned OFF_TS   = OFF_KV + 2 * SZ_KV;
constexpr unsigned WS_USED  = OFF_TS + 520 * KB;
DI constexpr unsigned FL_X(int c, int th)         { return (c * 16 + th) * 256u; }
DI constexpr unsigned FL_O(int L, int c, int th)  { return (c * 16 + 2 + L * 2 + th) * 256u; }
DI constexpr unsigned FL_X1(int L, int c)         { return (c * 16 + 6 + L) * 256u; }
DI constexpr unsigned FL_H(int L, int c, int p)   { return (c * 16 + 8 + L * 2 + p) * 256u; }
DI constexpr unsigned FL_W(int c)                 { return (c * 16 + 12) * 256u; }
constexpr unsigned FL_W2 = 13 * 256u;

#define XAHEAD 3
#define OPWN 5
constexpr float QSCALE = 0.18898223650461362f * 1.4426950408889634f;
constexpr float M_LIMIT = 48.0f;

template <int I, int N, class Fn> DI void sfor(Fn&& f) { if constexpr (I < N) { f(std::integral_constant<int, I>{}); sfor<I + 1, N>(f); } }

DI unsigned cvtpk(float lo, float hi) { f32x2 v = {lo, hi}; bf16x2 b = __builtin_convertvector(v, bf16x2); return __builtin_bit_cast(unsigned, b); }
DI float bflo(unsigned u) { return __uint_as_float(u << 16); }
DI float bfhi(unsigned u) { return __uint_as_float(u & 0xffff0000u); }
DI float dot2(unsigned a, unsigned b, float c) { return __builtin_amdgcn_fdot2_f32_bf16(__builtin_bit_cast(bf16x2, a), __builtin_bit_cast(bf16x2, b), c, false); }
DI float sum32(float v) { auto r = __builtin_amdgcn_permlane32_swap(__float_as_uint(v), __float_as_uint(v), false, false); return __uint_as_float(r[0]) + __uint_as_float(r[1]); }
DI float sum16(float v) { auto r = __builtin_amdgcn_permlane16_swap(__float_as_uint(v), __float_as_uint(v), false, false); return __uint_as_float(r[0]) + __uint_as_float(r[1]); }
DI float max32(float v) { auto r = __builtin_amdgcn_permlane32_swap(__float_as_uint(v), __float_as_uint(v), false, false); return fmaxf(__uint_as_float(r[0]), __uint_as_float(r[1])); }

DI unsigned umax16(unsigned v) {
  v = max(v, (unsigned)__builtin_amdgcn_update_dpp(0, (int)v, 0x128, 0xf, 0xf, false));
  v = max(v, (unsigned)__builtin_amdgcn_update_dpp(0, (int)v, 0x124, 0xf, 0xf, false));
  v = max(v, (unsigned)__builtin_amdgcn_update_dpp(0, (int)v, 0x122, 0xf, 0xf, false));
  v = max(v, (unsigned)__builtin_amdgcn_update_dpp(0, (int)v, 0x121, 0xf, 0xf, false));
  return v;
}
DI unsigned umax64(unsigned v) {
  v = umax16(v);
  auto a = __builtin_amdgcn_permlane16_swap(v, v, false, false); v = max(a[0], a[1]);
  auto b = __builtin_amdgcn_permlane32_swap(v, v, false, false); return max(b[0], b[1]);
}

typedef short s16x4 __attribute__((ext_vector_type(4)));
DI u32x2 trread(const char* p) { return __builtin_bit_cast(u32x2, __builtin_amdgcn_ds_read_tr16_b64_v4i16((__attribute__((address_space(3))) s16x4*)p)); }
DI u32x4 trfrag(const char* lo, const char* hi) { const u32x2 a = trread(lo), b = trread(hi); return u32x4{a[0], a[1], b[0], b[1]}; }
DI void row_stats(float s, float q, float& mean, float& rstd) {
  mean = s * (1.0f / D);
  const float var = fmaxf(q * (1.0f / D) - mean * mean, 0.f);
  rstd = __builtin_amdgcn_rsqf(var + 1e-5f);
}
DI float fragsum(const u32x4& f, float s) {
#pragma unroll
  for (int d = 0; d < 4; ++d) s = dot2(f[d], 0x3f803f80u, s);
  return s;
}
struct Args { const char *x, *wq, *wk, *wv, *wo, *wg, *wu, *wd; char* ws; char* out; };

#define ACLOB "a0","a1","a2","a3","a4","a5","a6","a7","a8","a9","a10","a11","a12","a13","a14","a15","a16","a17","a18","a19","a20","a21","a22","a23","a24","a25","a26","a27","a28","a29","a30","a31","a32","a33","a34","a35","a36","a37","a38","a39","a40","a41","a42","a43","a44","a45","a46","a47","a48","a49","a50","a51","a52","a53","a54","a55","a56","a57","a58","a59","a60","a61","a62","a63","a64","a65","a66","a67","a68","a69","a70","a71","a72","a73","a74","a75","a76","a77","a78","a79","a80","a81","a82","a83","a84","a85","a86","a87","a88","a89","a90","a91","a92","a93","a94","a95","a96","a97","a98","a99","a100","a101","a102","a103","a104","a105","a106","a107","a108","a109","a110","a111","a112","a113","a114","a115","a116","a117","a118","a119","a120","a121","a122","a123","a124","a125","a126","a127","a128","a129","a130","a131","a132","a133","a134","a135","a136","a137","a138","a139","a140","a141","a142","a143","a144","a145","a146","a147","a148","a149","a150","a151","a152","a153","a154","a155","a156","a157","a158","a159","a160","a161","a162","a163","a164","a165","a166","a167","a168","a169","a170","a171","a172","a173","a174","a175","a176","a177","a178","a179","a180","a181","a182","a183","a184","a185","a186","a187","a188","a189","a190","a191","a192","a193","a194","a195","a196","a197","a198","a199","a200","a201","a202","a203","a204","a205","a206","a207","a208","a209","a210","a211","a212","a213","a214","a215","a216","a217","a218","a219","a220","a221","a222","a223","a224","a225","a226","a227","a228","a229","a230","a231","a232","a233","a234","a235","a236","a237","a238","a239","a240","a241","a242","a243","a244","a245","a246","a247","a248","a249","a250","a251","a252","a253","a254","a255"

template <int IMM> DI void ld4(u32x4& d, unsigned vo, rsrc_t rs, unsigned so) {
  asm volatile("buffer_load_dwordx4 %0, %1, %2, %3 offen offset:%c4 sc1" : "=v"(d) : "v"(vo), "s"(rs), "s"(so), "i"(IMM));
}
template <int IMM> DI void ld2(f32x2& d, unsigned vo, rsrc_t rs, unsigned so) {
  asm volatile("buffer_load_dwordx2 %0, %1, %2, %3 offen offset:%c4 sc1" : "=v"(d) : "v"(vo), "s"(rs), "s"(so), "i"(IMM));
}
template <int IMM> DI void ld4i(u32x4& d, unsigned vo, rsrc_t rs, unsigned so) {
  asm volatile("buffer_load_dwordx4 %0, %1, %2, %3 offen offset:%c4" : "=v"(d) : "v"(vo), "s"(rs), "s"(so), "i"(IMM));
}
template <int IMM> DI void ld2i(u32x2& d, unsigned vo, rsrc_t rs, unsigned so) {
  asm volatile("buffer_load_dwordx2 %0, %1, %2, %3 offen offset:%c4" : "=v"(d) : "v"(vo), "s"(rs), "s"(so), "i"(IMM));
}
template <int IMM> DI void ld2u(u32x2& d, unsigned vo, rsrc_t rs, unsigned so) {
  asm volatile("buffer_load_dwordx2 %0, %1, %2, %3 offen offset:%c4 sc1" : "=v"(d) : "v"(vo), "s"(rs), "s"(so), "i"(IMM));
}
template <int IMM> DI void ld4f(f32x4& d, unsigned vo, rsrc_t rs, unsigned so) {
  asm volatile("buffer_load_dwordx4 %0, %1, %2, %3 offen offset:%c4 sc1" : "=v"(d) : "v"(vo), "s"(rs), "s"(so), "i"(IMM));
}
DI void ld1(float& d, unsigned vo, rsrc_t rs, unsigned so) {
  asm volatile("buffer_load_dword %0, %1, %2, %3 offen sc1" : "=v"(d) : "v"(vo), "s"(rs), "s"(so));
}
template <int AR, int IMM> DI void lda(unsigned vo, rsrc_t rs, unsigned so) {
  asm volatile("buffer_load_dwordx4 a[%c3:%c4], %0, %1, %2 offen offset:%c5 sc1" :: "v"(vo), "s"(rs), "s"(so), "i"(AR), "i"(AR + 3), "i"(IMM) : ACLOB);
}
template <int N> DI void ldl(unsigned vo, rsrc_t rs, unsigned so, unsigned lds) {
  asm volatile("s_mov_b32 m0, %3\n\ts_nop 0\n\tbuffer_load_dwordx4 %0, %1, %2 offen sc1 lds" :: "v"(vo), "s"(rs), "s"(so), "s"(lds) : "memory");
  if constexpr (N > 1) asm volatile("buffer_load_dwordx4 %0, %1, %2 offen offset:1024 sc1 lds" :: "v"(vo), "s"(rs), "s"(so) : "memory");
  if constexpr (N > 2) asm volatile("buffer_load_dwordx4 %0, %1, %2 offen offset:2048 sc1 lds" :: "v"(vo), "s"(rs), "s"(so) : "memory");
  if constexpr (N > 3) asm volatile("buffer_load_dwordx4 %0, %1, %2 offen offset:3072 sc1 lds" :: "v"(vo), "s"(rs), "s"(so) : "memory");
}
template <int IMM> DI void st4(const u32x4& d, unsigned vo, rsrc_t rs, unsigned so) {
  asm volatile("buffer_store_dwordx4 %0, %1, %2, %3 offen offset:%c4 sc1\n\ts_nop 1" :: "v"(d), "v"(vo), "s"(rs), "s"(so), "i"(IMM) : "memory");
}
template <int IMM> DI void st4p(const u32x4& d, unsigned vo, rsrc_t rs, unsigned so) {
  asm volatile("buffer_store_dwordx4 %0, %1, %2, %3 offen offset:%c4\n\ts_nop 1" :: "v"(d), "v"(vo), "s"(rs), "s"(so), "i"(IMM) : "memory");
}
template <int IMM> DI void st2(const u32x2& d, unsigned vo, rsrc_t rs, unsigned so) {
  asm volatile("buffer_store_dwordx2 %0, %1, %2, %3 offen offset:%c4 sc1" :: "v"(d), "v"(vo), "s"(rs), "s"(so), "i"(IMM) : "memory");
}
DI void st1(float d, unsigned vo, rsrc_t rs, unsigned so) {
  asm volatile("buffer_store_dword %0, %1, %2, %3 offen sc1" :: "v"(d), "v"(vo), "s"(rs), "s"(so) : "memory");
}
template <int N> DI void wv() { asm volatile("s_waitcnt vmcnt(%c0)" :: "i"(N) : "memory"); SB(); }
template <int N> DI void wl() { asm volatile("s_waitcnt lgkmcnt(%c0)" :: "i"(N) : "memory"); SB(); }
template <class T> DI void pin(T& x) { asm volatile("" : "+v"(x)); }
DI void bar() { asm volatile("s_waitcnt lgkmcnt(0)\n\ts_barrier" ::: "memory"); }
template <int N> DI void nop() { asm volatile("s_nop %c0" :: "i"(N)); SB(); }

template <int OFF> DI void trd(u32x2& r, unsigned a) { asm volatile("ds_read_b64_tr_b16 %0, %1 offset:%c2" : "=v"(r) : "v"(a), "i"(OFF)); }
template <int OFF> DI void dsr(u32x4& r, unsigned a) { asm volatile("ds_read_b128 %0, %1 offset:%c2" : "=v"(r) : "v"(a), "i"(OFF)); }

DI void m32(f32x16& c, const u32x4& a, const u32x4& b)  { asm volatile("v_mfma_f32_32x32x16_bf16 %0, %1, %2, %0" : "+v"(c) : "v"(a), "v"(b)); }
DI void m32z(f32x16& c, const u32x4& a, const u32x4& b) { asm volatile("v_mfma_f32_32x32x16_bf16 %0, %1, %2, 0" : "=&v"(c) : "v"(a), "v"(b)); }
template <int AR, bool Z> DI void m32a(f32x16& c, const u32x4& b) {
  if constexpr (Z) asm volatile("v_mfma_f32_32x32x16_bf16 %0, a[%c2:%c3], %1, 0" : "=&v"(c) : "v"(b), "i"(AR), "i"(AR + 3));
  else             asm volatile("v_mfma_f32_32x32x16_bf16 %0, a[%c2:%c3], %1, %0" : "+v"(c) : "v"(b), "i"(AR), "i"(AR + 3));
}
template <int AR, bool Z> DI void m32b(f32x16& c, const u32x4& a) {
  if constexpr (Z) asm volatile("v_mfma_f32_32x32x16_bf16 %0, %1, a[%c2:%c3], 0" : "=&v"(c) : "v"(a), "i"(AR), "i"(AR + 3));
  else             asm volatile("v_mfma_f32_32x32x16_bf16 %0, %1, a[%c2:%c3], %0" : "+v"(c) : "v"(a), "i"(AR), "i"(AR + 3));
}
template <bool Z> DI void m16(f32x4& c, const u32x4& a, const u32x4& b) {
  if constexpr (Z) asm volatile("v_mfma_f32_16x16x32_bf16 %0, %1, %2, 0" : "=&v"(c) : "v"(a), "v"(b));
  else             asm volatile("v_mfma_f32_16x16x32_bf16 %0, %1, %2, %0" : "+v"(c) : "v"(a), "v"(b));
}

template <bool Z, int AR> DI void m16a(f32x4& c, const u32x4& a) {
  if constexpr (Z) asm volatile("v_mfma_f32_16x16x32_bf16 %0, %1, a[%c2:%c3], 0" : "=&v"(c) : "v"(a), "i"(AR), "i"(AR + 3));
  else             asm volatile("v_mfma_f32_16x16x32_bf16 %0, %1, a[%c2:%c3], %0" : "+v"(c) : "v"(a), "i"(AR), "i"(AR + 3));
}
DI void ldflag(unsigned& v, rsrc_t rs, unsigned so) { asm volatile("buffer_load_dword %0, off, %1, %2 sc1" : "=v"(v) : "s"(rs), "s"(so)); }

struct Ctx {
  rsrc_t rs;
  unsigned tlo, thi;
  unsigned vl16;
  unsigned vg16;
  int w, l, c, m;
};
DI void fresh(Ctx& x) {
  asm volatile("" : "+s"(x.w), "+s"(x.c), "+s"(x.m));
  asm volatile("v_mbcnt_lo_u32_b32 %0, -1, 0\n\tv_mbcnt_hi_u32_b32 %0, -1, %0" : "=v"(x.l));
  x.vl16 = x.l * 16;
  const int g = x.l >> 4;
  x.vg16 = ((g >> 1) * 64 + (g & 1) * 32 + (x.l & 15)) * 16;
}
extern __shared__ __attribute__((aligned(16))) char lds[];
constexpr unsigned SCR = 112 * KB;

DI void pstamp(Ctx&) {}
DI void fstamp(Ctx&) {}
DI void stamp(Ctx&) {}
DI void stamp_flush(Ctx&) {}

DI void get_tag(Ctx& x) {
  unsigned lo, hi;
  asm volatile("v_accvgpr_read_b32 v255, a252\n\ts_nop 1\n\tv_readfirstlane_b32 %0, v255\n\tv_accvgpr_read_b32 v255, a253\n\ts_nop 1\n\tv_readfirstlane_b32 %1, v255\n\ts_nop 4"
               : "=s"(lo), "=s"(hi) :: "v255");
  const unsigned long long e = ((unsigned long long)hi << 32) | lo;
  unsigned long long n = e * 0x9E3779B97F4A7C15ull + 0x632BE59BD9B4E019ull;
  if (((n ^ e) >> 16) == 0) n += 0x10000;
  x.tlo = (unsigned)n; x.thi = (unsigned)(n >> 32);
}
DI bool wave0(const Ctx& x) { int w = x.w; asm volatile("" : "+s"(w)); return w == 0; }
DI void count(Ctx& x, unsigned fl, int n) {
  if (wave0(x)) {
    const unsigned long long mask = (1ull << n) - 1;
    unsigned long long keep;
    asm volatile("s_mov_b64 %0, exec\n\ts_mov_b64 exec, %5\n\tbuffer_atomic_add %1, %2, %3, %4 offen\n\ts_mov_b64 exec, %0"
                 : "=&s"(keep) : "v"(1u), "v"(x.l * 256), "s"(x.rs), "s"(OFF_FLAG + fl), "s"(mask) : "memory");
  }
}
DI void signal(Ctx& x, unsigned fl, int n) { bar(); count(x, fl, n); }
DI void await(Ctx& x, unsigned fl, unsigned target) {
  if (wave0(x)) {
#pragma nounroll
    for (unsigned spin = 0; spin < (1u << 18); ++spin) {
      unsigned v;
      asm volatile("buffer_load_dword %0, off, %1, %2 sc1\n\ts_waitcnt vmcnt(0)" : "=v"(v) : "s"(x.rs), "s"(OFF_FLAG + fl) : "memory");
      if ((unsigned)__builtin_amdgcn_readfirstlane(v) >= target) break;
      __builtin_amdgcn_s_sleep(1);
    }
  }
  bar();
}

DI void recheck(Ctx& x, unsigned v, unsigned fl, unsigned target) {
  pin(v);
#pragma nounroll
  for (unsigned spin = 0; (unsigned)__builtin_amdgcn_readfirstlane(v) < target && spin < (1u << 18); ++spin) {
    __builtin_amdgcn_s_sleep(2);
    asm volatile("buffer_load_dword %0, off, %1, %2 sc1\n\ts_waitcnt vmcnt(0)" : "=v"(v) : "s"(x.rs), "s"(OFF_FLAG + fl) : "memory");
  }
  SB();
}

DI void reduce_put(Ctx& x, const f32x16 (&acc)[4]) {
  sfor<1, 4>([&](auto k) {
    f32x4* p = (f32x4*)(lds + SCR + ((x.w ^ k) * 3 + k - 1) * 4096 + x.vl16);
#pragma unroll
    for (int q = 0; q < 4; ++q) p[q * 64] = f32x4{acc[k][4 * q], acc[k][4 * q + 1], acc[k][4 * q + 2], acc[k][4 * q + 3]};
  });
}
template <int K, int Q> DI void red_put1(Ctx& x, const f32x16& t) {
  asm volatile("ds_write_b128 %0, %1 offset:%c2" :: "v"(SCR + ((x.w ^ K) * 3 + K - 1) * 4096 + x.vl16),
               "v"(__builtin_shufflevector(t, t, 4 * Q, 4 * Q + 1, 4 * Q + 2, 4 * Q + 3)), "i"(Q * 1024) : "memory");
}
DI void reduce_get(Ctx& x, f32x16& own) {
  bar();
  const f32x4* p = (const f32x4*)(lds + SCR + x.w * 3 * 4096 + x.vl16);
#pragma unroll
  for (int k = 0; k < 3; ++k)
#pragma unroll
    for (int q = 0; q < 4; ++q) {
      f32x4 v = p[k * 256 + q * 64];
#pragma unroll
      for (int i = 0; i < 4; ++i) own[4 * q + i] += v[i];
    }
}

constexpr int A_WO = 184, A_WD = 0, A_GX = 224;
constexpr int dn_ks(int o) { return o < 20 ? 2 * ((o >> 1) % 5 + ((o >> 1) % 5 >= 2) + 7 * (o / 10)) + (o & 1) : 2 * (((o - 20) >> 1) * 4 - ((o - 20) >> 2) + 2) + (o & 1); }
static_assert(dn_ks(0) == 0 && dn_ks(4) == 6 && dn_ks(9) == 11 && dn_ks(10) == 14 && dn_ks(19) == 25 && dn_ks(20) == 4 && dn_ks(22) == 12 && dn_ks(24) == 18 && dn_ks(27) == 27, "");
struct Two { unsigned s0, s1; };
constexpr int SL_WQ = 56;
template <int J> DI void pf_wq(Ctx& x) {
  ldl<1>(x.vl16, x.rs, OFF_WQ + ((x.m & 7) * 56 + 14 * x.w + J) * KB, (SL_WQ + 14 * x.w + J) * KB);
}
DI Two base_wo(const Ctx& x) { const int ct = x.m % 7;
  return Two{OFF_WO + ((2 * ct + (x.w & 1)) * 32 + 8 * x.w) * KB, OFF_WO + ((2 * ct + ((x.w & 1) ^ 1)) * 32 + 8 * x.w) * KB}; }
DI void pf_wo(Ctx& x) {
  const Two b = base_wo(x);
  sfor<0, 8>([&](auto i) {
    lda<A_WO + 8 * i, (i & 3) * 1024>(x.vl16, x.rs, b.s0 + (i >> 2) * 4096);
    lda<A_WO + 8 * i + 4, (i & 3) * 1024>(x.vl16, x.rs, b.s1 + (i >> 2) * 4096);
  });
}
DI Two base_wd(const Ctx& x) { const int ct = x.m % 7;
  return Two{OFF_WD + ((2 * ct + (x.w & 1)) * 112 + 28 * x.w) * KB, OFF_WD + ((2 * ct + ((x.w & 1) ^ 1)) * 112 + 28 * x.w) * KB}; }
template <int I0, int I1> DI void pf_wd(Ctx& x, const Two& b) {
  sfor<I0, I1>([&](auto f) {
    constexpr int i = f >> 1;
    lda<A_WD + 4 * f, (i & 3) * 1024>(x.vl16, x.rs, ((f & 1) ? b.s1 : b.s0) + (i >> 2) * 4096);
  });
}
DI void pf_wgu1(Ctx& x) {
  for (int t = 0; t < 14; ++t) ldl<2>(x.vl16, x.rs, OFF_WGU + (x.m * 196 + 8 * t + 2 * x.w) * KB, (8 * t + 2 * x.w) * KB);
}
template <int J> DI void pf_wgu3(Ctx& x) {
  ldl<1>(x.vl16, x.rs, OFF_WGU + (x.m * 196 + 160 + 9 * x.w + J) * KB, (9 * x.w + J) * KB);
}

constexpr unsigned S_QP = SCR, S_KP = SCR + 16384, S_QF = SCR + 24576, S_ST = SCR + 32768, S_CS = SCR + 33792, S_KM = SCR + 36864, S_TR = SCR + 40960;
DI unsigned kv_finish(Ctx& x, const float (&raw)[16], const f32x4 (&cs)[4], float mean, float rstd, bool isv, unsigned so, int slot) {
  const int h = x.l >> 5, r = x.l & 31;
  u32x4 y[2];
#pragma unroll
  for (int d = 0; d < 8; ++d)
    y[d >> 2][d & 3] = cvtpk((raw[2 * d] - mean * cs[d >> 1][2 * (d & 1)]) * rstd, (raw[2 * d + 1] - mean * cs[d >> 1][2 * (d & 1) + 1]) * rstd);
  if (h) { y[1][2] = 0; y[1][3] = 0; }
  unsigned nb = 0;
  if (!isv) {
    float n2 = 0.f;
#pragma unroll
    for (int d = 0; d < 8; ++d) n2 = dot2(y[d >> 2][d & 3], y[d >> 2][d & 3], n2);
    n2 = sum32(n2);
    nb = umax64(n2 < 3.0e38f ? __float_as_uint(n2) : 0x7f800000u);
    nb = cvtpk(1.01f * __builtin_amdgcn_sqrtf(__uint_as_float(nb)), 0.f);
    if (h) y[1][2] = 0x3f80u;
  } else {
    if (h) y[1][2] = 0x3f80u;
    char* im = lds + S_TR + slot * 2048;
#pragma unroll
    for (int a = 0; a < 4; ++a) *(u32x2*)(im + r * 64 + 16 * a + 8 * h) = u32x2{y[a >> 1][2 * (a & 1)], y[a >> 1][2 * (a & 1) + 1]};
    asm volatile("s_waitcnt lgkmcnt(0)" ::: "memory");
    const char* tb = im + (4 * h + ((x.l >> 2) & 3)) * 64 + ((x.l >> 4) & 1) * 32 + (x.l & 3) * 8;
    y[0] = trfrag(tb, tb + 512); y[1] = trfrag(tb + 1024, tb + 1536);
  }
  st4<0>(y[0], x.vl16, x.rs, so); st4<1024>(y[1], x.vl16, x.rs, so);
  return nb;
}
DI void kv_publish(Ctx& x, int L, int p, unsigned km) {
  get_tag(x);
  st2<0>(u32x2{(x.tlo & 0xffff0000u) | km, x.thi}, x.l < 16 ? x.l * 1024 : ~0u, x.rs, OFF_SLOT + (L * 2048 + p) * 8);
}
DI void q_finish(Ctx& x, const f32x16& qa, const f32x4 (&cq)[4], float mean, float rstd) {
  const int h = x.l >> 5;
  u32x4 qf[2];
  const f32x4* p = (const f32x4*)(lds + S_QP + x.w * 4096 + x.vl16);
  const float sc = rstd * QSCALE;
#pragma unroll
  for (int q = 0; q < 4; ++q) {
    const f32x4 o = p[q * 64];
    float v[4];
#pragma unroll
    for (int i = 0; i < 4; ++i) v[i] = (qa[4 * q + i] + o[i] - mean * cq[q][i]) * sc;
    qf[q >> 1][2 * (q & 1)] = cvtpk(v[0], v[1]); qf[q >> 1][2 * (q & 1) + 1] = cvtpk(v[2], v[3]);
  }
  if (h) { qf[1][2] = 0; qf[1][3] = 0; }
  float qn2 = 0.f, qn3 = 0.f;
#pragma unroll
  for (int d = 0; d < 4; ++d) { qn2 = dot2(qf[0][d], qf[0][d], qn2); qn3 = dot2(qf[1][d], qf[1][d], qn3); }
  qn2 = sum32(qn2 + qn3);
  if (h) qf[1][2] = cvtpk(1.01f * __builtin_amdgcn_sqrtf(qn2), 0.f);
  ((u32x4*)(lds + S_QF + x.w * 2048 + x.vl16))[0] = qf[0];
  ((u32x4*)(lds + S_QF + x.w * 2048 + x.vl16))[64] = qf[1];
}
DI void put_tile(char* p, const f32x16& t) {
#pragma unroll
  for (int q = 0; q < 4; ++q) ((f32x4*)p)[q * 64] = f32x4{t[4 * q], t[4 * q + 1], t[4 * q + 2], t[4 * q + 3]};
}

constexpr unsigned IMG_Q = 0, IMG_T = 64 * KB;
DI rsrc_t mkrs(const char* p, unsigned n) { return __builtin_amdgcn_make_buffer_rsrc((char*)p, 0, n, 0x00020000); }

DI void ask_slots(Ctx& x, int L) {
  if (wave0(x)) asm volatile("buffer_load_dwordx4 a[248:251], %0, %1, %2 offen sc1" :: "v"(x.vl16), "s"(x.rs), "s"(OFF_SLOT + (L * 16 + x.c) * 1024) : ACLOB);
}
DI void kv_wait(Ctx& x, int L) {
  wv<0>();
  if (wave0(x)) {
    get_tag(x);
    unsigned s0, s1, s2, s3;
#pragma nounroll
    for (unsigned spin = 0; spin < (1u << 18); ++spin) {
      asm volatile("v_accvgpr_read_b32 %0, a248\n\tv_accvgpr_read_b32 %1, a249\n\tv_accvgpr_read_b32 %2, a250\n\tv_accvgpr_read_b32 %3, a251" : "=v"(s0), "=v"(s1), "=v"(s2), "=v"(s3));
      if (__all(s1 == x.thi && s3 == x.thi && (((s0 ^ x.tlo) | (s2 ^ x.tlo)) >> 16) == 0)) break;
      __builtin_amdgcn_s_sleep(2);
      ask_slots(x, L); wv<0>();
    }
    const unsigned km = umax64(max(s0 << 16, s2 << 16));
    if (x.l == 0) ((unsigned*)(lds + S_KM))[4] = km;
  }
  bar();
}
DI void ask_kv(Ctx& x, int L, f32x16 (&kf)[3]) {
  const unsigned kvs = OFF_KV + L * SZ_KV + 16 * x.w * 4096;
  asm volatile(".irp i,0,1,2,3\n\tbuffer_load_dwordx4 v[128+4*\\i:131+4*\\i], %3, %4, %5 offen offset:1024*\\i sc1\n\t.endr\n\t"
               ".irp i,0,1,2,3\n\tbuffer_load_dwordx4 v[144+4*\\i:147+4*\\i], %3, %4, %6 offen offset:1024*\\i sc1\n\t.endr\n\t"
               ".irp i,0,1,2,3\n\tbuffer_load_dwordx4 v[160+4*\\i:163+4*\\i], %3, %4, %7 offen offset:1024*\\i sc1\n\t.endr"
               : "={v[128:143]}"(kf[0]), "={v[144:159]}"(kf[1]), "={v[160:175]}"(kf[2]) : "{v204}"(x.vl16), "s"(x.rs), "s"(kvs), "s"(kvs + 4096), "s"(kvs + 8192));
}

DI void phase_pre(Ctx& x, const Args& a, f32x16 (&kf)[3]) {
  const int w = x.w, l = x.l, h = l >> 5, r = l & 31;
  const int th = x.m >> 3, hp = x.m & 7, tl = w & 1, kh = w >> 1, tt = 4 * x.c + 2 * th + tl, wi = 2 * x.c + th;
  const bool kvp = hp < 2;
  u32x4 xf[14];
  { const rsrc_t rx = mkrs(a.x, S * D * 2);
    if (kvp) {
      sfor<0, 16>([&](auto i) {
        const int c = r ^ ((2 * i + h) & 15);
        ldl<1>(c < 28 ? h * 896 + c * 16 : 0x80000000u, rx, (tt * 32 + 2 * i) * 896 + kh * 448, (16 * w + i) * KB);
      });
    } else {
      sfor<0, 14>([&](auto i) { ld4i<32 * i>(xf[i], r * 896 + h * 16, rx, tt * 32 * 896 + kh * 448); });
    } }
  if (x.m == 2) asm volatile("buffer_load_dword a116, %0, %1, 0 offen sc1" :: "v"(l < 16 ? OFF_FLAG + (x.c * 16 + l) * 256 : OFF_SLOT + ((l >> 3) & 1) * 16384 + x.c * 1024 + (l & 7) * 128), "s"(x.rs) : ACLOB);
  asm volatile("buffer_load_dwordx2 a[252:253], off, %0, %1 sc1" :: "s"(x.rs), "s"(OFF_TAG) : ACLOB);
  pstamp(x);
  const int task = wi * 6 + hp - 2;
  const rsrc_t rq = mkrs(a.wq, D * D * 2);
  auto ask_q = [&](auto i) {
    ldl<1>((8 * w + (l >> 3)) * 896 + (l & 7) * 16, rq, hp * 112 + i * 32 * 896, IMG_Q + (4 * i + w) * KB);
  };
  auto ask_t = [&](const char* tp, unsigned tn, unsigned ts, unsigned tb) {
    const rsrc_t rt = mkrs(tp, tn);
    const unsigned vo = (8 * w + (l >> 3)) * ts + (l & 7) * 16;
    sfor<0, 7>([&](auto i) { ldl<1>(vo, rt, tb + i * 32 * ts, IMG_T + (4 * i + w) * KB); });
  };
  const int tco = ((l >> 4) & 1) * 32 + (l & 3) * 8;
  const int sro = 4 * h + ((l >> 2) & 3);
  if (kvp) ask_t(hp ? a.wv : a.wk, D * 56, 128, 0);
  else if (task < 112) ask_t(task < 56 ? a.wg : a.wu, D * F * 2, F * 2, (task & 1) * 224 * F * 2 + ((task % 56) >> 1) * 128);
  else if (task < 168) ask_t(a.wd, D * F * 2, 896, ((task - 112) / 7) * 224 * 896 + ((task - 112) % 7) * 128);
  else ask_t(a.wo, task < 182 ? D * D * 2 : 0, 896, ((task - 168) / 7) * 224 * 896 + ((task - 168) % 7) * 128);
  wv<8>();
  if (kvp) {
    sfor<0, 14>([&](auto i) { dsr<0>(xf[i], w * 16 * KB + r * 512 + (((2 * i + h) ^ (r & 15)) << 4)); });
    wl<0>();
  }
  {
    float s = 0.f, q = 0.f, s1 = 0.f, q1 = 0.f;
    sfor<0, 14>([&](auto i) {
      if (!kvp) ask_q(i);
      pin(xf[i]);
      s = dot2(xf[i][0], 0x3f803f80u, s); q = dot2(xf[i][0], xf[i][0], q); s1 = dot2(xf[i][1], 0x3f803f80u, s1); q1 = dot2(xf[i][1], xf[i][1], q1);
      s = dot2(xf[i][2], 0x3f803f80u, s); q = dot2(xf[i][2], xf[i][2], q); s1 = dot2(xf[i][3], 0x3f803f80u, s1); q1 = dot2(xf[i][3], xf[i][3], q1);
      pin(s); pin(q); pin(s1); pin(q1);
    });
    s = sum32(s + s1); q = sum32(q + q1);
    if (h == 0) ((f32x2*)(lds + S_ST))[w * 32 + r] = f32x2{s, q};
  }
  if (kvp) wv<0>(); else wv<14>();
  bar();
  stamp(x);
  if (!kvp) {
    if (task < 112) {
      const int g = l >> 4, mat = task >= 56, gt = 4 * ((task % 56) >> 1) + w, m = gt / 7, j = gt % 7, lt = (m & 1) ? j : (j == 6 ? 0 : j + 1);
      const char* tb = lds + IMG_T + (16 * (g >> 1) + 4 * (g & 1) + ((l >> 2) & 3)) * 128 + 32 * w + (l & 3) * 8;
      const unsigned st = lt < 5 ? 10 * KB : 4 * KB, so = OFF_WGU + (m * 196 + (lt < 5 ? lt * 2 + mat : 140 + (lt - 5) * 2 + mat)) * KB + (task & 1) * 7 * st;
      u32x4 f[7];
      sfor<0, 7>([&](auto i) { f[i] = trfrag(tb + i * 4096, tb + i * 4096 + 1024); });
      float cs = 0.f;
      sfor<0, 7>([&](auto i) { st4<0>(f[i], x.vl16, x.rs, so + i * st); cs = fragsum(f[i], cs); });
      cs = sum32(sum16(cs));
      st1(cs, l < 16 ? l * 4 : ~0u, x.rs, OFF_CSGU + m * 2048 + (task & 1) * 1024 + (lt * 2 + mat) * 64);
    } else if (task < 168) {
      const int nt = 2 * ((task - 112) % 7) + (w & 1), rb = (task - 112) / 7;
      const char* tb = lds + IMG_T + sro * 128 + 64 * (w & 1) + tco;
      const unsigned so = OFF_WD + (nt * 112 + 28 * (rb >> 1)) * KB, sa = so + (rb & 1) * 10 * KB, sb = so + (20 + (rb & 1) * 4) * KB;
      u32x4 f[7];
      if (w < 2) {
        sfor<0, 7>([&](auto i) { const char* p = tb + (32 * (i >> 1) + 8 * (i & 1)) * 128; f[i] = trfrag(p, p + 2048); });
        sfor<0, 7>([&](auto i) { st4<0>(f[i], x.vl16, x.rs, i < 4 ? sa + i * KB : (i < 6 ? sb + (i - 4) * KB : sa + 4 * KB)); });
      } else {
        sfor<0, 7>([&](auto i) { const char* p = tb + (32 * ((i + 7) >> 1) + 8 * ((i + 7) & 1)) * 128; f[i] = trfrag(p, p + 2048); });
        sfor<0, 7>([&](auto i) { st4<0>(f[i], x.vl16, x.rs, i < 5 ? sa + (5 + i) * KB : sb + (i - 3) * KB); });
      }
      st1(0.f, ~0u, x.rs, 0);
    } else {
      const int nt = 2 * ((task - 168) % 7) + (w & 1);
      const char* tb = lds + IMG_T + (112 * (w >> 1) + sro) * 128 + 64 * (w & 1) + tco;
      const unsigned vo = task < 182 ? x.vl16 : ~0u - 4096;
      u32x4 f[8];
      sfor<0, 8>([&](auto i) { const char* p = tb + (28 * (i >> 1) + 16 * (i & 1)) * 128; f[i] = trfrag(p, p + 1024); if ((i & 1) && h) { f[i][2] = 0; f[i][3] = 0; } });
      sfor<0, 8>([&](auto i) { st4<(i & 3) * 1024>(f[i], vo, x.rs, OFF_WO + (nt * 32 + 16 * ((task - 168) / 7) + 8 * (w >> 1) + (i & ~3)) * KB); });
    }
  }
  pstamp(x);
  if (!kvp) { wv<8>(); bar(); }
  stamp(x);
  float mean, rstd;
  { const f32x2 p0 = ((const f32x2*)(lds + S_ST))[tl * 32 + r], p1 = ((const f32x2*)(lds + S_ST))[(tl + 2) * 32 + r];
    row_stats(p0[0] + p1[0], p0[1] + p1[1], mean, rstd); }
  float* csw = (float*)(lds + S_CS);
  const int tro = 8 * h + ((l >> 2) & 3);
  u32x4 ones = {0x3f803f80u, 0x3f803f80u, 0x3f803f80u, 0x3f803f80u};
  pin(ones);
  if (kvp) {
    f32x16 kv, cs;
    const unsigned tb = IMG_T + (224 * kh + tro) * 56 + tco;
    u32x2 f[14][2];
    auto rd = [&](auto i) { trd<i * 896>(f[i][0], tb); trd<i * 896 + 224>(f[i][1], tb); };
    sfor<0, 7>(rd);
    sfor<0, 14>([&](auto i) {
      if constexpr (i + 7 < 14) { wl<12>(); rd(std::integral_constant<int, i + 7>{}); } else wl<2 * (13 - i)>();
      pin(f[i][0]); pin(f[i][1]);
      u32x4 g = {f[i][0][0], f[i][0][1], f[i][1][0], f[i][1][1]};
      pin(g); nop<0>();
      if constexpr (i == 0) { m32z(kv, g, xf[i]); m32z(cs, g, ones); } else { m32(kv, g, xf[i]); m32(cs, g, ones); }
    });
    nop<11>(); pin(kv); pin(cs);
    if (r == 0) sfor<0, 4>([&](auto q) { *(f32x4*)(csw + (w * 3 + 2) * 32 + 8 * q + 4 * h) = f32x4{cs[4 * q], cs[4 * q + 1], cs[4 * q + 2], cs[4 * q + 3]}; });
    pstamp(x);
    if (kh) put_tile(lds + S_KP + tl * 4096 + x.vl16, kv);
    bar();
    if (!kh) {
      if (x.m == 0 && w == 0) st1(0.f, l < 16 ? l * 256 : ~0u, x.rs, OFF_FLAG + x.c * 4096);
      float raw[16]; f32x4 c4[4];
      sfor<0, 4>([&](auto q) {
        const f32x4 o = ((const f32x4*)(lds + S_KP + tl * 4096 + x.vl16))[q * 64];
        c4[q] = *(const f32x4*)(csw + (w * 3 + 2) * 32 + 8 * q + 4 * h) + *(const f32x4*)(csw + ((w + 2) * 3 + 2) * 32 + 8 * q + 4 * h);
#pragma unroll
        for (int i = 0; i < 4; ++i) raw[4 * q + i] = kv[4 * q + i] + o[i];
      });
      const unsigned km = kv_finish(x, raw, c4, mean, rstd, hp == 1, OFF_KV + (tt * 4 + 2 * hp) * KB, tl);
      pstamp(x);
      wv<0>();
      kv_publish(x, 0, 2 * tt + hp, km);
      if (tt == 0 && r == 0) sfor<0, 4>([&](auto q) { st4<32 * q>(__builtin_bit_cast(u32x4, c4[q]), h * 16, x.rs, OFF_CSQ + (16 + hp) * 128); });
    } else {
      sfor<0, 28>([&](auto i) { ldl<1>((16 * tl + 8 * (i & 1) + (l >> 3)) * 896 + (l & 7) * 16, rq, hp * 112 + (i >> 1) * 32 * 896, IMG_Q + (4 * (i >> 1) + 2 * tl + (i & 1)) * KB); });
      pstamp(x);
      wv<0>();
    }
    bar();
    pstamp(x);
  } else { pstamp(x); pstamp(x); pstamp(x); }
  f32x16 qa;
  {
    f32x16 q1, q2, cs;
    const unsigned t1 = IMG_Q + (224 * kh + tro) * 128 + tco + 56 * tl, t2 = IMG_Q + (224 * kh + tro) * 128 + tco + 56 * (tl ^ 1);
    u32x2 f[14][4];
    auto rd = [&](auto i) { trd<i * 2048>(f[i][0], t1); trd<i * 2048 + 512>(f[i][1], t1); trd<i * 2048>(f[i][2], t2); trd<i * 2048 + 512>(f[i][3], t2); };
    wl<0>();
    sfor<0, 3>(rd);
    sfor<0, 14>([&](auto i) {
      if constexpr (i + 3 < 14) { wl<8>(); rd(std::integral_constant<int, i + 3>{}); } else wl<4 * (13 - i)>();
      sfor<0, 4>([&](auto e) { pin(f[i][e]); });
      u32x4 g1 = {f[i][0][0], f[i][0][1], f[i][1][0], f[i][1][1]}, g2 = {f[i][2][0], f[i][2][1], f[i][3][0], f[i][3][1]};
      pin(g1); pin(g2); nop<0>();
      if constexpr (i == 0) { m32z(q1, g1, xf[i]); m32z(q2, g2, xf[i]); m32z(cs, g1, ones); } else { m32(q1, g1, xf[i]); m32(q2, g2, xf[i]); m32(cs, g1, ones); }
      if constexpr (i == 10) { ask_slots(x, 0); SB(); }
    });
    nop<11>(); pin(q1); pin(q2); pin(cs);
    pstamp(x);
    if (r == 0) sfor<0, 4>([&](auto q) { *(f32x4*)(csw + (w * 3) * 32 + 8 * q + 4 * h) = f32x4{cs[4 * q], cs[4 * q + 1], cs[4 * q + 2], cs[4 * q + 3]}; });
    if (kh == tl) { put_tile(lds + S_QP + (w ^ 2) * 4096 + x.vl16, q2); qa = q1; } else { put_tile(lds + S_QP + (w ^ 2) * 4096 + x.vl16, q1); qa = q2; }
  }
  if (th == 0 && x.c < 14) {
    const int f = 4 * x.c + w, hd = f >= 28, ks = f - 28 * hd;
    const char* tb = lds + IMG_Q + (16 * ks + sro) * 128 + tco + 56 * hd;
    st4<0>(trfrag(tb, tb + 1024), x.vl16, x.rs, OFF_WQ + (hp * 56 + f) * KB);
  }
  if (kvp && th == 1 && x.c < 7) {
    const char* tb = lds + IMG_T + (16 * (4 * x.c + w) + sro) * 56 + tco;
    st4<0>(trfrag(tb, tb + 448), x.vl16, x.rs, OFF_WQ + ((16 + hp) * 28 + 4 * x.c + w) * KB);
  }
  pstamp(x);
  kv_wait(x, 0);
  count(x, FL_W(x.c), 1);
  ask_kv(x, 0, kf);
  stamp(x);
  const bool own = x.c == hp && th == 0;
  {
    f32x4 cq[4];
    sfor<0, 4>([&](auto q) {
      cq[q] = *(const f32x4*)(csw + (kh * 3) * 32 + 8 * q + 4 * h) + *(const f32x4*)(csw + ((kh + 2) * 3) * 32 + 8 * q + 4 * h);
      if (own && tl == 0 && r == 0) st4<32 * q>(__builtin_bit_cast(u32x4, cq[q]), h * 16, x.rs, OFF_CSQ + (2 * hp + kh) * 128);
    });
    q_finish(x, qa, cq, mean, rstd);
  }
  pstamp(x);
}

DI void phase_front(Ctx& x, f32x16 (&kf)[3]) {
  const int w = x.w, l = x.l, h = l >> 5, r = l & 31;
  const int th = x.m >> 3, hp = x.m & 7, tl = w & 1, kh = w >> 1, tt = 4 * x.c + 2 * th + tl;
  const unsigned KV = OFF_KV + SZ_KV;
  {
    f32x4 cq[4], ck[4];
    sfor<0, 4>([&](auto q) { ld4f<32 * q>(cq[q], h * 16, x.rs, OFF_CSQ + (2 * hp + kh) * 128); ld4f<32 * q>(ck[q], h * 16, x.rs, OFF_CSQ + (16 + (hp & 1)) * 128); });
    wv<0>();
    await(x, FL_X(x.c, th), 7);
    stamp(x);
    u32x4 xf[14]; f32x2 sp[7];
    {
      const unsigned so = OFF_X + 2 * SZ_X + (tt * 28 + 14 * kh) * KB;
      sfor<0, 7>([&](auto i) { ld4<(i & 3) * 1024>(xf[i], x.vl16, x.rs, so + (i >> 2) * 4096); });
      sfor<0, 7>([&](auto i) { ld2<0>(sp[i], (h * 7 * 2048 + r) * 8, x.rs, OFF_ST + 2 * SZ_ST + (i * 2048 + tt * 32) * 8); });
      sfor<7, 14>([&](auto i) { ld4<(i & 3) * 1024>(xf[i], x.vl16, x.rs, so + (i >> 2) * 4096); });
    }
    float mean, rstd;
    auto stats = [&]() {
      sfor<0, 7>([&](auto i) { pin(sp[i]); });
      sfor<0, 4>([&](auto i) { pin(cq[i]); pin(ck[i]); });
      float s = 0.f, q = 0.f;
#pragma unroll
      for (int i = 0; i < 7; ++i) { s += sp[i][0]; q += sp[i][1]; }
      row_stats(sum32(s), sum32(q), mean, rstd);
      pin(mean); pin(rstd); SB();
    };
    f32x16 qa, qb;
    u32x4 wa[14], wb[14];
    const unsigned pa = x.vl16 + (SL_WQ + 28 * kh + 14 * kh) * KB, pb = x.vl16 + (SL_WQ + 28 * (kh ^ 1) + 14 * kh) * KB;
    if (hp < 2) {
      f32x16 kv;
      sfor<0, 14>([&](auto i) { dsr<i * 1024>(wa[i], x.vl16 + 14 * kh * KB); });
      sfor<0, 14>([&](auto i) {
        wv<(i < 7 ? 20 : 13) - i>(); wl<13 - i>();
        if constexpr (i == 0) m32z(kv, wa[i], xf[i]); else m32(kv, wa[i], xf[i]);
        if constexpr (i == 7) stats();
      });
      nop<11>(); pin(kv);
      if (kh) put_tile(lds + S_KP + tl * 4096 + x.vl16, kv);
      bar();
      unsigned km = 0;
      if (!kh) {
        float raw[16];
        sfor<0, 4>([&](auto q) {
          const f32x4 o = ((const f32x4*)(lds + S_KP + tl * 4096 + x.vl16))[q * 64];
#pragma unroll
          for (int i = 0; i < 4; ++i) raw[4 * q + i] = kv[4 * q + i] + o[i];
        });
        km = kv_finish(x, raw, ck, mean, rstd, hp == 1, KV + (tt * 4 + 2 * hp) * KB, tl);
      }
      wl<0>();
      sfor<0, 7>([&](auto i) { dsr<i * 1024>(wb[i], pb); dsr<i * 1024>(wa[i], pa); });
      sfor<0, 14>([&](auto i) {
        if constexpr (i < 7) wl<12>(); else wl<2 * (13 - i)>();
        if constexpr (i == 0) { m32z(qb, wb[i], xf[i]); m32z(qa, wa[i], xf[i]); } else { m32(qb, wb[i], xf[i]); m32(qa, wa[i], xf[i]); }
        if constexpr (i < 7) { dsr<(i + 7) * 1024>(wb[i + 7], pb); dsr<(i + 7) * 1024>(wa[i + 7], pa); }
        if constexpr (i == 8) { if (!kh) { wv<0>(); kv_publish(x, 1, 2 * tt + hp, km); } SB(); }
      });
    } else {
      sfor<0, 7>([&](auto i) { dsr<i * 1024>(wb[i], pb); dsr<i * 1024>(wa[i], pa); });
      sfor<0, 14>([&](auto i) {
        wv<(i < 7 ? 20 : 13) - i>();
        if constexpr (i < 7) wl<12>(); else wl<2 * (13 - i)>();
        if constexpr (i == 0) { m32z(qb, wb[i], xf[i]); m32z(qa, wa[i], xf[i]); } else { m32(qb, wb[i], xf[i]); m32(qa, wa[i], xf[i]); }
        if constexpr (i < 7) { dsr<(i + 7) * 1024>(wb[i + 7], pb); dsr<(i + 7) * 1024>(wa[i + 7], pa); }
        if constexpr (i == 7) stats();
      });
    }
    nop<11>();
    pin(qa); pin(qb);
    put_tile(lds + S_QP + (w ^ 2) * 4096 + x.vl16, qb);
    bar();
    q_finish(x, qa, cq, mean, rstd);
    stamp(x);
    ask_slots(x, 1);
    kv_wait(x, 1);
    ask_kv(x, 1, kf);
    stamp(x);
  }
}

DI void phase_att(Ctx& x, const int L, f32x16 (&kf)[3], f32x16 (&g2)[3]) {
  const int w = x.w, l = x.l, h = l >> 5, r = l & 31;
  const int th = x.m >> 3, hp = x.m & 7, tl = w & 1, kh = w >> 1, tt = 4 * x.c + 2 * th + tl;
  const unsigned kvs = OFF_KV + L * SZ_KV + 16 * w * 4096;
  stamp(x);
  if (L == 0) bar();
  stamp(x);
  unsigned kma = S_KM + 16; asm volatile("" : "+s"(kma));
  const float kmx = *(const float*)(lds + kma);
  u32x4 q[4][2];
#pragma unroll
  for (int k = 0; k < 4; ++k) {
    q[k][0] = ((const u32x4*)(lds + S_QF + (w ^ k) * 2048 + x.vl16))[0];
    q[k][1] = ((const u32x4*)(lds + S_QF + (w ^ k) * 2048 + x.vl16))[64];
  }
  bool safe = true;
  {
#pragma unroll
    for (int k = 0; k < 4; ++k) {
      const float mrow = bflo(q[k][1][2]) * kmx + 0.01f;
      safe = safe && (!h || mrow <= M_LIMIT);
      if (h) q[k][1][2] = cvtpk(-mrow, 0.f);
    }
  }
  const bool fast = __all(safe);
  stamp(x);
  f32x16 own;
  if (fast) {
    unsigned so_kv = kvs + 3 * 4096, so_w = OFF_WGU + (x.m * 196 + 2 * w) * KB, so_l = 2 * w * KB, spin = 1u << 16, fvo = (l & 15) * 4096;
    Two ab = base_wo(x);
    const Two wd = base_wd(x);
    asm volatile(
      ".macro GRP s, pa, pb\n\t"
      "v_exp_f32 v200, v[\\s]\n\tv_exp_f32 v201, v[\\s+1]\n\tv_cvt_pk_bf16_f32 v[\\pa], v202, v203\n\t"
      "v_exp_f32 v202, v[\\s+2]\n\tv_exp_f32 v203, v[\\s+3]\n\tv_cvt_pk_bf16_f32 v[\\pb], v200, v201\n\t"
      ".endm\n\t"
      ".macro MEM kind, i, ld\n\t"
      ".if \\kind == 1\n\t"
      "buffer_load_dwordx4 v[128+16*\\ld+4*\\i:128+16*\\ld+4*\\i+3], %[vo], %[rs], %[skv] offen offset:1024*\\i sc1\n\t"
      ".elseif \\kind == 5\n\t"
      "ds_write_b128 v205, v[4*\\i:4*\\i+3] offset:1024*\\i\n\t"
      ".elseif \\kind == 6\n\t"
      "ds_write_b128 v205, v[4:7] offset:1024\n\t"
      ".elseif \\kind == 7\n\t"
      "ds_write_b128 v205, v[12:15] offset:3072\n\t"
      ".elseif \\kind == 2\n\t"
      "buffer_load_dword v206, v205, %[rs], 0 offen offset:3072 sc1\n\t"
      ".elseif \\kind == 3\n\t"
      ".if att_n < 16 || att_n >= 56\n\t"
      ".if att_n < 102\n\t"
      ".if att_n < 16\n\t.set att_r, 184+4*att_n\n\t.else\n\t.set att_r, 4*(att_n-56)\n\t.endif\n\t"
      ".if att_n & 1\n\t"
      "buffer_load_dwordx4 a[att_r:att_r+3], %[vo], %[rs], %[ab1] offen offset:1024*((att_n>>1)&3) sc1\n\t"
      ".else\n\t"
      "buffer_load_dwordx4 a[att_r:att_r+3], %[vo], %[rs], %[ab0] offen offset:1024*((att_n>>1)&3) sc1\n\t"
      ".endif\n\t"
      ".if att_n == 15\n\t"
      "s_mov_b32 %[ab0], %[wd0]\n\ts_mov_b32 %[ab1], %[wd1]\n\t"
      ".elseif (att_n & 7) == 7\n\t"
      "s_add_u32 %[ab0], %[ab0], 4096\n\ts_add_u32 %[ab1], %[ab1], 4096\n\t"
      ".endif\n\t"
      ".endif\n\t"
      ".elseif att_n < 44\n\t"
      ".if att_n & 1\n\t"
      "buffer_load_dwordx4 %[vo], %[rs], %[sw] offen offset:1024 sc1 lds\n\t"
      "s_add_u32 %[sw], %[sw], 8192\n\ts_add_u32 m0, m0, 8192\n\t"
      ".else\n\t"
      "buffer_load_dwordx4 %[vo], %[rs], %[sw] offen sc1 lds\n\t"
      ".endif\n\t"
      ".else\n\t"
      "buffer_load_dwordx4 v[208+4*(att_n-44):208+4*(att_n-44)+3], %[vo], %[rs], %[sw] offen offset:1024*(att_n&1) sc1\n\t"
      ".if att_n & 1\n\ts_add_u32 %[sw], %[sw], 8192\n\t.endif\n\t"
      ".endif\n\t"
      ".set att_n, att_n+1\n\t"
      ".endif\n\t"
      ".endm\n\t"
      ".macro STEP sc, sn, k, nk, pk, tk, nt, pt, ld, m0k, m0i, m1k, m2k, m2i, m3k\n\t"
      "v_mfma_f32_32x32x16_bf16 v[\\sn:\\sn+15], v[128+16*\\nt:128+16*\\nt+3], v[64+8*\\nk:64+8*\\nk+3], 0\n\t"
      "MEM \\m0k, \\m0i, \\ld\n\t"
      "GRP \\sc, 199, 192\n\t"
      "v_mfma_f32_32x32x16_bf16 v[\\sn:\\sn+15], v[132+16*\\nt:132+16*\\nt+3], v[68+8*\\nk:68+8*\\nk+3], v[\\sn:\\sn+15]\n\t"
      "MEM \\m1k, 0, 0\n\t"
      "GRP \\sc+4, 193, 194\n\t"
      "v_mfma_f32_32x32x16_bf16 v[16*\\pk:16*\\pk+15], v[140+16*\\pt:140+16*\\pt+3], v[196:199], v[16*\\pk:16*\\pk+15]\n\t"
      "MEM \\m2k, \\m2i, \\ld\n\t"
      "GRP \\sc+8, 195, 196\n\t"
      "v_mfma_f32_32x32x16_bf16 v[16*\\k:16*\\k+15], v[136+16*\\tk:136+16*\\tk+3], v[192:195], v[16*\\k:16*\\k+15]\n\t"
      "MEM \\m3k, 0, 0\n\t"
      "GRP \\sc+12, 197, 198\n\t"
      ".endm\n\t"
      ".macro TILE tk, nx, pv, on, wait, p, fl=0\n\t"
      "STEP 96, 112, 0, 1, 3, \\tk, \\tk, \\pv, \\pv, \\p, 0, \\p, \\p, 0, \\fl\n\t"
      "STEP 112, 96, 1, 2, 0, \\tk, \\tk, \\tk, \\pv, \\on, 0, \\p, \\on, 1, 0\n\t"
      "STEP 96, 112, 2, 3, 1, \\tk, \\tk, \\tk, \\pv, \\on, 2, \\p, \\on, 3, 0\n\t"
      ".if \\on\n\ts_add_u32 %[skv], %[skv], 4096\n\t.endif\n\t"
      "s_waitcnt vmcnt(\\wait)\n\t"
      "STEP 112, 96, 3, 0, 2, \\tk, \\nx, \\tk, \\pv, \\p, 0, \\p, \\p, 0, 0\n\t"
      ".endm\n\t"
      ".set att_n, 0\n\t"
      "s_mov_b32 m0, %[sl]\n\t"
      "v_mov_b32 v196, 0\n\tv_mov_b32 v197, 0\n\tv_mov_b32 v198, 0\n\tv_mov_b32 v199, 0\n\tv_mov_b32 v202, 0\n\tv_mov_b32 v203, 0\n\t"
      "v_mov_b32 v188, 0\n\tv_mov_b32 v189, 0\n\tv_mov_b32 v190, 0\n\tv_mov_b32 v191, 0\n\t"
      "v_mfma_f32_32x32x16_bf16 v[0:15], v[196:199], v[196:199], 0\n\t"
      "v_mfma_f32_32x32x16_bf16 v[16:31], v[196:199], v[196:199], 0\n\t"
      "v_mfma_f32_32x32x16_bf16 v[32:47], v[196:199], v[196:199], 0\n\t"
      "v_mfma_f32_32x32x16_bf16 v[48:63], v[196:199], v[196:199], 0\n\t"
      "s_waitcnt vmcnt(8)\n\t"
      "v_mfma_f32_32x32x16_bf16 v[96:111], v[128:131], v[64:67], 0\n\t"
      "v_mfma_f32_32x32x16_bf16 v[96:111], v[132:135], v[68:71], v[96:111]\n\t"
      "s_nop 11\n\t"
      "TILE 0, 1, 3, 1, 8, 0\n\t"
      "TILE 1, 2, 0, 1, 8, 0\n\tTILE 2, 3, 1, 1, 9, 0, 2\n\t"
      "s_waitcnt vmcnt(4)\n\t"
      "1:\n\t"
      "v_cmp_gt_u32 vcc, 16, v206\n\t"
      "s_and_b64 vcc, exec, vcc\n\t"
      "s_cbranch_vccz 2f\n\t"
      "s_sleep 24\n\t"
      "buffer_load_dword v206, v205, %[rs], 0 offen offset:3072 sc1\n\t"
      "s_sub_u32 %[spin], %[spin], 1\n\t"
      "s_waitcnt vmcnt(0)\n\t"
      "s_cbranch_scc0 1b\n\t"
      "2:\n\t"
      "s_barrier\n\t"
      "TILE 3, 0, 2, 1, 14, 3\n\t"
      "TILE 0, 1, 3, 1, 21, 3\n\tTILE 1, 2, 0, 1, 24, 3\n\tTILE 2, 3, 1, 1, 24, 3\n\tTILE 3, 0, 2, 1, 24, 3\n\t"
      "TILE 0, 1, 3, 1, 24, 3\n\tTILE 1, 2, 0, 1, 24, 3\n\tTILE 2, 3, 1, 1, 24, 3\n\tTILE 3, 0, 2, 1, 24, 3\n\t"
      "TILE 0, 1, 3, 1, 24, 3\n\tTILE 1, 2, 0, 0, 20, 3\n\tTILE 2, 3, 1, 0, 16, 3\n\t"
      "v_add_u32 v205, %[d1], v204\n\tv_add_u32 v206, %[d2], v204\n\t"
      "STEP 96, 112, 0, 1, 3, 3, 3, 2, 0, 3, 0, 3, 3, 0, 3\n\t"
      "STEP 112, 96, 1, 2, 0, 3, 3, 3, 0, 3, 0, 3, 0, 0, 0\n\t"
      "STEP 96, 112, 2, 3, 1, 3, 3, 3, 0, 5, 0, 6, 5, 2, 7\n\t"
      "ds_write_b128 v206, v[16:19]\n\t"
      "GRP 112, 199, 192\n\t"
      "ds_write_b128 v206, v[20:23] offset:1024\n\t"
      "GRP 116, 193, 194\n\t"
      "v_mfma_f32_32x32x16_bf16 v[32:47], v[188:191], v[196:199], v[32:47]\n\t"
      "ds_write_b128 v206, v[24:27] offset:2048\n\t"
      "GRP 120, 195, 196\n\t"
      "v_mfma_f32_32x32x16_bf16 v[48:63], v[184:187], v[192:195], v[48:63]\n\t"
      "ds_write_b128 v206, v[28:31] offset:3072\n\t"
      "GRP 124, 197, 198\n\t"
      "v_add_u32 v200, %[d3], v204\n\t"
      "v_cvt_pk_bf16_f32 v199, v202, v203\n\t"
      "s_nop 1\n\t"
      "v_mfma_f32_32x32x16_bf16 v[48:63], v[188:191], v[196:199], v[48:63]\n\t"
      "ds_write_b128 v200, v[32:35]\n\tds_write_b128 v200, v[36:39] offset:1024\n\tds_write_b128 v200, v[40:43] offset:2048\n\tds_write_b128 v200, v[44:47] offset:3072\n\t"
      "s_nop 7\n\t"
      ".purgem GRP\n\t.purgem MEM\n\t.purgem STEP\n\t.purgem TILE"
      : "={v[48:63]}"(own), "+{v[128:143]}"(kf[0]), "+{v[144:159]}"(kf[1]), "+{v[160:175]}"(kf[2]),
        "={v[208:223]}"(g2[0]), "={v[224:239]}"(g2[1]), "={v[240:255]}"(g2[2]),
        [skv] "+s"(so_kv), [sw] "+s"(so_w), [ab0] "+s"(ab.s0), [ab1] "+s"(ab.s1), [spin] "+s"(spin), "+{v205}"(fvo)
      : "{v[64:67]}"(q[1][0]), "{v[68:71]}"(q[1][1]), "{v[72:75]}"(q[2][0]), "{v[76:79]}"(q[2][1]),
        "{v[80:83]}"(q[3][0]), "{v[84:87]}"(q[3][1]), "{v[88:91]}"(q[0][0]), "{v[92:95]}"(q[0][1]),
        [vo] "{v204}"(x.vl16), [d1] "s"(SCR + (w ^ 1) * 3 * 4096), [d2] "s"(SCR + ((w ^ 2) * 3 + 1) * 4096), [d3] "s"(SCR + ((w ^ 3) * 3 + 2) * 4096), [rs] "s"(x.rs), [sl] "s"(so_l), [wd0] "s"(wd.s0), [wd1] "s"(wd.s1)
      : "memory", "scc", "vcc",
        "v0","v1","v2","v3","v4","v5","v6","v7","v8","v9","v10","v11","v12","v13","v14","v15","v16","v17","v18","v19","v20","v21","v22","v23",
        "v24","v25","v26","v27","v28","v29","v30","v31","v32","v33","v34","v35","v36","v37","v38","v39","v40","v41","v42","v43","v44","v45","v46","v47",
        "v96","v97","v98","v99","v100","v101","v102","v103","v104","v105","v106","v107","v108","v109","v110","v111",
        "v112","v113","v114","v115","v116","v117","v118","v119","v120","v121","v122","v123","v124","v125","v126","v127",
        "v176","v177","v178","v179","v180","v181","v182","v183","v184","v185","v186","v187","v188","v189","v190","v191",
        "v192","v193","v194","v195","v196","v197","v198","v199","v200","v201","v202","v203","v206", ACLOB);
  } else {
    f32x16 o[4];
    wv<0>();
    pin(kf[0]); pin(kf[1]); pin(kf[2]);
    float mx[4];
#pragma unroll
    for (int k = 0; k < 4; ++k) {
      mx[k] = -3.0e38f; if (h) q[k][1][2] = 0; pin(q[k][1]);
#pragma unroll
      for (int i = 0; i < 16; ++i) o[k][i] = 0.f;
    }
#pragma nounroll
    for (unsigned spin = 0; spin < (1u << 16); ++spin) {
      float v;
      ld1(v, (l & 15) * 4096, x.rs, OFF_FLAG + FL_W(0)); wv<0>(); pin(v);
      if (__all(__float_as_uint(v) >= 16u)) break;
      __builtin_amdgcn_s_sleep(24);
    }
    pf_wo(x);
    pf_wgu1(x);
    sfor<0, 12>([&](auto j) {
      u32x4 t;
      ld4<1024 * (j & 1)>(t, x.vl16, x.rs, OFF_WGU + (x.m * 196 + 112 + 8 * (j >> 1) + 2 * w) * KB); wv<0>(); pin(t);
#pragma unroll
      for (int i = 0; i < 4; ++i) g2[j >> 2][4 * (j & 3) + i] = __uint_as_float(t[i]);
    });
    pf_wd<0, 46>(x, base_wd(x));
    for (int tk = 0; tk < 16; ++tk) {
      u32x4 kf[4];
      sfor<0, 4>([&](auto i) { ld4<i * 1024>(kf[i], x.vl16, x.rs, kvs + tk * 4096); });
      wv<0>();
      sfor<0, 4>([&](auto k) {
        f32x16 s;
        nop<1>();
        m32z(s, kf[0], q[k][0]); m32(s, kf[1], q[k][1]);
        nop<11>(); pin(s);
        float tm = s[0];
#pragma unroll
        for (int i = 1; i < 16; ++i) tm = fmaxf(tm, s[i]);
        tm = max32(tm);
        const float mn = fmaxf(mx[k], tm), al = __builtin_amdgcn_exp2f(mx[k] - mn);
        mx[k] = mn;
#pragma unroll
        for (int i = 0; i < 16; ++i) o[k][i] *= al;
        u32x4 p0, p1;
#pragma unroll
        for (int d = 0; d < 4; ++d) {
          p0[d] = cvtpk(__builtin_amdgcn_exp2f(s[2 * d] - mn), __builtin_amdgcn_exp2f(s[2 * d + 1] - mn));
          p1[d] = cvtpk(__builtin_amdgcn_exp2f(s[8 + 2 * d] - mn), __builtin_amdgcn_exp2f(s[9 + 2 * d] - mn));
        }
        pin(p0); pin(p1); pin(o[k]);
        nop<1>();
        m32(o[k], kf[2], p0); m32(o[k], kf[3], p1);
        nop<11>(); pin(o[k]);
      });
    }
    bar();
    float* mm = (float*)(lds + SCR);
#pragma unroll
    for (int k = 0; k < 4; ++k) if (h == 0) mm[((w ^ k) * 4 + w) * 32 + r] = mx[k];
    bar();
#pragma unroll
    for (int k = 0; k < 4; ++k) {
      const float* p = mm + (w ^ k) * 128 + r;
      const float g = fmaxf(fmaxf(p[0], p[32]), fmaxf(p[64], p[96])), f = __builtin_amdgcn_exp2f(mx[k] - g);
#pragma unroll
      for (int i = 0; i < 16; ++i) o[k][i] *= f;
    }
    bar();
    reduce_put(x, o);
    own = o[0];
  }
  stamp(x);
  reduce_get(x, own);
  pin(own); fstamp(x);
  {
    auto lr = __builtin_amdgcn_permlane32_swap(__float_as_uint(own[12]), __float_as_uint(own[12]), false, false);
    const float inv = __builtin_amdgcn_rcpf(__uint_as_float(lr[1]));
    u32x4 a, b;
#pragma unroll
    for (int d = 0; d < 4; ++d) { a[d] = cvtpk(own[2 * d] * inv, own[2 * d + 1] * inv); b[d] = cvtpk(own[8 + 2 * d] * inv, own[9 + 2 * d] * inv); }
    if (h) { b[2] = 0; b[3] = 0; }
    const unsigned so = OFF_O + L * SZ_O + (tt * 32 + 2 * (2 * hp + kh)) * KB;
    st4<0>(a, x.vl16, x.rs, so); st4<1024>(b, x.vl16, x.rs, so);
  }
  fstamp(x);
  wv<0>();
  fstamp(x);
  signal(x, FL_O(L, x.c, th), 1);
  if (L == 0 && th == 0) {
    if (x.c == hp) count(x, FL_W2, 1);
  }
}

template <bool FINAL> DI void res_epilogue(Ctx& x, const f32x16& acc, const u32x4 (&xr)[2], unsigned Xo, unsigned STo, int tt, int nt, char* out = nullptr) {
  const int h = x.l >> 5, r = x.l & 31;
  u32x4 y[2];
#pragma unroll
  for (int d = 0; d < 8; ++d) y[d >> 2][d & 3] = cvtpk(acc[2 * d] + bflo(xr[d >> 2][d & 3]), acc[2 * d + 1] + bfhi(xr[d >> 2][d & 3]));
  if constexpr (FINAL) {
#pragma unroll
    for (int s = 0; s < 2; ++s) {
      auto a = __builtin_amdgcn_permlane32_swap(y[s][0], y[s][2], false, false);
      auto b = __builtin_amdgcn_permlane32_swap(y[s][1], y[s][3], false, false);
      y[s] = u32x4{a[0], b[0], a[1], b[1]};
    }
    const unsigned vo = r * (D * 2) + h * 16, so = tt * 32 * D * 2 + nt * 64;
    const rsrc_t ro = mkrs(out, S * D * 2);
    st4p<0>(y[0], vo, ro, so); st4p<32>(y[1], vo, ro, so);
  } else {
    const unsigned so = Xo + (tt * 28 + 2 * nt) * KB;
    st4<0>(y[0], x.vl16, x.rs, so); st4<1024>(y[1], x.vl16, x.rs, so);
    float s = 0.f, q = 0.f, s1 = 0.f, q1 = 0.f;
#pragma unroll
    for (int d = 0; d < 4; ++d) {
      s = dot2(y[0][d], 0x3f803f80u, s); q = dot2(y[0][d], y[0][d], q); s1 = dot2(y[1][d], 0x3f803f80u, s1); q1 = dot2(y[1][d], y[1][d], q1);
    }
    s = sum32(s + s1); q = sum32(q + q1);
    if (h == 0) st2<0>(u32x2{__float_as_uint(s), __float_as_uint(q)}, r * 8, x.rs, STo + (nt * 2048 + tt * 32) * 8);
  }
}

DI void phase_oproj(Ctx& x, const int L, const Args& a, const f32x16 (&g2)[3]) {
  const int w = x.w;
  if (x.m < 14) {
    const int th = x.m / 7, ct = x.m % 7, tt0 = 4 * x.c + 2 * th, tt = tt0 + (w >> 1), nt = 2 * ct + (w & 1);
    u32x2 x0[4];
    if (L == 0) {
      const rsrc_t rx = mkrs(a.x, S * D * 2);
      sfor<0, 4>([&](auto i) { ld2i<16 * i>(x0[i], (x.l & 31) * 896 + (x.l >> 5) * 8, rx, tt * 32 * 896 + nt * 64); });
    } else {
      sfor<0, 4>([&](auto i) { ld2u<(i >> 1) * 1024 + (i & 1) * 8>(x0[i], x.vl16, x.rs, OFF_X + 2 * SZ_X + (tt * 28 + 2 * nt) * KB); });
    }
    await(x, FL_O(L, x.c, th), 8);
    stamp(x);
    u32x4 b[8][2];
    const unsigned s0 = OFF_O + L * SZ_O + (tt * 32 + 8 * w) * KB, s1 = OFF_O + L * SZ_O + ((tt0 + ((w >> 1) ^ 1)) * 32 + 8 * w) * KB;
    auto ld = [&](auto i) { ld4<(i & 3) * 1024>(b[i][0], x.vl16, x.rs, s0 + (i >> 2) * 4096); ld4<(i & 3) * 1024>(b[i][1], x.vl16, x.rs, s1 + (i >> 2) * 4096); };
    constexpr int WN = OPWN;
    sfor<0, WN>(ld);
    f32x16 acc[4];
    sfor<0, 8>([&](auto i) {
      constexpr int last = (i + WN - 1 < 7 ? i + WN - 1 : 7);
      wv<2 * (last - i) + 1>();
      m32a<A_WO + 8 * i, i == 0>(acc[0], b[i][0]); m32a<A_WO + 8 * i + 4, i == 0>(acc[1], b[i][0]);
      wv<2 * (last - i)>();
      m32a<A_WO + 8 * i, i == 0>(acc[2], b[i][1]); m32a<A_WO + 8 * i + 4, i == 0>(acc[3], b[i][1]);
      if constexpr (i + WN < 8) ld(std::integral_constant<int, i + WN>{});
    });
    nop<11>();
    sfor<0, 4>([&](auto k) { pin(acc[k]); pin(x0[k]); });
    stamp(x);
    reduce_put(x, acc); reduce_get(x, acc[0]);
    pin(acc[0]); fstamp(x);
    const u32x4 x2[2] = {u32x4{x0[0][0], x0[0][1], x0[1][0], x0[1][1]}, u32x4{x0[2][0], x0[2][1], x0[3][0], x0[3][1]}};
    res_epilogue<false>(x, acc[0], x2, OFF_X + (2 * L + 1) * SZ_X, OFF_ST + (2 * L + 1) * SZ_ST, tt, nt);
    fstamp(x);
    wv<0>();
    fstamp(x);
    signal(x, FL_X1(L, x.c), 1);
    fstamp(x);
  } else {
    stamp(x); stamp(x); fstamp(x); fstamp(x); fstamp(x); fstamp(x);
  }
#pragma unroll
  for (int j = 0; j < 12; ++j)
    *(f32x4*)(lds + (112 + 8 * (j >> 1) + 2 * w + (j & 1)) * KB + x.vl16) = f32x4{g2[j >> 2][4 * (j & 3)], g2[j >> 2][4 * (j & 3) + 1], g2[j >> 2][4 * (j & 3) + 2], g2[j >> 2][4 * (j & 3) + 3]};
}

constexpr int DWN = 7;
constexpr bool dn_half(int o) { return o == 4 || o == 5 || o == 14 || o == 15; }
struct DnSrc { unsigned s0, s1, l0, l1; };
DI DnSrc dn_src(const Ctx& x, int L) {
  const int tt0 = 4 * x.c + 2 * ((x.m / 7) & 1), tt = tt0 + (x.w >> 1), tto = tt0 + ((x.w >> 1) ^ 1);
  return DnSrc{OFF_H + L * SZ_H + (tt * 112 + 28 * x.w) * KB, OFF_H + L * SZ_H + (tto * 112 + 28 * x.w) * KB,
               OFF_HL + ((L * 64 + tt) * 16 + 4 * x.w) * KB, OFF_HL + ((L * 64 + tto) * 16 + 4 * x.w) * KB};
}
template <int O> DI void dn_ld(Ctx& x, const DnSrc& d, u32x4 (&b)[DWN][2]) {
  constexpr int i = dn_ks(O);
  if constexpr (dn_half(O)) {
    constexpr int imm = (O >= 14 ? 2048 : 0) + (O & 1) * 512;
    u32x2 lo, hi, lo1, hi1;
    ld2u<imm>(lo, x.l * 8, x.rs, d.l0); ld2u<imm + 1024>(hi, x.l * 8, x.rs, d.l0);
    ld2u<imm>(lo1, x.l * 8, x.rs, d.l1); ld2u<imm + 1024>(hi1, x.l * 8, x.rs, d.l1);
    b[O % DWN][0] = u32x4{lo[0], lo[1], hi[0], hi[1]}; b[O % DWN][1] = u32x4{lo1[0], lo1[1], hi1[0], hi1[1]};
  } else {
    ld4<(i & 3) * 1024>(b[O % DWN][0], x.vl16, x.rs, d.s0 + (i >> 2) * 4096); ld4<(i & 3) * 1024>(b[O % DWN][1], x.vl16, x.rs, d.s1 + (i >> 2) * 4096);
  }
}

DI void gu_tile(const f32x4 (&acc)[2][2], const u32x4 (&cs)[2], const float (&mean)[2], const float (&r2)[2], u32x2 (&hp)[2]) {
  const f32x4 cg = __builtin_bit_cast(f32x4, cs[0]), cu = __builtin_bit_cast(f32x4, cs[1]);
#pragma unroll
  for (int u = 0; u < 2; ++u) {
    const f32x2 nm = {-mean[u], -mean[u]}, rr = {r2[u], r2[u]};
#pragma unroll
    for (int e = 0; e < 2; ++e) {
      const f32x2 g = __builtin_elementwise_fma(nm, f32x2{cg[2 * e], cg[2 * e + 1]}, f32x2{acc[0][u][2 * e], acc[0][u][2 * e + 1]});
      const f32x2 v = __builtin_elementwise_fma(nm, f32x2{cu[2 * e], cu[2 * e + 1]}, f32x2{acc[1][u][2 * e], acc[1][u][2 * e + 1]});
      const f32x2 y = g * rr * v;
      hp[u][e] = cvtpk(y[0], y[1]);
    }
  }
}

DI void phase_gu(Ctx& x, const int L, u32x4 (&b)[DWN][2]) {
  const int w = x.w, l = x.l, g = l >> 4, c16 = l & 15, m = x.m, tt = 4 * x.c + w;
  if (L == 0 && wave0(x)) { unsigned v; ldflag(v, x.rs, OFF_FLAG + FL_W2); wv<0>(); recheck(x, v, FL_W2, 8); }
  await(x, FL_X1(L, x.c), 14);
  stamp(x);
  constexpr int NXV = 11;
  u32x4 xf[NXV][2]; f32x2 sp[2][4];
  const unsigned sx = OFF_X + (2 * L + 1) * SZ_X + tt * 28 * KB, st = OFF_ST + (2 * L + 1) * SZ_ST + tt * 32 * 8;
  auto ldx = [&](auto k, auto u) {
    if constexpr (k < NXV) ld4<(k & 1) * 2048 + 256 * u>(xf[k][u], x.vg16, x.rs, sx + (k >> 1) * 4096);
    else if constexpr (k < 14) lda<A_GX + 8 * (k - NXV) + 4 * u, (k & 1) * 2048 + 256 * u>(x.vg16, x.rs, sx + (k >> 1) * 4096);
    else { constexpr int i = k - 14; ld2<128 * u>(sp[u][i], (((i < 3 || g < 2) ? g + 4 * i : g) * 2048 + c16) * 8, x.rs, st); }
  };
  using I0 = std::integral_constant<int, 0>; using I1 = std::integral_constant<int, 1>;
  constexpr int XA = XAHEAD;
  sfor<0, XA>([&](auto k) { ldx(k, I0{}); ldx(k, I1{}); });
  const unsigned a0 = x.vl16, a1 = x.vl16 + 65536, a2 = x.vl16 + 131072;
  constexpr int DP = 6;
  u32x4 wf[8];
  auto rd = [&](auto ii) {
    constexpr int i = ii, slot = i >= 160 ? i - 160 : i;
    if constexpr (slot < 64) dsr<slot * 1024>(wf[i & 7], a0); else if constexpr (slot < 128) dsr<(slot - 64) * 1024>(wf[i & 7], a1); else dsr<(slot - 128) * 1024>(wf[i & 7], a2);
  };
  auto mm = [&](f32x4& c, const u32x4& a, auto k, auto u) {
    if constexpr (k < NXV) m16<k == 0>(c, a, xf[k][u]); else m16a<false, A_GX + 8 * (k - NXV) + 4 * u>(c, a);
  };
  const Two wd = base_wd(x);
  f32x4 aa[5][2][2], ch0, ch1;
  float mean[2], r2[2];
  sfor<0, DP>(rd);
  sfor<0, 140>([&](auto i) {
    constexpr int KS = i / 10, s = i % 10, t = s >> 1, mat = i & 1;
    using K = std::integral_constant<int, KS>;
    constexpr auto side = [](int k) { return (k >= 0 && k <= 3) || (k >= 5 && k <= 12); };
    constexpr auto all = [](int k) { return (k + XA < 14 ? 2 : 0) + ((k >= 0 && k <= 3) || (k >= 5 && k <= 9) ? 2 : 0) + (k == 5 ? 1 : 0) + (k >= 10 ? 3 : 0); };
    if constexpr (s == 0) {
      constexpr int cnt = [&] { int n = side(KS - XA) ? 1 : 0; for (int k = KS - XA + 1; k < KS; ++k) n += all(k); return n; }();
      wv<cnt>();
      if constexpr (KS == 5) asm volatile("s_barrier" ::: "memory");
    }
    if constexpr ((i & 1) == 0) { rd(std::integral_constant<int, i + DP>{}); rd(std::integral_constant<int, i + DP + 1>{}); wl<DP>(); }
    mm(aa[t][mat][0], wf[i & 7], K{}, I0{});
    if constexpr (s == 1 && KS + XA < 14) ldx(std::integral_constant<int, KS + XA>{}, I0{});
    if constexpr (s == 6 && KS + XA < 14) ldx(std::integral_constant<int, KS + XA>{}, I1{});
    if constexpr (KS <= 3 && s == 3) ldx(std::integral_constant<int, 14 + KS>{}, I0{});
    if constexpr (KS <= 3 && s == 8) ldx(std::integral_constant<int, 14 + KS>{}, I1{});
    if constexpr (KS == 5 && s == 3) asm volatile("buffer_load_dwordx4 %0, %2, %3, %4 offen sc1\n\tbuffer_load_dwordx4 %1, %2, %3, %4 offen offset:1024 sc1" : "=&v"(ch0), "=&v"(ch1) : "v"(x.vl16), "s"(x.rs), "s"(OFF_CSGU + m * 2048));
    if constexpr (KS == 11 && s == 2) { pin(ch0); pin(ch1); *(f32x4*)(lds + 40 * KB + x.vl16) = ch0 + ch1; }
    if constexpr (KS >= 5 && KS <= 9 && (s == 3 || s == 8) && !(KS == 5 && s == 3)) pf_wgu3<2 * (KS - 5) + (s == 8) - 1>(x);
    if constexpr (KS >= 10 && (s == 3 || s == 5 || s == 8) && 3 * (KS - 10) + (s == 3 ? 0 : (s == 5 ? 1 : 2)) < 10) {
      constexpr int f = 46 + 3 * (KS - 10) + (s == 3 ? 0 : (s == 5 ? 1 : 2));
      pf_wd<f, f + 1>(x, wd);
    }
    if constexpr ((KS == 10 || KS == 12) && s == 2) {
      constexpr int u = KS == 12;
      sfor<0, 4>([&](auto e) { pin(sp[u][e]); });
      float sm = sp[u][0][0] + sp[u][1][0] + sp[u][2][0], q = sp[u][0][1] + sp[u][1][1] + sp[u][2][1];
      if (g < 2) { sm += sp[u][3][0]; q += sp[u][3][1]; }
      sm = sum32(sum16(sm)); q = sum32(sum16(q));
      float rs; row_stats(sm, q, mean[u], rs); r2[u] = rs * rs;
      pin(mean[u]); pin(r2[u]);
    }
    mm(aa[t][mat][1], wf[i & 7], K{}, I1{}); SB();
  });
  fstamp(x);
  u32x4 cs[7][2];
  const unsigned csa = 40 * KB + g * 16;
  sfor<0, 10>([&](auto i) { dsr<64 * i>(cs[i >> 1][i & 1], csa); });
  wv<10>();
  asm volatile("s_barrier" ::: "memory");
  stamp(x);
  const unsigned H = OFF_H + L * SZ_H + tt * 112 * KB;
  const int gp = (m & 1) ? 7 * m + 1 : 7 * m;
  auto pair = [&](const f32x4 (&t0)[2][2], const f32x4 (&t1)[2][2], auto lt) {
    u32x2 h0[2], h1[2];
    gu_tile(t0, cs[lt], mean, r2, h0); gu_tile(t1, cs[lt + 1], mean, r2, h1);
    st4<0>(u32x4{h0[0][0], h0[0][1], h1[0][0], h1[0][1]}, x.vg16, x.rs, H + (gp + lt - 1) * 1024);
    st4<256>(u32x4{h0[1][0], h0[1][1], h1[1][0], h1[1][1]}, x.vg16, x.rs, H + (gp + lt - 1) * 1024);
  };
  {
    wl<0>();
    sfor<0, 20>([&](auto i) { pin(aa[i >> 2][(i >> 1) & 1][i & 1]); });
    sfor<0, 10>([&](auto i) { pin(cs[i >> 1][i & 1]); });
    u32x2 hl[2];
    gu_tile(aa[0], cs[0], mean, r2, hl);
    const unsigned sl = OFF_HL + ((L * 64 + tt) * 16 + m) * KB;
    st2<0>(hl[0], x.vg16 >> 1, x.rs, sl); st2<128>(hl[1], x.vg16 >> 1, x.rs, sl);
    pair(aa[1], aa[2], std::integral_constant<int, 1>{});
    pair(aa[3], aa[4], std::integral_constant<int, 3>{});
  }
  SB();
  sfor<10, 14>([&](auto i) { dsr<64 * i>(cs[i >> 1][i & 1], csa); });
  fstamp(x);
  f32x4 ab[2][2][2];
  unsigned fa;
  sfor<140, 196>([&](auto i) {
    constexpr int j = i - 140, KS = j / 4, t = (j % 4) >> 1, mat = i & 1;
    using K = std::integral_constant<int, KS>;
    if constexpr (j == 18) {
      fstamp(x);
      if (L == 0) wv<6>(); else wv<0>();
      asm volatile("s_barrier" ::: "memory");
      count(x, FL_H(L, x.c, 0), 1);
      SB();
      fstamp(x);
    }
    if constexpr ((i & 1) == 0) { if constexpr (i + DP < 196) { rd(std::integral_constant<int, i + DP>{}); rd(std::integral_constant<int, i + DP + 1>{}); wl<DP>(); } else wl<194 - i>(); }
    mm(ab[t][mat][0], wf[i & 7], K{}, I0{});
    if constexpr (j % 3 == 1 && j < 42) { if (L == 0) pf_wq<j / 3>(x); SB(); }
    if constexpr (j == 44) ldflag(fa, x.rs, OFF_FLAG + FL_H(L, x.c, 0));
    mm(ab[t][mat][1], wf[i & 7], K{}, I1{}); SB();
  });
  wv<0>();
  recheck(x, fa, FL_H(L, x.c, 0), 16);
  { const DnSrc d = dn_src(x, L); sfor<0, 3>([&](auto o) { dn_ld<o>(x, d, b); }); }
  stamp(x);
  sfor<0, 8>([&](auto i) { pin(ab[i >> 2][(i >> 1) & 1][i & 1]); });
  sfor<10, 14>([&](auto i) { pin(cs[i >> 1][i & 1]); });
  pair(ab[0], ab[1], std::integral_constant<int, 5>{});
}

DI void phase_down(Ctx& x, const int L, char* out, u32x4 (&b)[DWN][2]) {
  const int w = x.w;
  if (x.m >= 14) {
    wv<0>();
    sfor<0, 3>([&](auto o) { pin(b[o][0]); pin(b[o][1]); });
    signal(x, FL_H(L, x.c, 1), 1);
    stamp(x); fstamp(x); fstamp(x); stamp(x);
    return;
  }
  const int th = x.m / 7, ct = x.m % 7, tt = 4 * x.c + 2 * th + (w >> 1), nt = 2 * ct + (w & 1);
  stamp(x);
  constexpr int WN = DWN;
  u32x4 xr[2];
  const DnSrc d = dn_src(x, L);
  auto ld = [&](auto o) { dn_ld<o>(x, d, b); };
  sfor<3, WN>(ld);
  f32x16 acc[4];
  unsigned fb;
  sfor<0, 25>([&](auto i) {
    constexpr int last = i + WN - 1 < 27 ? i + WN - 1 : 27, ex = i <= 2 ? 2 : ((i >= 8 && i <= 13) ? 1 : ((i >= 21) ? 2 : 0));
    constexpr int later = [&] { int n = 0; for (int o = i + 1; o <= last; ++o) n += dn_half(o) ? 4 : 2; return n; }();
    wv<later + (dn_half(i) ? 2 : 1) + ex>();
    if constexpr (i == 3) signal(x, FL_H(L, x.c, 1), 1);
    m32a<A_WD + 8 * i, i == 0>(acc[0], b[i % WN][0]); m32a<A_WD + 8 * i + 4, i == 0>(acc[1], b[i % WN][0]);
    wv<later + ex>();
    m32a<A_WD + 8 * i, i == 0>(acc[2], b[i % WN][1]); m32a<A_WD + 8 * i + 4, i == 0>(acc[3], b[i % WN][1]);
    if constexpr (i == 7) ldflag(fb, x.rs, OFF_FLAG + FL_H(L, x.c, 1));
    if constexpr (i + WN == 20) { fstamp(x); wv<12>(); recheck(x, fb, FL_H(L, x.c, 1), 16); fstamp(x); }
    if constexpr (i + WN < 28) ld(std::integral_constant<int, i + WN>{});
    if constexpr (i + WN == 27) {
      const unsigned sr = OFF_X + (2 * L + 1) * SZ_X + (tt * 28 + 2 * nt) * KB;
      ld4<0>(xr[0], x.vl16, x.rs, sr); ld4<1024>(xr[1], x.vl16, x.rs, sr);
    }
  });
  wv<2>();
  sfor<1, 5>([&](auto t) {
    constexpr int k = t & 3;
    m32a<A_WD + 8 * 25 + 4 * (k & 1), false>(acc[k], b[25 % WN][k >> 1]);
    m32a<A_WD + 8 * 26 + 4 * (k & 1), false>(acc[k], b[26 % WN][k >> 1]);
    if constexpr (t > 1) { nop<(t == 2 ? 9 : 7)>(); red_put1<t - 1, 0>(x, acc[t - 1]); red_put1<t - 1, 1>(x, acc[t - 1]); }
    m32a<A_WD + 8 * 27 + 4 * (k & 1), false>(acc[k], b[27 % WN][k >> 1]);
    if constexpr (t > 1) { red_put1<t - 1, 2>(x, acc[t - 1]); red_put1<t - 1, 3>(x, acc[t - 1]); }
  });
  if (L == 0 && (x.m & 7) < 2) {
    ldl<4>(x.vl16, x.rs, OFF_WQ + ((16 + (x.m & 1)) * 28 + 7 * w) * KB, 7 * w * KB);
    ldl<3>(x.vl16, x.rs, OFF_WQ + ((16 + (x.m & 1)) * 28 + 7 * w + 4) * KB, (7 * w + 4) * KB);
  }
  nop<9>();
  pin(acc[0]);
  stamp(x);
  reduce_get(x, acc[0]);
  wv<0>();
  pin(xr[0]); pin(xr[1]);
  if (L > 0) {
    res_epilogue<true>(x, acc[0], xr, 0, 0, tt, nt, out);
  } else {
    res_epilogue<false>(x, acc[0], xr, OFF_X + 2 * SZ_X, OFF_ST + 2 * SZ_ST, tt, nt);
    wv<0>();
    signal(x, FL_X(x.c, th), 1);
  }
}

#define STAMP_SYNC
DI unsigned long long RT() { unsigned long long t; asm volatile("s_memrealtime %0\n\ts_waitcnt lgkmcnt(0)" : "=s"(t) :: "memory"); return t; }
__global__ __launch_bounds__(256, 1) void k_main(Args) {
  typedef unsigned long long u64x8 __attribute__((ext_vector_type(8)));
  typedef unsigned long long u64x2 __attribute__((ext_vector_type(2)));
  u64x8 pin8; u64x2 pws;
  asm volatile("s_load_dwordx16 s[8:23], %4, 0x0\n\ts_load_dwordx4 s[24:27], %4, 0x40\n\t"
               "s_getpc_b64 s[4:5]\n.Lk_begin:\n\t"
               "s_mov_b32 s6, .Lk_end-.Lk_begin\n\ts_mov_b32 s7, 0x20000\n\ts_and_b32 s5, s5, 0xffff\n\t"
               "v_lshlrev_b32 v1, 4, %2\n\t"
               "s_bfe_u32 s28, %3, 0x20003\n\ts_lshl_b32 s28, s28, 12\n\t"
               "buffer_load_dwordx4 a[112:115], v1, s[4:7], s28 offen\n\t"
               "s_bfe_u32 s28, %3, 0x30004\n\ts_lshl_b32 s28, s28, 13\n\t"
               "buffer_load_dwordx4 a[112:115], v1, s[4:7], s28 offen\n\t"
               "s_add_u32 s28, s28, 4096\n\t"
               "buffer_load_dwordx4 a[112:115], v1, s[4:7], s28 offen\n\t"
               "s_waitcnt lgkmcnt(0)"
               : "={s[8:23]}"(pin8), "={s[24:27]}"(pws) : "v"(threadIdx.x), "{s2}"(blockIdx.x), "{s[0:1]}"(__builtin_amdgcn_kernarg_segment_ptr())
               : "s4", "s5", "s6", "s7", "s28", "v1", "scc", ACLOB);
  const Args a = {(const char*)pin8[0], (const char*)pin8[1], (const char*)pin8[2], (const char*)pin8[3], (const char*)pin8[4], (const char*)pin8[5],
                  (const char*)pin8[6], (const char*)pin8[7], (char*)pws[0], (char*)pws[1]};
  Ctx x;
  x.rs = mkrs(a.ws, WS_USED);
  x.w = __builtin_amdgcn_readfirstlane(threadIdx.x >> 6);
  asm volatile("s_nop 4" : "+s"(x.w));
  { const int b = blockIdx.x; x.c = 2 * (b & 7) + (b >> 7); x.m = (b >> 3) & 15; }
  f32x16 kf[3];
  unsigned long long tm[8];
  tm[0] = RT();
  { Ctx y = x; fresh(y); stamp(y); phase_pre(y, a, kf); STAMP_SYNC }
  tm[1] = RT(); tm[2] = tm[3] = tm[4] = tm[5] = tm[6] = tm[7] = 0;
  for (int L = 0;; ++L) {
    f32x16 g2[3];
    int Lo = L; asm volatile("" : "+s"(Lo));
    { Ctx y = x; fresh(y); phase_att(y, Lo, kf, g2); stamp(y); STAMP_SYNC }
    if (L == 0) tm[2] = RT(); else tm[6] = RT();
    { Ctx y = x; fresh(y); phase_oproj(y, Lo, a, g2); stamp(y); STAMP_SYNC }
    if (L == 0) tm[3] = RT();
    { Ctx y = x; u32x4 b[DWN][2]; fresh(y); phase_gu(y, Lo, b); stamp(y); fresh(y); phase_down(y, Lo, a.out, b); stamp(y); STAMP_SYNC }
    if (L == 1) break;
    tm[4] = RT();
    { Ctx y = x; fresh(y); phase_front(y, kf); STAMP_SYNC }
    tm[5] = RT();
  }
  if (blockIdx.x == 16) { const unsigned long long t0 = RT(); while (RT() - t0 < tm[TB] - tm[TA]) {} }
  fresh(x);
  if (blockIdx.x == 0 && wave0(x)) {
    get_tag(x);
    if (x.l == 0) st2<0>(u32x2{x.tlo, x.thi}, 0, x.rs, OFF_TAG);
  }
  stamp_flush(x);
  asm volatile(".Lk_end:");
}

extern "C" void kernel_launch(void* const* d_in, const int* in_sizes, int n_in, void* d_out, int out_size, void* d_ws, size_t ws_size,
                              hipStream_t stream) {
  Args a;
  a.x = (const char*)d_in[0]; a.wq = (const char*)d_in[1]; a.wk = (const char*)d_in[2]; a.wv = (const char*)d_in[3];
  a.wo = (const char*)d_in[4]; a.wg = (const char*)d_in[6]; a.wu = (const char*)d_in[7]; a.wd = (const char*)d_in[8];
  a.ws = (char*)d_ws; a.out = (char*)d_out;
  (void)hipFuncSetAttribute((const void*)k_main, hipFuncAttributeMaxDynamicSharedMemorySize, 160 * 1024);
  k_main<<<256, 256, 160 * 1024, stream>>>(a);
}
```

```cpp
#define TA 1
#define TB 1
#include <hip/hip_runtime.h>
#include <stdint.h>
#include <type_traits>

typedef float    f32x16 __attribute__((ext_vector_type(16)));
typedef float    f32x4  __attribute__((ext_vector_type(4)));
typedef float    f32x2  __attribute__((ext_vector_type(2)));
typedef unsigned u32x4  __attribute__((ext_vector_type(4)));
typedef unsigned u32x2  __attribute__((ext_vector_type(2)));
typedef __bf16   bf16x2 __attribute__((ext_vector_type(2)));
typedef __amdgpu_buffer_rsrc_t rsrc_t;
#define DI __device__ __forceinline__
#define GAS __attribute__((address_space(1)))
#define SB() __builtin_amdgcn_sched_barrier(0)

constexpr int S = 2048, D = 448, F = 1792;
constexpr unsigned KB = 1024;
constexpr unsigned OFF_FLAG = 0;
constexpr unsigned OFF_CSQ  = 64 * KB;
constexpr unsigned OFF_CSGU = OFF_CSQ + 4 * KB;
constexpr unsigned OFF_SLOT = OFF_CSGU + 32 * KB;
constexpr unsigned OFF_TAG  = OFF_SLOT + 32 * KB;
constexpr unsigned OFF_WQ   = 160 * KB;
constexpr unsigned OFF_WO   = OFF_WQ + 18 * 28 * KB;
constexpr unsigned OFF_WGU  = OFF_WO + 14 * 32 * KB;
constexpr unsigned OFF_WD   = OFF_WGU + 16 * 196 * KB;
constexpr unsigned SZ_X = 64 * 28 * KB, SZ_ST = 14 * 2048 * 8, SZ_O = 64 * 32 * KB, SZ_H = 64 * 112 * KB, SZ_KV = 64 * 4 * KB;
constexpr unsigned OFF_X    = OFF_WD + 14 * 112 * KB;
constexpr unsigned OFF_ST   = OFF_X + 4 * SZ_X;
constexpr unsigned OFF_O    = OFF_ST + 4 * SZ_ST;
constexpr unsigned OFF_H    = OFF_O + 2 * SZ_O;
constexpr unsigned OFF_HL   = OFF_H + 2 * SZ_H;
constexpr unsigned OFF_KV   = OFF_HL + 2 * 1024 * KB;
constexpr unsigned OFF_TS   = OFF_KV + 2 * SZ_KV;
constexpr unsigned WS_USED  = OFF_TS + 520 * KB;
DI constexpr unsigned FL_X(int c, int th)         { return (c * 16 + th) * 256u; }
DI constexpr unsigned FL_O(int L, int c, int th)  { return (c * 16 + 2 + L * 2 + th) * 256u; }
DI constexpr unsigned FL_X1(int L, int c)         { return (c * 16 + 6 + L) * 256u; }
DI constexpr unsigned FL_H(int L, int c, int p)   { return (c * 16 + 8 + L * 2 + p) * 256u; }
DI constexpr unsigned FL_W(int c)                 { return (c * 16 + 12) * 256u; }
constexpr unsigned FL_W2 = 13 * 256u;

#define XAHEAD 3
#define OPWN 5
constexpr float QSCALE = 0.18898223650461362f * 1.4426950408889634f;
constexpr float M_LIMIT = 48.0f;

template <int I, int N, class Fn> DI void sfor(Fn&& f) { if constexpr (I < N) { f(std::integral_constant<int, I>{}); sfor<I + 1, N>(f); } }

DI unsigned cvtpk(float lo, float hi) { f32x2 v = {lo, hi}; bf16x2 b = __builtin_convertvector(v, bf16x2); return __builtin_bit_cast(unsigned, b); }
DI float bflo(unsigned u) { return __uint_as_float(u << 16); }
DI float bfhi(unsigned u) { return __uint_as_float(u & 0xffff0000u); }
DI float dot2(unsigned a, unsigned b, float c) { return __builtin_amdgcn_fdot2_f32_bf16(__builtin_bit_cast(bf16x2, a), __builtin_bit_cast(bf16x2, b), c, false); }
DI float sum32(float v) { auto r = __builtin_amdgcn_permlane32_swap(__float_as_uint(v), __float_as_uint(v), false, false); return __uint_as_float(r[0]) + __uint_as_float(r[1]); }
DI float sum16(float v) { auto r = __builtin_amdgcn_permlane16_swap(__float_as_uint(v), __float_as_uint(v), false, false); return __uint_as_float(r[0]) + __uint_as_float(r[1]); }
DI float max32(float v) { auto r = __builtin_amdgcn_permlane32_swap(__float_as_uint(v), __float_as_uint(v), false, false); return fmaxf(__uint_as_float(r[0]), __uint_as_float(r[1])); }

DI unsigned umax16(unsigned v) {
  v = max(v, (unsigned)__builtin_amdgcn_update_dpp(0, (int)v, 0x128, 0xf, 0xf, false));
  v = max(v, (unsigned)__builtin_amdgcn_update_dpp(0, (int)v, 0x124, 0xf, 0xf, false));
  v = max(v, (unsigned)__builtin_amdgcn_update_dpp(0, (int)v, 0x122, 0xf, 0xf, false));
  v = max(v, (unsigned)__builtin_amdgcn_update_dpp(0, (int)v, 0x121, 0xf, 0xf, false));
  return v;
}
DI unsigned umax64(unsigned v) {
  v = umax16(v);
  auto a = __builtin_amdgcn_permlane16_swap(v, v, false, false); v = max(a[0], a[1]);
  auto b = __builtin_amdgcn_permlane32_swap(v, v, false, false); return max(b[0], b[1]);
}

typedef short s16x4 __attribute__((ext_vector_type(4)));
DI u32x2 trread(const char* p) { return __builtin_bit_cast(u32x2, __builtin_amdgcn_ds_read_tr16_b64_v4i16((__attribute__((address_space(3))) s16x4*)p)); }
DI u32x4 trfrag(const char* lo, const char* hi) { const u32x2 a = trread(lo), b = trread(hi); return u32x4{a[0], a[1], b[0], b[1]}; }
DI void row_stats(float s, float q, float& mean, float& rstd) {
  mean = s * (1.0f / D);
  const float var = fmaxf(q * (1.0f / D) - mean * mean, 0.f);
  rstd = __builtin_amdgcn_rsqf(var + 1e-5f);
}
DI float fragsum(const u32x4& f, float s) {
#pragma unroll
  for (int d = 0; d < 4; ++d) s = dot2(f[d], 0x3f803f80u, s);
  return s;
}
struct Args { const char *x, *wq, *wk, *wv, *wo, *wg, *wu, *wd; char* ws; char* out; };

#define ACLOB "a0","a1","a2","a3","a4","a5","a6","a7","a8","a9","a10","a11","a12","a13","a14","a15","a16","a17","a18","a19","a20","a21","a22","a23","a24","a25","a26","a27","a28","a29","a30","a31","a32","a33","a34","a35","a36","a37","a38","a39","a40","a41","a42","a43","a44","a45","a46","a47","a48","a49","a50","a51","a52","a53","a54","a55","a56","a57","a58","a59","a60","a61","a62","a63","a64","a65","a66","a67","a68","a69","a70","a71","a72","a73","a74","a75","a76","a77","a78","a79","a80","a81","a82","a83","a84","a85","a86","a87","a88","a89","a90","a91","a92","a93","a94","a95","a96","a97","a98","a99","a100","a101","a102","a103","a104","a105","a106","a107","a108","a109","a110","a111","a112","a113","a114","a115","a116","a117","a118","a119","a120","a121","a122","a123","a124","a125","a126","a127","a128","a129","a130","a131","a132","a133","a134","a135","a136","a137","a138","a139","a140","a141","a142","a143","a144","a145","a146","a147","a148","a149","a150","a151","a152","a153","a154","a155","a156","a157","a158","a159","a160","a161","a162","a163","a164","a165","a166","a167","a168","a169","a170","a171","a172","a173","a174","a175","a176","a177","a178","a179","a180","a181","a182","a183","a184","a185","a186","a187","a188","a189","a190","a191","a192","a193","a194","a195","a196","a197","a198","a199","a200","a201","a202","a203","a204","a205","a206","a207","a208","a209","a210","a211","a212","a213","a214","a215","a216","a217","a218","a219","a220","a221","a222","a223","a224","a225","a226","a227","a228","a229","a230","a231","a232","a233","a234","a235","a236","a237","a238","a239","a240","a241","a242","a243","a244","a245","a246","a247","a248","a249","a250","a251","a252","a253","a254","a255"

template <int IMM> DI void ld4(u32x4& d, unsigned vo, rsrc_t rs, unsigned so) {
  asm volatile("buffer_load_dwordx4 %0, %1, %2, %3 offen offset:%c4 sc1" : "=v"(d) : "v"(vo), "s"(rs), "s"(so), "i"(IMM));
}
template <int IMM> DI void ld2(f32x2& d, unsigned vo, rsrc_t rs, unsigned so) {
  asm volatile("buffer_load_dwordx2 %0, %1, %2, %3 offen offset:%c4 sc1" : "=v"(d) : "v"(vo), "s"(rs), "s"(so), "i"(IMM));
}
template <int IMM> DI void ld4i(u32x4& d, unsigned vo, rsrc_t rs, unsigned so) {
  asm volatile("buffer_load_dwordx4 %0, %1, %2, %3 offen offset:%c4" : "=v"(d) : "v"(vo), "s"(rs), "s"(so), "i"(IMM));
}
template <int IMM> DI void ld2i(u32x2& d, unsigned vo, rsrc_t rs, unsigned so) {
  asm volatile("buffer_load_dwordx2 %0, %1, %2, %3 offen offset:%c4" : "=v"(d) : "v"(vo), "s"(rs), "s"(so), "i"(IMM));
}
template <int IMM> DI void ld2u(u32x2& d, unsigned vo, rsrc_t rs, unsigned so) {
  asm volatile("buffer_load_dwordx2 %0, %1, %2, %3 offen offset:%c4 sc1" : "=v"(d) : "v"(vo), "s"(rs), "s"(so), "i"(IMM));
}
template <int IMM> DI void ld4f(f32x4& d, unsigned vo, rsrc_t rs, unsigned so) {
  asm volatile("buffer_load_dwordx4 %0, %1, %2, %3 offen offset:%c4 sc1" : "=v"(d) : "v"(vo), "s"(rs), "s"(so), "i"(IMM));
}
DI void ld1(float& d, unsigned vo, rsrc_t rs, unsigned so) {
  asm volatile("buffer_load_dword %0, %1, %2, %3 offen sc1" : "=v"(d) : "v"(vo), "s"(rs), "s"(so));
}
template <int AR, int IMM> DI void lda(unsigned vo, rsrc_t rs, unsigned so) {
  asm volatile("buffer_load_dwordx4 a[%c3:%c4], %0, %1, %2 offen offset:%c5 sc1" :: "v"(vo), "s"(rs), "s"(so), "i"(AR), "i"(AR + 3), "i"(IMM) : ACLOB);
}
template <int N> DI void ldl(unsigned vo, rsrc_t rs, unsigned so, unsigned lds) {
  asm volatile("s_mov_b32 m0, %3\n\ts_nop 0\n\tbuffer_load_dwordx4 %0, %1, %2 offen sc1 lds" :: "v"(vo), "s"(rs), "s"(so), "s"(lds) : "memory");
  if constexpr (N > 1) asm volatile("buffer_load_dwordx4 %0, %1, %2 offen offset:1024 sc1 lds" :: "v"(vo), "s"(rs), "s"(so) : "memory");
  if constexpr (N > 2) asm volatile("buffer_load_dwordx4 %0, %1, %2 offen offset:2048 sc1 lds" :: "v"(vo), "s"(rs), "s"(so) : "memory");
  if constexpr (N > 3) asm volatile("buffer_load_dwordx4 %0, %1, %2 offen offset:3072 sc1 lds" :: "v"(vo), "s"(rs), "s"(so) : "memory");
}
template <int IMM> DI void st4(const u32x4& d, unsigned vo, rsrc_t rs, unsigned so) {
  asm volatile("buffer_store_dwordx4 %0, %1, %2, %3 offen offset:%c4 sc1\n\ts_nop 1" :: "v"(d), "v"(vo), "s"(rs), "s"(so), "i"(IMM) : "memory");
}
template <int IMM> DI void st4p(const u32x4& d, unsigned vo, rsrc_t rs, unsigned so) {
  asm volatile("buffer_store_dwordx4 %0, %1, %2, %3 offen offset:%c4\n\ts_nop 1" :: "v"(d), "v"(vo), "s"(rs), "s"(so), "i"(IMM) : "memory");
}
template <int IMM> DI void st2(const u32x2& d, unsigned vo, rsrc_t rs, unsigned so) {
  asm volatile("buffer_store_dwordx2 %0, %1, %2, %3 offen offset:%c4 sc1" :: "v"(d), "v"(vo), "s"(rs), "s"(so), "i"(IMM) : "memory");
}
DI void st1(float d, unsigned vo, rsrc_t rs, unsigned so) {
  asm volatile("buffer_store_dword %0, %1, %2, %3 offen sc1" :: "v"(d), "v"(vo), "s"(rs), "s"(so) : "memory");
}
template <int N> DI void wv() { asm volatile("s_waitcnt vmcnt(%c0)" :: "i"(N) : "memory"); SB(); }
template <int N> DI void wl() { asm volatile("s_waitcnt lgkmcnt(%c0)" :: "i"(N) : "memory"); SB(); }
template <class T> DI void pin(T& x) { asm volatile("" : "+v"(x)); }
DI void bar() { asm volatile("s_waitcnt lgkmcnt(0)\n\ts_barrier" ::: "memory"); }
template <int N> DI void nop() { asm volatile("s_nop %c0" :: "i"(N)); SB(); }

template <int OFF> DI void trd(u32x2& r, unsigned a) { asm volatile("ds_read_b64_tr_b16 %0, %1 offset:%c2" : "=v"(r) : "v"(a), "i"(OFF)); }
template <int OFF> DI void dsr(u32x4& r, unsigned a) { asm volatile("ds_read_b128 %0, %1 offset:%c2" : "=v"(r) : "v"(a), "i"(OFF)); }

DI void m32(f32x16& c, const u32x4& a, const u32x4& b)  { asm volatile("v_mfma_f32_32x32x16_bf16 %0, %1, %2, %0" : "+v"(c) : "v"(a), "v"(b)); }
DI void m32z(f32x16& c, const u32x4& a, const u32x4& b) { asm volatile("v_mfma_f32_32x32x16_bf16 %0, %1, %2, 0" : "=&v"(c) : "v"(a), "v"(b)); }
template <int AR, bool Z> DI void m32a(f32x16& c, const u32x4& b) {
  if constexpr (Z) asm volatile("v_mfma_f32_32x32x16_bf16 %0, a[%c2:%c3], %1, 0" : "=&v"(c) : "v"(b), "i"(AR), "i"(AR + 3));
  else             asm volatile("v_mfma_f32_32x32x16_bf16 %0, a[%c2:%c3], %1, %0" : "+v"(c) : "v"(b), "i"(AR), "i"(AR + 3));
}
template <int AR, bool Z> DI void m32b(f32x16& c, const u32x4& a) {
  if constexpr (Z) asm volatile("v_mfma_f32_32x32x16_bf16 %0, %1, a[%c2:%c3], 0" : "=&v"(c) : "v"(a), "i"(AR), "i"(AR + 3));
  else             asm volatile("v_mfma_f32_32x32x16_bf16 %0, %1, a[%c2:%c3], %0" : "+v"(c) : "v"(a), "i"(AR), "i"(AR + 3));
}
template <bool Z> DI void m16(f32x4& c, const u32x4& a, const u32x4& b) {
  if constexpr (Z) asm volatile("v_mfma_f32_16x16x32_bf16 %0, %1, %2, 0" : "=&v"(c) : "v"(a), "v"(b));
  else             asm volatile("v_mfma_f32_16x16x32_bf16 %0, %1, %2, %0" : "+v"(c) : "v"(a), "v"(b));
}

template <bool Z, int AR> DI void m16a(f32x4& c, const u32x4& a) {
  if constexpr (Z) asm volatile("v_mfma_f32_16x16x32_bf16 %0, %1, a[%c2:%c3], 0" : "=&v"(c) : "v"(a), "i"(AR), "i"(AR + 3));
  else             asm volatile("v_mfma_f32_16x16x32_bf16 %0, %1, a[%c2:%c3], %0" : "+v"(c) : "v"(a), "i"(AR), "i"(AR + 3));
}
DI void ldflag(unsigned& v, rsrc_t rs, unsigned so) { asm volatile("buffer_load_dword %0, off, %1, %2 sc1" : "=v"(v) : "s"(rs), "s"(so)); }

struct Ctx {
  rsrc_t rs;
  unsigned tlo, thi;
  unsigned vl16;
  unsigned vg16;
  int w, l, c, m;
};
DI void fresh(Ctx& x) {
  asm volatile("" : "+s"(x.w), "+s"(x.c), "+s"(x.m));
  asm volatile("v_mbcnt_lo_u32_b32 %0, -1, 0\n\tv_mbcnt_hi_u32_b32 %0, -1, %0" : "=v"(x.l));
  x.vl16 = x.l * 16;
  const int g = x.l >> 4;
  x.vg16 = ((g >> 1) * 64 + (g & 1) * 32 + (x.l & 15)) * 16;
}
extern __shared__ __attribute__((aligned(16))) char lds[];
constexpr unsigned SCR = 112 * KB;

DI void pstamp(Ctx&) {}
DI void fstamp(Ctx&) {}
DI void stamp(Ctx&) {}
DI void stamp_flush(Ctx&) {}

DI void get_tag(Ctx& x) {
  unsigned lo, hi;
  asm volatile("v_accvgpr_read_b32 v255, a252\n\ts_nop 1\n\tv_readfirstlane_b32 %0, v255\n\tv_accvgpr_read_b32 v255, a253\n\ts_nop 1\n\tv_readfirstlane_b32 %1, v255\n\ts_nop 4"
               : "=s"(lo), "=s"(hi) :: "v255");
  const unsigned long long e = ((unsigned long long)hi << 32) | lo;
  unsigned long long n = e * 0x9E3779B97F4A7C15ull + 0x632BE59BD9B4E019ull;
  if (((n ^ e) >> 16) == 0) n += 0x10000;
  x.tlo = (unsigned)n; x.thi = (unsigned)(n >> 32);
}
DI bool wave0(const Ctx& x) { int w = x.w; asm volatile("" : "+s"(w)); return w == 0; }
DI void count(Ctx& x, unsigned fl, int n) {
  if (wave0(x)) {
    const unsigned long long mask = (1ull << n) - 1;
    unsigned long long keep;
    asm volatile("s_mov_b64 %0, exec\n\ts_mov_b64 exec, %5\n\tbuffer_atomic_add %1, %2, %3, %4 offen\n\ts_mov_b64 exec, %0"
                 : "=&s"(keep) : "v"(1u), "v"(x.l * 256), "s"(x.rs), "s"(OFF_FLAG + fl), "s"(mask) : "memory");
  }
}
DI void signal(Ctx& x, unsigned fl, int n) { bar(); count(x, fl, n); }
DI void await(Ctx& x, unsigned fl, unsigned target) {
  if (wave0(x)) {
#pragma nounroll
    for (unsigned spin = 0; spin < (1u << 18); ++spin) {
      unsigned v;
      asm volatile("buffer_load_dword %0, off, %1, %2 sc1\n\ts_waitcnt vmcnt(0)" : "=v"(v) : "s"(x.rs), "s"(OFF_FLAG + fl) : "memory");
      if ((unsigned)__builtin_amdgcn_readfirstlane(v) >= target) break;
      __builtin_amdgcn_s_sleep(1);
    }
  }
  bar();
}

DI void recheck(Ctx& x, unsigned v, unsigned fl, unsigned target) {
  pin(v);
#pragma nounroll
  for (unsigned spin = 0; (unsigned)__builtin_amdgcn_readfirstlane(v) < target && spin < (1u << 18); ++spin) {
    __builtin_amdgcn_s_sleep(2);
    asm volatile("buffer_load_dword %0, off, %1, %2 sc1\n\ts_waitcnt vmcnt(0)" : "=v"(v) : "s"(x.rs), "s"(OFF_FLAG + fl) : "memory");
  }
  SB();
}

DI void reduce_put(Ctx& x, const f32x16 (&acc)[4]) {
  sfor<1, 4>([&](auto k) {
    f32x4* p = (f32x4*)(lds + SCR + ((x.w ^ k) * 3 + k - 1) * 4096 + x.vl16);
#pragma unroll
    for (int q = 0; q < 4; ++q) p[q * 64] = f32x4{acc[k][4 * q], acc[k][4 * q + 1], acc[k][4 * q + 2], acc[k][4 * q + 3]};
  });
}
template <int K, int Q> DI void red_put1(Ctx& x, const f32x16& t) {
  asm volatile("ds_write_b128 %0, %1 offset:%c2" :: "v"(SCR + ((x.w ^ K) * 3 + K - 1) * 4096 + x.vl16),
               "v"(__builtin_shufflevector(t, t, 4 * Q, 4 * Q + 1, 4 * Q + 2, 4 * Q + 3)), "i"(Q * 1024) : "memory");
}
DI void reduce_get(Ctx& x, f32x16& own) {
  bar();
  const f32x4* p = (const f32x4*)(lds + SCR + x.w * 3 * 4096 + x.vl16);
#pragma unroll
  for (int k = 0; k < 3; ++k)
#pragma unroll
    for (int q = 0; q < 4; ++q) {
      f32x4 v = p[k * 256 + q * 64];
#pragma unroll
      for (int i = 0; i < 4; ++i) own[4 * q + i] += v[i];
    }
}

constexpr int A_WO = 184, A_WD = 0, A_GX = 224;
constexpr int dn_ks(int o) { return o < 20 ? 2 * ((o >> 1) % 5 + ((o >> 1) % 5 >= 2) + 7 * (o / 10)) + (o & 1) : 2 * (((o - 20) >> 1) * 4 - ((o - 20) >> 2) + 2) + (o & 1); }
static_assert(dn_ks(0) == 0 && dn_ks(4) == 6 && dn_ks(9) == 11 && dn_ks(10) == 14 && dn_ks(19) == 25 && dn_ks(20) == 4 && dn_ks(22) == 12 && dn_ks(24) == 18 && dn_ks(27) == 27, "");
struct Two { unsigned s0, s1; };
constexpr int SL_WQ = 56;
template <int J> DI void pf_wq(Ctx& x) {
  ldl<1>(x.vl16, x.rs, OFF_WQ + ((x.m & 7) * 56 + 14 * x.w + J) * KB, (SL_WQ + 14 * x.w + J) * KB);
}
DI Two base_wo(const Ctx& x) { const int ct = x.m % 7;
  return Two{OFF_WO + ((2 * ct + (x.w & 1)) * 32 + 8 * x.w) * KB, OFF_WO + ((2 * ct + ((x.w & 1) ^ 1)) * 32 + 8 * x.w) * KB}; }
DI void pf_wo(Ctx& x) {
  const Two b = base_wo(x);
  sfor<0, 8>([&](auto i) {
    lda<A_WO + 8 * i, (i & 3) * 1024>(x.vl16, x.rs, b.s0 + (i >> 2) * 4096);
    lda<A_WO + 8 * i + 4, (i & 3) * 1024>(x.vl16, x.rs, b.s1 + (i >> 2) * 4096);
  });
}
DI Two base_wd(const Ctx& x) { const int ct = x.m % 7;
  return Two{OFF_WD + ((2 * ct + (x.w & 1)) * 112 + 28 * x.w) * KB, OFF_WD + ((2 * ct + ((x.w & 1) ^ 1)) * 112 + 28 * x.w) * KB}; }
template <int I0, int I1> DI void pf_wd(Ctx& x, const Two& b) {
  sfor<I0, I1>([&](auto f) {
    constexpr int i = f >> 1;
    lda<A_WD + 4 * f, (i & 3) * 1024>(x.vl16, x.rs, ((f & 1) ? b.s1 : b.s0) + (i >> 2) * 4096);
  });
}
DI void pf_wgu1(Ctx& x) {
  for (int t = 0; t < 14; ++t) ldl<2>(x.vl16, x.rs, OFF_WGU + (x.m * 196 + 8 * t + 2 * x.w) * KB, (8 * t + 2 * x.w) * KB);
}
template <int J> DI void pf_wgu3(Ctx& x) {
  ldl<1>(x.vl16, x.rs, OFF_WGU + (x.m * 196 + 160 + 9 * x.w + J) * KB, (9 * x.w + J) * KB);
}

constexpr unsigned S_QP = SCR, S_KP = SCR + 16384, S_QF = SCR + 24576, S_ST = SCR + 32768, S_CS = SCR + 33792, S_KM = SCR + 36864, S_TR = SCR + 40960;
DI unsigned kv_finish(Ctx& x, const float (&raw)[16], const f32x4 (&cs)[4], float mean, float rstd, bool isv, unsigned so, int slot) {
  const int h = x.l >> 5, r = x.l & 31;
  u32x4 y[2];
#pragma unroll
  for (int d = 0; d < 8; ++d)
    y[d >> 2][d & 3] = cvtpk((raw[2 * d] - mean * cs[d >> 1][2 * (d & 1)]) * rstd, (raw[2 * d + 1] - mean * cs[d >> 1][2 * (d & 1) + 1]) * rstd);
  if (h) { y[1][2] = 0; y[1][3] = 0; }
  unsigned nb = 0;
  if (!isv) {
    float n2 = 0.f;
#pragma unroll
    for (int d = 0; d < 8; ++d) n2 = dot2(y[d >> 2][d & 3], y[d >> 2][d & 3], n2);
    n2 = sum32(n2);
    nb = umax64(n2 < 3.0e38f ? __float_as_uint(n2) : 0x7f800000u);
    nb = cvtpk(1.01f * __builtin_amdgcn_sqrtf(__uint_as_float(nb)), 0.f);
    if (h) y[1][2] = 0x3f80u;
  } else {
    if (h) y[1][2] = 0x3f80u;
    char* im = lds + S_TR + slot * 2048;
#pragma unroll
    for (int a = 0; a < 4; ++a) *(u32x2*)(im + r * 64 + 16 * a + 8 * h) = u32x2{y[a >> 1][2 * (a & 1)], y[a >> 1][2 * (a & 1) + 1]};
    asm volatile("s_waitcnt lgkmcnt(0)" ::: "memory");
    const char* tb = im + (4 * h + ((x.l >> 2) & 3)) * 64 + ((x.l >> 4) & 1) * 32 + (x.l & 3) * 8;
    y[0] = trfrag(tb, tb + 512); y[1] = trfrag(tb + 1024, tb + 1536);
  }
  st4<0>(y[0], x.vl16, x.rs, so); st4<1024>(y[1], x.vl16, x.rs, so);
  return nb;
}
DI void kv_publish(Ctx& x, int L, int p, unsigned km) {
  get_tag(x);
  st2<0>(u32x2{(x.tlo & 0xffff0000u) | km, x.thi}, x.l < 16 ? x.l * 1024 : ~0u, x.rs, OFF_SLOT + (L * 2048 + p) * 8);
}
DI void q_finish(Ctx& x, const f32x16& qa, const f32x4 (&cq)[4], float mean, float rstd) {
  const int h = x.l >> 5;
  u32x4 qf[2];
  const f32x4* p = (const f32x4*)(lds + S_QP + x.w * 4096 + x.vl16);
  const float sc = rstd * QSCALE;
#pragma unroll
  for (int q = 0; q < 4; ++q) {
    const f32x4 o = p[q * 64];
    float v[4];
#pragma unroll
    for (int i = 0; i < 4; ++i) v[i] = (qa[4 * q + i] + o[i] - mean * cq[q][i]) * sc;
    qf[q >> 1][2 * (q & 1)] = cvtpk(v[0], v[1]); qf[q >> 1][2 * (q & 1) + 1] = cvtpk(v[2], v[3]);
  }
  if (h) { qf[1][2] = 0; qf[1][3] = 0; }
  float qn2 = 0.f, qn3 = 0.f;
#pragma unroll
  for (int d = 0; d < 4; ++d) { qn2 = dot2(qf[0][d], qf[0][d], qn2); qn3 = dot2(qf[1][d], qf[1][d], qn3); }
  qn2 = sum32(qn2 + qn3);
  if (h) qf[1][2] = cvtpk(1.01f * __builtin_amdgcn_sqrtf(qn2), 0.f);
  ((u32x4*)(lds + S_QF + x.w * 2048 + x.vl16))[0] = qf[0];
  ((u32x4*)(lds + S_QF + x.w * 2048 + x.vl16))[64] = qf[1];
}
DI void put_tile(char* p, const f32x16& t) {
#pragma unroll
  for (int q = 0; q < 4; ++q) ((f32x4*)p)[q * 64] = f32x4{t[4 * q], t[4 * q + 1], t[4 * q + 2], t[4 * q + 3]};
}

constexpr unsigned IMG_Q = 0, IMG_T = 64 * KB;
DI rsrc_t mkrs(const char* p, unsigned n) { return __builtin_amdgcn_make_buffer_rsrc((char*)p, 0, n, 0x00020000); }

DI void ask_slots(Ctx& x, int L) {
  if (wave0(x)) asm volatile("buffer_load_dwordx4 a[248:251], %0, %1, %2 offen sc1" :: "v"(x.vl16), "s"(x.rs), "s"(OFF_SLOT + (L * 16 + x.c) * 1024) : ACLOB);
}
DI void kv_wait(Ctx& x, int L) {
  wv<0>();
  if (wave0(x)) {
    get_tag(x);
    unsigned s0, s1, s2, s3;
#pragma nounroll
    for (unsigned spin = 0; spin < (1u << 18); ++spin) {
      asm volatile("v_accvgpr_read_b32 %0, a248\n\tv_accvgpr_read_b32 %1, a249\n\tv_accvgpr_read_b32 %2, a250\n\tv_accvgpr_read_b32 %3, a251" : "=v"(s0), "=v"(s1), "=v"(s2), "=v"(s3));
      if (__all(s1 == x.thi && s3 == x.thi && (((s0 ^ x.tlo) | (s2 ^ x.tlo)) >> 16) == 0)) break;
      __builtin_amdgcn_s_sleep(2);
      ask_slots(x, L); wv<0>();
    }
    const unsigned km = umax64(max(s0 << 16, s2 << 16));
    if (x.l == 0) ((unsigned*)(lds + S_KM))[4] = km;
  }
  bar();
}
DI void ask_kv(Ctx& x, int L, f32x16 (&kf)[3]) {
  const unsigned kvs = OFF_KV + L * SZ_KV + 16 * x.w * 4096;
  asm volatile(".irp i,0,1,2,3\n\tbuffer_load_dwordx4 v[128+4*\\i:131+4*\\i], %3, %4, %5 offen offset:1024*\\i sc1\n\t.endr\n\t"
               ".irp i,0,1,2,3\n\tbuffer_load_dwordx4 v[144+4*\\i:147+4*\\i], %3, %4, %6 offen offset:1024*\\i sc1\n\t.endr\n\t"
               ".irp i,0,1,2,3\n\tbuffer_load_dwordx4 v[160+4*\\i:163+4*\\i], %3, %4, %7 offen offset:1024*\\i sc1\n\t.endr"
               : "={v[128:143]}"(kf[0]), "={v[144:159]}"(kf[1]), "={v[160:175]}"(kf[2]) : "{v204}"(x.vl16), "s"(x.rs), "s"(kvs), "s"(kvs + 4096), "s"(kvs + 8192));
}

DI void phase_pre(Ctx& x, const Args& a, f32x16 (&kf)[3]) {
  const int w = x.w, l = x.l, h = l >> 5, r = l & 31;
  const int th = x.m >> 3, hp = x.m & 7, tl = w & 1, kh = w >> 1, tt = 4 * x.c + 2 * th + tl, wi = 2 * x.c + th;
  const bool kvp = hp < 2;
  u32x4 xf[14];
  { const rsrc_t rx = mkrs(a.x, S * D * 2);
    if (kvp) {
      sfor<0, 16>([&](auto i) {
        const int c = r ^ ((2 * i + h) & 15);
        ldl<1>(c < 28 ? h * 896 + c * 16 : 0x80000000u, rx, (tt * 32 + 2 * i) * 896 + kh * 448, (16 * w + i) * KB);
      });
    } else {
      sfor<0, 14>([&](auto i) { ld4i<32 * i>(xf[i], r * 896 + h * 16, rx, tt * 32 * 896 + kh * 448); });
    } }
  if (x.m == 2) asm volatile("buffer_load_dword a116, %0, %1, 0 offen sc1" :: "v"(l < 16 ? OFF_FLAG + (x.c * 16 + l) * 256 : OFF_SLOT + ((l >> 3) & 1) * 16384 + x.c * 1024 + (l & 7) * 128), "s"(x.rs) : ACLOB);
  asm volatile("buffer_load_dwordx2 a[252:253], off, %0, %1 sc1" :: "s"(x.rs), "s"(OFF_TAG) : ACLOB);
  pstamp(x);
  const int task = wi * 6 + hp - 2;
  const rsrc_t rq = mkrs(a.wq, D * D * 2);
  auto ask_q = [&](auto i) {
    ldl<1>((8 * w + (l >> 3)) * 896 + (l & 7) * 16, rq, hp * 112 + i * 32 * 896, IMG_Q + (4 * i + w) * KB);
  };
  auto ask_t = [&](const char* tp, unsigned tn, unsigned ts, unsigned tb) {
    const rsrc_t rt = mkrs(tp, tn);
    const unsigned vo = (8 * w + (l >> 3)) * ts + (l & 7) * 16;
    sfor<0, 7>([&](auto i) { ldl<1>(vo, rt, tb + i * 32 * ts, IMG_T + (4 * i + w) * KB); });
  };
  const int tco = ((l >> 4) & 1) * 32 + (l & 3) * 8;
  const int sro = 4 * h + ((l >> 2) & 3);
  if (kvp) ask_t(hp ? a.wv : a.wk, D * 56, 128, 0);
  else if (task < 112) ask_t(task < 56 ? a.wg : a.wu, D * F * 2, F * 2, (task & 1) * 224 * F * 2 + ((task % 56) >> 1) * 128);
  else if (task < 168) ask_t(a.wd, D * F * 2, 896, ((task - 112) / 7) * 224 * 896 + ((task - 112) % 7) * 128);
  else ask_t(a.wo, task < 182 ? D * D * 2 : 0, 896, ((task - 168) / 7) * 224 * 896 + ((task - 168) % 7) * 128);
  wv<8>();
  if (kvp) {
    sfor<0, 14>([&](auto i) { dsr<0>(xf[i], w * 16 * KB + r * 512 + (((2 * i + h) ^ (r & 15)) << 4)); });
    wl<0>();
  }
  {
    float s = 0.f, q = 0.f, s1 = 0.f, q1 = 0.f;
    sfor<0, 14>([&](auto i) {
      if (!kvp) ask_q(i);
      pin(xf[i]);
      s = dot2(xf[i][0], 0x3f803f80u, s); q = dot2(xf[i][0], xf[i][0], q); s1 = dot2(xf[i][1], 0x3f803f80u, s1); q1 = dot2(xf[i][1], xf[i][1], q1);
      s = dot2(xf[i][2], 0x3f803f80u, s); q = dot2(xf[i][2], xf[i][2], q); s1 = dot2(xf[i][3], 0x3f803f80u, s1); q1 = dot2(xf[i][3], xf[i][3], q1);
      pin(s); pin(q); pin(s1); pin(q1);
    });
    s = sum32(s + s1); q = sum32(q + q1);
    if (h == 0) ((f32x2*)(lds + S_ST))[w * 32 + r] = f32x2{s, q};
  }
  if (kvp) wv<0>(); else wv<14>();
  bar();
  stamp(x);
  if (!kvp) {
    if (task < 112) {
      const int g = l >> 4, mat = task >= 56, gt = 4 * ((task % 56) >> 1) + w, m = gt / 7, j = gt % 7, lt = (m & 1) ? j : (j == 6 ? 0 : j + 1);
      const char* tb = lds + IMG_T + (16 * (g >> 1) + 4 * (g & 1) + ((l >> 2) & 3)) * 128 + 32 * w + (l & 3) * 8;
      const unsigned st = lt < 5 ? 10 * KB : 4 * KB, so = OFF_WGU + (m * 196 + (lt < 5 ? lt * 2 + mat : 140 + (lt - 5) * 2 + mat)) * KB + (task & 1) * 7 * st;
      u32x4 f[7];
      sfor<0, 7>([&](auto i) { f[i] = trfrag(tb + i * 4096, tb + i * 4096 + 1024); });
      float cs = 0.f;
      sfor<0, 7>([&](auto i) { st4<0>(f[i], x.vl16, x.rs, so + i * st); cs = fragsum(f[i], cs); });
      cs = sum32(sum16(cs));
      st1(cs, l < 16 ? l * 4 : ~0u, x.rs, OFF_CSGU + m * 2048 + (task & 1) * 1024 + (lt * 2 + mat) * 64);
    } else if (task < 168) {
      const int nt = 2 * ((task - 112) % 7) + (w & 1), rb = (task - 112) / 7;
      const char* tb = lds + IMG_T + sro * 128 + 64 * (w & 1) + tco;
      const unsigned so = OFF_WD + (nt * 112 + 28 * (rb >> 1)) * KB, sa = so + (rb & 1) * 10 * KB, sb = so + (20 + (rb & 1) * 4) * KB;
      u32x4 f[7];
      if (w < 2) {
        sfor<0, 7>([&](auto i) { const char* p = tb + (32 * (i >> 1) + 8 * (i & 1)) * 128; f[i] = trfrag(p, p + 2048); });
        sfor<0, 7>([&](auto i) { st4<0>(f[i], x.vl16, x.rs, i < 4 ? sa + i * KB : (i < 6 ? sb + (i - 4) * KB : sa + 4 * KB)); });
      } else {
        sfor<0, 7>([&](auto i) { const char* p = tb + (32 * ((i + 7) >> 1) + 8 * ((i + 7) & 1)) * 128; f[i] = trfrag(p, p + 2048); });
        sfor<0, 7>([&](auto i) { st4<0>(f[i], x.vl16, x.rs, i < 5 ? sa + (5 + i) * KB : sb + (i - 3) * KB); });
      }
      st1(0.f, ~0u, x.rs, 0);
    } else {
      const int nt = 2 * ((task - 168) % 7) + (w & 1);
      const char* tb = lds + IMG_T + (112 * (w >> 1) + sro) * 128 + 64 * (w & 1) + tco;
      const unsigned vo = task < 182 ? x.vl16 : ~0u - 4096;
      u32x4 f[8];
      sfor<0, 8>([&](auto i) { const char* p = tb + (28 * (i >> 1) + 16 * (i & 1)) * 128; f[i] = trfrag(p, p + 1024); if ((i & 1) && h) { f[i][2] = 0; f[i][3] = 0; } });
      sfor<0, 8>([&](auto i) { st4<(i & 3) * 1024>(f[i], vo, x.rs, OFF_WO + (nt * 32 + 16 * ((task - 168) / 7) + 8 * (w >> 1) + (i & ~3)) * KB); });
    }
  }
  pstamp(x);
  if (!kvp) { wv<8>(); bar(); }
  stamp(x);
  float mean, rstd;
  { const f32x2 p0 = ((const f32x2*)(lds + S_ST))[tl * 32 + r], p1 = ((const f32x2*)(lds + S_ST))[(tl + 2) * 32 + r];
    row_stats(p0[0] + p1[0], p0[1] + p1[1], mean, rstd); }
  float* csw = (float*)(lds + S_CS);
  const int tro = 8 * h + ((l >> 2) & 3);
  u32x4 ones = {0x3f803f80u, 0x3f803f80u, 0x3f803f80u, 0x3f803f80u};
  pin(ones);
  if (kvp) {
    f32x16 kv, cs;
    const unsigned tb = IMG_T + (224 * kh + tro) * 56 + tco;
    u32x2 f[14][2];
    auto rd = [&](auto i) { trd<i * 896>(f[i][0], tb); trd<i * 896 + 224>(f[i][1], tb); };
    sfor<0, 7>(rd);
    sfor<0, 14>([&](auto i) {
      if constexpr (i + 7 < 14) { wl<12>(); rd(std::integral_constant<int, i + 7>{}); } else wl<2 * (13 - i)>();
      pin(f[i][0]); pin(f[i][1]);
      u32x4 g = {f[i][0][0], f[i][0][1], f[i][1][0], f[i][1][1]};
      pin(g); nop<0>();
      if constexpr (i == 0) { m32z(kv, g, xf[i]); m32z(cs, g, ones); } else { m32(kv, g, xf[i]); m32(cs, g, ones); }
    });
    nop<11>(); pin(kv); pin(cs);
    if (r == 0) sfor<0, 4>([&](auto q) { *(f32x4*)(csw + (w * 3 + 2) * 32 + 8 * q + 4 * h) = f32x4{cs[4 * q], cs[4 * q + 1], cs[4 * q + 2], cs[4 * q + 3]}; });
    pstamp(x);
    if (kh) put_tile(lds + S_KP + tl * 4096 + x.vl16, kv);
    bar();
    if (!kh) {
      if (x.m == 0 && w == 0) st1(0.f, l < 16 ? l * 256 : ~0u, x.rs, OFF_FLAG + x.c * 4096);
      float raw[16]; f32x4 c4[4];
      sfor<0, 4>([&](auto q) {
        const f32x4 o = ((const f32x4*)(lds + S_KP + tl * 4096 + x.vl16))[q * 64];
        c4[q] = *(const f32x4*)(csw + (w * 3 + 2) * 32 + 8 * q + 4 * h) + *(const f32x4*)(csw + ((w + 2) * 3 + 2) * 32 + 8 * q + 4 * h);
#pragma unroll
        for (int i = 0; i < 4; ++i) raw[4 * q + i] = kv[4 * q + i] + o[i];
      });
      const unsigned km = kv_finish(x, raw, c4, mean, rstd, hp == 1, OFF_KV + (tt * 4 + 2 * hp) * KB, tl);
      pstamp(x);
      wv<0>();
      kv_publish(x, 0, 2 * tt + hp, km);
      if (tt == 0 && r == 0) sfor<0, 4>([&](auto q) { st4<32 * q>(__builtin_bit_cast(u32x4, c4[q]), h * 16, x.rs, OFF_CSQ + (16 + hp) * 128); });
    } else {
      sfor<0, 28>([&](auto i) { ldl<1>((16 * tl + 8 * (i & 1) + (l >> 3)) * 896 + (l & 7) * 16, rq, hp * 112 + (i >> 1) * 32 * 896, IMG_Q + (4 * (i >> 1) + 2 * tl + (i & 1)) * KB); });
      pstamp(x);
      wv<0>();
    }
    bar();
    pstamp(x);
  } else { pstamp(x); pstamp(x); pstamp(x); }
  f32x16 qa;
  {
    f32x16 q1, q2, cs;
    const unsigned t1 = IMG_Q + (224 * kh + tro) * 128 + tco + 56 * tl, t2 = IMG_Q + (224 * kh + tro) * 128 + tco + 56 * (tl ^ 1);
    u32x2 f[14][4];
    auto rd = [&](auto i) { trd<i * 2048>(f[i][0], t1); trd<i * 2048 + 512>(f[i][1], t1); trd<i * 2048>(f[i][2], t2); trd<i * 2048 + 512>(f[i][3], t2); };
    wl<0>();
    sfor<0, 3>(rd);
    sfor<0, 14>([&](auto i) {
      if constexpr (i + 3 < 14) { wl<8>(); rd(std::integral_constant<int, i + 3>{}); } else wl<4 * (13 - i)>();
      sfor<0, 4>([&](auto e) { pin(f[i][e]); });
      u32x4 g1 = {f[i][0][0], f[i][0][1], f[i][1][0], f[i][1][1]}, g2 = {f[i][2][0], f[i][2][1], f[i][3][0], f[i][3][1]};
      pin(g1); pin(g2); nop<0>();
      if constexpr (i == 0) { m32z(q1, g1, xf[i]); m32z(q2, g2, xf[i]); m32z(cs, g1, ones); } else { m32(q1, g1, xf[i]); m32(q2, g2, xf[i]); m32(cs, g1, ones); }
      if constexpr (i == 10) { ask_slots(x, 0); SB(); }
    });
    nop<11>(); pin(q1); pin(q2); pin(cs);
    pstamp(x);
    if (r == 0) sfor<0, 4>([&](auto q) { *(f32x4*)(csw + (w * 3) * 32 + 8 * q + 4 * h) = f32x4{cs[4 * q], cs[4 * q + 1], cs[4 * q + 2], cs[4 * q + 3]}; });
    if (kh == tl) { put_tile(lds + S_QP + (w ^ 2) * 4096 + x.vl16, q2); qa = q1; } else { put_tile(lds + S_QP + (w ^ 2) * 4096 + x.vl16, q1); qa = q2; }
  }
  if (th == 0 && x.c < 14) {
    const int f = 4 * x.c + w, hd = f >= 28, ks = f - 28 * hd;
    const char* tb = lds + IMG_Q + (16 * ks + sro) * 128 + tco + 56 * hd;
    st4<0>(trfrag(tb, tb + 1024), x.vl16, x.rs, OFF_WQ + (hp * 56 + f) * KB);
  }
  if (kvp && th == 1 && x.c < 7) {
    const char* tb = lds + IMG_T + (16 * (4 * x.c + w) + sro) * 56 + tco;
    st4<0>(trfrag(tb, tb + 448), x.vl16, x.rs, OFF_WQ + ((16 + hp) * 28 + 4 * x.c + w) * KB);
  }
  pstamp(x);
  kv_wait(x, 0);
  count(x, FL_W(x.c), 1);
  ask_kv(x, 0, kf);
  stamp(x);
  const bool own = x.c == hp && th == 0;
  {
    f32x4 cq[4];
    sfor<0, 4>([&](auto q) {
      cq[q] = *(const f32x4*)(csw + (kh * 3) * 32 + 8 * q + 4 * h) + *(const f32x4*)(csw + ((kh + 2) * 3) * 32 + 8 * q + 4 * h);
      if (own && tl == 0 && r == 0) st4<32 * q>(__builtin_bit_cast(u32x4, cq[q]), h * 16, x.rs, OFF_CSQ + (2 * hp + kh) * 128);
    });
    q_finish(x, qa, cq, mean, rstd);
  }
  pstamp(x);
}

DI void phase_front(Ctx& x, f32x16 (&kf)[3]) {
  const int w = x.w, l = x.l, h = l >> 5, r = l & 31;
  const int th = x.m >> 3, hp = x.m & 7, tl = w & 1, kh = w >> 1, tt = 4 * x.c + 2 * th + tl;
  const unsigned KV = OFF_KV + SZ_KV;
  {
    f32x4 cq[4], ck[4];
    sfor<0, 4>([&](auto q) { ld4f<32 * q>(cq[q], h * 16, x.rs, OFF_CSQ + (2 * hp + kh) * 128); ld4f<32 * q>(ck[q], h * 16, x.rs, OFF_CSQ + (16 + (hp & 1)) * 128); });
    wv<0>();
    await(x, FL_X(x.c, th), 7);
    stamp(x);
    u32x4 xf[14]; f32x2 sp[7];
    {
      const unsigned so = OFF_X + 2 * SZ_X + (tt * 28 + 14 * kh) * KB;
      sfor<0, 7>([&](auto i) { ld4<(i & 3) * 1024>(xf[i], x.vl16, x.rs, so + (i >> 2) * 4096); });
      sfor<0, 7>([&](auto i) { ld2<0>(sp[i], (h * 7 * 2048 + r) * 8, x.rs, OFF_ST + 2 * SZ_ST + (i * 2048 + tt * 32) * 8); });
      sfor<7, 14>([&](auto i) { ld4<(i & 3) * 1024>(xf[i], x.vl16, x.rs, so + (i >> 2) * 4096); });
    }
    float mean, rstd;
    auto stats = [&]() {
      sfor<0, 7>([&](auto i) { pin(sp[i]); });
      sfor<0, 4>([&](auto i) { pin(cq[i]); pin(ck[i]); });
      float s = 0.f, q = 0.f;
#pragma unroll
      for (int i = 0; i < 7; ++i) { s += sp[i][0]; q += sp[i][1]; }
      row_stats(sum32(s), sum32(q), mean, rstd);
      pin(mean); pin(rstd); SB();
    };
    f32x16 qa, qb;
    u32x4 wa[14], wb[14];
    const unsigned pa = x.vl16 + (SL_WQ + 28 * kh + 14 * kh) * KB, pb = x.vl16 + (SL_WQ + 28 * (kh ^ 1) + 14 * kh) * KB;
    if (hp < 2) {
      f32x16 kv;
      sfor<0, 14>([&](auto i) { dsr<i * 1024>(wa[i], x.vl16 + 14 * kh * KB); });
      sfor<0, 14>([&](auto i) {
        wv<(i < 7 ? 20 : 13) - i>(); wl<13 - i>();
        if constexpr (i == 0) m32z(kv, wa[i], xf[i]); else m32(kv, wa[i], xf[i]);
        if constexpr (i == 7) stats();
      });
      nop<11>(); pin(kv);
      if (kh) put_tile(lds + S_KP + tl * 4096 + x.vl16, kv);
      bar();
      unsigned km = 0;
      if (!kh) {
        float raw[16];
        sfor<0, 4>([&](auto q) {
          const f32x4 o = ((const f32x4*)(lds + S_KP + tl * 4096 + x.vl16))[q * 64];
#pragma unroll
          for (int i = 0; i < 4; ++i) raw[4 * q + i] = kv[4 * q + i] + o[i];
        });
        km = kv_finish(x, raw, ck, mean, rstd, hp == 1, KV + (tt * 4 + 2 * hp) * KB, tl);
      }
      wl<0>();
      sfor<0, 7>([&](auto i) { dsr<i * 1024>(wb[i], pb); dsr<i * 1024>(wa[i], pa); });
      sfor<0, 14>([&](auto i) {
        if constexpr (i < 7) wl<12>(); else wl<2 * (13 - i)>();
        if constexpr (i == 0) { m32z(qb, wb[i], xf[i]); m32z(qa, wa[i], xf[i]); } else { m32(qb, wb[i], xf[i]); m32(qa, wa[i], xf[i]); }
        if constexpr (i < 7) { dsr<(i + 7) * 1024>(wb[i + 7], pb); dsr<(i + 7) * 1024>(wa[i + 7], pa); }
        if constexpr (i == 8) { if (!kh) { wv<0>(); kv_publish(x, 1, 2 * tt + hp, km); } SB(); }
      });
    } else {
      sfor<0, 7>([&](auto i) { dsr<i * 1024>(wb[i], pb); dsr<i * 1024>(wa[i], pa); });
      sfor<0, 14>([&](auto i) {
        wv<(i < 7 ? 20 : 13) - i>();
        if constexpr (i < 7) wl<12>(); else wl<2 * (13 - i)>();
        if constexpr (i == 0) { m32z(qb, wb[i], xf[i]); m32z(qa, wa[i], xf[i]); } else { m32(qb, wb[i], xf[i]); m32(qa, wa[i], xf[i]); }
        if constexpr (i < 7) { dsr<(i + 7) * 1024>(wb[i + 7], pb); dsr<(i + 7) * 1024>(wa[i + 7], pa); }
        if constexpr (i == 7) stats();
      });
    }
    nop<11>();
    pin(qa); pin(qb);
    put_tile(lds + S_QP + (w ^ 2) * 4096 + x.vl16, qb);
    bar();
    q_finish(x, qa, cq, mean, rstd);
    stamp(x);
    ask_slots(x, 1);
    kv_wait(x, 1);
    ask_kv(x, 1, kf);
    stamp(x);
  }
}

DI void phase_att(Ctx& x, const int L, f32x16 (&kf)[3], f32x16 (&g2)[3]) {
  const int w = x.w, l = x.l, h = l >> 5, r = l & 31;
  const int th = x.m >> 3, hp = x.m & 7, tl = w & 1, kh = w >> 1, tt = 4 * x.c + 2 * th + tl;
  const unsigned kvs = OFF_KV + L * SZ_KV + 16 * w * 4096;
  stamp(x);
  if (L == 0) bar();
  stamp(x);
  unsigned kma = S_KM + 16; asm volatile("" : "+s"(kma));
  const float kmx = *(const float*)(lds + kma);
  u32x4 q[4][2];
#pragma unroll
  for (int k = 0; k < 4; ++k) {
    q[k][0] = ((const u32x4*)(lds + S_QF + (w ^ k) * 2048 + x.vl16))[0];
    q[k][1] = ((const u32x4*)(lds + S_QF + (w ^ k) * 2048 + x.vl16))[64];
  }
  bool safe = true;
  {
#pragma unroll
    for (int k = 0; k < 4; ++k) {
      const float mrow = bflo(q[k][1][2]) * kmx + 0.01f;
      safe = safe && (!h || mrow <= M_LIMIT);
      if (h) q[k][1][2] = cvtpk(-mrow, 0.f);
    }
  }
  const bool fast = __all(safe);
  stamp(x);
  f32x16 own;
  if (fast) {
    unsigned so_kv = kvs + 3 * 4096, so_w = OFF_WGU + (x.m * 196 + 2 * w) * KB, so_l = 2 * w * KB, spin = 1u << 16, fvo = (l & 15) * 4096;
    Two ab = base_wo(x);
    const Two wd = base_wd(x);
    asm volatile(
      ".macro GRP s, pa, pb\n\t"
      "v_exp_f32 v200, v[\\s]\n\tv_exp_f32 v201, v[\\s+1]\n\tv_cvt_pk_bf16_f32 v[\\pa], v202, v203\n\t"
      "v_exp_f32 v202, v[\\s+2]\n\tv_exp_f32 v203, v[\\s+3]\n\tv_cvt_pk_bf16_f32 v[\\pb], v200, v201\n\t"
      ".endm\n\t"
      ".macro MEM kind, i, ld\n\t"
      ".if \\kind == 1\n\t"
      "buffer_load_dwordx4 v[128+16*\\ld+4*\\i:128+16*\\ld+4*\\i+3], %[vo], %[rs], %[skv] offen offset:1024*\\i sc1\n\t"
      ".elseif \\kind == 5\n\t"
      "ds_write_b128 v205, v[4*\\i:4*\\i+3] offset:1024*\\i\n\t"
      ".elseif \\kind == 6\n\t"
      "ds_write_b128 v205, v[4:7] offset:1024\n\t"
      ".elseif \\kind == 7\n\t"
      "ds_write_b128 v205, v[12:15] offset:3072\n\t"
      ".elseif \\kind == 2\n\t"
      "buffer_load_dword v206, v205, %[rs], 0 offen offset:3072 sc1\n\t"
      ".elseif \\kind == 3\n\t"
      ".if att_n < 16 || att_n >= 56\n\t"
      ".if att_n < 102\n\t"
      ".if att_n < 16\n\t.set att_r, 184+4*att_n\n\t.else\n\t.set att_r, 4*(att_n-56)\n\t.endif\n\t"
      ".if att_n & 1\n\t"
      "buffer_load_dwordx4 a[att_r:att_r+3], %[vo], %[rs], %[ab1] offen offset:1024*((att_n>>1)&3) sc1\n\t"
      ".else\n\t"
      "buffer_load_dwordx4 a[att_r:att_r+3], %[vo], %[rs], %[ab0] offen offset:1024*((att_n>>1)&3) sc1\n\t"
      ".endif\n\t"
      ".if att_n == 15\n\t"
      "s_mov_b32 %[ab0], %[wd0]\n\ts_mov_b32 %[ab1], %[wd1]\n\t"
      ".elseif (att_n & 7) == 7\n\t"
      "s_add_u32 %[ab0], %[ab0], 4096\n\ts_add_u32 %[ab1], %[ab1], 4096\n\t"
      ".endif\n\t"
      ".endif\n\t"
      ".elseif att_n < 44\n\t"
      ".if att_n & 1\n\t"
      "buffer_load_dwordx4 %[vo], %[rs], %[sw] offen offset:1024 sc1 lds\n\t"
      "s_add_u32 %[sw], %[sw], 8192\n\ts_add_u32 m0, m0, 8192\n\t"
      ".else\n\t"
      "buffer_load_dwordx4 %[vo], %[rs], %[sw] offen sc1 lds\n\t"
      ".endif\n\t"
      ".else\n\t"
      "buffer_load_dwordx4 v[208+4*(att_n-44):208+4*(att_n-44)+3], %[vo], %[rs], %[sw] offen offset:1024*(att_n&1) sc1\n\t"
      ".if att_n & 1\n\ts_add_u32 %[sw], %[sw], 8192\n\t.endif\n\t"
      ".endif\n\t"
      ".set att_n, att_n+1\n\t"
      ".endif\n\t"
      ".endm\n\t"
      ".macro STEP sc, sn, k, nk, pk, tk, nt, pt, ld, m0k, m0i, m1k, m2k, m2i, m3k\n\t"
      "v_mfma_f32_32x32x16_bf16 v[\\sn:\\sn+15], v[128+16*\\nt:128+16*\\nt+3], v[64+8*\\nk:64+8*\\nk+3], 0\n\t"
      "MEM \\m0k, \\m0i, \\ld\n\t"
      "GRP \\sc, 199, 192\n\t"
      "v_mfma_f32_32x32x16_bf16 v[\\sn:\\sn+15], v[132+16*\\nt:132+16*\\nt+3], v[68+8*\\nk:68+8*\\nk+3], v[\\sn:\\sn+15]\n\t"
      "MEM \\m1k, 0, 0\n\t"
      "GRP \\sc+4, 193, 194\n\t"
      "v_mfma_f32_32x32x16_bf16 v[16*\\pk:16*\\pk+15], v[140+16*\\pt:140+16*\\pt+3], v[196:199], v[16*\\pk:16*\\pk+15]\n\t"
      "MEM \\m2k, \\m2i, \\ld\n\t"
      "GRP \\sc+8, 195, 196\n\t"
      "v_mfma_f32_32x32x16_bf16 v[16*\\k:16*\\k+15], v[136+16*\\tk:136+16*\\tk+3], v[192:195], v[16*\\k:16*\\k+15]\n\t"
      "MEM \\m3k, 0, 0\n\t"
      "GRP \\sc+12, 197, 198\n\t"
      ".endm\n\t"
      ".macro TILE tk, nx, pv, on, wait, p, fl=0\n\t"
      "STEP 96, 112, 0, 1, 3, \\tk, \\tk, \\pv, \\pv, \\p, 0, \\p, \\p, 0, \\fl\n\t"
      "STEP 112, 96, 1, 2, 0, \\tk, \\tk, \\tk, \\pv, \\on, 0, \\p, \\on, 1, 0\n\t"
      "STEP 96, 112, 2, 3, 1, \\tk, \\tk, \\tk, \\pv, \\on, 2, \\p, \\on, 3, 0\n\t"
      ".if \\on\n\ts_add_u32 %[skv], %[skv], 4096\n\t.endif\n\t"
      "s_waitcnt vmcnt(\\wait)\n\t"
      "STEP 112, 96, 3, 0, 2, \\tk, \\nx, \\tk, \\pv, \\p, 0, \\p, \\p, 0, 0\n\t"
      ".endm\n\t"
      ".set att_n, 0\n\t"
      "s_mov_b32 m0, %[sl]\n\t"
      "v_mov_b32 v196, 0\n\tv_mov_b32 v197, 0\n\tv_mov_b32 v198, 0\n\tv_mov_b32 v199, 0\n\tv_mov_b32 v202, 0\n\tv_mov_b32 v203, 0\n\t"
      "v_mov_b32 v188, 0\n\tv_mov_b32 v189, 0\n\tv_mov_b32 v190, 0\n\tv_mov_b32 v191, 0\n\t"
      "v_mfma_f32_32x32x16_bf16 v[0:15], v[196:199], v[196:199], 0\n\t"
      "v_mfma_f32_32x32x16_bf16 v[16:31], v[196:199], v[196:199], 0\n\t"
      "v_mfma_f32_32x32x16_bf16 v[32:47], v[196:199], v[196:199], 0\n\t"
      "v_mfma_f32_32x32x16_bf16 v[48:63], v[196:199], v[196:199], 0\n\t"
      "s_waitcnt vmcnt(8)\n\t"
      "v_mfma_f32_32x32x16_bf16 v[96:111], v[128:131], v[64:67], 0\n\t"
      "v_mfma_f32_32x32x16_bf16 v[96:111], v[132:135], v[68:71], v[96:111]\n\t"
      "s_nop 11\n\t"
      "TILE 0, 1, 3, 1, 8, 0\n\t"
      "TILE 1, 2, 0, 1, 8, 0\n\tTILE 2, 3, 1, 1, 9, 0, 2\n\t"
      "s_waitcnt vmcnt(4)\n\t"
      "1:\n\t"
      "v_cmp_gt_u32 vcc, 16, v206\n\t"
      "s_and_b64 vcc, exec, vcc\n\t"
      "s_cbranch_vccz 2f\n\t"
      "s_sleep 24\n\t"
      "buffer_load_dword v206, v205, %[rs], 0 offen offset:3072 sc1\n\t"
      "s_sub_u32 %[spin], %[spin], 1\n\t"
      "s_waitcnt vmcnt(0)\n\t"
      "s_cbranch_scc0 1b\n\t"
      "2:\n\t"
      "s_barrier\n\t"
      "TILE 3, 0, 2, 1, 14, 3\n\t"
      "TILE 0, 1, 3, 1, 21, 3\n\tTILE 1, 2, 0, 1, 24, 3\n\tTILE 2, 3, 1, 1, 24, 3\n\tTILE 3, 0, 2, 1, 24, 3\n\t"
      "TILE 0, 1, 3, 1, 24, 3\n\tTILE 1, 2, 0, 1, 24, 3\n\tTILE 2, 3, 1, 1, 24, 3\n\tTILE 3, 0, 2, 1, 24, 3\n\t"
      "TILE 0, 1, 3, 1, 24, 3\n\tTILE 1, 2, 0, 0, 20, 3\n\tTILE 2, 3, 1, 0, 16, 3\n\t"
      "v_add_u32 v205, %[d1], v204\n\tv_add_u32 v206, %[d2], v204\n\t"
      "STEP 96, 112, 0, 1, 3, 3, 3, 2, 0, 3, 0, 3, 3, 0, 3\n\t"
      "STEP 112, 96, 1, 2, 0, 3, 3, 3, 0, 3, 0, 3, 0, 0, 0\n\t"
      "STEP 96, 112, 2, 3, 1, 3, 3, 3, 0, 5, 0, 6, 5, 2, 7\n\t"
      "ds_write_b128 v206, v[16:19]\n\t"
      "GRP 112, 199, 192\n\t"
      "ds_write_b128 v206, v[20:23] offset:1024\n\t"
      "GRP 116, 193, 194\n\t"
      "v_mfma_f32_32x32x16_bf16 v[32:47], v[188:191], v[196:199], v[32:47]\n\t"
      "ds_write_b128 v206, v[24:27] offset:2048\n\t"
      "GRP 120, 195, 196\n\t"
      "v_mfma_f32_32x32x16_bf16 v[48:63], v[184:187], v[192:195], v[48:63]\n\t"
      "ds_write_b128 v206, v[28:31] offset:3072\n\t"
      "GRP 124, 197, 198\n\t"
      "v_add_u32 v200, %[d3], v204\n\t"
      "v_cvt_pk_bf16_f32 v199, v202, v203\n\t"
      "s_nop 1\n\t"
      "v_mfma_f32_32x32x16_bf16 v[48:63], v[188:191], v[196:199], v[48:63]\n\t"
      "ds_write_b128 v200, v[32:35]\n\tds_write_b128 v200, v[36:39] offset:1024\n\tds_write_b128 v200, v[40:43] offset:2048\n\tds_write_b128 v200, v[44:47] offset:3072\n\t"
      "s_nop 7\n\t"
      ".purgem GRP\n\t.purgem MEM\n\t.purgem STEP\n\t.purgem TILE"
      : "={v[48:63]}"(own), "+{v[128:143]}"(kf[0]), "+{v[144:159]}"(kf[1]), "+{v[160:175]}"(kf[2]),
        "={v[208:223]}"(g2[0]), "={v[224:239]}"(g2[1]), "={v[240:255]}"(g2[2]),
        [skv] "+s"(so_kv), [sw] "+s"(so_w), [ab0] "+s"(ab.s0), [ab1] "+s"(ab.s1), [spin] "+s"(spin), "+{v205}"(fvo)
      : "{v[64:67]}"(q[1][0]), "{v[68:71]}"(q[1][1]), "{v[72:75]}"(q[2][0]), "{v[76:79]}"(q[2][1]),
        "{v[80:83]}"(q[3][0]), "{v[84:87]}"(q[3][1]), "{v[88:91]}"(q[0][0]), "{v[92:95]}"(q[0][1]),
        [vo] "{v204}"(x.vl16), [d1] "s"(SCR + (w ^ 1) * 3 * 4096), [d2] "s"(SCR + ((w ^ 2) * 3 + 1) * 4096), [d3] "s"(SCR + ((w ^ 3) * 3 + 2) * 4096), [rs] "s"(x.rs), [sl] "s"(so_l), [wd0] "s"(wd.s0), [wd1] "s"(wd.s1)
      : "memory", "scc", "vcc",
        "v0","v1","v2","v3","v4","v5","v6","v7","v8","v9","v10","v11","v12","v13","v14","v15","v16","v17","v18","v19","v20","v21","v22","v23",
        "v24","v25","v26","v27","v28","v29","v30","v31","v32","v33","v34","v35","v36","v37","v38","v39","v40","v41","v42","v43","v44","v45","v46","v47",
        "v96","v97","v98","v99","v100","v101","v102","v103","v104","v105","v106","v107","v108","v109","v110","v111",
        "v112","v113","v114","v115","v116","v117","v118","v119","v120","v121","v122","v123","v124","v125","v126","v127",
        "v176","v177","v178","v179","v180","v181","v182","v183","v184","v185","v186","v187","v188","v189","v190","v191",
        "v192","v193","v194","v195","v196","v197","v198","v199","v200","v201","v202","v203","v206", ACLOB);
  } else {
    f32x16 o[4];
    wv<0>();
    pin(kf[0]); pin(kf[1]); pin(kf[2]);
    float mx[4];
#pragma unroll
    for (int k = 0; k < 4; ++k) {
      mx[k] = -3.0e38f; if (h) q[k][1][2] = 0; pin(q[k][1]);
#pragma unroll
      for (int i = 0; i < 16; ++i) o[k][i] = 0.f;
    }
#pragma nounroll
    for (unsigned spin = 0; spin < (1u << 16); ++spin) {
      float v;
      ld1(v, (l & 15) * 4096, x.rs, OFF_FLAG + FL_W(0)); wv<0>(); pin(v);
      if (__all(__float_as_uint(v) >= 16u)) break;
      __builtin_amdgcn_s_sleep(24);
    }
    pf_wo(x);
    pf_wgu1(x);
    sfor<0, 12>([&](auto j) {
      u32x4 t;
      ld4<1024 * (j & 1)>(t, x.vl16, x.rs, OFF_WGU + (x.m * 196 + 112 + 8 * (j >> 1) + 2 * w) * KB); wv<0>(); pin(t);
#pragma unroll
      for (int i = 0; i < 4; ++i) g2[j >> 2][4 * (j & 3) + i] = __uint_as_float(t[i]);
    });
    pf_wd<0, 46>(x, base_wd(x));
    for (int tk = 0; tk < 16; ++tk) {
      u32x4 kf[4];
      sfor<0, 4>([&](auto i) { ld4<i * 1024>(kf[i], x.vl16, x.rs, kvs + tk * 4096); });
      wv<0>();
      sfor<0, 4>([&](auto k) {
        f32x16 s;
        nop<1>();
        m32z(s, kf[0], q[k][0]); m32(s, kf[1], q[k][1]);
        nop<11>(); pin(s);
        float tm = s[0];
#pragma unroll
        for (int i = 1; i < 16; ++i) tm = fmaxf(tm, s[i]);
        tm = max32(tm);
        const float mn = fmaxf(mx[k], tm), al = __builtin_amdgcn_exp2f(mx[k] - mn);
        mx[k] = mn;
#pragma unroll
        for (int i = 0; i < 16; ++i) o[k][i] *= al;
        u32x4 p0, p1;
#pragma unroll
        for (int d = 0; d < 4; ++d) {
          p0[d] = cvtpk(__builtin_amdgcn_exp2f(s[2 * d] - mn), __builtin_amdgcn_exp2f(s[2 * d + 1] - mn));
          p1[d] = cvtpk(__builtin_amdgcn_exp2f(s[8 + 2 * d] - mn), __builtin_amdgcn_exp2f(s[9 + 2 * d] - mn));
        }
        pin(p0); pin(p1); pin(o[k]);
        nop<1>();
        m32(o[k], kf[2], p0); m32(o[k], kf[3], p1);
        nop<11>(); pin(o[k]);
      });
    }
    bar();
    float* mm = (float*)(lds + SCR);
#pragma unroll
    for (int k = 0; k < 4; ++k) if (h == 0) mm[((w ^ k) * 4 + w) * 32 + r] = mx[k];
    bar();
#pragma unroll
    for (int k = 0; k < 4; ++k) {
      const float* p = mm + (w ^ k) * 128 + r;
      const float g = fmaxf(fmaxf(p[0], p[32]), fmaxf(p[64], p[96])), f = __builtin_amdgcn_exp2f(mx[k] - g);
#pragma unroll
      for (int i = 0; i < 16; ++i) o[k][i] *= f;
    }
    bar();
    reduce_put(x, o);
    own = o[0];
  }
  stamp(x);
  reduce_get(x, own);
  pin(own); fstamp(x);
  {
    auto lr = __builtin_amdgcn_permlane32_swap(__float_as_uint(own[12]), __float_as_uint(own[12]), false, false);
    const float inv = __builtin_amdgcn_rcpf(__uint_as_float(lr[1]));
    u32x4 a, b;
#pragma unroll
    for (int d = 0; d < 4; ++d) { a[d] = cvtpk(own[2 * d] * inv, own[2 * d + 1] * inv); b[d] = cvtpk(own[8 + 2 * d] * inv, own[9 + 2 * d] * inv); }
    if (h) { b[2] = 0; b[3] = 0; }
    const unsigned so = OFF_O + L * SZ_O + (tt * 32 + 2 * (2 * hp + kh)) * KB;
    st4<0>(a, x.vl16, x.rs, so); st4<1024>(b, x.vl16, x.rs, so);
  }
  fstamp(x);
  wv<0>();
  fstamp(x);
  signal(x, FL_O(L, x.c, th), 1);
  if (L == 0 && th == 0) {
    if (x.c == hp) count(x, FL_W2, 1);
  }
}

template <bool FINAL> DI void res_epilogue(Ctx& x, const f32x16& acc, const u32x4 (&xr)[2], unsigned Xo, unsigned STo, int tt, int nt, char* out = nullptr) {
  const int h = x.l >> 5, r = x.l & 31;
  u32x4 y[2];
#pragma unroll
  for (int d = 0; d < 8; ++d) y[d >> 2][d & 3] = cvtpk(acc[2 * d] + bflo(xr[d >> 2][d & 3]), acc[2 * d + 1] + bfhi(xr[d >> 2][d & 3]));
  if constexpr (FINAL) {
#pragma unroll
    for (int s = 0; s < 2; ++s) {
      auto a = __builtin_amdgcn_permlane32_swap(y[s][0], y[s][2], false, false);
      auto b = __builtin_amdgcn_permlane32_swap(y[s][1], y[s][3], false, false);
      y[s] = u32x4{a[0], b[0], a[1], b[1]};
    }
    const unsigned vo = r * (D * 2) + h * 16, so = tt * 32 * D * 2 + nt * 64;
    const rsrc_t ro = mkrs(out, S * D * 2);
    st4p<0>(y[0], vo, ro, so); st4p<32>(y[1], vo, ro, so);
  } else {
    const unsigned so = Xo + (tt * 28 + 2 * nt) * KB;
    st4<0>(y[0], x.vl16, x.rs, so); st4<1024>(y[1], x.vl16, x.rs, so);
    float s = 0.f, q = 0.f, s1 = 0.f, q1 = 0.f;
#pragma unroll
    for (int d = 0; d < 4; ++d) {
      s = dot2(y[0][d], 0x3f803f80u, s); q = dot2(y[0][d], y[0][d], q); s1 = dot2(y[1][d], 0x3f803f80u, s1); q1 = dot2(y[1][d], y[1][d], q1);
    }
    s = sum32(s + s1); q = sum32(q + q1);
    if (h == 0) st2<0>(u32x2{__float_as_uint(s), __float_as_uint(q)}, r * 8, x.rs, STo + (nt * 2048 + tt * 32) * 8);
  }
}

DI void phase_oproj(Ctx& x, const int L, const Args& a, const f32x16 (&g2)[3]) {
  const int w = x.w;
  if (x.m < 14) {
    const int th = x.m / 7, ct = x.m % 7, tt0 = 4 * x.c + 2 * th, tt = tt0 + (w >> 1), nt = 2 * ct + (w & 1);
    u32x2 x0[4];
    if (L == 0) {
      const rsrc_t rx = mkrs(a.x, S * D * 2);
      sfor<0, 4>([&](auto i) { ld2i<16 * i>(x0[i], (x.l & 31) * 896 + (x.l >> 5) * 8, rx, tt * 32 * 896 + nt * 64); });
    } else {
      sfor<0, 4>([&](auto i) { ld2u<(i >> 1) * 1024 + (i & 1) * 8>(x0[i], x.vl16, x.rs, OFF_X + 2 * SZ_X + (tt * 28 + 2 * nt) * KB); });
    }
    await(x, FL_O(L, x.c, th), 8);
    stamp(x);
    u32x4 b[8][2];
    const unsigned s0 = OFF_O + L * SZ_O + (tt * 32 + 8 * w) * KB, s1 = OFF_O + L * SZ_O + ((tt0 + ((w >> 1) ^ 1)) * 32 + 8 * w) * KB;
    auto ld = [&](auto i) { ld4<(i & 3) * 1024>(b[i][0], x.vl16, x.rs, s0 + (i >> 2) * 4096); ld4<(i & 3) * 1024>(b[i][1], x.vl16, x.rs, s1 + (i >> 2) * 4096); };
    constexpr int WN = OPWN;
    sfor<0, WN>(ld);
    f32x16 acc[4];
    sfor<0, 8>([&](auto i) {
      constexpr int last = (i + WN - 1 < 7 ? i + WN - 1 : 7);
      wv<2 * (last - i) + 1>();
      m32a<A_WO + 8 * i, i == 0>(acc[0], b[i][0]); m32a<A_WO + 8 * i + 4, i == 0>(acc[1], b[i][0]);
      wv<2 * (last - i)>();
      m32a<A_WO + 8 * i, i == 0>(acc[2], b[i][1]); m32a<A_WO + 8 * i + 4, i == 0>(acc[3], b[i][1]);
      if constexpr (i + WN < 8) ld(std::integral_constant<int, i + WN>{});
    });
    nop<11>();
    sfor<0, 4>([&](auto k) { pin(acc[k]); pin(x0[k]); });
    stamp(x);
    reduce_put(x, acc); reduce_get(x, acc[0]);
    pin(acc[0]); fstamp(x);
    const u32x4 x2[2] = {u32x4{x0[0][0], x0[0][1], x0[1][0], x0[1][1]}, u32x4{x0[2][0], x0[2][1], x0[3][0], x0[3][1]}};
    res_epilogue<false>(x, acc[0], x2, OFF_X + (2 * L + 1) * SZ_X, OFF_ST + (2 * L + 1) * SZ_ST, tt, nt);
    fstamp(x);
    wv<0>();
    fstamp(x);
    signal(x, FL_X1(L, x.c), 1);
    fstamp(x);
  } else {
    stamp(x); stamp(x); fstamp(x); fstamp(x); fstamp(x); fstamp(x);
  }
#pragma unroll
  for (int j = 0; j < 12; ++j)
    *(f32x4*)(lds + (112 + 8 * (j >> 1) + 2 * w + (j & 1)) * KB + x.vl16) = f32x4{g2[j >> 2][4 * (j & 3)], g2[j >> 2][4 * (j & 3) + 1], g2[j >> 2][4 * (j & 3) + 2], g2[j >> 2][4 * (j & 3) + 3]};
}

constexpr int DWN = 7;
constexpr bool dn_half(int o) { return o == 4 || o == 5 || o == 14 || o == 15; }
struct DnSrc { unsigned s0, s1, l0, l1; };
DI DnSrc dn_src(const Ctx& x, int L) {
  const int tt0 = 4 * x.c + 2 * ((x.m / 7) & 1), tt = tt0 + (x.w >> 1), tto = tt0 + ((x.w >> 1) ^ 1);
  return DnSrc{OFF_H + L * SZ_H + (tt * 112 + 28 * x.w) * KB, OFF_H + L * SZ_H + (tto * 112 + 28 * x.w) * KB,
               OFF_HL + ((L * 64 + tt) * 16 + 4 * x.w) * KB, OFF_HL + ((L * 64 + tto) * 16 + 4 * x.w) * KB};
}
template <int O> DI void dn_ld(Ctx& x, const DnSrc& d, u32x4 (&b)[DWN][2]) {
  constexpr int i = dn_ks(O);
  if constexpr (dn_half(O)) {
    constexpr int imm = (O >= 14 ? 2048 : 0) + (O & 1) * 512;
    u32x2 lo, hi, lo1, hi1;
    ld2u<imm>(lo, x.l * 8, x.rs, d.l0); ld2u<imm + 1024>(hi, x.l * 8, x.rs, d.l0);
    ld2u<imm>(lo1, x.l * 8, x.rs, d.l1); ld2u<imm + 1024>(hi1, x.l * 8, x.rs, d.l1);
    b[O % DWN][0] = u32x4{lo[0], lo[1], hi[0], hi[1]}; b[O % DWN][1] = u32x4{lo1[0], lo1[1], hi1[0], hi1[1]};
  } else {
    ld4<(i & 3) * 1024>(b[O % DWN][0], x.vl16, x.rs, d.s0 + (i >> 2) * 4096); ld4<(i & 3) * 1024>(b[O % DWN][1], x.vl16, x.rs, d.s1 + (i >> 2) * 4096);
  }
}

DI void gu_tile(const f32x4 (&acc)[2][2], const u32x4 (&cs)[2], const float (&mean)[2], const float (&r2)[2], u32x2 (&hp)[2]) {
  const f32x4 cg = __builtin_bit_cast(f32x4, cs[0]), cu = __builtin_bit_cast(f32x4, cs[1]);
#pragma unroll
  for (int u = 0; u < 2; ++u) {
    const f32x2 nm = {-mean[u], -mean[u]}, rr = {r2[u], r2[u]};
#pragma unroll
    for (int e = 0; e < 2; ++e) {
      const f32x2 g = __builtin_elementwise_fma(nm, f32x2{cg[2 * e], cg[2 * e + 1]}, f32x2{acc[0][u][2 * e], acc[0][u][2 * e + 1]});
      const f32x2 v = __builtin_elementwise_fma(nm, f32x2{cu[2 * e], cu[2 * e + 1]}, f32x2{acc[1][u][2 * e], acc[1][u][2 * e + 1]});
      const f32x2 y = g * rr * v;
      hp[u][e] = cvtpk(y[0], y[1]);
    }
  }
}

DI void phase_gu(Ctx& x, const int L, u32x4 (&b)[DWN][2]) {
  const int w = x.w, l = x.l, g = l >> 4, c16 = l & 15, m = x.m, tt = 4 * x.c + w;
  if (L == 0 && wave0(x)) { unsigned v; ldflag(v, x.rs, OFF_FLAG + FL_W2); wv<0>(); recheck(x, v, FL_W2, 8); }
  await(x, FL_X1(L, x.c), 14);
  stamp(x);
  constexpr int NXV = 11;
  u32x4 xf[NXV][2]; f32x2 sp[2][4];
  const unsigned sx = OFF_X + (2 * L + 1) * SZ_X + tt * 28 * KB, st = OFF_ST + (2 * L + 1) * SZ_ST + tt * 32 * 8;
  auto ldx = [&](auto k, auto u) {
    if constexpr (k < NXV) ld4<(k & 1) * 2048 + 256 * u>(xf[k][u], x.vg16, x.rs, sx + (k >> 1) * 4096);
    else if constexpr (k < 14) lda<A_GX + 8 * (k - NXV) + 4 * u, (k & 1) * 2048 + 256 * u>(x.vg16, x.rs, sx + (k >> 1) * 4096);
    else { constexpr int i = k - 14; ld2<128 * u>(sp[u][i], (((i < 3 || g < 2) ? g + 4 * i : g) * 2048 + c16) * 8, x.rs, st); }
  };
  using I0 = std::integral_constant<int, 0>; using I1 = std::integral_constant<int, 1>;
  constexpr int XA = XAHEAD;
  sfor<0, XA>([&](auto k) { ldx(k, I0{}); ldx(k, I1{}); });
  const unsigned a0 = x.vl16, a1 = x.vl16 + 65536, a2 = x.vl16 + 131072;
  constexpr int DP = 6;
  u32x4 wf[8];
  auto rd = [&](auto ii) {
    constexpr int i = ii, slot = i >= 160 ? i - 160 : i;
    if constexpr (slot < 64) dsr<slot * 1024>(wf[i & 7], a0); else if constexpr (slot < 128) dsr<(slot - 64) * 1024>(wf[i & 7], a1); else dsr<(slot - 128) * 1024>(wf[i & 7], a2);
  };
  auto mm = [&](f32x4& c, const u32x4& a, auto k, auto u) {
    if constexpr (k < NXV) m16<k == 0>(c, a, xf[k][u]); else m16a<false, A_GX + 8 * (k - NXV) + 4 * u>(c, a);
  };
  const Two wd = base_wd(x);
  f32x4 aa[5][2][2], ch0, ch1;
  float mean[2], r2[2];
  sfor<0, DP>(rd);
  sfor<0, 140>([&](auto i) {
    constexpr int KS = i / 10, s = i % 10, t = s >> 1, mat = i & 1;
    using K = std::integral_constant<int, KS>;
    constexpr auto side = [](int k) { return (k >= 0 && k <= 3) || (k >= 5 && k <= 12); };
    constexpr auto all = [](int k) { return (k + XA < 14 ? 2 : 0) + ((k >= 0 && k <= 3) || (k >= 5 && k <= 9) ? 2 : 0) + (k == 5 ? 1 : 0) + (k >= 10 ? 3 : 0); };
    if constexpr (s == 0) {
      constexpr int cnt = [&] { int n = side(KS - XA) ? 1 : 0; for (int k = KS - XA + 1; k < KS; ++k) n += all(k); return n; }();
      wv<cnt>();
      if constexpr (KS == 5) asm volatile("s_barrier" ::: "memory");
    }
    if constexpr ((i & 1) == 0) { rd(std::integral_constant<int, i + DP>{}); rd(std::integral_constant<int, i + DP + 1>{}); wl<DP>(); }
    mm(aa[t][mat][0], wf[i & 7], K{}, I0{});
    if constexpr (s == 1 && KS + XA < 14) ldx(std::integral_constant<int, KS + XA>{}, I0{});
    if constexpr (s == 6 && KS + XA < 14) ldx(std::integral_constant<int, KS + XA>{}, I1{});
    if constexpr (KS <= 3 && s == 3) ldx(std::integral_constant<int, 14 + KS>{}, I0{});
    if constexpr (KS <= 3 && s == 8) ldx(std::integral_constant<int, 14 + KS>{}, I1{});
    if constexpr (KS == 5 && s == 3) asm volatile("buffer_load_dwordx4 %0, %2, %3, %4 offen sc1\n\tbuffer_load_dwordx4 %1, %2, %3, %4 offen offset:1024 sc1" : "=&v"(ch0), "=&v"(ch1) : "v"(x.vl16), "s"(x.rs), "s"(OFF_CSGU + m * 2048));
    if constexpr (KS == 11 && s == 2) { pin(ch0); pin(ch1); *(f32x4*)(lds + 40 * KB + x.vl16) = ch0 + ch1; }
    if constexpr (KS >= 5 && KS <= 9 && (s == 3 || s == 8) && !(KS == 5 && s == 3)) pf_wgu3<2 * (KS - 5) + (s == 8) - 1>(x);
    if constexpr (KS >= 10 && (s == 3 || s == 5 || s == 8) && 3 * (KS - 10) + (s == 3 ? 0 : (s == 5 ? 1 : 2)) < 10) {
      constexpr int f = 46 + 3 * (KS - 10) + (s == 3 ? 0 : (s == 5 ? 1 : 2));
      pf_wd<f, f + 1>(x, wd);
    }
    if constexpr ((KS == 10 || KS == 12) && s == 2) {
      constexpr int u = KS == 12;
      sfor<0, 4>([&](auto e) { pin(sp[u][e]); });
      float sm = sp[u][0][0] + sp[u][1][0] + sp[u][2][0], q = sp[u][0][1] + sp[u][1][1] + sp[u][2][1];
      if (g < 2) { sm += sp[u][3][0]; q += sp[u][3][1]; }
      sm = sum32(sum16(sm)); q = sum32(sum16(q));
      float rs; row_stats(sm, q, mean[u], rs); r2[u] = rs * rs;
      pin(mean[u]); pin(r2[u]);
    }
    mm(aa[t][mat][1], wf[i & 7], K{}, I1{}); SB();
  });
  fstamp(x);
  u32x4 cs[7][2];
  const unsigned csa = 40 * KB + g * 16;
  sfor<0, 10>([&](auto i) { dsr<64 * i>(cs[i >> 1][i & 1], csa); });
  wv<10>();
  asm volatile("s_barrier" ::: "memory");
  stamp(x);
  const unsigned H = OFF_H + L * SZ_H + tt * 112 * KB;
  const int gp = (m & 1) ? 7 * m + 1 : 7 * m;
  auto pair = [&](const f32x4 (&t0)[2][2], const f32x4 (&t1)[2][2], auto lt) {
    u32x2 h0[2], h1[2];
    gu_tile(t0, cs[lt], mean, r2, h0); gu_tile(t1, cs[lt + 1], mean, r2, h1);
    st4<0>(u32x4{h0[0][0], h0[0][1], h1[0][0], h1[0][1]}, x.vg16, x.rs, H + (gp + lt - 1) * 1024);
    st4<256>(u32x4{h0[1][0], h0[1][1], h1[1][0], h1[1][1]}, x.vg16, x.rs, H + (gp + lt - 1) * 1024);
  };
  {
    wl<0>();
    sfor<0, 20>([&](auto i) { pin(aa[i >> 2][(i >> 1) & 1][i & 1]); });
    sfor<0, 10>([&](auto i) { pin(cs[i >> 1][i & 1]); });
    u32x2 hl[2];
    gu_tile(aa[0], cs[0], mean, r2, hl);
    const unsigned sl = OFF_HL + ((L * 64 + tt) * 16 + m) * KB;
    st2<0>(hl[0], x.vg16 >> 1, x.rs, sl); st2<128>(hl[1], x.vg16 >> 1, x.rs, sl);
    pair(aa[1], aa[2], std::integral_constant<int, 1>{});
    pair(aa[3], aa[4], std::integral_constant<int, 3>{});
  }
  SB();
  sfor<10, 14>([&](auto i) { dsr<64 * i>(cs[i >> 1][i & 1], csa); });
  fstamp(x);
  f32x4 ab[2][2][2];
  unsigned fa;
  sfor<140, 196>([&](auto i) {
    constexpr int j = i - 140, KS = j / 4, t = (j % 4) >> 1, mat = i & 1;
    using K = std::integral_constant<int, KS>;
    if constexpr (j == 18) {
      fstamp(x);
      if (L == 0) wv<6>(); else wv<0>();
      asm volatile("s_barrier" ::: "memory");
      count(x, FL_H(L, x.c, 0), 1);
      SB();
      fstamp(x);
    }
    if constexpr ((i & 1) == 0) { if constexpr (i + DP < 196) { rd(std::integral_constant<int, i + DP>{}); rd(std::integral_constant<int, i + DP + 1>{}); wl<DP>(); } else wl<194 - i>(); }
    mm(ab[t][mat][0], wf[i & 7], K{}, I0{});
    if constexpr (j % 3 == 1 && j < 42) { if (L == 0) pf_wq<j / 3>(x); SB(); }
    if constexpr (j == 44) ldflag(fa, x.rs, OFF_FLAG + FL_H(L, x.c, 0));
    mm(ab[t][mat][1], wf[i & 7], K{}, I1{}); SB();
  });
  wv<0>();
  recheck(x, fa, FL_H(L, x.c, 0), 16);
  { const DnSrc d = dn_src(x, L); sfor<0, 3>([&](auto o) { dn_ld<o>(x, d, b); }); }
  stamp(x);
  sfor<0, 8>([&](auto i) { pin(ab[i >> 2][(i >> 1) & 1][i & 1]); });
  sfor<10, 14>([&](auto i) { pin(cs[i >> 1][i & 1]); });
  pair(ab[0], ab[1], std::integral_constant<int, 5>{});
}

DI void phase_down(Ctx& x, const int L, char* out, u32x4 (&b)[DWN][2]) {
  const int w = x.w;
  if (x.m >= 14) {
    wv<0>();
    sfor<0, 3>([&](auto o) { pin(b[o][0]); pin(b[o][1]); });
    signal(x, FL_H(L, x.c, 1), 1);
    stamp(x); fstamp(x); fstamp(x); stamp(x);
    return;
  }
  const int th = x.m / 7, ct = x.m % 7, tt = 4 * x.c + 2 * th + (w >> 1), nt = 2 * ct + (w & 1);
  stamp(x);
  constexpr int WN = DWN;
  u32x4 xr[2];
  const DnSrc d = dn_src(x, L);
  auto ld = [&](auto o) { dn_ld<o>(x, d, b); };
  sfor<3, WN>(ld);
  f32x16 acc[4];
  unsigned fb;
  sfor<0, 25>([&](auto i) {
    constexpr int last = i + WN - 1 < 27 ? i + WN - 1 : 27, ex = i <= 2 ? 2 : ((i >= 8 && i <= 13) ? 1 : ((i >= 21) ? 2 : 0));
    constexpr int later = [&] { int n = 0; for (int o = i + 1; o <= last; ++o) n += dn_half(o) ? 4 : 2; return n; }();
    wv<later + (dn_half(i) ? 2 : 1) + ex>();
    if constexpr (i == 3) signal(x, FL_H(L, x.c, 1), 1);
    m32a<A_WD + 8 * i, i == 0>(acc[0], b[i % WN][0]); m32a<A_WD + 8 * i + 4, i == 0>(acc[1], b[i % WN][0]);
    wv<later + ex>();
    m32a<A_WD + 8 * i, i == 0>(acc[2], b[i % WN][1]); m32a<A_WD + 8 * i + 4, i == 0>(acc[3], b[i % WN][1]);
    if constexpr (i == 7) ldflag(fb, x.rs, OFF_FLAG + FL_H(L, x.c, 1));
    if constexpr (i + WN == 20) { fstamp(x); wv<12>(); recheck(x, fb, FL_H(L, x.c, 1), 16); fstamp(x); }
    if constexpr (i + WN < 28) ld(std::integral_constant<int, i + WN>{});
    if constexpr (i + WN == 27) {
      const unsigned sr = OFF_X + (2 * L + 1) * SZ_X + (tt * 28 + 2 * nt) * KB;
      ld4<0>(xr[0], x.vl16, x.rs, sr); ld4<1024>(xr[1], x.vl16, x.rs, sr);
    }
  });
  wv<2>();
  sfor<1, 5>([&](auto t) {
    constexpr int k = t & 3;
    m32a<A_WD + 8 * 25 + 4 * (k & 1), false>(acc[k], b[25 % WN][k >> 1]);
    m32a<A_WD + 8 * 26 + 4 * (k & 1), false>(acc[k], b[26 % WN][k >> 1]);
    if constexpr (t > 1) { nop<(t == 2 ? 9 : 7)>(); red_put1<t - 1, 0>(x, acc[t - 1]); red_put1<t - 1, 1>(x, acc[t - 1]); }
    m32a<A_WD + 8 * 27 + 4 * (k & 1), false>(acc[k], b[27 % WN][k >> 1]);
    if constexpr (t > 1) { red_put1<t - 1, 2>(x, acc[t - 1]); red_put1<t - 1, 3>(x, acc[t - 1]); }
  });
  if (L == 0 && (x.m & 7) < 2) {
    ldl<4>(x.vl16, x.rs, OFF_WQ + ((16 + (x.m & 1)) * 28 + 7 * w) * KB, 7 * w * KB);
    ldl<3>(x.vl16, x.rs, OFF_WQ + ((16 + (x.m & 1)) * 28 + 7 * w + 4) * KB, (7 * w + 4) * KB);
  }
  nop<9>();
  pin(acc[0]);
  stamp(x);
  reduce_get(x, acc[0]);
  wv<0>();
  pin(xr[0]); pin(xr[1]);
  if (L > 0) {
    res_epilogue<true>(x, acc[0], xr, 0, 0, tt, nt, out);
  } else {
    res_epilogue<false>(x, acc[0], xr, OFF_X + 2 * SZ_X, OFF_ST + 2 * SZ_ST, tt, nt);
    wv<0>();
    signal(x, FL_X(x.c, th), 1);
  }
}

#define STAMP_SYNC
DI unsigned RT() { unsigned long long t; asm volatile("s_memrealtime %0\n\ts_waitcnt lgkmcnt(0)" : "=s"(t) :: "memory"); return (unsigned)t; }
DI void TM(int k) {
  if (k == TA) { unsigned t = RT(); asm volatile("v_accvgpr_write_b32 a254, %0" :: "s"(t) : "a254"); }
  if (k == TB) { unsigned t = RT(); asm volatile("v_accvgpr_write_b32 a255, %0" :: "s"(t) : "a255"); }
}
__global__ __launch_bounds__(256, 1) void k_main(Args) {
  typedef unsigned long long u64x8 __attribute__((ext_vector_type(8)));
  typedef unsigned long long u64x2 __attribute__((ext_vector_type(2)));
  u64x8 pin8; u64x2 pws;
  asm volatile("s_load_dwordx16 s[8:23], %4, 0x0\n\ts_load_dwordx4 s[24:27], %4, 0x40\n\t"
               "s_getpc_b64 s[4:5]\n.Lk_begin:\n\t"
               "s_mov_b32 s6, .Lk_end-.Lk_begin\n\ts_mov_b32 s7, 0x20000\n\ts_and_b32 s5, s5, 0xffff\n\t"
               "v_lshlrev_b32 v1, 4, %2\n\t"
               "s_bfe_u32 s28, %3, 0x20003\n\ts_lshl_b32 s28, s28, 12\n\t"
               "buffer_load_dwordx4 a[112:115], v1, s[4:7], s28 offen\n\t"
               "s_bfe_u32 s28, %3, 0x30004\n\ts_lshl_b32 s28, s28, 13\n\t"
               "buffer_load_dwordx4 a[112:115], v1, s[4:7], s28 offen\n\t"
               "s_add_u32 s28, s28, 4096\n\t"
               "buffer_load_dwordx4 a[112:115], v1, s[4:7], s28 offen\n\t"
               "s_waitcnt lgkmcnt(0)"
               : "={s[8:23]}"(pin8), "={s[24:27]}"(pws) : "v"(threadIdx.x), "{s2}"(blockIdx.x), "{s[0:1]}"(__builtin_amdgcn_kernarg_segment_ptr())
               : "s4", "s5", "s6", "s7", "s28", "v1", "scc", ACLOB);
  const Args a = {(const char*)pin8[0], (const char*)pin8[1], (const char*)pin8[2], (const char*)pin8[3], (const char*)pin8[4], (const char*)pin8[5],
                  (const char*)pin8[6], (const char*)pin8[7], (char*)pws[0], (char*)pws[1]};
  Ctx x;
  x.rs = mkrs(a.ws, WS_USED);
  x.w = __builtin_amdgcn_readfirstlane(threadIdx.x >> 6);
  asm volatile("s_nop 4" : "+s"(x.w));
  { const int b = blockIdx.x; x.c = 2 * (b & 7) + (b >> 7); x.m = (b >> 3) & 15; }
  f32x16 kf[3];
  { Ctx y = x; fresh(y); stamp(y); phase_pre(y, a, kf); STAMP_SYNC }
  for (int L = 0;; ++L) {
    f32x16 g2[3];
    int Lo = L; asm volatile("" : "+s"(Lo));
    TM(8 * Lo + 0);
    { Ctx y = x; fresh(y); phase_att(y, Lo, kf, g2); stamp(y); STAMP_SYNC }
    TM(8 * Lo + 1);
    { Ctx y = x; fresh(y); phase_oproj(y, Lo, a, g2); stamp(y); STAMP_SYNC }
    TM(8 * Lo + 2);
    { Ctx y = x; u32x4 b[DWN][2]; fresh(y); phase_gu(y, Lo, b); TM(8 * Lo + 3); fresh(y); phase_down(y, Lo, a.out, b); stamp(y); STAMP_SYNC }
    TM(8 * Lo + 4);
    if (L == 1) break;
    { Ctx y = x; fresh(y); phase_front(y, kf); STAMP_SYNC }
  }
  if (blockIdx.x == 16) {
    unsigned ta, tb;
    asm volatile("s_nop 4\n\tv_accvgpr_read_b32 v254, a254\n\tv_accvgpr_read_b32 v255, a255\n\ts_nop 1\n\tv_readfirstlane_b32 %0, v254\n\tv_readfirstlane_b32 %1, v255" : "=s"(ta), "=s"(tb) :: "v254", "v255");
    const unsigned t0 = RT(); while (RT() - t0 < tb - ta) {}
  }
  fresh(x);
  if (blockIdx.x == 0 && wave0(x)) {
    get_tag(x);
    if (x.l == 0) st2<0>(u32x2{x.tlo, x.thi}, 0, x.rs, OFF_TAG);
  }
  stamp_flush(x);
  asm volatile(".Lk_end:");
}

extern "C" void kernel_launch(void* const* d_in, const int* in_sizes, int n_in, void* d_out, int out_size, void* d_ws, size_t ws_size,
                              hipStream_t stream) {
  Args a;
  a.x = (const char*)d_in[0]; a.wq = (const char*)d_in[1]; a.wk = (const char*)d_in[2]; a.wv = (const char*)d_in[3];
  a.wo = (const char*)d_in[4]; a.wg = (const char*)d_in[6]; a.wu = (const char*)d_in[7]; a.wd = (const char*)d_in[8];
  a.ws = (char*)d_ws; a.out = (char*)d_out;
  (void)hipFuncSetAttribute((const void*)k_main, hipFuncAttributeMaxDynamicSharedMemorySize, 160 * 1024);
  k_main<<<256, 256, 160 * 1024, stream>>>(a);
}
```

```cpp
#include <hip/hip_runtime.h>
#include <stdint.h>
#include <type_traits>

typedef float    f32x16 __attribute__((ext_vector_type(16)));
typedef float    f32x4  __attribute__((ext_vector_type(4)));
typedef float    f32x2  __attribute__((ext_vector_type(2)));
typedef unsigned u32x4  __attribute__((ext_vector_type(4)));
typedef unsigned u32x2  __attribute__((ext_vector_type(2)));
typedef __bf16   bf16x2 __attribute__((ext_vector_type(2)));
typedef __amdgpu_buffer_rsrc_t rsrc_t;
#define DI __device__ __forceinline__
#define SB() __builtin_amdgcn_sched_barrier(0)

constexpr int S = 2048, D = 448, F = 1792;
constexpr unsigned KB = 1024;
constexpr unsigned OFF_FLAG = 0;
constexpr unsigned OFF_CSQ  = 64 * KB;
constexpr unsigned OFF_CSGU = OFF_CSQ + 4 * KB;
constexpr unsigned OFF_SLOT = OFF_CSGU + 32 * KB;
constexpr unsigned OFF_TAG  = OFF_SLOT + 32 * KB;
constexpr unsigned OFF_WQ   = 160 * KB;
constexpr unsigned OFF_WO   = OFF_WQ + 18 * 28 * KB;
constexpr unsigned OFF_WGU  = OFF_WO + 14 * 32 * KB;
constexpr unsigned OFF_WD   = OFF_WGU + 16 * 196 * KB;
constexpr unsigned SZ_X = 64 * 28 * KB, SZ_ST = 14 * 2048 * 8, SZ_O = 64 * 32 * KB, SZ_H = 64 * 112 * KB, SZ_KV = 64 * 4 * KB;
constexpr unsigned OFF_X    = OFF_WD + 14 * 112 * KB;
constexpr unsigned OFF_ST   = OFF_X + 4 * SZ_X;
constexpr unsigned OFF_O    = OFF_ST + 4 * SZ_ST;
constexpr unsigned OFF_H    = OFF_O + 2 * SZ_O;
constexpr unsigned OFF_HL   = OFF_H + 2 * SZ_H;
constexpr unsigned OFF_KV   = OFF_HL + 2 * 1024 * KB;
constexpr unsigned WS_USED  = OFF_KV + 2 * SZ_KV;
DI constexpr unsigned FL_X(int c, int th)         { return (c * 16 + th) * 256u; }
DI constexpr unsigned FL_O(int L, int c, int th)  { return (c * 16 + 2 + L * 2 + th) * 256u; }
DI constexpr unsigned FL_X1(int L, int c)         { return (c * 16 + 6 + L) * 256u; }
DI constexpr unsigned FL_H(int L, int c, int p)   { return (c * 16 + 8 + L * 2 + p) * 256u; }
DI constexpr unsigned FL_W(int c)                 { return (c * 16 + 12) * 256u; }
constexpr unsigned FL_W2 = 13 * 256u;

constexpr float QSCALE = 0.18898223650461362f * 1.4426950408889634f;
constexpr float M_LIMIT = 48.0f;

template <int I, int N, class Fn> DI void sfor(Fn&& f) { if constexpr (I < N) { f(std::integral_constant<int, I>{}); sfor<I + 1, N>(f); } }

DI unsigned cvtpk(float lo, float hi) { f32x2 v = {lo, hi}; bf16x2 b = __builtin_convertvector(v, bf16x2); return __builtin_bit_cast(unsigned, b); }
DI float bflo(unsigned u) { return __uint_as_float(u << 16); }
DI float bfhi(unsigned u) { return __uint_as_float(u & 0xffff0000u); }
DI float dot2(unsigned a, unsigned b, float c) { return __builtin_amdgcn_fdot2_f32_bf16(__builtin_bit_cast(bf16x2, a), __builtin_bit_cast(bf16x2, b), c, false); }
DI float sum32(float v) { auto r = __builtin_amdgcn_permlane32_swap(__float_as_uint(v), __float_as_uint(v), false, false); return __uint_as_float(r[0]) + __uint_as_float(r[1]); }
DI float sum16(float v) { auto r = __builtin_amdgcn_permlane16_swap(__float_as_uint(v), __float_as_uint(v), false, false); return __uint_as_float(r[0]) + __uint_as_float(r[1]); }
DI float max32(float v) { auto r = __builtin_amdgcn_permlane32_swap(__float_as_uint(v), __float_as_uint(v), false, false); return fmaxf(__uint_as_float(r[0]), __uint_as_float(r[1])); }

DI unsigned umax16(unsigned v) {
  v = max(v, (unsigned)__builtin_amdgcn_update_dpp(0, (int)v, 0x128, 0xf, 0xf, false));
  v = max(v, (unsigned)__builtin_amdgcn_update_dpp(0, (int)v, 0x124, 0xf, 0xf, false));
  v = max(v, (unsigned)__builtin_amdgcn_update_dpp(0, (int)v, 0x122, 0xf, 0xf, false));
  v = max(v, (unsigned)__builtin_amdgcn_update_dpp(0, (int)v, 0x121, 0xf, 0xf, false));
  return v;
}
DI unsigned umax64(unsigned v) {
  v = umax16(v);
  auto a = __builtin_amdgcn_permlane16_swap(v, v, false, false); v = max(a[0], a[1]);
  auto b = __builtin_amdgcn_permlane32_swap(v, v, false, false); return max(b[0], b[1]);
}

typedef short s16x4 __attribute__((ext_vector_type(4)));
DI u32x2 trread(const char* p) { return __builtin_bit_cast(u32x2, __builtin_amdgcn_ds_read_tr16_b64_v4i16((__attribute__((address_space(3))) s16x4*)p)); }
DI u32x4 trfrag(const char* lo, const char* hi) { const u32x2 a = trread(lo), b = trread(hi); return u32x4{a[0], a[1], b[0], b[1]}; }
DI void row_stats(float s, float q, float& mean, float& rstd) {
  mean = s * (1.0f / D);
  const float var = fmaxf(q * (1.0f / D) - mean * mean, 0.f);
  rstd = __builtin_amdgcn_rsqf(var + 1e-5f);
}
DI float fragsum(const u32x4& f, float s) {
#pragma unroll
  for (int d = 0; d < 4; ++d) s = dot2(f[d], 0x3f803f80u, s);
  return s;
}
struct Args { const char *x, *wq, *wk, *wv, *wo, *wg, *wu, *wd; char* ws; char* out; };

#define ACLOB "a0","a1","a2","a3","a4","a5","a6","a7","a8","a9","a10","a11","a12","a13","a14","a15","a16","a17","a18","a19","a20","a21","a22","a23","a24","a25","a26","a27","a28","a29","a30","a31","a32","a33","a34","a35","a36","a37","a38","a39","a40","a41","a42","a43","a44","a45","a46","a47","a48","a49","a50","a51","a52","a53","a54","a55","a56","a57","a58","a59","a60","a61","a62","a63","a64","a65","a66","a67","a68","a69","a70","a71","a72","a73","a74","a75","a76","a77","a78","a79","a80","a81","a82","a83","a84","a85","a86","a87","a88","a89","a90","a91","a92","a93","a94","a95","a96","a97","a98","a99","a100","a101","a102","a103","a104","a105","a106","a107","a108","a109","a110","a111","a112","a113","a114","a115","a116","a117","a118","a119","a120","a121","a122","a123","a124","a125","a126","a127","a128","a129","a130","a131","a132","a133","a134","a135","a136","a137","a138","a139","a140","a141","a142","a143","a144","a145","a146","a147","a148","a149","a150","a151","a152","a153","a154","a155","a156","a157","a158","a159","a160","a161","a162","a163","a164","a165","a166","a167","a168","a169","a170","a171","a172","a173","a174","a175","a176","a177","a178","a179","a180","a181","a182","a183","a184","a185","a186","a187","a188","a189","a190","a191","a192","a193","a194","a195","a196","a197","a198","a199","a200","a201","a202","a203","a204","a205","a206","a207","a208","a209","a210","a211","a212","a213","a214","a215","a216","a217","a218","a219","a220","a221","a222","a223","a224","a225","a226","a227","a228","a229","a230","a231","a232","a233","a234","a235","a236","a237","a238","a239","a240","a241","a242","a243","a244","a245","a246","a247","a248","a249","a250","a251","a252","a253","a254","a255"

template <int IMM> DI void ld4(u32x4& d, unsigned vo, rsrc_t rs, unsigned so) {
  asm volatile("buffer_load_dwordx4 %0, %1, %2, %3 offen offset:%c4 sc1" : "=v"(d) : "v"(vo), "s"(rs), "s"(so), "i"(IMM));
}
template <int IMM> DI void ld2(f32x2& d, unsigned vo, rsrc_t rs, unsigned so) {
  asm volatile("buffer_load_dwordx2 %0, %1, %2, %3 offen offset:%c4 sc1" : "=v"(d) : "v"(vo), "s"(rs), "s"(so), "i"(IMM));
}
template <int IMM> DI void ld2i(u32x2& d, unsigned vo, rsrc_t rs, unsigned so) {
  asm volatile("buffer_load_dwordx2 %0, %1, %2, %3 offen offset:%c4" : "=v"(d) : "v"(vo), "s"(rs), "s"(so), "i"(IMM));
}
template <int IMM> DI void ld2u(u32x2& d, unsigned vo, rsrc_t rs, unsigned so) {
  asm volatile("buffer_load_dwordx2 %0, %1, %2, %3 offen offset:%c4 sc1" : "=v"(d) : "v"(vo), "s"(rs), "s"(so), "i"(IMM));
}
template <int IMM> DI void ld4f(f32x4& d, unsigned vo, rsrc_t rs, unsigned so) {
  asm volatile("buffer_load_dwordx4 %0, %1, %2, %3 offen offset:%c4 sc1" : "=v"(d) : "v"(vo), "s"(rs), "s"(so), "i"(IMM));
}
DI void ld1(float& d, unsigned vo, rsrc_t rs, unsigned so) {
  asm volatile("buffer_load_dword %0, %1, %2, %3 offen sc1" : "=v"(d) : "v"(vo), "s"(rs), "s"(so));
}
template <int AR, int IMM> DI void lda(unsigned vo, rsrc_t rs, unsigned so) {
  asm volatile("buffer_load_dwordx4 a[%c3:%c4], %0, %1, %2 offen offset:%c5 sc1" :: "v"(vo), "s"(rs), "s"(so), "i"(AR), "i"(AR + 3), "i"(IMM) : ACLOB);
}
template <int N> DI void ldl(unsigned vo, rsrc_t rs, unsigned so, unsigned lds) {
  asm volatile("s_mov_b32 m0, %3\n\ts_nop 0\n\tbuffer_load_dwordx4 %0, %1, %2 offen sc1 lds" :: "v"(vo), "s"(rs), "s"(so), "s"(lds) : "memory");
  if constexpr (N > 1) asm volatile("buffer_load_dwordx4 %0, %1, %2 offen offset:1024 sc1 lds" :: "v"(vo), "s"(rs), "s"(so) : "memory");
  if constexpr (N > 2) asm volatile("buffer_load_dwordx4 %0, %1, %2 offen offset:2048 sc1 lds" :: "v"(vo), "s"(rs), "s"(so) : "memory");
  if constexpr (N > 3) asm volatile("buffer_load_dwordx4 %0, %1, %2 offen offset:3072 sc1 lds" :: "v"(vo), "s"(rs), "s"(so) : "memory");
}
template <int IMM> DI void st4(const u32x4& d, unsigned vo, rsrc_t rs, unsigned so) {
  asm volatile("buffer_store_dwordx4 %0, %1, %2, %3 offen offset:%c4 sc1\n\ts_nop 1" :: "v"(d), "v"(vo), "s"(rs), "s"(so), "i"(IMM) : "memory");
}
template <int IMM> DI void st4p(const u32x4& d, unsigned vo, rsrc_t rs, unsigned so) {
  asm volatile("buffer_store_dwordx4 %0, %1, %2, %3 offen offset:%c4\n\ts_nop 1" :: "v"(d), "v"(vo), "s"(rs), "s"(so), "i"(IMM) : "memory");
}
template <int IMM> DI void st2(const u32x2& d, unsigned vo, rsrc_t rs, unsigned so) {
  asm volatile("buffer_store_dwordx2 %0, %1, %2, %3 offen offset:%c4 sc1" :: "v"(d), "v"(vo), "s"(rs), "s"(so), "i"(IMM) : "memory");
}
DI void st1(float d, unsigned vo, rsrc_t rs, unsigned so) {
  asm volatile("buffer_store_dword %0, %1, %2, %3 offen sc1" :: "v"(d), "v"(vo), "s"(rs), "s"(so) : "memory");
}
template <int N> DI void wv() { asm volatile("s_waitcnt vmcnt(%c0)" :: "i"(N) : "memory"); SB(); }
template <int N> DI void wl() { asm volatile("s_waitcnt lgkmcnt(%c0)" :: "i"(N) : "memory"); SB(); }
template <class T> DI void pin(T& x) { asm volatile("" : "+v"(x)); }
DI void bar() { asm volatile("s_waitcnt lgkmcnt(0)\n\ts_barrier" ::: "memory"); }
template <int N> DI void nop() { asm volatile("s_nop %c0" :: "i"(N)); SB(); }

template <int OFF> DI void trd(u32x2& r, unsigned a) { asm volatile("ds_read_b64_tr_b16 %0, %1 offset:%c2" : "=v"(r) : "v"(a), "i"(OFF)); }
template <int OFF> DI void dsr(u32x4& r, unsigned a) { asm volatile("ds_read_b128 %0, %1 offset:%c2" : "=v"(r) : "v"(a), "i"(OFF)); }

DI void m32(f32x16& c, const u32x4& a, const u32x4& b)  { asm volatile("v_mfma_f32_32x32x16_bf16 %0, %1, %2, %0" : "+v"(c) : "v"(a), "v"(b)); }
DI void m32z(f32x16& c, const u32x4& a, const u32x4& b) { asm volatile("v_mfma_f32_32x32x16_bf16 %0, %1, %2, 0" : "=&v"(c) : "v"(a), "v"(b)); }
template <int AR, bool Z> DI void m32a(f32x16& c, const u32x4& b) {
  if constexpr (Z) asm volatile("v_mfma_f32_32x32x16_bf16 %0, a[%c2:%c3], %1, 0" : "=&v"(c) : "v"(b), "i"(AR), "i"(AR + 3));
  else             asm volatile("v_mfma_f32_32x32x16_bf16 %0, a[%c2:%c3], %1, %0" : "+v"(c) : "v"(b), "i"(AR), "i"(AR + 3));
}
template <bool Z> DI void m16(f32x4& c, const u32x4& a, const u32x4& b) {
  if constexpr (Z) asm volatile("v_mfma_f32_16x16x32_bf16 %0, %1, %2, 0" : "=&v"(c) : "v"(a), "v"(b));
  else             asm volatile("v_mfma_f32_16x16x32_bf16 %0, %1, %2, %0" : "+v"(c) : "v"(a), "v"(b));
}

template <bool Z, int AR> DI void m16a(f32x4& c, const u32x4& a) {
  if constexpr (Z) asm volatile("v_mfma_f32_16x16x32_bf16 %0, %1, a[%c2:%c3], 0" : "=&v"(c) : "v"(a), "i"(AR), "i"(AR + 3));
  else             asm volatile("v_mfma_f32_16x16x32_bf16 %0, %1, a[%c2:%c3], %0" : "+v"(c) : "v"(a), "i"(AR), "i"(AR + 3));
}
DI void ldflag(unsigned& v, rsrc_t rs, unsigned so) { asm volatile("buffer_load_dword %0, off, %1, %2 sc1" : "=v"(v) : "s"(rs), "s"(so)); }

struct Ctx {
  rsrc_t rs;
  unsigned tlo, thi;
  unsigned vl16;
  unsigned vg16;
  int w, l, c, m;
};
DI void fresh(Ctx& x) {
  asm volatile("" : "+s"(x.w), "+s"(x.c), "+s"(x.m));
  asm volatile("v_mbcnt_lo_u32_b32 %0, -1, 0\n\tv_mbcnt_hi_u32_b32 %0, -1, %0" : "=v"(x.l));
  x.vl16 = x.l * 16;
  const int g = x.l >> 4;
  x.vg16 = ((g >> 1) * 64 + (g & 1) * 32 + (x.l & 15)) * 16;
}
extern __shared__ __attribute__((aligned(16))) char lds[];
constexpr unsigned SCR = 112 * KB;

DI void get_tag(Ctx& x) {
  unsigned lo, hi;
  asm volatile("v_accvgpr_read_b32 v255, a252\n\ts_nop 1\n\tv_readfirstlane_b32 %0, v255\n\tv_accvgpr_read_b32 v255, a253\n\ts_nop 1\n\tv_readfirstlane_b32 %1, v255\n\ts_nop 4"
               : "=s"(lo), "=s"(hi) :: "v255");
  const unsigned long long e = ((unsigned long long)hi << 32) | lo;
  unsigned long long n = e * 0x9E3779B97F4A7C15ull + 0x632BE59BD9B4E019ull;
  if (((n ^ e) >> 16) == 0) n += 0x10000;
  x.tlo = (unsigned)n; x.thi = (unsigned)(n >> 32);
}
DI bool wave0(const Ctx& x) { int w = x.w; asm volatile("" : "+s"(w)); return w == 0; }
DI void count(Ctx& x, unsigned fl, int n) {
  if (wave0(x)) {
    const unsigned long long mask = (1ull << n) - 1;
    unsigned long long keep;
    asm volatile("s_mov_b64 %0, exec\n\ts_mov_b64 exec, %5\n\tbuffer_atomic_add %1, %2, %3, %4 offen\n\ts_mov_b64 exec, %0"
                 : "=&s"(keep) : "v"(1u), "v"(x.l * 256), "s"(x.rs), "s"(OFF_FLAG + fl), "s"(mask) : "memory");
  }
}
DI void signal(Ctx& x, unsigned fl, int n) { bar(); count(x, fl, n); }
DI void await(Ctx& x, unsigned fl, unsigned target) {
  if (wave0(x)) {
#pragma nounroll
    for (unsigned spin = 0; spin < (1u << 18); ++spin) {
      unsigned v;
      asm volatile("buffer_load_dword %0, off, %1, %2 sc1\n\ts_waitcnt vmcnt(0)" : "=v"(v) : "s"(x.rs), "s"(OFF_FLAG + fl) : "memory");
      if ((unsigned)__builtin_amdgcn_readfirstlane(v) >= target) break;
      __builtin_amdgcn_s_sleep(1);
    }
  }
  bar();
}

DI void recheck(Ctx& x, unsigned v, unsigned fl, unsigned target) {
  pin(v);
#pragma nounroll
  for (unsigned spin = 0; (unsigned)__builtin_amdgcn_readfirstlane(v) < target && spin < (1u << 18); ++spin) {
    __builtin_amdgcn_s_sleep(2);
    asm volatile("buffer_load_dword %0, off, %1, %2 sc1\n\ts_waitcnt vmcnt(0)" : "=v"(v) : "s"(x.rs), "s"(OFF_FLAG + fl) : "memory");
  }
  SB();
}

DI void reduce_put(Ctx& x, const f32x16 (&acc)[4]) {
  sfor<1, 4>([&](auto k) {
    f32x4* p = (f32x4*)(lds + SCR + ((x.w ^ k) * 3 + k - 1) * 4096 + x.vl16);
#pragma unroll
    for (int q = 0; q < 4; ++q) p[q * 64] = f32x4{acc[k][4 * q], acc[k][4 * q + 1], acc[k][4 * q + 2], acc[k][4 * q + 3]};
  });
}
template <int K, int Q> DI void red_put1(Ctx& x, const f32x16& t) {
  asm volatile("ds_write_b128 %0, %1 offset:%c2" :: "v"(SCR + ((x.w ^ K) * 3 + K - 1) * 4096 + x.vl16),
               "v"(__builtin_shufflevector(t, t, 4 * Q, 4 * Q + 1, 4 * Q + 2, 4 * Q + 3)), "i"(Q * 1024) : "memory");
}
DI void reduce_get(Ctx& x, f32x16& own) {
  bar();
  const f32x4* p = (const f32x4*)(lds + SCR + x.w * 3 * 4096 + x.vl16);
#pragma unroll
  for (int k = 0; k < 3; ++k)
#pragma unroll
    for (int q = 0; q < 4; ++q) {
      f32x4 v = p[k * 256 + q * 64];
#pragma unroll
      for (int i = 0; i < 4; ++i) own[4 * q + i] += v[i];
    }
}

constexpr int A_WO = 184, A_WD = 0, A_GX = 224;
constexpr int dn_ks(int o) { return o < 20 ? 2 * ((o >> 1) % 5 + ((o >> 1) % 5 >= 2) + 7 * (o / 10)) + (o & 1) : 2 * (((o - 20) >> 1) * 4 - ((o - 20) >> 2) + 2) + (o & 1); }
static_assert(dn_ks(0) == 0 && dn_ks(4) == 6 && dn_ks(9) == 11 && dn_ks(10) == 14 && dn_ks(19) == 25 && dn_ks(20) == 4 && dn_ks(22) == 12 && dn_ks(24) == 18 && dn_ks(27) == 27, "");
struct Two { unsigned s0, s1; };
constexpr int SL_WQ = 56;
template <int J> DI void pf_wq(Ctx& x) {
  ldl<1>(x.vl16, x.rs, OFF_WQ + ((x.m & 7) * 56 + 14 * x.w + J) * KB, (SL_WQ + 14 * x.w + J) * KB);
}
DI Two base_wo(const Ctx& x) { const int ct = x.m % 7;
  return Two{OFF_WO + ((2 * ct + (x.w & 1)) * 32 + 8 * x.w) * KB, OFF_WO + ((2 * ct + ((x.w & 1) ^ 1)) * 32 + 8 * x.w) * KB}; }
DI void pf_wo(Ctx& x) {
  const Two b = base_wo(x);
  sfor<0, 8>([&](auto i) {
    lda<A_WO + 8 * i, (i & 3) * 1024>(x.vl16, x.rs, b.s0 + (i >> 2) * 4096);
    lda<A_WO + 8 * i + 4, (i & 3) * 1024>(x.vl16, x.rs, b.s1 + (i >> 2) * 4096);
  });
}
DI Two base_wd(const Ctx& x) { const int ct = x.m % 7;
  return Two{OFF_WD + ((2 * ct + (x.w & 1)) * 112 + 28 * x.w) * KB, OFF_WD + ((2 * ct + ((x.w & 1) ^ 1)) * 112 + 28 * x.w) * KB}; }
template <int I0, int I1> DI void pf_wd(Ctx& x, const Two& b) {
  sfor<I0, I1>([&](auto f) {
    constexpr int i = f >> 1;
    lda<A_WD + 4 * f, (i & 3) * 1024>(x.vl16, x.rs, ((f & 1) ? b.s1 : b.s0) + (i >> 2) * 4096);
  });
}
DI void pf_wgu1(Ctx& x) {
  for (int t = 0; t < 14; ++t) ldl<2>(x.vl16, x.rs, OFF_WGU + (x.m * 196 + 8 * t + 2 * x.w) * KB, (8 * t + 2 * x.w) * KB);
}
template <int J> DI void pf_wgu3(Ctx& x) {
  ldl<1>(x.vl16, x.rs, OFF_WGU + (x.m * 196 + 160 + 9 * x.w + J) * KB, (9 * x.w + J) * KB);
}

constexpr unsigned S_QP = SCR, S_KP = SCR + 16384, S_QF = SCR + 24576, S_ST = SCR + 37888, S_CS = SCR + 38912, S_KM = SCR + 36864, S_TR = SCR + 40960;
DI unsigned kv_finish(Ctx& x, const float (&raw)[16], const f32x4 (&cs)[4], float mean, float rstd, bool isv, unsigned so, int slot) {
  const int h = x.l >> 5, r = x.l & 31;
  u32x4 y[2];
#pragma unroll
  for (int d = 0; d < 8; ++d)
    y[d >> 2][d & 3] = cvtpk((raw[2 * d] - mean * cs[d >> 1][2 * (d & 1)]) * rstd, (raw[2 * d + 1] - mean * cs[d >> 1][2 * (d & 1) + 1]) * rstd);
  if (h) { y[1][2] = 0; y[1][3] = 0; }
  unsigned nb = 0;
  if (!isv) {
    u32x4 y1 = y[1];
    if (h) y1[2] = 0x3f80u;
    st4<0>(y[0], x.vl16, x.rs, so); st4<1024>(y1, x.vl16, x.rs, so);
    float n2 = 0.f, n3 = 0.f;
#pragma unroll
    for (int d = 0; d < 4; ++d) { n2 = dot2(y[0][d], y[0][d], n2); n3 = dot2(y[1][d], y[1][d], n3); }
    n2 = sum32(n2 + n3);
    nb = umax64(n2 < 3.0e38f ? __float_as_uint(n2) : 0x7f800000u);
    nb = cvtpk(1.01f * __builtin_amdgcn_sqrtf(__uint_as_float(nb)), 0.f);
  } else {
    if (h) y[1][2] = 0x3f80u;
    char* im = lds + S_TR + slot * 2048;
#pragma unroll
    for (int a = 0; a < 4; ++a) *(u32x2*)(im + r * 64 + 16 * a + 8 * h) = u32x2{y[a >> 1][2 * (a & 1)], y[a >> 1][2 * (a & 1) + 1]};
    asm volatile("s_waitcnt lgkmcnt(0)" ::: "memory");
    const char* tb = im + (4 * h + ((x.l >> 2) & 3)) * 64 + ((x.l >> 4) & 1) * 32 + (x.l & 3) * 8;
    const u32x4 v0 = trfrag(tb, tb + 512), v1 = trfrag(tb + 1024, tb + 1536);
    st4<0>(v0, x.vl16, x.rs, so); st4<1024>(v1, x.vl16, x.rs, so);
  }
  return nb;
}
DI void kv_publish(Ctx& x, int L, int p, unsigned km) {
  get_tag(x);
  st2<0>(u32x2{(x.tlo & 0xffff0000u) | km, x.thi}, x.l < 16 ? x.l * 1024 : ~0u, x.rs, OFF_SLOT + (L * 2048 + p) * 8);
}
DI void q_finish(Ctx& x, const f32x16& qa, const f32x4 (&cq)[4], float mean, float rstd) {
  const int h = x.l >> 5;
  u32x4 qf[2];
  const f32x4* p = (const f32x4*)(lds + S_QP + x.w * 4096 + x.vl16);
  const float sc = rstd * QSCALE;
#pragma unroll
  for (int q = 0; q < 4; ++q) {
    const f32x4 o = p[q * 64];
    float v[4];
#pragma unroll
    for (int i = 0; i < 4; ++i) v[i] = (qa[4 * q + i] + o[i] - mean * cq[q][i]) * sc;
    qf[q >> 1][2 * (q & 1)] = cvtpk(v[0], v[1]); qf[q >> 1][2 * (q & 1) + 1] = cvtpk(v[2], v[3]);
  }
  if (h) { qf[1][2] = 0; qf[1][3] = 0; }
  float qn2 = 0.f, qn3 = 0.f;
#pragma unroll
  for (int d = 0; d < 4; ++d) { qn2 = dot2(qf[0][d], qf[0][d], qn2); qn3 = dot2(qf[1][d], qf[1][d], qn3); }
  qn2 = sum32(qn2 + qn3);
  if (h) qf[1][2] = cvtpk(1.01f * __builtin_amdgcn_sqrtf(qn2), 0.f);
  ((u32x4*)(lds + S_QF + x.w * 2048 + x.vl16))[0] = qf[0];
  ((u32x4*)(lds + S_QF + x.w * 2048 + x.vl16))[64] = qf[1];
}
DI void put_tile(char* p, const f32x16& t) {
#pragma unroll
  for (int q = 0; q < 4; ++q) ((f32x4*)p)[q * 64] = f32x4{t[4 * q], t[4 * q + 1], t[4 * q + 2], t[4 * q + 3]};
}

constexpr unsigned IMG_Q = 0, IMG_T = 56 * KB;
DI rsrc_t mkrs(const char* p, unsigned n) { return __builtin_amdgcn_make_buffer_rsrc((char*)p, 0, n, 0x00020000); }

DI void ask_slots(Ctx& x, int L) {
  if (wave0(x)) asm volatile("buffer_load_dwordx4 a[248:251], %0, %1, %2 offen sc1" :: "v"(x.vl16), "s"(x.rs), "s"(OFF_SLOT + (L * 16 + x.c) * 1024) : ACLOB);
}
DI void kv_wait(Ctx& x, int L) {
  if (wave0(x)) get_tag(x);
  wv<0>();
  if (wave0(x)) {
    unsigned s0, s1, s2, s3;
#pragma nounroll
    for (unsigned spin = 0; spin < (1u << 18); ++spin) {
      asm volatile("v_accvgpr_read_b32 %0, a248\n\tv_accvgpr_read_b32 %1, a249\n\tv_accvgpr_read_b32 %2, a250\n\tv_accvgpr_read_b32 %3, a251" : "=v"(s0), "=v"(s1), "=v"(s2), "=v"(s3));
      if (__all(s1 == x.thi && s3 == x.thi && (((s0 ^ x.tlo) | (s2 ^ x.tlo)) >> 16) == 0)) break;
      __builtin_amdgcn_s_sleep(2);
      ask_slots(x, L); wv<0>();
    }
    const unsigned km = umax64(max(s0 << 16, s2 << 16));
    if (x.l == 0) ((unsigned*)(lds + S_KM))[4] = km;
  }
  bar();
}
DI void ask_kv(Ctx& x, int L, f32x16 (&kf)[3]) {
  const unsigned kvs = OFF_KV + L * SZ_KV + 16 * x.w * 4096;
  asm volatile(".irp i,0,1,2,3\n\tbuffer_load_dwordx4 v[128+4*\\i:131+4*\\i], %3, %4, %5 offen offset:1024*\\i sc1\n\t.endr\n\t"
               ".irp i,0,1,2,3\n\tbuffer_load_dwordx4 v[144+4*\\i:147+4*\\i], %3, %4, %6 offen offset:1024*\\i sc1\n\t.endr\n\t"
               ".irp i,0,1,2,3\n\tbuffer_load_dwordx4 v[160+4*\\i:163+4*\\i], %3, %4, %7 offen offset:1024*\\i sc1\n\t.endr"
               : "={v[128:143]}"(kf[0]), "={v[144:159]}"(kf[1]), "={v[160:175]}"(kf[2]) : "{v204}"(x.vl16), "s"(x.rs), "s"(kvs), "s"(kvs + 4096), "s"(kvs + 8192));
}

DI void phase_pre(Ctx& x, const Args& a, f32x16 (&kf)[3]) {
  const int w = x.w, l = x.l, h = l >> 5, r = l & 31;
  const int th = x.m >> 3, hp = x.m & 7, tl = w & 1, kh = w >> 1, tt = 4 * x.c + 2 * th + tl, wi = 2 * x.c + th;
  const bool kvp = hp < 2;
  u32x4 xf[14];
  { const rsrc_t rx = mkrs(a.x, S * D * 2);
    sfor<0, 16>([&](auto i) {
      const int c = r ^ ((2 * i + h) & 15);
      ldl<1>(c < 28 ? h * 896 + c * 16 : 0x80000000u, rx, (tt * 32 + 2 * i) * 896 + kh * 448, (84 + 16 * w + i) * KB);
    }); }
  if (x.m == 2) asm volatile("buffer_load_dword a116, %0, %1, 0 offen sc1" :: "v"(l < 16 ? OFF_FLAG + (x.c * 16 + l) * 256 : OFF_SLOT + ((l >> 3) & 1) * 16384 + x.c * 1024 + (l & 7) * 128), "s"(x.rs) : ACLOB);
  asm volatile("buffer_load_dwordx2 a[252:253], off, %0, %1 sc1" :: "s"(x.rs), "s"(OFF_TAG) : ACLOB);
  const int task = wi * 6 + hp - 2;
  const rsrc_t rq = mkrs(a.wq, D * D * 2);
  auto ask_q = [&](auto i) {
    ldl<1>((8 * w + (l >> 3)) * 896 + (l & 7) * 16, rq, hp * 112 + i * 32 * 896, IMG_Q + (4 * i + w) * KB);
  };
  auto ask_t = [&](const char* tp, unsigned tn, unsigned ts, unsigned tb) {
    const rsrc_t rt = mkrs(tp, tn);
    const unsigned vo = (8 * w + (l >> 3)) * ts + (l & 7) * 16;
    sfor<0, 7>([&](auto i) { ldl<1>(vo, rt, tb + i * 32 * ts, IMG_T + (4 * i + w) * KB); });
  };
  const int tco = ((l >> 4) & 1) * 32 + (l & 3) * 8;
  const int sro = 4 * h + ((l >> 2) & 3);
  if (kvp) ask_t(hp ? a.wv : a.wk, D * 56, 128, 0);
  else if (task < 112) ask_t(task < 56 ? a.wg : a.wu, D * F * 2, F * 2, (task & 1) * 224 * F * 2 + ((task % 56) >> 1) * 128);
  else if (task < 168) ask_t(a.wd, D * F * 2, 896, ((task - 112) / 7) * 224 * 896 + ((task - 112) % 7) * 128);
  else ask_t(a.wo, task < 182 ? D * D * 2 : 0, 896, ((task - 168) / 7) * 224 * 896 + ((task - 168) % 7) * 128);
  wv<8>();
  sfor<0, 14>([&](auto i) { dsr<0>(xf[i], (84 + w * 16) * KB + r * 512 + (((2 * i + h) ^ (r & 15)) << 4)); });
  wl<0>();
  {
    float s = 0.f, q = 0.f, s1 = 0.f, q1 = 0.f;
    sfor<0, 14>([&](auto i) {
      ask_q(i);
      pin(xf[i]);
      s = dot2(xf[i][0], 0x3f803f80u, s); q = dot2(xf[i][0], xf[i][0], q); s1 = dot2(xf[i][1], 0x3f803f80u, s1); q1 = dot2(xf[i][1], xf[i][1], q1);
      s = dot2(xf[i][2], 0x3f803f80u, s); q = dot2(xf[i][2], xf[i][2], q); s1 = dot2(xf[i][3], 0x3f803f80u, s1); q1 = dot2(xf[i][3], xf[i][3], q1);
      pin(s); pin(q); pin(s1); pin(q1);
    });
    s = sum32(s + s1); q = sum32(q + q1);
    if (h == 0) ((f32x2*)(lds + S_ST))[w * 32 + r] = f32x2{s, q};
  }
  wv<14>();
  bar();
  if (!kvp) {
    if (task < 112) {
      const int g = l >> 4, mat = task >= 56, gt = 4 * ((task % 56) >> 1) + w, m = gt / 7, j = gt % 7, lt = (m & 1) ? j : (j == 6 ? 0 : j + 1);
      const char* tb = lds + IMG_T + (16 * (g >> 1) + 4 * (g & 1) + ((l >> 2) & 3)) * 128 + 32 * w + (l & 3) * 8;
      const unsigned st = lt < 5 ? 10 * KB : 4 * KB, so = OFF_WGU + (m * 196 + (lt < 5 ? lt * 2 + mat : 140 + (lt - 5) * 2 + mat)) * KB + (task & 1) * 7 * st;
      u32x4 f[7];
      sfor<0, 7>([&](auto i) { f[i] = trfrag(tb + i * 4096, tb + i * 4096 + 1024); });
      float cs = 0.f;
      sfor<0, 7>([&](auto i) { st4<0>(f[i], x.vl16, x.rs, so + i * st); cs = fragsum(f[i], cs); });
      cs = sum32(sum16(cs));
      st1(cs, l < 16 ? l * 4 : ~0u, x.rs, OFF_CSGU + m * 2048 + (task & 1) * 1024 + (lt * 2 + mat) * 64);
    } else if (task < 168) {
      const int nt = 2 * ((task - 112) % 7) + (w & 1), rb = (task - 112) / 7;
      const char* tb = lds + IMG_T + sro * 128 + 64 * (w & 1) + tco;
      const unsigned so = OFF_WD + (nt * 112 + 28 * (rb >> 1)) * KB, sa = so + (rb & 1) * 10 * KB, sb = so + (20 + (rb & 1) * 4) * KB;
      u32x4 f[7];
      if (w < 2) {
        sfor<0, 7>([&](auto i) { const char* p = tb + (32 * (i >> 1) + 8 * (i & 1)) * 128; f[i] = trfrag(p, p + 2048); });
        sfor<0, 7>([&](auto i) { st4<0>(f[i], x.vl16, x.rs, i < 4 ? sa + i * KB : (i < 6 ? sb + (i - 4) * KB : sa + 4 * KB)); });
      } else {
        sfor<0, 7>([&](auto i) { const char* p = tb + (32 * ((i + 7) >> 1) + 8 * ((i + 7) & 1)) * 128; f[i] = trfrag(p, p + 2048); });
        sfor<0, 7>([&](auto i) { st4<0>(f[i], x.vl16, x.rs, i < 5 ? sa + (5 + i) * KB : sb + (i - 3) * KB); });
      }
      st1(0.f, ~0u, x.rs, 0);
    } else {
      const int nt = 2 * ((task - 168) % 7) + (w & 1);
      const char* tb = lds + IMG_T + (112 * (w >> 1) + sro) * 128 + 64 * (w & 1) + tco;
      const unsigned vo = task < 182 ? x.vl16 : ~0u - 4096;
      u32x4 f[8];
      sfor<0, 8>([&](auto i) { const char* p = tb + (28 * (i >> 1) + 16 * (i & 1)) * 128; f[i] = trfrag(p, p + 1024); if ((i & 1) && h) { f[i][2] = 0; f[i][3] = 0; } });
      sfor<0, 8>([&](auto i) { st4<(i & 3) * 1024>(f[i], vo, x.rs, OFF_WO + (nt * 32 + 16 * ((task - 168) / 7) + 8 * (w >> 1) + (i & ~3)) * KB); });
    }
  }
  if (!kvp) { wv<8>(); bar(); }
  float mean, rstd;
  { const f32x2 p0 = ((const f32x2*)(lds + S_ST))[tl * 32 + r], p1 = ((const f32x2*)(lds + S_ST))[(tl + 2) * 32 + r];
    row_stats(p0[0] + p1[0], p0[1] + p1[1], mean, rstd); }
  float* csw = (float*)(lds + S_CS);
  const int tro = 8 * h + ((l >> 2) & 3);
  u32x4 ones = {0x3f803f80u, 0x3f803f80u, 0x3f803f80u, 0x3f803f80u};
  pin(ones);
  if (kvp) {
    f32x16 kv, cs;
    const unsigned tb = IMG_T + (224 * kh + tro) * 56 + tco;
    u32x2 f[14][2];
    auto rd = [&](auto i) { trd<i * 896>(f[i][0], tb); trd<i * 896 + 224>(f[i][1], tb); };
    sfor<0, 7>(rd);
    sfor<0, 14>([&](auto i) {
      if constexpr (i + 7 < 14) { wl<12>(); rd(std::integral_constant<int, i + 7>{}); } else wl<2 * (13 - i)>();
      pin(f[i][0]); pin(f[i][1]);
      u32x4 g = {f[i][0][0], f[i][0][1], f[i][1][0], f[i][1][1]};
      pin(g); nop<0>();
      if constexpr (i == 0) { m32z(kv, g, xf[i]); m32z(cs, g, ones); } else { m32(kv, g, xf[i]); m32(cs, g, ones); }
    });
    nop<11>(); pin(kv); pin(cs);
    if (r == 0) sfor<0, 4>([&](auto q) { *(f32x4*)(csw + (w * 3 + 2) * 32 + 8 * q + 4 * h) = f32x4{cs[4 * q], cs[4 * q + 1], cs[4 * q + 2], cs[4 * q + 3]}; });
    if (kh) put_tile(lds + S_KP + tl * 4096 + x.vl16, kv);
    bar();
    if (!kh) {
      if (x.m == 0 && w == 0) st1(0.f, l < 16 ? l * 256 : ~0u, x.rs, OFF_FLAG + x.c * 4096);
      float raw[16]; f32x4 c4[4];
      sfor<0, 4>([&](auto q) {
        const f32x4 o = ((const f32x4*)(lds + S_KP + tl * 4096 + x.vl16))[q * 64];
        c4[q] = *(const f32x4*)(csw + (w * 3 + 2) * 32 + 8 * q + 4 * h) + *(const f32x4*)(csw + ((w + 2) * 3 + 2) * 32 + 8 * q + 4 * h);
#pragma unroll
        for (int i = 0; i < 4; ++i) raw[4 * q + i] = kv[4 * q + i] + o[i];
      });
      const unsigned km = kv_finish(x, raw, c4, mean, rstd, hp == 1, OFF_KV + (tt * 4 + 2 * hp) * KB, tl);
      wv<0>();
      kv_publish(x, 0, 2 * tt + hp, km);
      if (tt == 0 && r == 0) sfor<0, 4>([&](auto q) { st4<32 * q>(__builtin_bit_cast(u32x4, c4[q]), h * 16, x.rs, OFF_CSQ + (16 + hp) * 128); });
    } else {
      wv<0>();
    }
    bar();
  }
  f32x16 qa;
  {
    f32x16 q1, q2, cs;
    const unsigned t1 = IMG_Q + (224 * kh + tro) * 128 + tco + 56 * tl, t2 = IMG_Q + (224 * kh + tro) * 128 + tco + 56 * (tl ^ 1);
    u32x2 f[14][4];
    auto rd = [&](auto i) { trd<i * 2048>(f[i][0], t1); trd<i * 2048 + 512>(f[i][1], t1); trd<i * 2048>(f[i][2], t2); trd<i * 2048 + 512>(f[i][3], t2); };
    wl<0>();
    sfor<0, 3>(rd);
    sfor<0, 14>([&](auto i) {
      if constexpr (i + 3 < 14) { wl<8>(); rd(std::integral_constant<int, i + 3>{}); } else wl<4 * (13 - i)>();
      sfor<0, 4>([&](auto e) { pin(f[i][e]); });
      u32x4 g1 = {f[i][0][0], f[i][0][1], f[i][1][0], f[i][1][1]}, g2 = {f[i][2][0], f[i][2][1], f[i][3][0], f[i][3][1]};
      pin(g1); pin(g2); nop<0>();
      if constexpr (i == 0) { m32z(q1, g1, xf[i]); m32z(q2, g2, xf[i]); m32z(cs, g1, ones); } else { m32(q1, g1, xf[i]); m32(q2, g2, xf[i]); m32(cs, g1, ones); }
    });
    nop<11>(); pin(q1); pin(q2); pin(cs);
    if (r == 0) sfor<0, 4>([&](auto q) { *(f32x4*)(csw + (w * 3) * 32 + 8 * q + 4 * h) = f32x4{cs[4 * q], cs[4 * q + 1], cs[4 * q + 2], cs[4 * q + 3]}; });
    if (kh == tl) { put_tile(lds + S_QP + (w ^ 2) * 4096 + x.vl16, q2); qa = q1; } else { put_tile(lds + S_QP + (w ^ 2) * 4096 + x.vl16, q1); qa = q2; }
  }
  if (th == 0 && x.c < 14) {
    const int f = 4 * x.c + w, hd = f >= 28, ks = f - 28 * hd;
    const char* tb = lds + IMG_Q + (16 * ks + sro) * 128 + tco + 56 * hd;
    st4<0>(trfrag(tb, tb + 1024), x.vl16, x.rs, OFF_WQ + (hp * 56 + f) * KB);
  }
  if (kvp && th == 1 && x.c < 7) {
    const char* tb = lds + IMG_T + (16 * (4 * x.c + w) + sro) * 56 + tco;
    st4<0>(trfrag(tb, tb + 448), x.vl16, x.rs, OFF_WQ + ((16 + hp) * 28 + 4 * x.c + w) * KB);
  }
  if (!kvp) __builtin_amdgcn_s_sleep(12);
  ask_slots(x, 0);
  kv_wait(x, 0);
  count(x, FL_W(x.c), 1);
  ask_kv(x, 0, kf);
  const bool own = x.c == hp && th == 0;
  {
    f32x4 cq[4];
    sfor<0, 4>([&](auto q) {
      cq[q] = *(const f32x4*)(csw + (kh * 3) * 32 + 8 * q + 4 * h) + *(const f32x4*)(csw + ((kh + 2) * 3) * 32 + 8 * q + 4 * h);
      if (own && tl == 0 && r == 0) st4<32 * q>(__builtin_bit_cast(u32x4, cq[q]), h * 16, x.rs, OFF_CSQ + (2 * hp + kh) * 128);
    });
    q_finish(x, qa, cq, mean, rstd);
  }
}

DI void phase_front(Ctx& x, f32x16 (&kf)[3]) {
  const int w = x.w, l = x.l, h = l >> 5, r = l & 31;
  const int th = x.m >> 3, hp = x.m & 7, tl = w & 1, kh = w >> 1, tt = 4 * x.c + 2 * th + tl;
  const unsigned KV = OFF_KV + SZ_KV;
  {
    f32x4 cq[4], ck[4];
    sfor<0, 4>([&](auto q) { ld4f<32 * q>(cq[q], h * 16, x.rs, OFF_CSQ + (2 * hp + kh) * 128); ld4f<32 * q>(ck[q], h * 16, x.rs, OFF_CSQ + (16 + (hp & 1)) * 128); });
    wv<0>();
    await(x, FL_X(x.c, th), 7);
    u32x4 xf[14]; f32x2 sp[7];
    {
      const unsigned so = OFF_X + 2 * SZ_X + (tt * 28 + 14 * kh) * KB;
      sfor<0, 7>([&](auto i) { ld4<(i & 3) * 1024>(xf[i], x.vl16, x.rs, so + (i >> 2) * 4096); });
      sfor<0, 7>([&](auto i) { ld2<0>(sp[i], (h * 7 * 2048 + r) * 8, x.rs, OFF_ST + 2 * SZ_ST + (i * 2048 + tt * 32) * 8); });
      sfor<7, 14>([&](auto i) { ld4<(i & 3) * 1024>(xf[i], x.vl16, x.rs, so + (i >> 2) * 4096); });
    }
    float mean, rstd;
    auto stats = [&]() {
      sfor<0, 7>([&](auto i) { pin(sp[i]); });
      sfor<0, 4>([&](auto i) { pin(cq[i]); pin(ck[i]); });
      float s = 0.f, q = 0.f;
#pragma unroll
      for (int i = 0; i < 7; ++i) { s += sp[i][0]; q += sp[i][1]; }
      row_stats(sum32(s), sum32(q), mean, rstd);
      pin(mean); pin(rstd); SB();
    };
    f32x16 qa, qb;
    u32x4 wa[14], wb[14];
    const unsigned pa = x.vl16 + (SL_WQ + 28 * kh + 14 * kh) * KB, pb = x.vl16 + (SL_WQ + 28 * (kh ^ 1) + 14 * kh) * KB;
    if (hp < 2) {
      f32x16 kv;
      sfor<0, 14>([&](auto i) { dsr<i * 1024>(wa[i], x.vl16 + 14 * kh * KB); });
      sfor<0, 14>([&](auto i) {
        wv<(i < 7 ? 20 : 13) - i>(); wl<13 - i>();
        if constexpr (i == 0) m32z(kv, wa[i], xf[i]); else m32(kv, wa[i], xf[i]);
        if constexpr (i == 7) stats();
      });
      nop<11>(); pin(kv);
      if (kh) put_tile(lds + S_KP + tl * 4096 + x.vl16, kv);
      bar();
      unsigned km = 0;
      if (!kh) {
        float raw[16];
        sfor<0, 4>([&](auto q) {
          const f32x4 o = ((const f32x4*)(lds + S_KP + tl * 4096 + x.vl16))[q * 64];
#pragma unroll
          for (int i = 0; i < 4; ++i) raw[4 * q + i] = kv[4 * q + i] + o[i];
        });
        km = kv_finish(x, raw, ck, mean, rstd, hp == 1, KV + (tt * 4 + 2 * hp) * KB, tl);
      }
      wl<0>();
      sfor<0, 7>([&](auto i) { dsr<i * 1024>(wb[i], pb); dsr<i * 1024>(wa[i], pa); });
      sfor<0, 14>([&](auto i) {
        if constexpr (i < 7) wl<12>(); else wl<2 * (13 - i)>();
        if constexpr (i == 0) { m32z(qb, wb[i], xf[i]); m32z(qa, wa[i], xf[i]); } else { m32(qb, wb[i], xf[i]); m32(qa, wa[i], xf[i]); }
        if constexpr (i < 7) { dsr<(i + 7) * 1024>(wb[i + 7], pb); dsr<(i + 7) * 1024>(wa[i + 7], pa); }
        if constexpr (i == 6) { if (!kh) { wv<0>(); kv_publish(x, 1, 2 * tt + hp, km); } SB(); }
      });
    } else {
      sfor<0, 7>([&](auto i) { dsr<i * 1024>(wb[i], pb); dsr<i * 1024>(wa[i], pa); });
      sfor<0, 14>([&](auto i) {
        wv<(i < 7 ? 20 : 13) - i>();
        if constexpr (i < 7) wl<12>(); else wl<2 * (13 - i)>();
        if constexpr (i == 0) { m32z(qb, wb[i], xf[i]); m32z(qa, wa[i], xf[i]); } else { m32(qb, wb[i], xf[i]); m32(qa, wa[i], xf[i]); }
        if constexpr (i < 7) { dsr<(i + 7) * 1024>(wb[i + 7], pb); dsr<(i + 7) * 1024>(wa[i + 7], pa); }
        if constexpr (i == 7) stats();
      });
    }
    nop<11>();
    pin(qa); pin(qb);
    put_tile(lds + S_QP + (w ^ 2) * 4096 + x.vl16, qb);
    bar();
    q_finish(x, qa, cq, mean, rstd);
    if (hp >= 2) __builtin_amdgcn_s_sleep(36);
    ask_slots(x, 1);
    kv_wait(x, 1);
    ask_kv(x, 1, kf);
  }
}

DI void phase_att(Ctx& x, const int L, f32x16 (&kf)[3], f32x16 (&g2)[3]) {
  const int w = x.w, l = x.l, h = l >> 5, r = l & 31;
  const int th = x.m >> 3, hp = x.m & 7, tl = w & 1, kh = w >> 1, tt = 4 * x.c + 2 * th + tl;
  const unsigned kvs = OFF_KV + L * SZ_KV + 16 * w * 4096;
  if (L == 0) bar();
  unsigned kma = S_KM + 16; asm volatile("" : "+s"(kma));
  const float kmx = *(const float*)(lds + kma);
  u32x4 q[4][2];
#pragma unroll
  for (int k = 0; k < 4; ++k) {
    q[k][0] = ((const u32x4*)(lds + S_QF + (w ^ k) * 2048 + x.vl16))[0];
    q[k][1] = ((const u32x4*)(lds + S_QF + (w ^ k) * 2048 + x.vl16))[64];
  }
  bool safe = true;
  {
#pragma unroll
    for (int k = 0; k < 4; ++k) {
      const float mrow = bflo(q[k][1][2]) * kmx + 0.01f;
      safe = safe && (!h || mrow <= M_LIMIT);
      if (h) q[k][1][2] = cvtpk(-mrow, 0.f);
    }
  }
  const bool fast = __all(safe);
  f32x16 own;
  if (fast) {
    unsigned so_kv = kvs + 3 * 4096, so_w = OFF_WGU + (x.m * 196 + 2 * w) * KB, so_l = 2 * w * KB, so_g = OFF_WGU + (x.m * 196 + 112 + 12 * w) * KB, spin = 1u << 16, fvo = (l & 15) * 4096;
    Two ab = base_wo(x);
    const Two wd = base_wd(x);
    asm volatile(
      ".macro GRP s, pa, pb\n\t"
      "v_exp_f32 v200, v[\\s]\n\tv_exp_f32 v201, v[\\s+1]\n\tv_cvt_pk_bf16_f32 v[\\pa], v202, v203\n\t"
      "v_exp_f32 v202, v[\\s+2]\n\tv_exp_f32 v203, v[\\s+3]\n\tv_cvt_pk_bf16_f32 v[\\pb], v200, v201\n\t"
      ".endm\n\t"
      ".macro MEM kind, i, ld\n\t"
      ".if \\kind == 1\n\t"
      "buffer_load_dwordx4 v[128+16*\\ld+4*\\i:128+16*\\ld+4*\\i+3], %[vo], %[rs], %[skv] offen offset:1024*\\i sc1\n\t"
      ".elseif \\kind == 5\n\t"
      "ds_write_b128 v205, v[4*\\i:4*\\i+3] offset:1024*\\i\n\t"
      ".elseif \\kind == 6\n\t"
      "ds_write_b128 v205, v[4:7] offset:1024\n\t"
      ".elseif \\kind == 7\n\t"
      "ds_write_b128 v205, v[12:15] offset:3072\n\t"
      ".elseif \\kind == 2\n\t"
      "buffer_load_dword v206, v205, %[rs], 0 offen offset:3072 sc1\n\t"
      ".elseif \\kind == 3\n\t"
      ".if att_n < 16 || att_n >= 56\n\t"
      ".if att_n < 102\n\t"
      ".if att_n < 16\n\t.set att_r, 184+4*att_n\n\t.else\n\t.set att_r, 4*(att_n-56)\n\t.endif\n\t"
      ".if att_n & 1\n\t"
      "buffer_load_dwordx4 a[att_r:att_r+3], %[vo], %[rs], %[ab1] offen offset:1024*((att_n>>1)&3) sc1\n\t"
      ".else\n\t"
      "buffer_load_dwordx4 a[att_r:att_r+3], %[vo], %[rs], %[ab0] offen offset:1024*((att_n>>1)&3) sc1\n\t"
      ".endif\n\t"
      ".if att_n == 15\n\t"
      "s_mov_b32 %[ab0], %[wd0]\n\ts_mov_b32 %[ab1], %[wd1]\n\t"
      ".elseif (att_n & 7) == 7\n\t"
      "s_add_u32 %[ab0], %[ab0], 4096\n\ts_add_u32 %[ab1], %[ab1], 4096\n\t"
      ".endif\n\t"
      ".endif\n\t"
      ".elseif att_n < 44\n\t"
      ".if att_n & 1\n\t"
      "buffer_load_dwordx4 %[vo], %[rs], %[sw] offen offset:1024 sc1 lds\n\t"
      "s_add_u32 %[sw], %[sw], 8192\n\ts_add_u32 m0, m0, 8192\n\t"
      ".else\n\t"
      "buffer_load_dwordx4 %[vo], %[rs], %[sw] offen sc1 lds\n\t"
      ".endif\n\t"
      ".else\n\t"
      "buffer_load_dwordx4 v[208+4*(att_n-44):208+4*(att_n-44)+3], %[vo], %[rs], %[sg] offen offset:1024*(att_n&3) sc1\n\t"
      ".if (att_n & 3) == 3\n\ts_add_u32 %[sg], %[sg], 4096\n\t.endif\n\t"
      ".endif\n\t"
      ".set att_n, att_n+1\n\t"
      ".endif\n\t"
      ".endm\n\t"
      ".macro STEP sc, sn, k, nk, pk, tk, nt, pt, ld, m0k, m0i, m1k, m2k, m2i, m3k\n\t"
      "v_mfma_f32_32x32x16_bf16 v[\\sn:\\sn+15], v[128+16*\\nt:128+16*\\nt+3], v[64+8*\\nk:64+8*\\nk+3], 0\n\t"
      "MEM \\m0k, \\m0i, \\ld\n\t"
      "GRP \\sc, 199, 192\n\t"
      "v_mfma_f32_32x32x16_bf16 v[\\sn:\\sn+15], v[132+16*\\nt:132+16*\\nt+3], v[68+8*\\nk:68+8*\\nk+3], v[\\sn:\\sn+15]\n\t"
      "MEM \\m1k, 0, 0\n\t"
      "GRP \\sc+4, 193, 194\n\t"
      "v_mfma_f32_32x32x16_bf16 v[16*\\pk:16*\\pk+15], v[140+16*\\pt:140+16*\\pt+3], v[196:199], v[16*\\pk:16*\\pk+15]\n\t"
      "MEM \\m2k, \\m2i, \\ld\n\t"
      "GRP \\sc+8, 195, 196\n\t"
      "v_mfma_f32_32x32x16_bf16 v[16*\\k:16*\\k+15], v[136+16*\\tk:136+16*\\tk+3], v[192:195], v[16*\\k:16*\\k+15]\n\t"
      "MEM \\m3k, 0, 0\n\t"
      "GRP \\sc+12, 197, 198\n\t"
      ".endm\n\t"
      ".macro TILE tk, nx, pv, on, wait, p, fl=0\n\t"
      "STEP 96, 112, 0, 1, 3, \\tk, \\tk, \\pv, \\pv, \\p, 0, \\p, \\p, 0, \\fl\n\t"
      "STEP 112, 96, 1, 2, 0, \\tk, \\tk, \\tk, \\pv, \\on, 0, \\p, \\on, 1, 0\n\t"
      "STEP 96, 112, 2, 3, 1, \\tk, \\tk, \\tk, \\pv, \\on, 2, \\p, \\on, 3, 0\n\t"
      ".if \\on\n\ts_add_u32 %[skv], %[skv], 4096\n\t.endif\n\t"
      "s_waitcnt vmcnt(\\wait)\n\t"
      "STEP 112, 96, 3, 0, 2, \\tk, \\nx, \\tk, \\pv, \\p, 0, \\p, \\p, 0, 0\n\t"
      ".endm\n\t"
      ".set att_n, 0\n\t"
      "s_mov_b32 m0, %[sl]\n\t"
      "v_mov_b32 v196, 0\n\tv_mov_b32 v197, 0\n\tv_mov_b32 v198, 0\n\tv_mov_b32 v199, 0\n\tv_mov_b32 v202, 0\n\tv_mov_b32 v203, 0\n\t"
      "v_mov_b32 v188, 0\n\tv_mov_b32 v189, 0\n\tv_mov_b32 v190, 0\n\tv_mov_b32 v191, 0\n\t"
      "v_mfma_f32_32x32x16_bf16 v[0:15], v[196:199], v[196:199], 0\n\t"
      "v_mfma_f32_32x32x16_bf16 v[16:31], v[196:199], v[196:199], 0\n\t"
      "v_mfma_f32_32x32x16_bf16 v[32:47], v[196:199], v[196:199], 0\n\t"
      "v_mfma_f32_32x32x16_bf16 v[48:63], v[196:199], v[196:199], 0\n\t"
      "s_waitcnt vmcnt(8)\n\t"
      "v_mfma_f32_32x32x16_bf16 v[96:111], v[128:131], v[64:67], 0\n\t"
      "v_mfma_f32_32x32x16_bf16 v[96:111], v[132:135], v[68:71], v[96:111]\n\t"
      "s_nop 11\n\t"
      "TILE 0, 1, 3, 1, 8, 0\n\t"
      "TILE 1, 2, 0, 1, 8, 0\n\tTILE 2, 3, 1, 1, 9, 0, 2\n\t"
      "s_waitcnt vmcnt(4)\n\t"
      "1:\n\t"
      "v_cmp_gt_u32 vcc, 16, v206\n\t"
      "s_and_b64 vcc, exec, vcc\n\t"
      "s_cbranch_vccz 2f\n\t"
      "s_sleep 24\n\t"
      "buffer_load_dword v206, v205, %[rs], 0 offen offset:3072 sc1\n\t"
      "s_sub_u32 %[spin], %[spin], 1\n\t"
      "s_waitcnt vmcnt(0)\n\t"
      "s_cbranch_scc0 1b\n\t"
      "2:\n\t"
      "s_barrier\n\t"
      "TILE 3, 0, 2, 1, 14, 3\n\t"
      "TILE 0, 1, 3, 1, 21, 3\n\tTILE 1, 2, 0, 1, 24, 3\n\tTILE 2, 3, 1, 1, 24, 3\n\tTILE 3, 0, 2, 1, 24, 3\n\t"
      "TILE 0, 1, 3, 1, 24, 3\n\tTILE 1, 2, 0, 1, 24, 3\n\tTILE 2, 3, 1, 1, 24, 3\n\tTILE 3, 0, 2, 1, 24, 3\n\t"
      "TILE 0, 1, 3, 1, 24, 3\n\tTILE 1, 2, 0, 0, 20, 3\n\tTILE 2, 3, 1, 0, 16, 3\n\t"
      "v_add_u32 v205, %[d1], v204\n\tv_add_u32 v206, %[d2], v204\n\t"
      "STEP 96, 112, 0, 1, 3, 3, 3, 2, 0, 3, 0, 3, 3, 0, 3\n\t"
      "STEP 112, 96, 1, 2, 0, 3, 3, 3, 0, 3, 0, 3, 0, 0, 0\n\t"
      "STEP 96, 112, 2, 3, 1, 3, 3, 3, 0, 5, 0, 6, 5, 2, 7\n\t"
      "ds_write_b128 v206, v[16:19]\n\t"
      "GRP 112, 199, 192\n\t"
      "ds_write_b128 v206, v[20:23] offset:1024\n\t"
      "GRP 116, 193, 194\n\t"
      "v_mfma_f32_32x32x16_bf16 v[32:47], v[188:191], v[196:199], v[32:47]\n\t"
      "ds_write_b128 v206, v[24:27] offset:2048\n\t"
      "GRP 120, 195, 196\n\t"
      "v_mfma_f32_32x32x16_bf16 v[48:63], v[184:187], v[192:195], v[48:63]\n\t"
      "ds_write_b128 v206, v[28:31] offset:3072\n\t"
      "GRP 124, 197, 198\n\t"
      "v_add_u32 v200, %[d3], v204\n\t"
      "v_cvt_pk_bf16_f32 v199, v202, v203\n\t"
      "s_nop 1\n\t"
      "v_mfma_f32_32x32x16_bf16 v[48:63], v[188:191], v[196:199], v[48:63]\n\t"
      "ds_write_b128 v200, v[32:35]\n\tds_write_b128 v200, v[36:39] offset:1024\n\tds_write_b128 v200, v[40:43] offset:2048\n\tds_write_b128 v200, v[44:47] offset:3072\n\t"
      "s_nop 7\n\t"
      ".purgem GRP\n\t.purgem MEM\n\t.purgem STEP\n\t.purgem TILE"
      : "={v[48:63]}"(own), "+{v[128:143]}"(kf[0]), "+{v[144:159]}"(kf[1]), "+{v[160:175]}"(kf[2]),
        "={v[208:223]}"(g2[0]), "={v[224:239]}"(g2[1]), "={v[240:255]}"(g2[2]),
        [skv] "+s"(so_kv), [sw] "+s"(so_w), [ab0] "+s"(ab.s0), [ab1] "+s"(ab.s1), [spin] "+s"(spin), [sg] "+s"(so_g), "+{v205}"(fvo)
      : "{v[64:67]}"(q[1][0]), "{v[68:71]}"(q[1][1]), "{v[72:75]}"(q[2][0]), "{v[76:79]}"(q[2][1]),
        "{v[80:83]}"(q[3][0]), "{v[84:87]}"(q[3][1]), "{v[88:91]}"(q[0][0]), "{v[92:95]}"(q[0][1]),
        [vo] "{v204}"(x.vl16), [d1] "s"(SCR + (w ^ 1) * 3 * 4096), [d2] "s"(SCR + ((w ^ 2) * 3 + 1) * 4096), [d3] "s"(SCR + ((w ^ 3) * 3 + 2) * 4096), [rs] "s"(x.rs), [sl] "s"(so_l), [wd0] "s"(wd.s0), [wd1] "s"(wd.s1)
      : "memory", "scc", "vcc",
        "v0","v1","v2","v3","v4","v5","v6","v7","v8","v9","v10","v11","v12","v13","v14","v15","v16","v17","v18","v19","v20","v21","v22","v23",
        "v24","v25","v26","v27","v28","v29","v30","v31","v32","v33","v34","v35","v36","v37","v38","v39","v40","v41","v42","v43","v44","v45","v46","v47",
        "v96","v97","v98","v99","v100","v101","v102","v103","v104","v105","v106","v107","v108","v109","v110","v111",
        "v112","v113","v114","v115","v116","v117","v118","v119","v120","v121","v122","v123","v124","v125","v126","v127",
        "v176","v177","v178","v179","v180","v181","v182","v183","v184","v185","v186","v187","v188","v189","v190","v191",
        "v192","v193","v194","v195","v196","v197","v198","v199","v200","v201","v202","v203","v206", ACLOB);
  } else {
    f32x16 o[4];
    wv<0>();
    pin(kf[0]); pin(kf[1]); pin(kf[2]);
    float mx[4];
#pragma unroll
    for (int k = 0; k < 4; ++k) {
      mx[k] = -3.0e38f; if (h) q[k][1][2] = 0; pin(q[k][1]);
#pragma unroll
      for (int i = 0; i < 16; ++i) o[k][i] = 0.f;
    }
#pragma nounroll
    for (unsigned spin = 0; spin < (1u << 16); ++spin) {
      float v;
      ld1(v, (l & 15) * 4096, x.rs, OFF_FLAG + FL_W(0)); wv<0>(); pin(v);
      if (__all(__float_as_uint(v) >= 16u)) break;
      __builtin_amdgcn_s_sleep(24);
    }
    pf_wo(x);
    pf_wgu1(x);
    sfor<0, 12>([&](auto j) {
      u32x4 t;
      ld4<0>(t, x.vl16, x.rs, OFF_WGU + (x.m * 196 + 112 + 12 * w + j) * KB); wv<0>(); pin(t);
#pragma unroll
      for (int i = 0; i < 4; ++i) g2[j >> 2][4 * (j & 3) + i] = __uint_as_float(t[i]);
    });
    pf_wd<0, 46>(x, base_wd(x));
    for (int tk = 0; tk < 16; ++tk) {
      u32x4 kf[4];
      sfor<0, 4>([&](auto i) { ld4<i * 1024>(kf[i], x.vl16, x.rs, kvs + tk * 4096); });
      wv<0>();
      sfor<0, 4>([&](auto k) {
        f32x16 s;
        nop<1>();
        m32z(s, kf[0], q[k][0]); m32(s, kf[1], q[k][1]);
        nop<11>(); pin(s);
        float tm = s[0];
#pragma unroll
        for (int i = 1; i < 16; ++i) tm = fmaxf(tm, s[i]);
        tm = max32(tm);
        const float mn = fmaxf(mx[k], tm), al = __builtin_amdgcn_exp2f(mx[k] - mn);
        mx[k] = mn;
#pragma unroll
        for (int i = 0; i < 16; ++i) o[k][i] *= al;
        u32x4 p0, p1;
#pragma unroll
        for (int d = 0; d < 4; ++d) {
          p0[d] = cvtpk(__builtin_amdgcn_exp2f(s[2 * d] - mn), __builtin_amdgcn_exp2f(s[2 * d + 1] - mn));
          p1[d] = cvtpk(__builtin_amdgcn_exp2f(s[8 + 2 * d] - mn), __builtin_amdgcn_exp2f(s[9 + 2 * d] - mn));
        }
        pin(p0); pin(p1); pin(o[k]);
        nop<1>();
        m32(o[k], kf[2], p0); m32(o[k], kf[3], p1);
        nop<11>(); pin(o[k]);
      });
    }
    bar();
    float* mm = (float*)(lds + SCR);
#pragma unroll
    for (int k = 0; k < 4; ++k) if (h == 0) mm[((w ^ k) * 4 + w) * 32 + r] = mx[k];
    bar();
#pragma unroll
    for (int k = 0; k < 4; ++k) {
      const float* p = mm + (w ^ k) * 128 + r;
      const float g = fmaxf(fmaxf(p[0], p[32]), fmaxf(p[64], p[96])), f = __builtin_amdgcn_exp2f(mx[k] - g);
#pragma unroll
      for (int i = 0; i < 16; ++i) o[k][i] *= f;
    }
    bar();
    reduce_put(x, o);
    own = o[0];
  }
  reduce_get(x, own);
  pin(own);
  {
    auto lr = __builtin_amdgcn_permlane32_swap(__float_as_uint(own[12]), __float_as_uint(own[12]), false, false);
    const float inv = __builtin_amdgcn_rcpf(__uint_as_float(lr[1]));
    u32x4 a, b;
#pragma unroll
    for (int d = 0; d < 4; ++d) { a[d] = cvtpk(own[2 * d] * inv, own[2 * d + 1] * inv); b[d] = cvtpk(own[8 + 2 * d] * inv, own[9 + 2 * d] * inv); }
    if (h) { b[2] = 0; b[3] = 0; }
    const unsigned so = OFF_O + L * SZ_O + (tt * 32 + 2 * (2 * hp + kh)) * KB;
    st4<0>(a, x.vl16, x.rs, so); st4<1024>(b, x.vl16, x.rs, so);
  }
  wv<0>();
  signal(x, FL_O(L, x.c, th), 1);
  if (L == 0 && th == 0) {
    if (x.c == hp) count(x, FL_W2, 1);
  }
}

template <bool FINAL> DI void res_epilogue(Ctx& x, const f32x16& acc, const u32x4 (&xr)[2], unsigned Xo, unsigned STo, int tt, int nt, char* out = nullptr) {
  const int h = x.l >> 5, r = x.l & 31;
  u32x4 y[2];
#pragma unroll
  for (int d = 0; d < 8; ++d) y[d >> 2][d & 3] = cvtpk(acc[2 * d] + bflo(xr[d >> 2][d & 3]), acc[2 * d + 1] + bfhi(xr[d >> 2][d & 3]));
  if constexpr (FINAL) {
#pragma unroll
    for (int s = 0; s < 2; ++s) {
      auto a = __builtin_amdgcn_permlane32_swap(y[s][0], y[s][2], false, false);
      auto b = __builtin_amdgcn_permlane32_swap(y[s][1], y[s][3], false, false);
      y[s] = u32x4{a[0], b[0], a[1], b[1]};
    }
    const unsigned vo = r * (D * 2) + h * 16, so = tt * 32 * D * 2 + nt * 64;
    const rsrc_t ro = mkrs(out, S * D * 2);
    st4p<0>(y[0], vo, ro, so); st4p<32>(y[1], vo, ro, so);
  } else {
    const unsigned so = Xo + (tt * 28 + 2 * nt) * KB;
    st4<0>(y[0], x.vl16, x.rs, so); st4<1024>(y[1], x.vl16, x.rs, so);
    float s = 0.f, q = 0.f, s1 = 0.f, q1 = 0.f;
#pragma unroll
    for (int d = 0; d < 4; ++d) {
      s = dot2(y[0][d], 0x3f803f80u, s); q = dot2(y[0][d], y[0][d], q); s1 = dot2(y[1][d], 0x3f803f80u, s1); q1 = dot2(y[1][d], y[1][d], q1);
    }
    s = sum32(s + s1); q = sum32(q + q1);
    if (h == 0) st2<0>(u32x2{__float_as_uint(s), __float_as_uint(q)}, r * 8, x.rs, STo + (nt * 2048 + tt * 32) * 8);
  }
}

DI void phase_oproj(Ctx& x, const int L, const Args& a, const f32x16 (&g2)[3]) {
  const int w = x.w;
  auto put_g2 = [&]() {
#pragma unroll
    for (int j = 0; j < 12; ++j)
      *(f32x4*)(lds + (112 + 12 * w + j) * KB + x.vl16) = f32x4{g2[j >> 2][4 * (j & 3)], g2[j >> 2][4 * (j & 3) + 1], g2[j >> 2][4 * (j & 3) + 2], g2[j >> 2][4 * (j & 3) + 3]};
  };
  if (x.m < 14) {
    const int th = x.m / 7, ct = x.m % 7, tt0 = 4 * x.c + 2 * th, tt = tt0 + (w >> 1), nt = 2 * ct + (w & 1);
    u32x2 x0[4];
    if (L == 0) {
      const rsrc_t rx = mkrs(a.x, S * D * 2);
      sfor<0, 4>([&](auto i) { ld2i<16 * i>(x0[i], (x.l & 31) * 896 + (x.l >> 5) * 8, rx, tt * 32 * 896 + nt * 64); });
    } else {
      sfor<0, 4>([&](auto i) { ld2u<(i >> 1) * 1024 + (i & 1) * 8>(x0[i], x.vl16, x.rs, OFF_X + 2 * SZ_X + (tt * 28 + 2 * nt) * KB); });
    }
    await(x, FL_O(L, x.c, th), 8);
    u32x4 b[8][2];
    const unsigned s0 = OFF_O + L * SZ_O + (tt * 32 + 8 * w) * KB, s1 = OFF_O + L * SZ_O + ((tt0 + ((w >> 1) ^ 1)) * 32 + 8 * w) * KB;
    auto ld = [&](auto i) { ld4<(i & 3) * 1024>(b[i][0], x.vl16, x.rs, s0 + (i >> 2) * 4096); ld4<(i & 3) * 1024>(b[i][1], x.vl16, x.rs, s1 + (i >> 2) * 4096); };
    constexpr int WN = 5;
    sfor<0, WN>(ld);
    f32x16 acc[4];
    sfor<0, 8>([&](auto i) {
      constexpr int last = (i + WN - 1 < 7 ? i + WN - 1 : 7);
      wv<2 * (last - i) + 1>();
      m32a<A_WO + 8 * i, i == 0>(acc[0], b[i][0]); m32a<A_WO + 8 * i + 4, i == 0>(acc[1], b[i][0]);
      wv<2 * (last - i)>();
      m32a<A_WO + 8 * i, i == 0>(acc[2], b[i][1]); m32a<A_WO + 8 * i + 4, i == 0>(acc[3], b[i][1]);
      if constexpr (i + WN < 8) ld(std::integral_constant<int, i + WN>{});
    });
    nop<11>();
    sfor<0, 4>([&](auto k) { pin(acc[k]); pin(x0[k]); });
    reduce_put(x, acc); reduce_get(x, acc[0]);
    pin(acc[0]);
    const u32x4 x2[2] = {u32x4{x0[0][0], x0[0][1], x0[1][0], x0[1][1]}, u32x4{x0[2][0], x0[2][1], x0[3][0], x0[3][1]}};
    res_epilogue<false>(x, acc[0], x2, OFF_X + (2 * L + 1) * SZ_X, OFF_ST + (2 * L + 1) * SZ_ST, tt, nt);
    put_g2();
    wv<0>();
    signal(x, FL_X1(L, x.c), 1);
  } else {
    put_g2();
  }
}

constexpr int DWN = 7;
constexpr bool dn_half(int o) { return o == 4 || o == 5 || o == 14 || o == 15; }
struct DnSrc { unsigned s0, s1, l0, l1; };
DI DnSrc dn_src(const Ctx& x, int L) {
  const int tt0 = 4 * x.c + 2 * ((x.m / 7) & 1), tt = tt0 + (x.w >> 1), tto = tt0 + ((x.w >> 1) ^ 1);
  return DnSrc{OFF_H + L * SZ_H + (tt * 112 + 28 * x.w) * KB, OFF_H + L * SZ_H + (tto * 112 + 28 * x.w) * KB,
               OFF_HL + ((L * 64 + tt) * 16 + 4 * x.w) * KB, OFF_HL + ((L * 64 + tto) * 16 + 4 * x.w) * KB};
}
template <int O> DI void dn_ld(Ctx& x, const DnSrc& d, u32x4 (&b)[DWN][2]) {
  constexpr int i = dn_ks(O);
  if constexpr (dn_half(O)) {
    constexpr int imm = (O >= 14 ? 2048 : 0) + (O & 1) * 512;
    u32x2 lo, hi, lo1, hi1;
    ld2u<imm>(lo, x.l * 8, x.rs, d.l0); ld2u<imm + 1024>(hi, x.l * 8, x.rs, d.l0);
    ld2u<imm>(lo1, x.l * 8, x.rs, d.l1); ld2u<imm + 1024>(hi1, x.l * 8, x.rs, d.l1);
    b[O % DWN][0] = u32x4{lo[0], lo[1], hi[0], hi[1]}; b[O % DWN][1] = u32x4{lo1[0], lo1[1], hi1[0], hi1[1]};
  } else {
    ld4<(i & 3) * 1024>(b[O % DWN][0], x.vl16, x.rs, d.s0 + (i >> 2) * 4096); ld4<(i & 3) * 1024>(b[O % DWN][1], x.vl16, x.rs, d.s1 + (i >> 2) * 4096);
  }
}

DI void gu_tile(const f32x4 (&acc)[2][2], const u32x4 (&cs)[2], const float (&mean)[2], const float (&r2)[2], u32x2 (&hp)[2]) {
  const f32x4 cg = __builtin_bit_cast(f32x4, cs[0]), cu = __builtin_bit_cast(f32x4, cs[1]);
#pragma unroll
  for (int u = 0; u < 2; ++u) {
    const f32x2 nm = {-mean[u], -mean[u]}, rr = {r2[u], r2[u]};
#pragma unroll
    for (int e = 0; e < 2; ++e) {
      const f32x2 g = __builtin_elementwise_fma(nm, f32x2{cg[2 * e], cg[2 * e + 1]}, f32x2{acc[0][u][2 * e], acc[0][u][2 * e + 1]});
      const f32x2 v = __builtin_elementwise_fma(nm, f32x2{cu[2 * e], cu[2 * e + 1]}, f32x2{acc[1][u][2 * e], acc[1][u][2 * e + 1]});
      const f32x2 y = g * rr * v;
      hp[u][e] = cvtpk(y[0], y[1]);
    }
  }
}

DI void phase_gu(Ctx& x, const int L, u32x4 (&b)[DWN][2]) {
  const int w = x.w, l = x.l, g = l >> 4, c16 = l & 15, m = x.m, tt = 4 * x.c + w;
  if (L == 0 && wave0(x)) { unsigned v; ldflag(v, x.rs, OFF_FLAG + FL_W2); wv<0>(); recheck(x, v, FL_W2, 8); }
  await(x, FL_X1(L, x.c), 14);
  constexpr int NXV = 11;
  u32x4 xf[NXV][2]; f32x2 sp[2][4];
  const unsigned sx = OFF_X + (2 * L + 1) * SZ_X + tt * 28 * KB, st = OFF_ST + (2 * L + 1) * SZ_ST + tt * 32 * 8;
  auto ldx = [&](auto k, auto u) {
    if constexpr (k < NXV) ld4<(k & 1) * 2048 + 256 * u>(xf[k][u], x.vg16, x.rs, sx + (k >> 1) * 4096);
    else if constexpr (k < 14) lda<A_GX + 8 * (k - NXV) + 4 * u, (k & 1) * 2048 + 256 * u>(x.vg16, x.rs, sx + (k >> 1) * 4096);
    else { constexpr int i = k - 14; ld2<128 * u>(sp[u][i], (((i < 3 || g < 2) ? g + 4 * i : g) * 2048 + c16) * 8, x.rs, st); }
  };
  using I0 = std::integral_constant<int, 0>; using I1 = std::integral_constant<int, 1>;
  constexpr int XA = 3;
  sfor<0, XA>([&](auto k) { ldx(k, I0{}); ldx(k, I1{}); });
  const unsigned a0 = x.vl16, a1 = x.vl16 + 65536, a2 = x.vl16 + 131072;
  constexpr int DP = 6;
  u32x4 wf[8];
  auto rd = [&](auto ii) {
    constexpr int i = ii, slot = i >= 160 ? i - 160 : i;
    if constexpr (slot < 64) dsr<slot * 1024>(wf[i & 7], a0); else if constexpr (slot < 128) dsr<(slot - 64) * 1024>(wf[i & 7], a1); else dsr<(slot - 128) * 1024>(wf[i & 7], a2);
  };
  auto mm = [&](f32x4& c, const u32x4& a, auto k, auto u) {
    if constexpr (k < NXV) m16<k == 0>(c, a, xf[k][u]); else m16a<false, A_GX + 8 * (k - NXV) + 4 * u>(c, a);
  };
  const Two wd = base_wd(x);
  f32x4 aa[5][2][2], ch0, ch1;
  float mean[2], r2[2];
  sfor<0, DP>(rd);
  sfor<0, 140>([&](auto i) {
    constexpr int KS = i / 10, s = i % 10, t = s >> 1, mat = i & 1;
    using K = std::integral_constant<int, KS>;
    constexpr auto side = [](int k) { return (k >= 0 && k <= 3) || (k >= 5 && k <= 12); };
    constexpr auto all = [](int k) { return (k + XA < 14 ? 2 : 0) + ((k >= 0 && k <= 3) || (k >= 5 && k <= 9) ? 2 : 0) + (k == 5 ? 1 : 0) + (k >= 10 ? 3 : 0); };
    if constexpr (s == 0) {
      constexpr int cnt = [&] { int n = side(KS - XA) ? 1 : 0; for (int k = KS - XA + 1; k < KS; ++k) n += all(k); return n; }();
      wv<cnt>();
      if constexpr (KS == 5) asm volatile("s_barrier" ::: "memory");
    }
    if constexpr ((i & 1) == 0) { rd(std::integral_constant<int, i + DP>{}); rd(std::integral_constant<int, i + DP + 1>{}); wl<DP>(); }
    mm(aa[t][mat][0], wf[i & 7], K{}, I0{});
    if constexpr (s == 1 && KS + XA < 14) ldx(std::integral_constant<int, KS + XA>{}, I0{});
    if constexpr (s == 6 && KS + XA < 14) ldx(std::integral_constant<int, KS + XA>{}, I1{});
    if constexpr (KS <= 3 && s == 3) ldx(std::integral_constant<int, 14 + KS>{}, I0{});
    if constexpr (KS <= 3 && s == 8) ldx(std::integral_constant<int, 14 + KS>{}, I1{});
    if constexpr (KS == 5 && s == 3) asm volatile("buffer_load_dwordx4 %0, %2, %3, %4 offen sc1\n\tbuffer_load_dwordx4 %1, %2, %3, %4 offen offset:1024 sc1" : "=&v"(ch0), "=&v"(ch1) : "v"(x.vl16), "s"(x.rs), "s"(OFF_CSGU + m * 2048));
    if constexpr (KS == 11 && s == 2) { pin(ch0); pin(ch1); *(f32x4*)(lds + 40 * KB + x.vl16) = ch0 + ch1; }
    if constexpr (KS >= 5 && KS <= 9 && (s == 3 || s == 8) && !(KS == 5 && s == 3)) pf_wgu3<2 * (KS - 5) + (s == 8) - 1>(x);
    if constexpr (KS >= 10 && (s == 3 || s == 5 || s == 8) && 3 * (KS - 10) + (s == 3 ? 0 : (s == 5 ? 1 : 2)) < 10) {
      constexpr int f = 46 + 3 * (KS - 10) + (s == 3 ? 0 : (s == 5 ? 1 : 2));
      pf_wd<f, f + 1>(x, wd);
    }
    if constexpr ((KS == 10 || KS == 12) && s == 2) {
      constexpr int u = KS == 12;
      sfor<0, 4>([&](auto e) { pin(sp[u][e]); });
      float sm = sp[u][0][0] + sp[u][1][0] + sp[u][2][0], q = sp[u][0][1] + sp[u][1][1] + sp[u][2][1];
      if (g < 2) { sm += sp[u][3][0]; q += sp[u][3][1]; }
      sm = sum32(sum16(sm)); q = sum32(sum16(q));
      float rs; row_stats(sm, q, mean[u], rs); r2[u] = rs * rs;
      pin(mean[u]); pin(r2[u]);
    }
    mm(aa[t][mat][1], wf[i & 7], K{}, I1{}); SB();
  });
  u32x4 cs[7][2];
  const unsigned csa = 40 * KB + g * 16;
  sfor<0, 10>([&](auto i) { dsr<64 * i>(cs[i >> 1][i & 1], csa); });
  wv<10>();
  asm volatile("s_barrier" ::: "memory");
  const unsigned H = OFF_H + L * SZ_H + tt * 112 * KB;
  const int gp = (m & 1) ? 7 * m + 1 : 7 * m;
  auto pair = [&](const f32x4 (&t0)[2][2], const f32x4 (&t1)[2][2], auto lt) {
    u32x2 h0[2], h1[2];
    gu_tile(t0, cs[lt], mean, r2, h0); gu_tile(t1, cs[lt + 1], mean, r2, h1);
    st4<0>(u32x4{h0[0][0], h0[0][1], h1[0][0], h1[0][1]}, x.vg16, x.rs, H + (gp + lt - 1) * 1024);
    st4<256>(u32x4{h0[1][0], h0[1][1], h1[1][0], h1[1][1]}, x.vg16, x.rs, H + (gp + lt - 1) * 1024);
  };
  {
    wl<0>();
    sfor<0, 20>([&](auto i) { pin(aa[i >> 2][(i >> 1) & 1][i & 1]); });
    sfor<0, 10>([&](auto i) { pin(cs[i >> 1][i & 1]); });
    u32x2 hl[2];
    gu_tile(aa[0], cs[0], mean, r2, hl);
    const unsigned sl = OFF_HL + ((L * 64 + tt) * 16 + m) * KB;
    st2<0>(hl[0], x.vg16 >> 1, x.rs, sl); st2<128>(hl[1], x.vg16 >> 1, x.rs, sl);
    pair(aa[1], aa[2], std::integral_constant<int, 1>{});
    pair(aa[3], aa[4], std::integral_constant<int, 3>{});
  }
  SB();
  sfor<10, 14>([&](auto i) { dsr<64 * i>(cs[i >> 1][i & 1], csa); });
  f32x4 ab[2][2][2];
  unsigned fa;
  sfor<140, 196>([&](auto i) {
    constexpr int j = i - 140, KS = j / 4, t = (j % 4) >> 1, mat = i & 1;
    using K = std::integral_constant<int, KS>;
    if constexpr (j == 18) {
      if (L == 0) wv<6>(); else wv<0>();
      asm volatile("s_barrier" ::: "memory");
      count(x, FL_H(L, x.c, 0), 1);
      SB();
    }
    if constexpr ((i & 1) == 0) { if constexpr (i + DP < 196) { rd(std::integral_constant<int, i + DP>{}); rd(std::integral_constant<int, i + DP + 1>{}); wl<DP>(); } else wl<194 - i>(); }
    mm(ab[t][mat][0], wf[i & 7], K{}, I0{});
    if constexpr (j % 3 == 1 && j < 42) { if (L == 0) pf_wq<j / 3>(x); SB(); }
    if constexpr (j == 44) ldflag(fa, x.rs, OFF_FLAG + FL_H(L, x.c, 0));
    mm(ab[t][mat][1], wf[i & 7], K{}, I1{}); SB();
  });
  wv<0>();
  recheck(x, fa, FL_H(L, x.c, 0), 16);
  { const DnSrc d = dn_src(x, L); sfor<0, 3>([&](auto o) { dn_ld<o>(x, d, b); }); }
  sfor<0, 8>([&](auto i) { pin(ab[i >> 2][(i >> 1) & 1][i & 1]); });
  sfor<10, 14>([&](auto i) { pin(cs[i >> 1][i & 1]); });
  pair(ab[0], ab[1], std::integral_constant<int, 5>{});
}

DI void phase_down(Ctx& x, const int L, char* out, u32x4 (&b)[DWN][2]) {
  const int w = x.w;
  if (x.m >= 14) {
    wv<0>();
    sfor<0, 3>([&](auto o) { pin(b[o][0]); pin(b[o][1]); });
    signal(x, FL_H(L, x.c, 1), 1);
    return;
  }
  const int th = x.m / 7, ct = x.m % 7, tt = 4 * x.c + 2 * th + (w >> 1), nt = 2 * ct + (w & 1);
  constexpr int WN = DWN;
  u32x4 xr[2];
  const DnSrc d = dn_src(x, L);
  auto ld = [&](auto o) { dn_ld<o>(x, d, b); };
  sfor<3, WN>(ld);
  f32x16 acc[4];
  unsigned fb;
  sfor<0, 25>([&](auto i) {
    constexpr int last = i + WN - 1 < 27 ? i + WN - 1 : 27, ex = i <= 2 ? 2 : ((i >= 8 && i <= 13) ? 1 : ((i >= 21) ? 2 : 0));
    constexpr int later = [&] { int n = 0; for (int o = i + 1; o <= last; ++o) n += dn_half(o) ? 4 : 2; return n; }();
    wv<later + (dn_half(i) ? 2 : 1) + ex>();
    if constexpr (i == 3) signal(x, FL_H(L, x.c, 1), 1);
    m32a<A_WD + 8 * i, i == 0>(acc[0], b[i % WN][0]); m32a<A_WD + 8 * i + 4, i == 0>(acc[1], b[i % WN][0]);
    wv<later + ex>();
    m32a<A_WD + 8 * i, i == 0>(acc[2], b[i % WN][1]); m32a<A_WD + 8 * i + 4, i == 0>(acc[3], b[i % WN][1]);
    if constexpr (i == 7) ldflag(fb, x.rs, OFF_FLAG + FL_H(L, x.c, 1));
    if constexpr (i + WN == 20) { wv<12>(); recheck(x, fb, FL_H(L, x.c, 1), 16); }
    if constexpr (i + WN < 28) ld(std::integral_constant<int, i + WN>{});
    if constexpr (i + WN == 27) {
      const unsigned sr = OFF_X + (2 * L + 1) * SZ_X + (tt * 28 + 2 * nt) * KB;
      ld4<0>(xr[0], x.vl16, x.rs, sr); ld4<1024>(xr[1], x.vl16, x.rs, sr);
    }
  });
  wv<2>();
  sfor<1, 5>([&](auto t) {
    constexpr int k = t & 3;
    m32a<A_WD + 8 * 25 + 4 * (k & 1), false>(acc[k], b[25 % WN][k >> 1]);
    m32a<A_WD + 8 * 26 + 4 * (k & 1), false>(acc[k], b[26 % WN][k >> 1]);
    if constexpr (t > 1) { nop<(t == 2 ? 9 : 7)>(); red_put1<t - 1, 0>(x, acc[t - 1]); red_put1<t - 1, 1>(x, acc[t - 1]); }
    m32a<A_WD + 8 * 27 + 4 * (k & 1), false>(acc[k], b[27 % WN][k >> 1]);
    if constexpr (t > 1) { red_put1<t - 1, 2>(x, acc[t - 1]); red_put1<t - 1, 3>(x, acc[t - 1]); }
  });
  if (L == 0 && (x.m & 7) < 2) {
    ldl<4>(x.vl16, x.rs, OFF_WQ + ((16 + (x.m & 1)) * 28 + 7 * w) * KB, 7 * w * KB);
    ldl<3>(x.vl16, x.rs, OFF_WQ + ((16 + (x.m & 1)) * 28 + 7 * w + 4) * KB, (7 * w + 4) * KB);
  }
  nop<9>();
  pin(acc[0]);
  reduce_get(x, acc[0]);
  wv<0>();
  pin(xr[0]); pin(xr[1]);
  if (L > 0) {
    res_epilogue<true>(x, acc[0], xr, 0, 0, tt, nt, out);
  } else {
    res_epilogue<false>(x, acc[0], xr, OFF_X + 2 * SZ_X, OFF_ST + 2 * SZ_ST, tt, nt);
    wv<0>();
    signal(x, FL_X(x.c, th), 1);
  }
}

__global__ __launch_bounds__(256, 1) void k_main(Args) {
  typedef unsigned long long u64x8 __attribute__((ext_vector_type(8)));
  typedef unsigned long long u64x2 __attribute__((ext_vector_type(2)));
  u64x8 pin8; u64x2 pws;
  asm volatile("s_load_dwordx16 s[8:23], %4, 0x0\n\ts_load_dwordx4 s[24:27], %4, 0x40\n\t"
               "s_getpc_b64 s[4:5]\n.Lk_begin:\n\t"
               "s_mov_b32 s6, .Lk_end-.Lk_begin\n\ts_mov_b32 s7, 0x20000\n\ts_and_b32 s5, s5, 0xffff\n\t"
               "v_lshlrev_b32 v1, 4, %2\n\t"
               "s_bfe_u32 s28, %3, 0x20003\n\ts_lshl_b32 s28, s28, 12\n\t"
               "buffer_load_dwordx4 a[112:115], v1, s[4:7], s28 offen\n\t"
               "s_bfe_u32 s28, %3, 0x30004\n\ts_lshl_b32 s28, s28, 13\n\t"
               "buffer_load_dwordx4 a[112:115], v1, s[4:7], s28 offen\n\t"
               "s_add_u32 s28, s28, 4096\n\t"
               "buffer_load_dwordx4 a[112:115], v1, s[4:7], s28 offen\n\t"
               "s_waitcnt lgkmcnt(0)"
               : "={s[8:23]}"(pin8), "={s[24:27]}"(pws) : "v"(threadIdx.x), "{s2}"(blockIdx.x), "{s[0:1]}"(__builtin_amdgcn_kernarg_segment_ptr())
               : "s4", "s5", "s6", "s7", "s28", "v1", "scc", ACLOB);
  const Args a = {(const char*)pin8[0], (const char*)pin8[1], (const char*)pin8[2], (const char*)pin8[3], (const char*)pin8[4], (const char*)pin8[5],
                  (const char*)pin8[6], (const char*)pin8[7], (char*)pws[0], (char*)pws[1]};
  Ctx x;
  x.rs = mkrs(a.ws, WS_USED);
  x.w = __builtin_amdgcn_readfirstlane(threadIdx.x >> 6);
  asm volatile("s_nop 4" : "+s"(x.w));
  { const int b = blockIdx.x; x.c = 2 * (b & 7) + (b >> 7); x.m = (b >> 3) & 15; }
  f32x16 kf[3];
  { Ctx y = x; fresh(y); phase_pre(y, a, kf); }
  for (int L = 0;; ++L) {
    f32x16 g2[3];
    int Lo = L; asm volatile("" : "+s"(Lo));
    { Ctx y = x; fresh(y); phase_att(y, Lo, kf, g2); }
    { Ctx y = x; fresh(y); phase_oproj(y, Lo, a, g2); }
    { Ctx y = x; u32x4 b[DWN][2]; fresh(y); phase_gu(y, Lo, b); fresh(y); phase_down(y, Lo, a.out, b); }
    if (L == 1) break;
    { Ctx y = x; fresh(y); phase_front(y, kf); }
  }
  fresh(x);
  if (blockIdx.x == 0 && wave0(x)) {
    get_tag(x);
    if (x.l == 0) st2<0>(u32x2{x.tlo, x.thi}, 0, x.rs, OFF_TAG);
  }
  asm volatile(".Lk_end:");
}

extern "C" void kernel_launch(void* const* d_in, const int* in_sizes, int n_in, void* d_out, int out_size, void* d_ws, size_t ws_size,
                              hipStream_t stream) {
  Args a;
  a.x = (const char*)d_in[0]; a.wq = (const char*)d_in[1]; a.wk = (const char*)d_in[2]; a.wv = (const char*)d_in[3];
  a.wo = (const char*)d_in[4]; a.wg = (const char*)d_in[6]; a.wu = (const char*)d_in[7]; a.wd = (const char*)d_in[8];
  a.ws = (char*)d_ws; a.out = (char*)d_out;
  (void)hipFuncSetAttribute((const void*)k_main, hipFuncAttributeMaxDynamicSharedMemorySize, 160 * 1024);
  k_main<<<256, 256, 160 * 1024, stream>>>(a);
}
```

```cpp
#define TA 1
#define TB 1
#include <hip/hip_runtime.h>
#include <stdint.h>
#include <type_traits>

typedef float    f32x16 __attribute__((ext_vector_type(16)));
typedef float    f32x4  __attribute__((ext_vector_type(4)));
typedef float    f32x2  __attribute__((ext_vector_type(2)));
typedef unsigned u32x4  __attribute__((ext_vector_type(4)));
typedef unsigned u32x2  __attribute__((ext_vector_type(2)));
typedef __bf16   bf16x2 __attribute__((ext_vector_type(2)));
typedef __amdgpu_buffer_rsrc_t rsrc_t;
#define DI __device__ __forceinline__
#define SB() __builtin_amdgcn_sched_barrier(0)

constexpr int S = 2048, D = 448, F = 1792;
constexpr unsigned KB = 1024;
constexpr unsigned OFF_FLAG = 0;
constexpr unsigned OFF_CSQ  = 64 * KB;
constexpr unsigned OFF_CSGU = OFF_CSQ + 4 * KB;
constexpr unsigned OFF_SLOT = OFF_CSGU + 32 * KB;
constexpr unsigned OFF_TAG  = OFF_SLOT + 32 * KB;
constexpr unsigned OFF_WQ   = 160 * KB;
constexpr unsigned OFF_WO   = OFF_WQ + 18 * 28 * KB;
constexpr unsigned OFF_WGU  = OFF_WO + 14 * 32 * KB;
constexpr unsigned OFF_WD   = OFF_WGU + 16 * 196 * KB;
constexpr unsigned SZ_X = 64 * 28 * KB, SZ_ST = 14 * 2048 * 8, SZ_O = 64 * 32 * KB, SZ_H = 64 * 112 * KB, SZ_KV = 64 * 4 * KB;
constexpr unsigned OFF_X    = OFF_WD + 14 * 112 * KB;
constexpr unsigned OFF_ST   = OFF_X + 4 * SZ_X;
constexpr unsigned OFF_O    = OFF_ST + 4 * SZ_ST;
constexpr unsigned OFF_H    = OFF_O + 2 * SZ_O;
constexpr unsigned OFF_HL   = OFF_H + 2 * SZ_H;
constexpr unsigned OFF_KV   = OFF_HL + 2 * 1024 * KB;
constexpr unsigned OFF_TS   = OFF_KV + 2 * SZ_KV;
constexpr unsigned WS_USED  = OFF_TS + 520 * KB;
DI constexpr unsigned FL_X(int c, int th)         { return (c * 16 + th) * 256u; }
DI constexpr unsigned FL_O(int L, int c, int th)  { return (c * 16 + 2 + L * 2 + th) * 256u; }
DI constexpr unsigned FL_X1(int L, int c)         { return (c * 16 + 6 + L) * 256u; }
DI constexpr unsigned FL_H(int L, int c, int p)   { return (c * 16 + 8 + L * 2 + p) * 256u; }
DI constexpr unsigned FL_W(int c)                 { return (c * 16 + 12) * 256u; }
constexpr unsigned FL_W2 = 13 * 256u;

constexpr float QSCALE = 0.18898223650461362f * 1.4426950408889634f;
constexpr float M_LIMIT = 48.0f;

template <int I, int N, class Fn> DI void sfor(Fn&& f) { if constexpr (I < N) { f(std::integral_constant<int, I>{}); sfor<I + 1, N>(f); } }

DI unsigned cvtpk(float lo, float hi) { f32x2 v = {lo, hi}; bf16x2 b = __builtin_convertvector(v, bf16x2); return __builtin_bit_cast(unsigned, b); }
DI float bflo(unsigned u) { return __uint_as_float(u << 16); }
DI float bfhi(unsigned u) { return __uint_as_float(u & 0xffff0000u); }
DI float dot2(unsigned a, unsigned b, float c) { return __builtin_amdgcn_fdot2_f32_bf16(__builtin_bit_cast(bf16x2, a), __builtin_bit_cast(bf16x2, b), c, false); }
DI float sum32(float v) { auto r = __builtin_amdgcn_permlane32_swap(__float_as_uint(v), __float_as_uint(v), false, false); return __uint_as_float(r[0]) + __uint_as_float(r[1]); }
DI float sum16(float v) { auto r = __builtin_amdgcn_permlane16_swap(__float_as_uint(v), __float_as_uint(v), false, false); return __uint_as_float(r[0]) + __uint_as_float(r[1]); }
DI float max32(float v) { auto r = __builtin_amdgcn_permlane32_swap(__float_as_uint(v), __float_as_uint(v), false, false); return fmaxf(__uint_as_float(r[0]), __uint_as_float(r[1])); }

DI unsigned umax16(unsigned v) {
  v = max(v, (unsigned)__builtin_amdgcn_update_dpp(0, (int)v, 0x128, 0xf, 0xf, false));
  v = max(v, (unsigned)__builtin_amdgcn_update_dpp(0, (int)v, 0x124, 0xf, 0xf, false));
  v = max(v, (unsigned)__builtin_amdgcn_update_dpp(0, (int)v, 0x122, 0xf, 0xf, false));
  v = max(v, (unsigned)__builtin_amdgcn_update_dpp(0, (int)v, 0x121, 0xf, 0xf, false));
  return v;
}
DI unsigned umax64(unsigned v) {
  v = umax16(v);
  auto a = __builtin_amdgcn_permlane16_swap(v, v, false, false); v = max(a[0], a[1]);
  auto b = __builtin_amdgcn_permlane32_swap(v, v, false, false); return max(b[0], b[1]);
}

typedef short s16x4 __attribute__((ext_vector_type(4)));
DI u32x2 trread(const char* p) { return __builtin_bit_cast(u32x2, __builtin_amdgcn_ds_read_tr16_b64_v4i16((__attribute__((address_space(3))) s16x4*)p)); }
DI u32x4 trfrag(const char* lo, const char* hi) { const u32x2 a = trread(lo), b = trread(hi); return u32x4{a[0], a[1], b[0], b[1]}; }
DI void row_stats(float s, float q, float& mean, float& rstd) {
  mean = s * (1.0f / D);
  const float var = fmaxf(q * (1.0f / D) - mean * mean, 0.f);
  rstd = __builtin_amdgcn_rsqf(var + 1e-5f);
}
DI float fragsum(const u32x4& f, float s) {
#pragma unroll
  for (int d = 0; d < 4; ++d) s = dot2(f[d], 0x3f803f80u, s);
  return s;
}
struct Args { const char *x, *wq, *wk, *wv, *wo, *wg, *wu, *wd; char* ws; char* out; };

#define ACLOB "a0","a1","a2","a3","a4","a5","a6","a7","a8","a9","a10","a11","a12","a13","a14","a15","a16","a17","a18","a19","a20","a21","a22","a23","a24","a25","a26","a27","a28","a29","a30","a31","a32","a33","a34","a35","a36","a37","a38","a39","a40","a41","a42","a43","a44","a45","a46","a47","a48","a49","a50","a51","a52","a53","a54","a55","a56","a57","a58","a59","a60","a61","a62","a63","a64","a65","a66","a67","a68","a69","a70","a71","a72","a73","a74","a75","a76","a77","a78","a79","a80","a81","a82","a83","a84","a85","a86","a87","a88","a89","a90","a91","a92","a93","a94","a95","a96","a97","a98","a99","a100","a101","a102","a103","a104","a105","a106","a107","a108","a109","a110","a111","a112","a113","a114","a115","a116","a117","a118","a119","a120","a121","a122","a123","a124","a125","a126","a127","a128","a129","a130","a131","a132","a133","a134","a135","a136","a137","a138","a139","a140","a141","a142","a143","a144","a145","a146","a147","a148","a149","a150","a151","a152","a153","a154","a155","a156","a157","a158","a159","a160","a161","a162","a163","a164","a165","a166","a167","a168","a169","a170","a171","a172","a173","a174","a175","a176","a177","a178","a179","a180","a181","a182","a183","a184","a185","a186","a187","a188","a189","a190","a191","a192","a193","a194","a195","a196","a197","a198","a199","a200","a201","a202","a203","a204","a205","a206","a207","a208","a209","a210","a211","a212","a213","a214","a215","a216","a217","a218","a219","a220","a221","a222","a223","a224","a225","a226","a227","a228","a229","a230","a231","a232","a233","a234","a235","a236","a237","a238","a239","a240","a241","a242","a243","a244","a245","a246","a247","a248","a249","a250","a251","a252","a253","a254","a255"

template <int IMM> DI void ld4(u32x4& d, unsigned vo, rsrc_t rs, unsigned so) {
  asm volatile("buffer_load_dwordx4 %0, %1, %2, %3 offen offset:%c4 sc1" : "=v"(d) : "v"(vo), "s"(rs), "s"(so), "i"(IMM));
}
template <int IMM> DI void ld2(f32x2& d, unsigned vo, rsrc_t rs, unsigned so) {
  asm volatile("buffer_load_dwordx2 %0, %1, %2, %3 offen offset:%c4 sc1" : "=v"(d) : "v"(vo), "s"(rs), "s"(so), "i"(IMM));
}
template <int IMM> DI void ld2i(u32x2& d, unsigned vo, rsrc_t rs, unsigned so) {
  asm volatile("buffer_load_dwordx2 %0, %1, %2, %3 offen offset:%c4" : "=v"(d) : "v"(vo), "s"(rs), "s"(so), "i"(IMM));
}
template <int IMM> DI void ld2u(u32x2& d, unsigned vo, rsrc_t rs, unsigned so) {
  asm volatile("buffer_load_dwordx2 %0, %1, %2, %3 offen offset:%c4 sc1" : "=v"(d) : "v"(vo), "s"(rs), "s"(so), "i"(IMM));
}
template <int IMM> DI void ld4f(f32x4& d, unsigned vo, rsrc_t rs, unsigned so) {
  asm volatile("buffer_load_dwordx4 %0, %1, %2, %3 offen offset:%c4 sc1" : "=v"(d) : "v"(vo), "s"(rs), "s"(so), "i"(IMM));
}
DI void ld1(float& d, unsigned vo, rsrc_t rs, unsigned so) {
  asm volatile("buffer_load_dword %0, %1, %2, %3 offen sc1" : "=v"(d) : "v"(vo), "s"(rs), "s"(so));
}
template <int AR, int IMM> DI void lda(unsigned vo, rsrc_t rs, unsigned so) {
  asm volatile("buffer_load_dwordx4 a[%c3:%c4], %0, %1, %2 offen offset:%c5 sc1" :: "v"(vo), "s"(rs), "s"(so), "i"(AR), "i"(AR + 3), "i"(IMM) : ACLOB);
}
template <int N> DI void ldl(unsigned vo, rsrc_t rs, unsigned so, unsigned lds) {
  asm volatile("s_mov_b32 m0, %3\n\ts_nop 0\n\tbuffer_load_dwordx4 %0, %1, %2 offen sc1 lds" :: "v"(vo), "s"(rs), "s"(so), "s"(lds) : "memory");
  if constexpr (N > 1) asm volatile("buffer_load_dwordx4 %0, %1, %2 offen offset:1024 sc1 lds" :: "v"(vo), "s"(rs), "s"(so) : "memory");
  if constexpr (N > 2) asm volatile("buffer_load_dwordx4 %0, %1, %2 offen offset:2048 sc1 lds" :: "v"(vo), "s"(rs), "s"(so) : "memory");
  if constexpr (N > 3) asm volatile("buffer_load_dwordx4 %0, %1, %2 offen offset:3072 sc1 lds" :: "v"(vo), "s"(rs), "s"(so) : "memory");
}
template <int IMM> DI void st4(const u32x4& d, unsigned vo, rsrc_t rs, unsigned so) {
  asm volatile("buffer_store_dwordx4 %0, %1, %2, %3 offen offset:%c4 sc1\n\ts_nop 1" :: "v"(d), "v"(vo), "s"(rs), "s"(so), "i"(IMM) : "memory");
}
template <int IMM> DI void st4p(const u32x4& d, unsigned vo, rsrc_t rs, unsigned so) {
  asm volatile("buffer_store_dwordx4 %0, %1, %2, %3 offen offset:%c4\n\ts_nop 1" :: "v"(d), "v"(vo), "s"(rs), "s"(so), "i"(IMM) : "memory");
}
template <int IMM> DI void st2(const u32x2& d, unsigned vo, rsrc_t rs, unsigned so) {
  asm volatile("buffer_store_dwordx2 %0, %1, %2, %3 offen offset:%c4 sc1" :: "v"(d), "v"(vo), "s"(rs), "s"(so), "i"(IMM) : "memory");
}
DI void st1(float d, unsigned vo, rsrc_t rs, unsigned so) {
  asm volatile("buffer_store_dword %0, %1, %2, %3 offen sc1" :: "v"(d), "v"(vo), "s"(rs), "s"(so) : "memory");
}
template <int N> DI void wv() { asm volatile("s_waitcnt vmcnt(%c0)" :: "i"(N) : "memory"); SB(); }
template <int N> DI void wl() { asm volatile("s_waitcnt lgkmcnt(%c0)" :: "i"(N) : "memory"); SB(); }
template <class T> DI void pin(T& x) { asm volatile("" : "+v"(x)); }
DI void bar() { asm volatile("s_waitcnt lgkmcnt(0)\n\ts_barrier" ::: "memory"); }
template <int N> DI void nop() { asm volatile("s_nop %c0" :: "i"(N)); SB(); }

template <int OFF> DI void trd(u32x2& r, unsigned a) { asm volatile("ds_read_b64_tr_b16 %0, %1 offset:%c2" : "=v"(r) : "v"(a), "i"(OFF)); }
template <int OFF> DI void dsr(u32x4& r, unsigned a) { asm volatile("ds_read_b128 %0, %1 offset:%c2" : "=v"(r) : "v"(a), "i"(OFF)); }

DI void m32(f32x16& c, const u32x4& a, const u32x4& b)  { asm volatile("v_mfma_f32_32x32x16_bf16 %0, %1, %2, %0" : "+v"(c) : "v"(a), "v"(b)); }
DI void m32z(f32x16& c, const u32x4& a, const u32x4& b) { asm volatile("v_mfma_f32_32x32x16_bf16 %0, %1, %2, 0" : "=&v"(c) : "v"(a), "v"(b)); }
template <int AR, bool Z> DI void m32a(f32x16& c, const u32x4& b) {
  if constexpr (Z) asm volatile("v_mfma_f32_32x32x16_bf16 %0, a[%c2:%c3], %1, 0" : "=&v"(c) : "v"(b), "i"(AR), "i"(AR + 3));
  else             asm volatile("v_mfma_f32_32x32x16_bf16 %0, a[%c2:%c3], %1, %0" : "+v"(c) : "v"(b), "i"(AR), "i"(AR + 3));
}
template <bool Z> DI void m16(f32x4& c, const u32x4& a, const u32x4& b) {
  if constexpr (Z) asm volatile("v_mfma_f32_16x16x32_bf16 %0, %1, %2, 0" : "=&v"(c) : "v"(a), "v"(b));
  else             asm volatile("v_mfma_f32_16x16x32_bf16 %0, %1, %2, %0" : "+v"(c) : "v"(a), "v"(b));
}

template <bool Z, int AR> DI void m16a(f32x4& c, const u32x4& a) {
  if constexpr (Z) asm volatile("v_mfma_f32_16x16x32_bf16 %0, %1, a[%c2:%c3], 0" : "=&v"(c) : "v"(a), "i"(AR), "i"(AR + 3));
  else             asm volatile("v_mfma_f32_16x16x32_bf16 %0, %1, a[%c2:%c3], %0" : "+v"(c) : "v"(a), "i"(AR), "i"(AR + 3));
}
DI void ldflag(unsigned& v, rsrc_t rs, unsigned so) { asm volatile("buffer_load_dword %0, off, %1, %2 sc1" : "=v"(v) : "s"(rs), "s"(so)); }

struct Ctx {
  rsrc_t rs;
  unsigned tlo, thi;
  unsigned vl16;
  unsigned vg16;
  int w, l, c, m;
};
DI void fresh(Ctx& x) {
  asm volatile("" : "+s"(x.w), "+s"(x.c), "+s"(x.m));
  asm volatile("v_mbcnt_lo_u32_b32 %0, -1, 0\n\tv_mbcnt_hi_u32_b32 %0, -1, %0" : "=v"(x.l));
  x.vl16 = x.l * 16;
  const int g = x.l >> 4;
  x.vg16 = ((g >> 1) * 64 + (g & 1) * 32 + (x.l & 15)) * 16;
}
extern __shared__ __attribute__((aligned(16))) char lds[];
constexpr unsigned SCR = 112 * KB;

DI void pstamp(Ctx&) {}
DI void fstamp(Ctx&) {}
DI void stamp(Ctx&) {}
DI void stamp_flush(Ctx&) {}

DI void get_tag(Ctx& x) {
  unsigned lo, hi;
  asm volatile("v_accvgpr_read_b32 v255, a252\n\ts_nop 1\n\tv_readfirstlane_b32 %0, v255\n\tv_accvgpr_read_b32 v255, a253\n\ts_nop 1\n\tv_readfirstlane_b32 %1, v255\n\ts_nop 4"
               : "=s"(lo), "=s"(hi) :: "v255");
  const unsigned long long e = ((unsigned long long)hi << 32) | lo;
  unsigned long long n = e * 0x9E3779B97F4A7C15ull + 0x632BE59BD9B4E019ull;
  if (((n ^ e) >> 16) == 0) n += 0x10000;
  x.tlo = (unsigned)n; x.thi = (unsigned)(n >> 32);
}
DI bool wave0(const Ctx& x) { int w = x.w; asm volatile("" : "+s"(w)); return w == 0; }
DI void count(Ctx& x, unsigned fl, int n) {
  if (wave0(x)) {
    const unsigned long long mask = (1ull << n) - 1;
    unsigned long long keep;
    asm volatile("s_mov_b64 %0, exec\n\ts_mov_b64 exec, %5\n\tbuffer_atomic_add %1, %2, %3, %4 offen\n\ts_mov_b64 exec, %0"
                 : "=&s"(keep) : "v"(1u), "v"(x.l * 256), "s"(x.rs), "s"(OFF_FLAG + fl), "s"(mask) : "memory");
  }
}
DI void signal(Ctx& x, unsigned fl, int n) { bar(); count(x, fl, n); }
DI void await(Ctx& x, unsigned fl, unsigned target) {
  if (wave0(x)) {
#pragma nounroll
    for (unsigned spin = 0; spin < (1u << 18); ++spin) {
      unsigned v;
      asm volatile("buffer_load_dword %0, off, %1, %2 sc1\n\ts_waitcnt vmcnt(0)" : "=v"(v) : "s"(x.rs), "s"(OFF_FLAG + fl) : "memory");
      if ((unsigned)__builtin_amdgcn_readfirstlane(v) >= target) break;
      __builtin_amdgcn_s_sleep(1);
    }
  }
  bar();
}

DI void recheck(Ctx& x, unsigned v, unsigned fl, unsigned target) {
  pin(v);
#pragma nounroll
  for (unsigned spin = 0; (unsigned)__builtin_amdgcn_readfirstlane(v) < target && spin < (1u << 18); ++spin) {
    __builtin_amdgcn_s_sleep(2);
    asm volatile("buffer_load_dword %0, off, %1, %2 sc1\n\ts_waitcnt vmcnt(0)" : "=v"(v) : "s"(x.rs), "s"(OFF_FLAG + fl) : "memory");
  }
  SB();
}

DI void reduce_put(Ctx& x, const f32x16 (&acc)[4]) {
  sfor<1, 4>([&](auto k) {
    f32x4* p = (f32x4*)(lds + SCR + ((x.w ^ k) * 3 + k - 1) * 4096 + x.vl16);
#pragma unroll
    for (int q = 0; q < 4; ++q) p[q * 64] = f32x4{acc[k][4 * q], acc[k][4 * q + 1], acc[k][4 * q + 2], acc[k][4 * q + 3]};
  });
}
template <int K, int Q> DI void red_put1(Ctx& x, const f32x16& t) {
  asm volatile("ds_write_b128 %0, %1 offset:%c2" :: "v"(SCR + ((x.w ^ K) * 3 + K - 1) * 4096 + x.vl16),
               "v"(__builtin_shufflevector(t, t, 4 * Q, 4 * Q + 1, 4 * Q + 2, 4 * Q + 3)), "i"(Q * 1024) : "memory");
}
DI void reduce_get(Ctx& x, f32x16& own) {
  bar();
  const f32x4* p = (const f32x4*)(lds + SCR + x.w * 3 * 4096 + x.vl16);
#pragma unroll
  for (int k = 0; k < 3; ++k)
#pragma unroll
    for (int q = 0; q < 4; ++q) {
      f32x4 v = p[k * 256 + q * 64];
#pragma unroll
      for (int i = 0; i < 4; ++i) own[4 * q + i] += v[i];
    }
}

constexpr int A_WO = 184, A_WD = 0, A_GX = 224;
constexpr int dn_ks(int o) { return o < 20 ? 2 * ((o >> 1) % 5 + ((o >> 1) % 5 >= 2) + 7 * (o / 10)) + (o & 1) : 2 * (((o - 20) >> 1) * 4 - ((o - 20) >> 2) + 2) + (o & 1); }
static_assert(dn_ks(0) == 0 && dn_ks(4) == 6 && dn_ks(9) == 11 && dn_ks(10) == 14 && dn_ks(19) == 25 && dn_ks(20) == 4 && dn_ks(22) == 12 && dn_ks(24) == 18 && dn_ks(27) == 27, "");
struct Two { unsigned s0, s1; };
constexpr int SL_WQ = 56;
template <int J> DI void pf_wq(Ctx& x) {
  ldl<1>(x.vl16, x.rs, OFF_WQ + ((x.m & 7) * 56 + 14 * x.w + J) * KB, (SL_WQ + 14 * x.w + J) * KB);
}
DI Two base_wo(const Ctx& x) { const int ct = x.m % 7;
  return Two{OFF_WO + ((2 * ct + (x.w & 1)) * 32 + 8 * x.w) * KB, OFF_WO + ((2 * ct + ((x.w & 1) ^ 1)) * 32 + 8 * x.w) * KB}; }
DI void pf_wo(Ctx& x) {
  const Two b = base_wo(x);
  sfor<0, 8>([&](auto i) {
    lda<A_WO + 8 * i, (i & 3) * 1024>(x.vl16, x.rs, b.s0 + (i >> 2) * 4096);
    lda<A_WO + 8 * i + 4, (i & 3) * 1024>(x.vl16, x.rs, b.s1 + (i >> 2) * 4096);
  });
}
DI Two base_wd(const Ctx& x) { const int ct = x.m % 7;
  return Two{OFF_WD + ((2 * ct + (x.w & 1)) * 112 + 28 * x.w) * KB, OFF_WD + ((2 * ct + ((x.w & 1) ^ 1)) * 112 + 28 * x.w) * KB}; }
template <int I0, int I1> DI void pf_wd(Ctx& x, const Two& b) {
  sfor<I0, I1>([&](auto f) {
    constexpr int i = f >> 1;
    lda<A_WD + 4 * f, (i & 3) * 1024>(x.vl16, x.rs, ((f & 1) ? b.s1 : b.s0) + (i >> 2) * 4096);
  });
}
DI void pf_wgu1(Ctx& x) {
  for (int t = 0; t < 14; ++t) ldl<2>(x.vl16, x.rs, OFF_WGU + (x.m * 196 + 8 * t + 2 * x.w) * KB, (8 * t + 2 * x.w) * KB);
}
template <int J> DI void pf_wgu3(Ctx& x) {
  ldl<1>(x.vl16, x.rs, OFF_WGU + (x.m * 196 + 160 + 9 * x.w + J) * KB, (9 * x.w + J) * KB);
}

constexpr unsigned S_QP = SCR, S_KP = SCR + 16384, S_QF = SCR + 24576, S_ST = SCR + 37888, S_CS = SCR + 38912, S_KM = SCR + 36864, S_TR = SCR + 40960;
DI unsigned kv_finish(Ctx& x, const float (&raw)[16], const f32x4 (&cs)[4], float mean, float rstd, bool isv, unsigned so, int slot) {
  const int h = x.l >> 5, r = x.l & 31;
  u32x4 y[2];
#pragma unroll
  for (int d = 0; d < 8; ++d)
    y[d >> 2][d & 3] = cvtpk((raw[2 * d] - mean * cs[d >> 1][2 * (d & 1)]) * rstd, (raw[2 * d + 1] - mean * cs[d >> 1][2 * (d & 1) + 1]) * rstd);
  if (h) { y[1][2] = 0; y[1][3] = 0; }
  unsigned nb = 0;
  if (!isv) {
    u32x4 y1 = y[1];
    if (h) y1[2] = 0x3f80u;
    st4<0>(y[0], x.vl16, x.rs, so); st4<1024>(y1, x.vl16, x.rs, so);
    float n2 = 0.f, n3 = 0.f;
#pragma unroll
    for (int d = 0; d < 4; ++d) { n2 = dot2(y[0][d], y[0][d], n2); n3 = dot2(y[1][d], y[1][d], n3); }
    n2 = sum32(n2 + n3);
    nb = umax64(n2 < 3.0e38f ? __float_as_uint(n2) : 0x7f800000u);
    nb = cvtpk(1.01f * __builtin_amdgcn_sqrtf(__uint_as_float(nb)), 0.f);
  } else {
    if (h) y[1][2] = 0x3f80u;
    char* im = lds + S_TR + slot * 2048;
#pragma unroll
    for (int a = 0; a < 4; ++a) *(u32x2*)(im + r * 64 + 16 * a + 8 * h) = u32x2{y[a >> 1][2 * (a & 1)], y[a >> 1][2 * (a & 1) + 1]};
    asm volatile("s_waitcnt lgkmcnt(0)" ::: "memory");
    const char* tb = im + (4 * h + ((x.l >> 2) & 3)) * 64 + ((x.l >> 4) & 1) * 32 + (x.l & 3) * 8;
    const u32x4 v0 = trfrag(tb, tb + 512), v1 = trfrag(tb + 1024, tb + 1536);
    st4<0>(v0, x.vl16, x.rs, so); st4<1024>(v1, x.vl16, x.rs, so);
  }
  return nb;
}
DI void kv_publish(Ctx& x, int L, int p, unsigned km) {
  get_tag(x);
  st2<0>(u32x2{(x.tlo & 0xffff0000u) | km, x.thi}, x.l < 16 ? x.l * 1024 : ~0u, x.rs, OFF_SLOT + (L * 2048 + p) * 8);
}
DI void q_finish(Ctx& x, const f32x16& qa, const f32x4 (&cq)[4], float mean, float rstd) {
  const int h = x.l >> 5;
  u32x4 qf[2];
  const f32x4* p = (const f32x4*)(lds + S_QP + x.w * 4096 + x.vl16);
  const float sc = rstd * QSCALE;
#pragma unroll
  for (int q = 0; q < 4; ++q) {
    const f32x4 o = p[q * 64];
    float v[4];
#pragma unroll
    for (int i = 0; i < 4; ++i) v[i] = (qa[4 * q + i] + o[i] - mean * cq[q][i]) * sc;
    qf[q >> 1][2 * (q & 1)] = cvtpk(v[0], v[1]); qf[q >> 1][2 * (q & 1) + 1] = cvtpk(v[2], v[3]);
  }
  if (h) { qf[1][2] = 0; qf[1][3] = 0; }
  float qn2 = 0.f, qn3 = 0.f;
#pragma unroll
  for (int d = 0; d < 4; ++d) { qn2 = dot2(qf[0][d], qf[0][d], qn2); qn3 = dot2(qf[1][d], qf[1][d], qn3); }
  qn2 = sum32(qn2 + qn3);
  if (h) qf[1][2] = cvtpk(1.01f * __builtin_amdgcn_sqrtf(qn2), 0.f);
  ((u32x4*)(lds + S_QF + x.w * 2048 + x.vl16))[0] = qf[0];
  ((u32x4*)(lds + S_QF + x.w * 2048 + x.vl16))[64] = qf[1];
}
DI void put_tile(char* p, const f32x16& t) {
#pragma unroll
  for (int q = 0; q < 4; ++q) ((f32x4*)p)[q * 64] = f32x4{t[4 * q], t[4 * q + 1], t[4 * q + 2], t[4 * q + 3]};
}

constexpr unsigned IMG_Q = 0, IMG_T = 56 * KB;
DI rsrc_t mkrs(const char* p, unsigned n) { return __builtin_amdgcn_make_buffer_rsrc((char*)p, 0, n, 0x00020000); }

DI void ask_slots(Ctx& x, int L) {
  if (wave0(x)) asm volatile("buffer_load_dwordx4 a[248:251], %0, %1, %2 offen sc1" :: "v"(x.vl16), "s"(x.rs), "s"(OFF_SLOT + (L * 16 + x.c) * 1024) : ACLOB);
}
DI void kv_wait(Ctx& x, int L) {
  if (wave0(x)) get_tag(x);
  wv<0>();
  if (wave0(x)) {
    unsigned s0, s1, s2, s3;
#pragma nounroll
    for (unsigned spin = 0; spin < (1u << 18); ++spin) {
      asm volatile("v_accvgpr_read_b32 %0, a248\n\tv_accvgpr_read_b32 %1, a249\n\tv_accvgpr_read_b32 %2, a250\n\tv_accvgpr_read_b32 %3, a251" : "=v"(s0), "=v"(s1), "=v"(s2), "=v"(s3));
      if (__all(s1 == x.thi && s3 == x.thi && (((s0 ^ x.tlo) | (s2 ^ x.tlo)) >> 16) == 0)) break;
      __builtin_amdgcn_s_sleep(2);
      ask_slots(x, L); wv<0>();
    }
    const unsigned km = umax64(max(s0 << 16, s2 << 16));
    if (x.l == 0) ((unsigned*)(lds + S_KM))[4] = km;
  }
  bar();
}
DI void ask_kv(Ctx& x, int L, f32x16 (&kf)[3]) {
  const unsigned kvs = OFF_KV + L * SZ_KV + 16 * x.w * 4096;
  asm volatile(".irp i,0,1,2,3\n\tbuffer_load_dwordx4 v[128+4*\\i:131+4*\\i], %3, %4, %5 offen offset:1024*\\i sc1\n\t.endr\n\t"
               ".irp i,0,1,2,3\n\tbuffer_load_dwordx4 v[144+4*\\i:147+4*\\i], %3, %4, %6 offen offset:1024*\\i sc1\n\t.endr\n\t"
               ".irp i,0,1,2,3\n\tbuffer_load_dwordx4 v[160+4*\\i:163+4*\\i], %3, %4, %7 offen offset:1024*\\i sc1\n\t.endr"
               : "={v[128:143]}"(kf[0]), "={v[144:159]}"(kf[1]), "={v[160:175]}"(kf[2]) : "{v204}"(x.vl16), "s"(x.rs), "s"(kvs), "s"(kvs + 4096), "s"(kvs + 8192));
}

DI void phase_pre(Ctx& x, const Args& a, f32x16 (&kf)[3]) {
  const int w = x.w, l = x.l, h = l >> 5, r = l & 31;
  const int th = x.m >> 3, hp = x.m & 7, tl = w & 1, kh = w >> 1, tt = 4 * x.c + 2 * th + tl, wi = 2 * x.c + th;
  const bool kvp = hp < 2;
  u32x4 xf[14];
  { const rsrc_t rx = mkrs(a.x, S * D * 2);
    sfor<0, 16>([&](auto i) {
      const int c = r ^ ((2 * i + h) & 15);
      ldl<1>(c < 28 ? h * 896 + c * 16 : 0x80000000u, rx, (tt * 32 + 2 * i) * 896 + kh * 448, (84 + 16 * w + i) * KB);
    }); }
  if (x.m == 2) asm volatile("buffer_load_dword a116, %0, %1, 0 offen sc1" :: "v"(l < 16 ? OFF_FLAG + (x.c * 16 + l) * 256 : OFF_SLOT + ((l >> 3) & 1) * 16384 + x.c * 1024 + (l & 7) * 128), "s"(x.rs) : ACLOB);
  asm volatile("buffer_load_dwordx2 a[252:253], off, %0, %1 sc1" :: "s"(x.rs), "s"(OFF_TAG) : ACLOB);
  pstamp(x);
  const int task = wi * 6 + hp - 2;
  const rsrc_t rq = mkrs(a.wq, D * D * 2);
  auto ask_q = [&](auto i) {
    ldl<1>((8 * w + (l >> 3)) * 896 + (l & 7) * 16, rq, hp * 112 + i * 32 * 896, IMG_Q + (4 * i + w) * KB);
  };
  auto ask_t = [&](const char* tp, unsigned tn, unsigned ts, unsigned tb) {
    const rsrc_t rt = mkrs(tp, tn);
    const unsigned vo = (8 * w + (l >> 3)) * ts + (l & 7) * 16;
    sfor<0, 7>([&](auto i) { ldl<1>(vo, rt, tb + i * 32 * ts, IMG_T + (4 * i + w) * KB); });
  };
  const int tco = ((l >> 4) & 1) * 32 + (l & 3) * 8;
  const int sro = 4 * h + ((l >> 2) & 3);
  if (kvp) ask_t(hp ? a.wv : a.wk, D * 56, 128, 0);
  else if (task < 112) ask_t(task < 56 ? a.wg : a.wu, D * F * 2, F * 2, (task & 1) * 224 * F * 2 + ((task % 56) >> 1) * 128);
  else if (task < 168) ask_t(a.wd, D * F * 2, 896, ((task - 112) / 7) * 224 * 896 + ((task - 112) % 7) * 128);
  else ask_t(a.wo, task < 182 ? D * D * 2 : 0, 896, ((task - 168) / 7) * 224 * 896 + ((task - 168) % 7) * 128);
  wv<8>();
  sfor<0, 14>([&](auto i) { dsr<0>(xf[i], (84 + w * 16) * KB + r * 512 + (((2 * i + h) ^ (r & 15)) << 4)); });
  wl<0>();
  {
    float s = 0.f, q = 0.f, s1 = 0.f, q1 = 0.f;
    sfor<0, 14>([&](auto i) {
      ask_q(i);
      pin(xf[i]);
      s = dot2(xf[i][0], 0x3f803f80u, s); q = dot2(xf[i][0], xf[i][0], q); s1 = dot2(xf[i][1], 0x3f803f80u, s1); q1 = dot2(xf[i][1], xf[i][1], q1);
      s = dot2(xf[i][2], 0x3f803f80u, s); q = dot2(xf[i][2], xf[i][2], q); s1 = dot2(xf[i][3], 0x3f803f80u, s1); q1 = dot2(xf[i][3], xf[i][3], q1);
      pin(s); pin(q); pin(s1); pin(q1);
    });
    s = sum32(s + s1); q = sum32(q + q1);
    if (h == 0) ((f32x2*)(lds + S_ST))[w * 32 + r] = f32x2{s, q};
  }
  wv<14>();
  bar();
  stamp(x);
  if (!kvp) {
    if (task < 112) {
      const int g = l >> 4, mat = task >= 56, gt = 4 * ((task % 56) >> 1) + w, m = gt / 7, j = gt % 7, lt = (m & 1) ? j : (j == 6 ? 0 : j + 1);
      const char* tb = lds + IMG_T + (16 * (g >> 1) + 4 * (g & 1) + ((l >> 2) & 3)) * 128 + 32 * w + (l & 3) * 8;
      const unsigned st = lt < 5 ? 10 * KB : 4 * KB, so = OFF_WGU + (m * 196 + (lt < 5 ? lt * 2 + mat : 140 + (lt - 5) * 2 + mat)) * KB + (task & 1) * 7 * st;
      u32x4 f[7];
      sfor<0, 7>([&](auto i) { f[i] = trfrag(tb + i * 4096, tb + i * 4096 + 1024); });
      float cs = 0.f;
      sfor<0, 7>([&](auto i) { st4<0>(f[i], x.vl16, x.rs, so + i * st); cs = fragsum(f[i], cs); });
      cs = sum32(sum16(cs));
      st1(cs, l < 16 ? l * 4 : ~0u, x.rs, OFF_CSGU + m * 2048 + (task & 1) * 1024 + (lt * 2 + mat) * 64);
    } else if (task < 168) {
      const int nt = 2 * ((task - 112) % 7) + (w & 1), rb = (task - 112) / 7;
      const char* tb = lds + IMG_T + sro * 128 + 64 * (w & 1) + tco;
      const unsigned so = OFF_WD + (nt * 112 + 28 * (rb >> 1)) * KB, sa = so + (rb & 1) * 10 * KB, sb = so + (20 + (rb & 1) * 4) * KB;
      u32x4 f[7];
      if (w < 2) {
        sfor<0, 7>([&](auto i) { const char* p = tb + (32 * (i >> 1) + 8 * (i & 1)) * 128; f[i] = trfrag(p, p + 2048); });
        sfor<0, 7>([&](auto i) { st4<0>(f[i], x.vl16, x.rs, i < 4 ? sa + i * KB : (i < 6 ? sb + (i - 4) * KB : sa + 4 * KB)); });
      } else {
        sfor<0, 7>([&](auto i) { const char* p = tb + (32 * ((i + 7) >> 1) + 8 * ((i + 7) & 1)) * 128; f[i] = trfrag(p, p + 2048); });
        sfor<0, 7>([&](auto i) { st4<0>(f[i], x.vl16, x.rs, i < 5 ? sa + (5 + i) * KB : sb + (i - 3) * KB); });
      }
      st1(0.f, ~0u, x.rs, 0);
    } else {
      const int nt = 2 * ((task - 168) % 7) + (w & 1);
      const char* tb = lds + IMG_T + (112 * (w >> 1) + sro) * 128 + 64 * (w & 1) + tco;
      const unsigned vo = task < 182 ? x.vl16 : ~0u - 4096;
      u32x4 f[8];
      sfor<0, 8>([&](auto i) { const char* p = tb + (28 * (i >> 1) + 16 * (i & 1)) * 128; f[i] = trfrag(p, p + 1024); if ((i & 1) && h) { f[i][2] = 0; f[i][3] = 0; } });
      sfor<0, 8>([&](auto i) { st4<(i & 3) * 1024>(f[i], vo, x.rs, OFF_WO + (nt * 32 + 16 * ((task - 168) / 7) + 8 * (w >> 1) + (i & ~3)) * KB); });
    }
  }
  pstamp(x);
  if (!kvp) { wv<8>(); bar(); }
  stamp(x);
  float mean, rstd;
  { const f32x2 p0 = ((const f32x2*)(lds + S_ST))[tl * 32 + r], p1 = ((const f32x2*)(lds + S_ST))[(tl + 2) * 32 + r];
    row_stats(p0[0] + p1[0], p0[1] + p1[1], mean, rstd); }
  float* csw = (float*)(lds + S_CS);
  const int tro = 8 * h + ((l >> 2) & 3);
  u32x4 ones = {0x3f803f80u, 0x3f803f80u, 0x3f803f80u, 0x3f803f80u};
  pin(ones);
  if (kvp) {
    f32x16 kv, cs;
    const unsigned tb = IMG_T + (224 * kh + tro) * 56 + tco;
    u32x2 f[14][2];
    auto rd = [&](auto i) { trd<i * 896>(f[i][0], tb); trd<i * 896 + 224>(f[i][1], tb); };
    sfor<0, 7>(rd);
    sfor<0, 14>([&](auto i) {
      if constexpr (i + 7 < 14) { wl<12>(); rd(std::integral_constant<int, i + 7>{}); } else wl<2 * (13 - i)>();
      pin(f[i][0]); pin(f[i][1]);
      u32x4 g = {f[i][0][0], f[i][0][1], f[i][1][0], f[i][1][1]};
      pin(g); nop<0>();
      if constexpr (i == 0) { m32z(kv, g, xf[i]); m32z(cs, g, ones); } else { m32(kv, g, xf[i]); m32(cs, g, ones); }
    });
    nop<11>(); pin(kv); pin(cs);
    if (r == 0) sfor<0, 4>([&](auto q) { *(f32x4*)(csw + (w * 3 + 2) * 32 + 8 * q + 4 * h) = f32x4{cs[4 * q], cs[4 * q + 1], cs[4 * q + 2], cs[4 * q + 3]}; });
    pstamp(x);
    if (kh) put_tile(lds + S_KP + tl * 4096 + x.vl16, kv);
    bar();
    if (!kh) {
      if (x.m == 0 && w == 0) st1(0.f, l < 16 ? l * 256 : ~0u, x.rs, OFF_FLAG + x.c * 4096);
      float raw[16]; f32x4 c4[4];
      sfor<0, 4>([&](auto q) {
        const f32x4 o = ((const f32x4*)(lds + S_KP + tl * 4096 + x.vl16))[q * 64];
        c4[q] = *(const f32x4*)(csw + (w * 3 + 2) * 32 + 8 * q + 4 * h) + *(const f32x4*)(csw + ((w + 2) * 3 + 2) * 32 + 8 * q + 4 * h);
#pragma unroll
        for (int i = 0; i < 4; ++i) raw[4 * q + i] = kv[4 * q + i] + o[i];
      });
      const unsigned km = kv_finish(x, raw, c4, mean, rstd, hp == 1, OFF_KV + (tt * 4 + 2 * hp) * KB, tl);
      pstamp(x);
      wv<0>();
      kv_publish(x, 0, 2 * tt + hp, km);
      if (tt == 0 && r == 0) sfor<0, 4>([&](auto q) { st4<32 * q>(__builtin_bit_cast(u32x4, c4[q]), h * 16, x.rs, OFF_CSQ + (16 + hp) * 128); });
    } else {
      pstamp(x);
      wv<0>();
    }
    bar();
    pstamp(x);
  } else { pstamp(x); pstamp(x); pstamp(x); }
  f32x16 qa;
  {
    f32x16 q1, q2, cs;
    const unsigned t1 = IMG_Q + (224 * kh + tro) * 128 + tco + 56 * tl, t2 = IMG_Q + (224 * kh + tro) * 128 + tco + 56 * (tl ^ 1);
    u32x2 f[14][4];
    auto rd = [&](auto i) { trd<i * 2048>(f[i][0], t1); trd<i * 2048 + 512>(f[i][1], t1); trd<i * 2048>(f[i][2], t2); trd<i * 2048 + 512>(f[i][3], t2); };
    wl<0>();
    sfor<0, 3>(rd);
    sfor<0, 14>([&](auto i) {
      if constexpr (i + 3 < 14) { wl<8>(); rd(std::integral_constant<int, i + 3>{}); } else wl<4 * (13 - i)>();
      sfor<0, 4>([&](auto e) { pin(f[i][e]); });
      u32x4 g1 = {f[i][0][0], f[i][0][1], f[i][1][0], f[i][1][1]}, g2 = {f[i][2][0], f[i][2][1], f[i][3][0], f[i][3][1]};
      pin(g1); pin(g2); nop<0>();
      if constexpr (i == 0) { m32z(q1, g1, xf[i]); m32z(q2, g2, xf[i]); m32z(cs, g1, ones); } else { m32(q1, g1, xf[i]); m32(q2, g2, xf[i]); m32(cs, g1, ones); }
    });
    nop<11>(); pin(q1); pin(q2); pin(cs);
    pstamp(x);
    if (r == 0) sfor<0, 4>([&](auto q) { *(f32x4*)(csw + (w * 3) * 32 + 8 * q + 4 * h) = f32x4{cs[4 * q], cs[4 * q + 1], cs[4 * q + 2], cs[4 * q + 3]}; });
    if (kh == tl) { put_tile(lds + S_QP + (w ^ 2) * 4096 + x.vl16, q2); qa = q1; } else { put_tile(lds + S_QP + (w ^ 2) * 4096 + x.vl16, q1); qa = q2; }
  }
  if (th == 0 && x.c < 14) {
    const int f = 4 * x.c + w, hd = f >= 28, ks = f - 28 * hd;
    const char* tb = lds + IMG_Q + (16 * ks + sro) * 128 + tco + 56 * hd;
    st4<0>(trfrag(tb, tb + 1024), x.vl16, x.rs, OFF_WQ + (hp * 56 + f) * KB);
  }
  if (kvp && th == 1 && x.c < 7) {
    const char* tb = lds + IMG_T + (16 * (4 * x.c + w) + sro) * 56 + tco;
    st4<0>(trfrag(tb, tb + 448), x.vl16, x.rs, OFF_WQ + ((16 + hp) * 28 + 4 * x.c + w) * KB);
  }
  pstamp(x);
  if (!kvp) __builtin_amdgcn_s_sleep(12);
  ask_slots(x, 0);
  kv_wait(x, 0);
  count(x, FL_W(x.c), 1);
  ask_kv(x, 0, kf);
  stamp(x);
  const bool own = x.c == hp && th == 0;
  {
    f32x4 cq[4];
    sfor<0, 4>([&](auto q) {
      cq[q] = *(const f32x4*)(csw + (kh * 3) * 32 + 8 * q + 4 * h) + *(const f32x4*)(csw + ((kh + 2) * 3) * 32 + 8 * q + 4 * h);
      if (own && tl == 0 && r == 0) st4<32 * q>(__builtin_bit_cast(u32x4, cq[q]), h * 16, x.rs, OFF_CSQ + (2 * hp + kh) * 128);
    });
    q_finish(x, qa, cq, mean, rstd);
  }
  pstamp(x);
}

DI void phase_front(Ctx& x, f32x16 (&kf)[3]) {
  const int w = x.w, l = x.l, h = l >> 5, r = l & 31;
  const int th = x.m >> 3, hp = x.m & 7, tl = w & 1, kh = w >> 1, tt = 4 * x.c + 2 * th + tl;
  const unsigned KV = OFF_KV + SZ_KV;
  {
    f32x4 cq[4], ck[4];
    sfor<0, 4>([&](auto q) { ld4f<32 * q>(cq[q], h * 16, x.rs, OFF_CSQ + (2 * hp + kh) * 128); ld4f<32 * q>(ck[q], h * 16, x.rs, OFF_CSQ + (16 + (hp & 1)) * 128); });
    wv<0>();
    await(x, FL_X(x.c, th), 7);
    stamp(x);
    u32x4 xf[14]; f32x2 sp[7];
    {
      const unsigned so = OFF_X + 2 * SZ_X + (tt * 28 + 14 * kh) * KB;
      sfor<0, 7>([&](auto i) { ld4<(i & 3) * 1024>(xf[i], x.vl16, x.rs, so + (i >> 2) * 4096); });
      sfor<0, 7>([&](auto i) { ld2<0>(sp[i], (h * 7 * 2048 + r) * 8, x.rs, OFF_ST + 2 * SZ_ST + (i * 2048 + tt * 32) * 8); });
      sfor<7, 14>([&](auto i) { ld4<(i & 3) * 1024>(xf[i], x.vl16, x.rs, so + (i >> 2) * 4096); });
    }
    float mean, rstd;
    auto stats = [&]() {
      sfor<0, 7>([&](auto i) { pin(sp[i]); });
      sfor<0, 4>([&](auto i) { pin(cq[i]); pin(ck[i]); });
      float s = 0.f, q = 0.f;
#pragma unroll
      for (int i = 0; i < 7; ++i) { s += sp[i][0]; q += sp[i][1]; }
      row_stats(sum32(s), sum32(q), mean, rstd);
      pin(mean); pin(rstd); SB();
    };
    f32x16 qa, qb;
    u32x4 wa[14], wb[14];
    const unsigned pa = x.vl16 + (SL_WQ + 28 * kh + 14 * kh) * KB, pb = x.vl16 + (SL_WQ + 28 * (kh ^ 1) + 14 * kh) * KB;
    if (hp < 2) {
      f32x16 kv;
      sfor<0, 14>([&](auto i) { dsr<i * 1024>(wa[i], x.vl16 + 14 * kh * KB); });
      sfor<0, 14>([&](auto i) {
        wv<(i < 7 ? 20 : 13) - i>(); wl<13 - i>();
        if constexpr (i == 0) m32z(kv, wa[i], xf[i]); else m32(kv, wa[i], xf[i]);
        if constexpr (i == 7) stats();
      });
      nop<11>(); pin(kv);
      if (kh) put_tile(lds + S_KP + tl * 4096 + x.vl16, kv);
      bar();
      unsigned km = 0;
      if (!kh) {
        float raw[16];
        sfor<0, 4>([&](auto q) {
          const f32x4 o = ((const f32x4*)(lds + S_KP + tl * 4096 + x.vl16))[q * 64];
#pragma unroll
          for (int i = 0; i < 4; ++i) raw[4 * q + i] = kv[4 * q + i] + o[i];
        });
        km = kv_finish(x, raw, ck, mean, rstd, hp == 1, KV + (tt * 4 + 2 * hp) * KB, tl);
      }
      wl<0>();
      sfor<0, 7>([&](auto i) { dsr<i * 1024>(wb[i], pb); dsr<i * 1024>(wa[i], pa); });
      sfor<0, 14>([&](auto i) {
        if constexpr (i < 7) wl<12>(); else wl<2 * (13 - i)>();
        if constexpr (i == 0) { m32z(qb, wb[i], xf[i]); m32z(qa, wa[i], xf[i]); } else { m32(qb, wb[i], xf[i]); m32(qa, wa[i], xf[i]); }
        if constexpr (i < 7) { dsr<(i + 7) * 1024>(wb[i + 7], pb); dsr<(i + 7) * 1024>(wa[i + 7], pa); }
        if constexpr (i == 6) { if (!kh) { wv<0>(); kv_publish(x, 1, 2 * tt + hp, km); } SB(); }
      });
    } else {
      sfor<0, 7>([&](auto i) { dsr<i * 1024>(wb[i], pb); dsr<i * 1024>(wa[i], pa); });
      sfor<0, 14>([&](auto i) {
        wv<(i < 7 ? 20 : 13) - i>();
        if constexpr (i < 7) wl<12>(); else wl<2 * (13 - i)>();
        if constexpr (i == 0) { m32z(qb, wb[i], xf[i]); m32z(qa, wa[i], xf[i]); } else { m32(qb, wb[i], xf[i]); m32(qa, wa[i], xf[i]); }
        if constexpr (i < 7) { dsr<(i + 7) * 1024>(wb[i + 7], pb); dsr<(i + 7) * 1024>(wa[i + 7], pa); }
        if constexpr (i == 7) stats();
      });
    }
    nop<11>();
    pin(qa); pin(qb);
    put_tile(lds + S_QP + (w ^ 2) * 4096 + x.vl16, qb);
    bar();
    q_finish(x, qa, cq, mean, rstd);
    stamp(x);
    if (hp >= 2) __builtin_amdgcn_s_sleep(36);
    ask_slots(x, 1);
    kv_wait(x, 1);
    ask_kv(x, 1, kf);
    stamp(x);
  }
}

DI void phase_att(Ctx& x, const int L, f32x16 (&kf)[3], f32x16 (&g2)[3]) {
  const int w = x.w, l = x.l, h = l >> 5, r = l & 31;
  const int th = x.m >> 3, hp = x.m & 7, tl = w & 1, kh = w >> 1, tt = 4 * x.c + 2 * th + tl;
  const unsigned kvs = OFF_KV + L * SZ_KV + 16 * w * 4096;
  stamp(x);
  if (L == 0) bar();
  stamp(x);
  unsigned kma = S_KM + 16; asm volatile("" : "+s"(kma));
  const float kmx = *(const float*)(lds + kma);
  u32x4 q[4][2];
#pragma unroll
  for (int k = 0; k < 4; ++k) {
    q[k][0] = ((const u32x4*)(lds + S_QF + (w ^ k) * 2048 + x.vl16))[0];
    q[k][1] = ((const u32x4*)(lds + S_QF + (w ^ k) * 2048 + x.vl16))[64];
  }
  bool safe = true;
  {
#pragma unroll
    for (int k = 0; k < 4; ++k) {
      const float mrow = bflo(q[k][1][2]) * kmx + 0.01f;
      safe = safe && (!h || mrow <= M_LIMIT);
      if (h) q[k][1][2] = cvtpk(-mrow, 0.f);
    }
  }
  const bool fast = __all(safe);
  stamp(x);
  f32x16 own;
  if (fast) {
    unsigned so_kv = kvs + 3 * 4096, so_w = OFF_WGU + (x.m * 196 + 2 * w) * KB, so_l = 2 * w * KB, so_g = OFF_WGU + (x.m * 196 + 112 + 12 * w) * KB, spin = 1u << 16, fvo = (l & 15) * 4096;
    Two ab = base_wo(x);
    const Two wd = base_wd(x);
    asm volatile(
      ".macro GRP s, pa, pb\n\t"
      "v_exp_f32 v200, v[\\s]\n\tv_exp_f32 v201, v[\\s+1]\n\tv_cvt_pk_bf16_f32 v[\\pa], v202, v203\n\t"
      "v_exp_f32 v202, v[\\s+2]\n\tv_exp_f32 v203, v[\\s+3]\n\tv_cvt_pk_bf16_f32 v[\\pb], v200, v201\n\t"
      ".endm\n\t"
      ".macro MEM kind, i, ld\n\t"
      ".if \\kind == 1\n\t"
      "buffer_load_dwordx4 v[128+16*\\ld+4*\\i:128+16*\\ld+4*\\i+3], %[vo], %[rs], %[skv] offen offset:1024*\\i sc1\n\t"
      ".elseif \\kind == 5\n\t"
      "ds_write_b128 v205, v[4*\\i:4*\\i+3] offset:1024*\\i\n\t"
      ".elseif \\kind == 6\n\t"
      "ds_write_b128 v205, v[4:7] offset:1024\n\t"
      ".elseif \\kind == 7\n\t"
      "ds_write_b128 v205, v[12:15] offset:3072\n\t"
      ".elseif \\kind == 2\n\t"
      "buffer_load_dword v206, v205, %[rs], 0 offen offset:3072 sc1\n\t"
      ".elseif \\kind == 3\n\t"
      ".if att_n < 16 || att_n >= 56\n\t"
      ".if att_n < 102\n\t"
      ".if att_n < 16\n\t.set att_r, 184+4*att_n\n\t.else\n\t.set att_r, 4*(att_n-56)\n\t.endif\n\t"
      ".if att_n & 1\n\t"
      "buffer_load_dwordx4 a[att_r:att_r+3], %[vo], %[rs], %[ab1] offen offset:1024*((att_n>>1)&3) sc1\n\t"
      ".else\n\t"
      "buffer_load_dwordx4 a[att_r:att_r+3], %[vo], %[rs], %[ab0] offen offset:1024*((att_n>>1)&3) sc1\n\t"
      ".endif\n\t"
      ".if att_n == 15\n\t"
      "s_mov_b32 %[ab0], %[wd0]\n\ts_mov_b32 %[ab1], %[wd1]\n\t"
      ".elseif (att_n & 7) == 7\n\t"
      "s_add_u32 %[ab0], %[ab0], 4096\n\ts_add_u32 %[ab1], %[ab1], 4096\n\t"
      ".endif\n\t"
      ".endif\n\t"
      ".elseif att_n < 44\n\t"
      ".if att_n & 1\n\t"
      "buffer_load_dwordx4 %[vo], %[rs], %[sw] offen offset:1024 sc1 lds\n\t"
      "s_add_u32 %[sw], %[sw], 8192\n\ts_add_u32 m0, m0, 8192\n\t"
      ".else\n\t"
      "buffer_load_dwordx4 %[vo], %[rs], %[sw] offen sc1 lds\n\t"
      ".endif\n\t"
      ".else\n\t"
      "buffer_load_dwordx4 v[208+4*(att_n-44):208+4*(att_n-44)+3], %[vo], %[rs], %[sg] offen offset:1024*(att_n&3) sc1\n\t"
      ".if (att_n & 3) == 3\n\ts_add_u32 %[sg], %[sg], 4096\n\t.endif\n\t"
      ".endif\n\t"
      ".set att_n, att_n+1\n\t"
      ".endif\n\t"
      ".endm\n\t"
      ".macro STEP sc, sn, k, nk, pk, tk, nt, pt, ld, m0k, m0i, m1k, m2k, m2i, m3k\n\t"
      "v_mfma_f32_32x32x16_bf16 v[\\sn:\\sn+15], v[128+16*\\nt:128+16*\\nt+3], v[64+8*\\nk:64+8*\\nk+3], 0\n\t"
      "MEM \\m0k, \\m0i, \\ld\n\t"
      "GRP \\sc, 199, 192\n\t"
      "v_mfma_f32_32x32x16_bf16 v[\\sn:\\sn+15], v[132+16*\\nt:132+16*\\nt+3], v[68+8*\\nk:68+8*\\nk+3], v[\\sn:\\sn+15]\n\t"
      "MEM \\m1k, 0, 0\n\t"
      "GRP \\sc+4, 193, 194\n\t"
      "v_mfma_f32_32x32x16_bf16 v[16*\\pk:16*\\pk+15], v[140+16*\\pt:140+16*\\pt+3], v[196:199], v[16*\\pk:16*\\pk+15]\n\t"
      "MEM \\m2k, \\m2i, \\ld\n\t"
      "GRP \\sc+8, 195, 196\n\t"
      "v_mfma_f32_32x32x16_bf16 v[16*\\k:16*\\k+15], v[136+16*\\tk:136+16*\\tk+3], v[192:195], v[16*\\k:16*\\k+15]\n\t"
      "MEM \\m3k, 0, 0\n\t"
      "GRP \\sc+12, 197, 198\n\t"
      ".endm\n\t"
      ".macro TILE tk, nx, pv, on, wait, p, fl=0\n\t"
      "STEP 96, 112, 0, 1, 3, \\tk, \\tk, \\pv, \\pv, \\p, 0, \\p, \\p, 0, \\fl\n\t"
      "STEP 112, 96, 1, 2, 0, \\tk, \\tk, \\tk, \\pv, \\on, 0, \\p, \\on, 1, 0\n\t"
      "STEP 96, 112, 2, 3, 1, \\tk, \\tk, \\tk, \\pv, \\on, 2, \\p, \\on, 3, 0\n\t"
      ".if \\on\n\ts_add_u32 %[skv], %[skv], 4096\n\t.endif\n\t"
      "s_waitcnt vmcnt(\\wait)\n\t"
      "STEP 112, 96, 3, 0, 2, \\tk, \\nx, \\tk, \\pv, \\p, 0, \\p, \\p, 0, 0\n\t"
      ".endm\n\t"
      ".set att_n, 0\n\t"
      "s_mov_b32 m0, %[sl]\n\t"
      "v_mov_b32 v196, 0\n\tv_mov_b32 v197, 0\n\tv_mov_b32 v198, 0\n\tv_mov_b32 v199, 0\n\tv_mov_b32 v202, 0\n\tv_mov_b32 v203, 0\n\t"
      "v_mov_b32 v188, 0\n\tv_mov_b32 v189, 0\n\tv_mov_b32 v190, 0\n\tv_mov_b32 v191, 0\n\t"
      "v_mfma_f32_32x32x16_bf16 v[0:15], v[196:199], v[196:199], 0\n\t"
      "v_mfma_f32_32x32x16_bf16 v[16:31], v[196:199], v[196:199], 0\n\t"
      "v_mfma_f32_32x32x16_bf16 v[32:47], v[196:199], v[196:199], 0\n\t"
      "v_mfma_f32_32x32x16_bf16 v[48:63], v[196:199], v[196:199], 0\n\t"
      "s_waitcnt vmcnt(8)\n\t"
      "v_mfma_f32_32x32x16_bf16 v[96:111], v[128:131], v[64:67], 0\n\t"
      "v_mfma_f32_32x32x16_bf16 v[96:111], v[132:135], v[68:71], v[96:111]\n\t"
      "s_nop 11\n\t"
      "TILE 0, 1, 3, 1, 8, 0\n\t"
      "TILE 1, 2, 0, 1, 8, 0\n\tTILE 2, 3, 1, 1, 9, 0, 2\n\t"
      "s_waitcnt vmcnt(4)\n\t"
      "1:\n\t"
      "v_cmp_gt_u32 vcc, 16, v206\n\t"
      "s_and_b64 vcc, exec, vcc\n\t"
      "s_cbranch_vccz 2f\n\t"
      "s_sleep 24\n\t"
      "buffer_load_dword v206, v205, %[rs], 0 offen offset:3072 sc1\n\t"
      "s_sub_u32 %[spin], %[spin], 1\n\t"
      "s_waitcnt vmcnt(0)\n\t"
      "s_cbranch_scc0 1b\n\t"
      "2:\n\t"
      "s_barrier\n\t"
      "TILE 3, 0, 2, 1, 14, 3\n\t"
      "TILE 0, 1, 3, 1, 21, 3\n\tTILE 1, 2, 0, 1, 24, 3\n\tTILE 2, 3, 1, 1, 24, 3\n\tTILE 3, 0, 2, 1, 24, 3\n\t"
      "TILE 0, 1, 3, 1, 24, 3\n\tTILE 1, 2, 0, 1, 24, 3\n\tTILE 2, 3, 1, 1, 24, 3\n\tTILE 3, 0, 2, 1, 24, 3\n\t"
      "TILE 0, 1, 3, 1, 24, 3\n\tTILE 1, 2, 0, 0, 20, 3\n\tTILE 2, 3, 1, 0, 16, 3\n\t"
      "v_add_u32 v205, %[d1], v204\n\tv_add_u32 v206, %[d2], v204\n\t"
      "STEP 96, 112, 0, 1, 3, 3, 3, 2, 0, 3, 0, 3, 3, 0, 3\n\t"
      "STEP 112, 96, 1, 2, 0, 3, 3, 3, 0, 3, 0, 3, 0, 0, 0\n\t"
      "STEP 96, 112, 2, 3, 1, 3, 3, 3, 0, 5, 0, 6, 5, 2, 7\n\t"
      "ds_write_b128 v206, v[16:19]\n\t"
      "GRP 112, 199, 192\n\t"
      "ds_write_b128 v206, v[20:23] offset:1024\n\t"
      "GRP 116, 193, 194\n\t"
      "v_mfma_f32_32x32x16_bf16 v[32:47], v[188:191], v[196:199], v[32:47]\n\t"
      "ds_write_b128 v206, v[24:27] offset:2048\n\t"
      "GRP 120, 195, 196\n\t"
      "v_mfma_f32_32x32x16_bf16 v[48:63], v[184:187], v[192:195], v[48:63]\n\t"
      "ds_write_b128 v206, v[28:31] offset:3072\n\t"
      "GRP 124, 197, 198\n\t"
      "v_add_u32 v200, %[d3], v204\n\t"
      "v_cvt_pk_bf16_f32 v199, v202, v203\n\t"
      "s_nop 1\n\t"
      "v_mfma_f32_32x32x16_bf16 v[48:63], v[188:191], v[196:199], v[48:63]\n\t"
      "ds_write_b128 v200, v[32:35]\n\tds_write_b128 v200, v[36:39] offset:1024\n\tds_write_b128 v200, v[40:43] offset:2048\n\tds_write_b128 v200, v[44:47] offset:3072\n\t"
      "s_nop 7\n\t"
      ".purgem GRP\n\t.purgem MEM\n\t.purgem STEP\n\t.purgem TILE"
      : "={v[48:63]}"(own), "+{v[128:143]}"(kf[0]), "+{v[144:159]}"(kf[1]), "+{v[160:175]}"(kf[2]),
        "={v[208:223]}"(g2[0]), "={v[224:239]}"(g2[1]), "={v[240:255]}"(g2[2]),
        [skv] "+s"(so_kv), [sw] "+s"(so_w), [ab0] "+s"(ab.s0), [ab1] "+s"(ab.s1), [spin] "+s"(spin), [sg] "+s"(so_g), "+{v205}"(fvo)
      : "{v[64:67]}"(q[1][0]), "{v[68:71]}"(q[1][1]), "{v[72:75]}"(q[2][0]), "{v[76:79]}"(q[2][1]),
        "{v[80:83]}"(q[3][0]), "{v[84:87]}"(q[3][1]), "{v[88:91]}"(q[0][0]), "{v[92:95]}"(q[0][1]),
        [vo] "{v204}"(x.vl16), [d1] "s"(SCR + (w ^ 1) * 3 * 4096), [d2] "s"(SCR + ((w ^ 2) * 3 + 1) * 4096), [d3] "s"(SCR + ((w ^ 3) * 3 + 2) * 4096), [rs] "s"(x.rs), [sl] "s"(so_l), [wd0] "s"(wd.s0), [wd1] "s"(wd.s1)
      : "memory", "scc", "vcc",
        "v0","v1","v2","v3","v4","v5","v6","v7","v8","v9","v10","v11","v12","v13","v14","v15","v16","v17","v18","v19","v20","v21","v22","v23",
        "v24","v25","v26","v27","v28","v29","v30","v31","v32","v33","v34","v35","v36","v37","v38","v39","v40","v41","v42","v43","v44","v45","v46","v47",
        "v96","v97","v98","v99","v100","v101","v102","v103","v104","v105","v106","v107","v108","v109","v110","v111",
        "v112","v113","v114","v115","v116","v117","v118","v119","v120","v121","v122","v123","v124","v125","v126","v127",
        "v176","v177","v178","v179","v180","v181","v182","v183","v184","v185","v186","v187","v188","v189","v190","v191",
        "v192","v193","v194","v195","v196","v197","v198","v199","v200","v201","v202","v203","v206", ACLOB);
  } else {
    f32x16 o[4];
    wv<0>();
    pin(kf[0]); pin(kf[1]); pin(kf[2]);
    float mx[4];
#pragma unroll
    for (int k = 0; k < 4; ++k) {
      mx[k] = -3.0e38f; if (h) q[k][1][2] = 0; pin(q[k][1]);
#pragma unroll
      for (int i = 0; i < 16; ++i) o[k][i] = 0.f;
    }
#pragma nounroll
    for (unsigned spin = 0; spin < (1u << 16); ++spin) {
      float v;
      ld1(v, (l & 15) * 4096, x.rs, OFF_FLAG + FL_W(0)); wv<0>(); pin(v);
      if (__all(__float_as_uint(v) >= 16u)) break;
      __builtin_amdgcn_s_sleep(24);
    }
    pf_wo(x);
    pf_wgu1(x);
    sfor<0, 12>([&](auto j) {
      u32x4 t;
      ld4<0>(t, x.vl16, x.rs, OFF_WGU + (x.m * 196 + 112 + 12 * w + j) * KB); wv<0>(); pin(t);
#pragma unroll
      for (int i = 0; i < 4; ++i) g2[j >> 2][4 * (j & 3) + i] = __uint_as_float(t[i]);
    });
    pf_wd<0, 46>(x, base_wd(x));
    for (int tk = 0; tk < 16; ++tk) {
      u32x4 kf[4];
      sfor<0, 4>([&](auto i) { ld4<i * 1024>(kf[i], x.vl16, x.rs, kvs + tk * 4096); });
      wv<0>();
      sfor<0, 4>([&](auto k) {
        f32x16 s;
        nop<1>();
        m32z(s, kf[0], q[k][0]); m32(s, kf[1], q[k][1]);
        nop<11>(); pin(s);
        float tm = s[0];
#pragma unroll
        for (int i = 1; i < 16; ++i) tm = fmaxf(tm, s[i]);
        tm = max32(tm);
        const float mn = fmaxf(mx[k], tm), al = __builtin_amdgcn_exp2f(mx[k] - mn);
        mx[k] = mn;
#pragma unroll
        for (int i = 0; i < 16; ++i) o[k][i] *= al;
        u32x4 p0, p1;
#pragma unroll
        for (int d = 0; d < 4; ++d) {
          p0[d] = cvtpk(__builtin_amdgcn_exp2f(s[2 * d] - mn), __builtin_amdgcn_exp2f(s[2 * d + 1] - mn));
          p1[d] = cvtpk(__builtin_amdgcn_exp2f(s[8 + 2 * d] - mn), __builtin_amdgcn_exp2f(s[9 + 2 * d] - mn));
        }
        pin(p0); pin(p1); pin(o[k]);
        nop<1>();
        m32(o[k], kf[2], p0); m32(o[k], kf[3], p1);
        nop<11>(); pin(o[k]);
      });
    }
    bar();
    float* mm = (float*)(lds + SCR);
#pragma unroll
    for (int k = 0; k < 4; ++k) if (h == 0) mm[((w ^ k) * 4 + w) * 32 + r] = mx[k];
    bar();
#pragma unroll
    for (int k = 0; k < 4; ++k) {
      const float* p = mm + (w ^ k) * 128 + r;
      const float g = fmaxf(fmaxf(p[0], p[32]), fmaxf(p[64], p[96])), f = __builtin_amdgcn_exp2f(mx[k] - g);
#pragma unroll
      for (int i = 0; i < 16; ++i) o[k][i] *= f;
    }
    bar();
    reduce_put(x, o);
    own = o[0];
  }
  stamp(x);
  reduce_get(x, own);
  pin(own); fstamp(x);
  {
    auto lr = __builtin_amdgcn_permlane32_swap(__float_as_uint(own[12]), __float_as_uint(own[12]), false, false);
    const float inv = __builtin_amdgcn_rcpf(__uint_as_float(lr[1]));
    u32x4 a, b;
#pragma unroll
    for (int d = 0; d < 4; ++d) { a[d] = cvtpk(own[2 * d] * inv, own[2 * d + 1] * inv); b[d] = cvtpk(own[8 + 2 * d] * inv, own[9 + 2 * d] * inv); }
    if (h) { b[2] = 0; b[3] = 0; }
    const unsigned so = OFF_O + L * SZ_O + (tt * 32 + 2 * (2 * hp + kh)) * KB;
    st4<0>(a, x.vl16, x.rs, so); st4<1024>(b, x.vl16, x.rs, so);
  }
  fstamp(x);
  wv<0>();
  fstamp(x);
  signal(x, FL_O(L, x.c, th), 1);
  if (L == 0 && th == 0) {
    if (x.c == hp) count(x, FL_W2, 1);
  }
}

template <bool FINAL> DI void res_epilogue(Ctx& x, const f32x16& acc, const u32x4 (&xr)[2], unsigned Xo, unsigned STo, int tt, int nt, char* out = nullptr) {
  const int h = x.l >> 5, r = x.l & 31;
  u32x4 y[2];
#pragma unroll
  for (int d = 0; d < 8; ++d) y[d >> 2][d & 3] = cvtpk(acc[2 * d] + bflo(xr[d >> 2][d & 3]), acc[2 * d + 1] + bfhi(xr[d >> 2][d & 3]));
  if constexpr (FINAL) {
#pragma unroll
    for (int s = 0; s < 2; ++s) {
      auto a = __builtin_amdgcn_permlane32_swap(y[s][0], y[s][2], false, false);
      auto b = __builtin_amdgcn_permlane32_swap(y[s][1], y[s][3], false, false);
      y[s] = u32x4{a[0], b[0], a[1], b[1]};
    }
    const unsigned vo = r * (D * 2) + h * 16, so = tt * 32 * D * 2 + nt * 64;
    const rsrc_t ro = mkrs(out, S * D * 2);
    st4p<0>(y[0], vo, ro, so); st4p<32>(y[1], vo, ro, so);
  } else {
    const unsigned so = Xo + (tt * 28 + 2 * nt) * KB;
    st4<0>(y[0], x.vl16, x.rs, so); st4<1024>(y[1], x.vl16, x.rs, so);
    float s = 0.f, q = 0.f, s1 = 0.f, q1 = 0.f;
#pragma unroll
    for (int d = 0; d < 4; ++d) {
      s = dot2(y[0][d], 0x3f803f80u, s); q = dot2(y[0][d], y[0][d], q); s1 = dot2(y[1][d], 0x3f803f80u, s1); q1 = dot2(y[1][d], y[1][d], q1);
    }
    s = sum32(s + s1); q = sum32(q + q1);
    if (h == 0) st2<0>(u32x2{__float_as_uint(s), __float_as_uint(q)}, r * 8, x.rs, STo + (nt * 2048 + tt * 32) * 8);
  }
}

DI void phase_oproj(Ctx& x, const int L, const Args& a, const f32x16 (&g2)[3]) {
  const int w = x.w;
  auto put_g2 = [&]() {
#pragma unroll
    for (int j = 0; j < 12; ++j)
      *(f32x4*)(lds + (112 + 12 * w + j) * KB + x.vl16) = f32x4{g2[j >> 2][4 * (j & 3)], g2[j >> 2][4 * (j & 3) + 1], g2[j >> 2][4 * (j & 3) + 2], g2[j >> 2][4 * (j & 3) + 3]};
  };
  if (x.m < 14) {
    const int th = x.m / 7, ct = x.m % 7, tt0 = 4 * x.c + 2 * th, tt = tt0 + (w >> 1), nt = 2 * ct + (w & 1);
    u32x2 x0[4];
    if (L == 0) {
      const rsrc_t rx = mkrs(a.x, S * D * 2);
      sfor<0, 4>([&](auto i) { ld2i<16 * i>(x0[i], (x.l & 31) * 896 + (x.l >> 5) * 8, rx, tt * 32 * 896 + nt * 64); });
    } else {
      sfor<0, 4>([&](auto i) { ld2u<(i >> 1) * 1024 + (i & 1) * 8>(x0[i], x.vl16, x.rs, OFF_X + 2 * SZ_X + (tt * 28 + 2 * nt) * KB); });
    }
    await(x, FL_O(L, x.c, th), 8);
    stamp(x);
    u32x4 b[8][2];
    const unsigned s0 = OFF_O + L * SZ_O + (tt * 32 + 8 * w) * KB, s1 = OFF_O + L * SZ_O + ((tt0 + ((w >> 1) ^ 1)) * 32 + 8 * w) * KB;
    auto ld = [&](auto i) { ld4<(i & 3) * 1024>(b[i][0], x.vl16, x.rs, s0 + (i >> 2) * 4096); ld4<(i & 3) * 1024>(b[i][1], x.vl16, x.rs, s1 + (i >> 2) * 4096); };
    constexpr int WN = 5;
    sfor<0, WN>(ld);
    f32x16 acc[4];
    sfor<0, 8>([&](auto i) {
      constexpr int last = (i + WN - 1 < 7 ? i + WN - 1 : 7);
      wv<2 * (last - i) + 1>();
      m32a<A_WO + 8 * i, i == 0>(acc[0], b[i][0]); m32a<A_WO + 8 * i + 4, i == 0>(acc[1], b[i][0]);
      wv<2 * (last - i)>();
      m32a<A_WO + 8 * i, i == 0>(acc[2], b[i][1]); m32a<A_WO + 8 * i + 4, i == 0>(acc[3], b[i][1]);
      if constexpr (i + WN < 8) ld(std::integral_constant<int, i + WN>{});
    });
    nop<11>();
    sfor<0, 4>([&](auto k) { pin(acc[k]); pin(x0[k]); });
    stamp(x);
    reduce_put(x, acc); reduce_get(x, acc[0]);
    pin(acc[0]); fstamp(x);
    const u32x4 x2[2] = {u32x4{x0[0][0], x0[0][1], x0[1][0], x0[1][1]}, u32x4{x0[2][0], x0[2][1], x0[3][0], x0[3][1]}};
    res_epilogue<false>(x, acc[0], x2, OFF_X + (2 * L + 1) * SZ_X, OFF_ST + (2 * L + 1) * SZ_ST, tt, nt);
    fstamp(x);
    put_g2();
    wv<0>();
    fstamp(x);
    signal(x, FL_X1(L, x.c), 1);
    fstamp(x);
  } else {
    put_g2();
    stamp(x); stamp(x); fstamp(x); fstamp(x); fstamp(x); fstamp(x);
  }
}

constexpr int DWN = 7;
constexpr bool dn_half(int o) { return o == 4 || o == 5 || o == 14 || o == 15; }
struct DnSrc { unsigned s0, s1, l0, l1; };
DI DnSrc dn_src(const Ctx& x, int L) {
  const int tt0 = 4 * x.c + 2 * ((x.m / 7) & 1), tt = tt0 + (x.w >> 1), tto = tt0 + ((x.w >> 1) ^ 1);
  return DnSrc{OFF_H + L * SZ_H + (tt * 112 + 28 * x.w) * KB, OFF_H + L * SZ_H + (tto * 112 + 28 * x.w) * KB,
               OFF_HL + ((L * 64 + tt) * 16 + 4 * x.w) * KB, OFF_HL + ((L * 64 + tto) * 16 + 4 * x.w) * KB};
}
template <int O> DI void dn_ld(Ctx& x, const DnSrc& d, u32x4 (&b)[DWN][2]) {
  constexpr int i = dn_ks(O);
  if constexpr (dn_half(O)) {
    constexpr int imm = (O >= 14 ? 2048 : 0) + (O & 1) * 512;
    u32x2 lo, hi, lo1, hi1;
    ld2u<imm>(lo, x.l * 8, x.rs, d.l0); ld2u<imm + 1024>(hi, x.l * 8, x.rs, d.l0);
    ld2u<imm>(lo1, x.l * 8, x.rs, d.l1); ld2u<imm + 1024>(hi1, x.l * 8, x.rs, d.l1);
    b[O % DWN][0] = u32x4{lo[0], lo[1], hi[0], hi[1]}; b[O % DWN][1] = u32x4{lo1[0], lo1[1], hi1[0], hi1[1]};
  } else {
    ld4<(i & 3) * 1024>(b[O % DWN][0], x.vl16, x.rs, d.s0 + (i >> 2) * 4096); ld4<(i & 3) * 1024>(b[O % DWN][1], x.vl16, x.rs, d.s1 + (i >> 2) * 4096);
  }
}

DI void gu_tile(const f32x4 (&acc)[2][2], const u32x4 (&cs)[2], const float (&mean)[2], const float (&r2)[2], u32x2 (&hp)[2]) {
  const f32x4 cg = __builtin_bit_cast(f32x4, cs[0]), cu = __builtin_bit_cast(f32x4, cs[1]);
#pragma unroll
  for (int u = 0; u < 2; ++u) {
    const f32x2 nm = {-mean[u], -mean[u]}, rr = {r2[u], r2[u]};
#pragma unroll
    for (int e = 0; e < 2; ++e) {
      const f32x2 g = __builtin_elementwise_fma(nm, f32x2{cg[2 * e], cg[2 * e + 1]}, f32x2{acc[0][u][2 * e], acc[0][u][2 * e + 1]});
      const f32x2 v = __builtin_elementwise_fma(nm, f32x2{cu[2 * e], cu[2 * e + 1]}, f32x2{acc[1][u][2 * e], acc[1][u][2 * e + 1]});
      const f32x2 y = g * rr * v;
      hp[u][e] = cvtpk(y[0], y[1]);
    }
  }
}

DI void phase_gu(Ctx& x, const int L, u32x4 (&b)[DWN][2]) {
  const int w = x.w, l = x.l, g = l >> 4, c16 = l & 15, m = x.m, tt = 4 * x.c + w;
  if (L == 0 && wave0(x)) { unsigned v; ldflag(v, x.rs, OFF_FLAG + FL_W2); wv<0>(); recheck(x, v, FL_W2, 8); }
  await(x, FL_X1(L, x.c), 14);
  stamp(x);
  constexpr int NXV = 11;
  u32x4 xf[NXV][2]; f32x2 sp[2][4];
  const unsigned sx = OFF_X + (2 * L + 1) * SZ_X + tt * 28 * KB, st = OFF_ST + (2 * L + 1) * SZ_ST + tt * 32 * 8;
  auto ldx = [&](auto k, auto u) {
    if constexpr (k < NXV) ld4<(k & 1) * 2048 + 256 * u>(xf[k][u], x.vg16, x.rs, sx + (k >> 1) * 4096);
    else if constexpr (k < 14) lda<A_GX + 8 * (k - NXV) + 4 * u, (k & 1) * 2048 + 256 * u>(x.vg16, x.rs, sx + (k >> 1) * 4096);
    else { constexpr int i = k - 14; ld2<128 * u>(sp[u][i], (((i < 3 || g < 2) ? g + 4 * i : g) * 2048 + c16) * 8, x.rs, st); }
  };
  using I0 = std::integral_constant<int, 0>; using I1 = std::integral_constant<int, 1>;
  constexpr int XA = 3;
  sfor<0, XA>([&](auto k) { ldx(k, I0{}); ldx(k, I1{}); });
  const unsigned a0 = x.vl16, a1 = x.vl16 + 65536, a2 = x.vl16 + 131072;
  constexpr int DP = 6;
  u32x4 wf[8];
  auto rd = [&](auto ii) {
    constexpr int i = ii, slot = i >= 160 ? i - 160 : i;
    if constexpr (slot < 64) dsr<slot * 1024>(wf[i & 7], a0); else if constexpr (slot < 128) dsr<(slot - 64) * 1024>(wf[i & 7], a1); else dsr<(slot - 128) * 1024>(wf[i & 7], a2);
  };
  auto mm = [&](f32x4& c, const u32x4& a, auto k, auto u) {
    if constexpr (k < NXV) m16<k == 0>(c, a, xf[k][u]); else m16a<false, A_GX + 8 * (k - NXV) + 4 * u>(c, a);
  };
  const Two wd = base_wd(x);
  f32x4 aa[5][2][2], ch0, ch1;
  float mean[2], r2[2];
  sfor<0, DP>(rd);
  sfor<0, 140>([&](auto i) {
    constexpr int KS = i / 10, s = i % 10, t = s >> 1, mat = i & 1;
    using K = std::integral_constant<int, KS>;
    constexpr auto side = [](int k) { return (k >= 0 && k <= 3) || (k >= 5 && k <= 12); };
    constexpr auto all = [](int k) { return (k + XA < 14 ? 2 : 0) + ((k >= 0 && k <= 3) || (k >= 5 && k <= 9) ? 2 : 0) + (k == 5 ? 1 : 0) + (k >= 10 ? 3 : 0); };
    if constexpr (s == 0) {
      constexpr int cnt = [&] { int n = side(KS - XA) ? 1 : 0; for (int k = KS - XA + 1; k < KS; ++k) n += all(k); return n; }();
      wv<cnt>();
      if constexpr (KS == 5) asm volatile("s_barrier" ::: "memory");
    }
    if constexpr ((i & 1) == 0) { rd(std::integral_constant<int, i + DP>{}); rd(std::integral_constant<int, i + DP + 1>{}); wl<DP>(); }
    mm(aa[t][mat][0], wf[i & 7], K{}, I0{});
    if constexpr (s == 1 && KS + XA < 14) ldx(std::integral_constant<int, KS + XA>{}, I0{});
    if constexpr (s == 6 && KS + XA < 14) ldx(std::integral_constant<int, KS + XA>{}, I1{});
    if constexpr (KS <= 3 && s == 3) ldx(std::integral_constant<int, 14 + KS>{}, I0{});
    if constexpr (KS <= 3 && s == 8) ldx(std::integral_constant<int, 14 + KS>{}, I1{});
    if constexpr (KS == 5 && s == 3) asm volatile("buffer_load_dwordx4 %0, %2, %3, %4 offen sc1\n\tbuffer_load_dwordx4 %1, %2, %3, %4 offen offset:1024 sc1" : "=&v"(ch0), "=&v"(ch1) : "v"(x.vl16), "s"(x.rs), "s"(OFF_CSGU + m * 2048));
    if constexpr (KS == 11 && s == 2) { pin(ch0); pin(ch1); *(f32x4*)(lds + 40 * KB + x.vl16) = ch0 + ch1; }
    if constexpr (KS >= 5 && KS <= 9 && (s == 3 || s == 8) && !(KS == 5 && s == 3)) pf_wgu3<2 * (KS - 5) + (s == 8) - 1>(x);
    if constexpr (KS >= 10 && (s == 3 || s == 5 || s == 8) && 3 * (KS - 10) + (s == 3 ? 0 : (s == 5 ? 1 : 2)) < 10) {
      constexpr int f = 46 + 3 * (KS - 10) + (s == 3 ? 0 : (s == 5 ? 1 : 2));
      pf_wd<f, f + 1>(x, wd);
    }
    if constexpr ((KS == 10 || KS == 12) && s == 2) {
      constexpr int u = KS == 12;
      sfor<0, 4>([&](auto e) { pin(sp[u][e]); });
      float sm = sp[u][0][0] + sp[u][1][0] + sp[u][2][0], q = sp[u][0][1] + sp[u][1][1] + sp[u][2][1];
      if (g < 2) { sm += sp[u][3][0]; q += sp[u][3][1]; }
      sm = sum32(sum16(sm)); q = sum32(sum16(q));
      float rs; row_stats(sm, q, mean[u], rs); r2[u] = rs * rs;
      pin(mean[u]); pin(r2[u]);
    }
    mm(aa[t][mat][1], wf[i & 7], K{}, I1{}); SB();
  });
  fstamp(x);
  u32x4 cs[7][2];
  const unsigned csa = 40 * KB + g * 16;
  sfor<0, 10>([&](auto i) { dsr<64 * i>(cs[i >> 1][i & 1], csa); });
  wv<10>();
  asm volatile("s_barrier" ::: "memory");
  stamp(x);
  const unsigned H = OFF_H + L * SZ_H + tt * 112 * KB;
  const int gp = (m & 1) ? 7 * m + 1 : 7 * m;
  auto pair = [&](const f32x4 (&t0)[2][2], const f32x4 (&t1)[2][2], auto lt) {
    u32x2 h0[2], h1[2];
    gu_tile(t0, cs[lt], mean, r2, h0); gu_tile(t1, cs[lt + 1], mean, r2, h1);
    st4<0>(u32x4{h0[0][0], h0[0][1], h1[0][0], h1[0][1]}, x.vg16, x.rs, H + (gp + lt - 1) * 1024);
    st4<256>(u32x4{h0[1][0], h0[1][1], h1[1][0], h1[1][1]}, x.vg16, x.rs, H + (gp + lt - 1) * 1024);
  };
  {
    wl<0>();
    sfor<0, 20>([&](auto i) { pin(aa[i >> 2][(i >> 1) & 1][i & 1]); });
    sfor<0, 10>([&](auto i) { pin(cs[i >> 1][i & 1]); });
    u32x2 hl[2];
    gu_tile(aa[0], cs[0], mean, r2, hl);
    const unsigned sl = OFF_HL + ((L * 64 + tt) * 16 + m) * KB;
    st2<0>(hl[0], x.vg16 >> 1, x.rs, sl); st2<128>(hl[1], x.vg16 >> 1, x.rs, sl);
    pair(aa[1], aa[2], std::integral_constant<int, 1>{});
    pair(aa[3], aa[4], std::integral_constant<int, 3>{});
  }
  SB();
  sfor<10, 14>([&](auto i) { dsr<64 * i>(cs[i >> 1][i & 1], csa); });
  fstamp(x);
  f32x4 ab[2][2][2];
  unsigned fa;
  sfor<140, 196>([&](auto i) {
    constexpr int j = i - 140, KS = j / 4, t = (j % 4) >> 1, mat = i & 1;
    using K = std::integral_constant<int, KS>;
    if constexpr (j == 18) {
      fstamp(x);
      if (L == 0) wv<6>(); else wv<0>();
      asm volatile("s_barrier" ::: "memory");
      count(x, FL_H(L, x.c, 0), 1);
      SB();
      fstamp(x);
    }
    if constexpr ((i & 1) == 0) { if constexpr (i + DP < 196) { rd(std::integral_constant<int, i + DP>{}); rd(std::integral_constant<int, i + DP + 1>{}); wl<DP>(); } else wl<194 - i>(); }
    mm(ab[t][mat][0], wf[i & 7], K{}, I0{});
    if constexpr (j % 3 == 1 && j < 42) { if (L == 0) pf_wq<j / 3>(x); SB(); }
    if constexpr (j == 44) ldflag(fa, x.rs, OFF_FLAG + FL_H(L, x.c, 0));
    mm(ab[t][mat][1], wf[i & 7], K{}, I1{}); SB();
  });
  wv<0>();
  recheck(x, fa, FL_H(L, x.c, 0), 16);
  { const DnSrc d = dn_src(x, L); sfor<0, 3>([&](auto o) { dn_ld<o>(x, d, b); }); }
  stamp(x);
  sfor<0, 8>([&](auto i) { pin(ab[i >> 2][(i >> 1) & 1][i & 1]); });
  sfor<10, 14>([&](auto i) { pin(cs[i >> 1][i & 1]); });
  pair(ab[0], ab[1], std::integral_constant<int, 5>{});
}

DI void phase_down(Ctx& x, const int L, char* out, u32x4 (&b)[DWN][2]) {
  const int w = x.w;
  if (x.m >= 14) {
    wv<0>();
    sfor<0, 3>([&](auto o) { pin(b[o][0]); pin(b[o][1]); });
    signal(x, FL_H(L, x.c, 1), 1);
    stamp(x); fstamp(x); fstamp(x); stamp(x);
    return;
  }
  const int th = x.m / 7, ct = x.m % 7, tt = 4 * x.c + 2 * th + (w >> 1), nt = 2 * ct + (w & 1);
  stamp(x);
  constexpr int WN = DWN;
  u32x4 xr[2];
  const DnSrc d = dn_src(x, L);
  auto ld = [&](auto o) { dn_ld<o>(x, d, b); };
  sfor<3, WN>(ld);
  f32x16 acc[4];
  unsigned fb;
  sfor<0, 25>([&](auto i) {
    constexpr int last = i + WN - 1 < 27 ? i + WN - 1 : 27, ex = i <= 2 ? 2 : ((i >= 8 && i <= 13) ? 1 : ((i >= 21) ? 2 : 0));
    constexpr int later = [&] { int n = 0; for (int o = i + 1; o <= last; ++o) n += dn_half(o) ? 4 : 2; return n; }();
    wv<later + (dn_half(i) ? 2 : 1) + ex>();
    if constexpr (i == 3) signal(x, FL_H(L, x.c, 1), 1);
    m32a<A_WD + 8 * i, i == 0>(acc[0], b[i % WN][0]); m32a<A_WD + 8 * i + 4, i == 0>(acc[1], b[i % WN][0]);
    wv<later + ex>();
    m32a<A_WD + 8 * i, i == 0>(acc[2], b[i % WN][1]); m32a<A_WD + 8 * i + 4, i == 0>(acc[3], b[i % WN][1]);
    if constexpr (i == 7) ldflag(fb, x.rs, OFF_FLAG + FL_H(L, x.c, 1));
    if constexpr (i + WN == 20) { fstamp(x); wv<12>(); recheck(x, fb, FL_H(L, x.c, 1), 16); fstamp(x); }
    if constexpr (i + WN < 28) ld(std::integral_constant<int, i + WN>{});
    if constexpr (i + WN == 27) {
      const unsigned sr = OFF_X + (2 * L + 1) * SZ_X + (tt * 28 + 2 * nt) * KB;
      ld4<0>(xr[0], x.vl16, x.rs, sr); ld4<1024>(xr[1], x.vl16, x.rs, sr);
    }
  });
  wv<2>();
  sfor<1, 5>([&](auto t) {
    constexpr int k = t & 3;
    m32a<A_WD + 8 * 25 + 4 * (k & 1), false>(acc[k], b[25 % WN][k >> 1]);
    m32a<A_WD + 8 * 26 + 4 * (k & 1), false>(acc[k], b[26 % WN][k >> 1]);
    if constexpr (t > 1) { nop<(t == 2 ? 9 : 7)>(); red_put1<t - 1, 0>(x, acc[t - 1]); red_put1<t - 1, 1>(x, acc[t - 1]); }
    m32a<A_WD + 8 * 27 + 4 * (k & 1), false>(acc[k], b[27 % WN][k >> 1]);
    if constexpr (t > 1) { red_put1<t - 1, 2>(x, acc[t - 1]); red_put1<t - 1, 3>(x, acc[t - 1]); }
  });
  if (L == 0 && (x.m & 7) < 2) {
    ldl<4>(x.vl16, x.rs, OFF_WQ + ((16 + (x.m & 1)) * 28 + 7 * w) * KB, 7 * w * KB);
    ldl<3>(x.vl16, x.rs, OFF_WQ + ((16 + (x.m & 1)) * 28 + 7 * w + 4) * KB, (7 * w + 4) * KB);
  }
  nop<9>();
  pin(acc[0]);
  stamp(x);
  reduce_get(x, acc[0]);
  wv<0>();
  pin(xr[0]); pin(xr[1]);
  if (L > 0) {
    res_epilogue<true>(x, acc[0], xr, 0, 0, tt, nt, out);
  } else {
    res_epilogue<false>(x, acc[0], xr, OFF_X + 2 * SZ_X, OFF_ST + 2 * SZ_ST, tt, nt);
    wv<0>();
    signal(x, FL_X(x.c, th), 1);
  }
}

#define STAMP_SYNC
DI unsigned RT() { unsigned long long t; asm volatile("s_memrealtime %0\n\ts_waitcnt lgkmcnt(0)" : "=s"(t) :: "memory"); return (unsigned)t; }
DI void TM(int k) {
  if (k == TA) { unsigned t = RT(); asm volatile("v_accvgpr_write_b32 a254, %0" :: "s"(t) : "a254"); }
  if (k == TB) { unsigned t = RT(); asm volatile("v_accvgpr_write_b32 a255, %0" :: "s"(t) : "a255"); }
}
__global__ __launch_bounds__(256, 1) void k_main(Args) {
  typedef unsigned long long u64x8 __attribute__((ext_vector_type(8)));
  typedef unsigned long long u64x2 __attribute__((ext_vector_type(2)));
  u64x8 pin8; u64x2 pws;
  asm volatile("s_load_dwordx16 s[8:23], %4, 0x0\n\ts_load_dwordx4 s[24:27], %4, 0x40\n\t"
               "s_getpc_b64 s[4:5]\n.Lk_begin:\n\t"
               "s_mov_b32 s6, .Lk_end-.Lk_begin\n\ts_mov_b32 s7, 0x20000\n\ts_and_b32 s5, s5, 0xffff\n\t"
               "v_lshlrev_b32 v1, 4, %2\n\t"
               "s_bfe_u32 s28, %3, 0x20003\n\ts_lshl_b32 s28, s28, 12\n\t"
               "buffer_load_dwordx4 a[112:115], v1, s[4:7], s28 offen\n\t"
               "s_bfe_u32 s28, %3, 0x30004\n\ts_lshl_b32 s28, s28, 13\n\t"
               "buffer_load_dwordx4 a[112:115], v1, s[4:7], s28 offen\n\t"
               "s_add_u32 s28, s28, 4096\n\t"
               "buffer_load_dwordx4 a[112:115], v1, s[4:7], s28 offen\n\t"
               "s_waitcnt lgkmcnt(0)"
               : "={s[8:23]}"(pin8), "={s[24:27]}"(pws) : "v"(threadIdx.x), "{s2}"(blockIdx.x), "{s[0:1]}"(__builtin_amdgcn_kernarg_segment_ptr())
               : "s4", "s5", "s6", "s7", "s28", "v1", "scc", ACLOB);
  const Args a = {(const char*)pin8[0], (const char*)pin8[1], (const char*)pin8[2], (const char*)pin8[3], (const char*)pin8[4], (const char*)pin8[5],
                  (const char*)pin8[6], (const char*)pin8[7], (char*)pws[0], (char*)pws[1]};
  Ctx x;
  x.rs = mkrs(a.ws, WS_USED);
  x.w = __builtin_amdgcn_readfirstlane(threadIdx.x >> 6);
  asm volatile("s_nop 4" : "+s"(x.w));
  { const int b = blockIdx.x; x.c = 2 * (b & 7) + (b >> 7); x.m = (b >> 3) & 15; }
  TM(-1);
  f32x16 kf[3];
  { Ctx y = x; fresh(y); stamp(y); phase_pre(y, a, kf); STAMP_SYNC }
  for (int L = 0;; ++L) {
    f32x16 g2[3];
    int Lo = L; asm volatile("" : "+s"(Lo));
    TM(8 * Lo + 0);
    { Ctx y = x; fresh(y); phase_att(y, Lo, kf, g2); stamp(y); STAMP_SYNC }
    TM(8 * Lo + 1);
    { Ctx y = x; fresh(y); phase_oproj(y, Lo, a, g2); stamp(y); STAMP_SYNC }
    TM(8 * Lo + 2);
    { Ctx y = x; u32x4 b[DWN][2]; fresh(y); phase_gu(y, Lo, b); TM(8 * Lo + 3); fresh(y); phase_down(y, Lo, a.out, b); stamp(y); STAMP_SYNC }
    TM(8 * Lo + 4);
    if (L == 1) break;
    { Ctx y = x; fresh(y); phase_front(y, kf); STAMP_SYNC }
  }
  if (blockIdx.x == 16) {
    unsigned ta, tb;
    asm volatile("s_nop 4\n\tv_accvgpr_read_b32 v254, a254\n\tv_accvgpr_read_b32 v255, a255\n\ts_nop 1\n\tv_readfirstlane_b32 %0, v254\n\tv_readfirstlane_b32 %1, v255" : "=s"(ta), "=s"(tb) :: "v254", "v255");
    const unsigned t0 = RT(); while (RT() - t0 < tb - ta) {}
  }
  fresh(x);
  if (blockIdx.x == 0 && wave0(x)) {
    get_tag(x);
    if (x.l == 0) st2<0>(u32x2{x.tlo, x.thi}, 0, x.rs, OFF_TAG);
  }
  stamp_flush(x);
  asm volatile(".Lk_end:");
}

extern "C" void kernel_launch(void* const* d_in, const int* in_sizes, int n_in, void* d_out, int out_size, void* d_ws, size_t ws_size,
                              hipStream_t stream) {
  Args a;
  a.x = (const char*)d_in[0]; a.wq = (const char*)d_in[1]; a.wk = (const char*)d_in[2]; a.wv = (const char*)d_in[3];
  a.wo = (const char*)d_in[4]; a.wg = (const char*)d_in[6]; a.wu = (const char*)d_in[7]; a.wd = (const char*)d_in[8];
  a.ws = (char*)d_ws; a.out = (char*)d_out;
  (void)hipFuncSetAttribute((const void*)k_main, hipFuncAttributeMaxDynamicSharedMemorySize, 160 * 1024);
  k_main<<<256, 256, 160 * 1024, stream>>>(a);
}
```

```cpp
#include <hip/hip_runtime.h>
#include <stdint.h>
#include <type_traits>

typedef float    f32x16 __attribute__((ext_vector_type(16)));
typedef float    f32x4  __attribute__((ext_vector_type(4)));
typedef float    f32x2  __attribute__((ext_vector_type(2)));
typedef unsigned u32x4  __attribute__((ext_vector_type(4)));
typedef unsigned u32x2  __attribute__((ext_vector_type(2)));
typedef __bf16   bf16x2 __attribute__((ext_vector_type(2)));
typedef __amdgpu_buffer_rsrc_t rsrc_t;
#define DI __device__ __forceinline__
#define SB() __builtin_amdgcn_sched_barrier(0)

constexpr int S = 2048, D = 448, F = 1792;
constexpr unsigned KB = 1024;
constexpr unsigned OFF_FLAG = 0;
constexpr unsigned OFF_CSQ  = 64 * KB;
constexpr unsigned OFF_CSGU = OFF_CSQ + 4 * KB;
constexpr unsigned OFF_SLOT = OFF_CSGU + 32 * KB;
constexpr unsigned OFF_TAG  = OFF_SLOT + 32 * KB;
constexpr unsigned OFF_WQ   = 160 * KB;
constexpr unsigned OFF_WO   = OFF_WQ + 18 * 28 * KB;
constexpr unsigned OFF_WGU  = OFF_WO + 14 * 32 * KB;
constexpr unsigned OFF_WD   = OFF_WGU + 16 * 196 * KB;
constexpr unsigned SZ_X = 64 * 28 * KB, SZ_ST = 14 * 2048 * 8, SZ_O = 64 * 32 * KB, SZ_H = 64 * 112 * KB, SZ_KV = 64 * 4 * KB;
constexpr unsigned OFF_X    = OFF_WD + 14 * 112 * KB;
constexpr unsigned OFF_ST   = OFF_X + 4 * SZ_X;
constexpr unsigned OFF_O    = OFF_ST + 4 * SZ_ST;
constexpr unsigned OFF_H    = OFF_O + 2 * SZ_O;
constexpr unsigned OFF_HL   = OFF_H + 2 * SZ_H;
constexpr unsigned OFF_KV   = OFF_HL + 2 * 1024 * KB;
constexpr unsigned OFF_TS   = OFF_KV + 2 * SZ_KV;
constexpr unsigned WS_USED  = OFF_TS + 520 * KB;
DI constexpr unsigned FL_X(int c, int th)         { return (c * 16 + th) * 256u; }
DI constexpr unsigned FL_O(int L, int c, int th)  { return (c * 16 + 2 + L * 2 + th) * 256u; }
DI constexpr unsigned FL_X1(int L, int c)         { return (c * 16 + 6 + L) * 256u; }
DI constexpr unsigned FL_H(int L, int c, int p)   { return (c * 16 + 8 + L * 2 + p) * 256u; }
DI constexpr unsigned FL_W(int c)                 { return (c * 16 + 12) * 256u; }
constexpr unsigned FL_W2 = 13 * 256u;

constexpr float QSCALE = 0.18898223650461362f * 1.4426950408889634f;
constexpr float M_LIMIT = 48.0f;

template <int I, int N, class Fn> DI void sfor(Fn&& f) { if constexpr (I < N) { f(std::integral_constant<int, I>{}); sfor<I + 1, N>(f); } }

DI unsigned cvtpk(float lo, float hi) { f32x2 v = {lo, hi}; bf16x2 b = __builtin_convertvector(v, bf16x2); return __builtin_bit_cast(unsigned, b); }
DI float bflo(unsigned u) { return __uint_as_float(u << 16); }
DI float bfhi(unsigned u) { return __uint_as_float(u & 0xffff0000u); }
DI float dot2(unsigned a, unsigned b, float c) { return __builtin_amdgcn_fdot2_f32_bf16(__builtin_bit_cast(bf16x2, a), __builtin_bit_cast(bf16x2, b), c, false); }
DI float sum32(float v) { auto r = __builtin_amdgcn_permlane32_swap(__float_as_uint(v), __float_as_uint(v), false, false); return __uint_as_float(r[0]) + __uint_as_float(r[1]); }
DI float sum16(float v) { auto r = __builtin_amdgcn_permlane16_swap(__float_as_uint(v), __float_as_uint(v), false, false); return __uint_as_float(r[0]) + __uint_as_float(r[1]); }
DI float max32(float v) { auto r = __builtin_amdgcn_permlane32_swap(__float_as_uint(v), __float_as_uint(v), false, false); return fmaxf(__uint_as_float(r[0]), __uint_as_float(r[1])); }

DI unsigned umax16(unsigned v) {
  v = max(v, (unsigned)__builtin_amdgcn_update_dpp(0, (int)v, 0x128, 0xf, 0xf, false));
  v = max(v, (unsigned)__builtin_amdgcn_update_dpp(0, (int)v, 0x124, 0xf, 0xf, false));
  v = max(v, (unsigned)__builtin_amdgcn_update_dpp(0, (int)v, 0x122, 0xf, 0xf, false));
  v = max(v, (unsigned)__builtin_amdgcn_update_dpp(0, (int)v, 0x121, 0xf, 0xf, false));
  return v;
}
DI unsigned umax64(unsigned v) {
  v = umax16(v);
  auto a = __builtin_amdgcn_permlane16_swap(v, v, false, false); v = max(a[0], a[1]);
  auto b = __builtin_amdgcn_permlane32_swap(v, v, false, false); return max(b[0], b[1]);
}

typedef short s16x4 __attribute__((ext_vector_type(4)));
DI u32x2 trread(const char* p) { return __builtin_bit_cast(u32x2, __builtin_amdgcn_ds_read_tr16_b64_v4i16((__attribute__((address_space(3))) s16x4*)p)); }
DI u32x4 trfrag(const char* lo, const char* hi) { const u32x2 a = trread(lo), b = trread(hi); return u32x4{a[0], a[1], b[0], b[1]}; }
DI void row_stats(float s, float q, float& mean, float& rstd) {
  mean = s * (1.0f / D);
  const float var = fmaxf(q * (1.0f / D) - mean * mean, 0.f);
  rstd = __builtin_amdgcn_rsqf(var + 1e-5f);
}
DI float fragsum(const u32x4& f, float s) {
#pragma unroll
  for (int d = 0; d < 4; ++d) s = dot2(f[d], 0x3f803f80u, s);
  return s;
}
struct Args { const char *x, *wq, *wk, *wv, *wo, *wg, *wu, *wd; char* ws; char* out; };

#define ACLOB "a0","a1","a2","a3","a4","a5","a6","a7","a8","a9","a10","a11","a12","a13","a14","a15","a16","a17","a18","a19","a20","a21","a22","a23","a24","a25","a26","a27","a28","a29","a30","a31","a32","a33","a34","a35","a36","a37","a38","a39","a40","a41","a42","a43","a44","a45","a46","a47","a48","a49","a50","a51","a52","a53","a54","a55","a56","a57","a58","a59","a60","a61","a62","a63","a64","a65","a66","a67","a68","a69","a70","a71","a72","a73","a74","a75","a76","a77","a78","a79","a80","a81","a82","a83","a84","a85","a86","a87","a88","a89","a90","a91","a92","a93","a94","a95","a96","a97","a98","a99","a100","a101","a102","a103","a104","a105","a106","a107","a108","a109","a110","a111","a112","a113","a114","a115","a116","a117","a118","a119","a120","a121","a122","a123","a124","a125","a126","a127","a128","a129","a130","a131","a132","a133","a134","a135","a136","a137","a138","a139","a140","a141","a142","a143","a144","a145","a146","a147","a148","a149","a150","a151","a152","a153","a154","a155","a156","a157","a158","a159","a160","a161","a162","a163","a164","a165","a166","a167","a168","a169","a170","a171","a172","a173","a174","a175","a176","a177","a178","a179","a180","a181","a182","a183","a184","a185","a186","a187","a188","a189","a190","a191","a192","a193","a194","a195","a196","a197","a198","a199","a200","a201","a202","a203","a204","a205","a206","a207","a208","a209","a210","a211","a212","a213","a214","a215","a216","a217","a218","a219","a220","a221","a222","a223","a224","a225","a226","a227","a228","a229","a230","a231","a232","a233","a234","a235","a236","a237","a238","a239","a240","a241","a242","a243","a244","a245","a246","a247","a248","a249","a250","a251","a252","a253","a254","a255"

template <int IMM> DI void ld4(u32x4& d, unsigned vo, rsrc_t rs, unsigned so) {
  asm volatile("buffer_load_dwordx4 %0, %1, %2, %3 offen offset:%c4 sc1" : "=v"(d) : "v"(vo), "s"(rs), "s"(so), "i"(IMM));
}
template <int IMM> DI void ld2(f32x2& d, unsigned vo, rsrc_t rs, unsigned so) {
  asm volatile("buffer_load_dwordx2 %0, %1, %2, %3 offen offset:%c4 sc1" : "=v"(d) : "v"(vo), "s"(rs), "s"(so), "i"(IMM));
}
template <int IMM> DI void ld2i(u32x2& d, unsigned vo, rsrc_t rs, unsigned so) {
  asm volatile("buffer_load_dwordx2 %0, %1, %2, %3 offen offset:%c4" : "=v"(d) : "v"(vo), "s"(rs), "s"(so), "i"(IMM));
}
template <int IMM> DI void ld2u(u32x2& d, unsigned vo, rsrc_t rs, unsigned so) {
  asm volatile("buffer_load_dwordx2 %0, %1, %2, %3 offen offset:%c4 sc1" : "=v"(d) : "v"(vo), "s"(rs), "s"(so), "i"(IMM));
}
template <int IMM> DI void ld4f(f32x4& d, unsigned vo, rsrc_t rs, unsigned so) {
  asm volatile("buffer_load_dwordx4 %0, %1, %2, %3 offen offset:%c4 sc1" : "=v"(d) : "v"(vo), "s"(rs), "s"(so), "i"(IMM));
}
DI void ld1(float& d, unsigned vo, rsrc_t rs, unsigned so) {
  asm volatile("buffer_load_dword %0, %1, %2, %3 offen sc1" : "=v"(d) : "v"(vo), "s"(rs), "s"(so));
}
template <int AR, int IMM> DI void lda(unsigned vo, rsrc_t rs, unsigned so) {
  asm volatile("buffer_load_dwordx4 a[%c3:%c4], %0, %1, %2 offen offset:%c5 sc1" :: "v"(vo), "s"(rs), "s"(so), "i"(AR), "i"(AR + 3), "i"(IMM) : ACLOB);
}
template <int N> DI void ldl(unsigned vo, rsrc_t rs, unsigned so, unsigned lds) {
  asm volatile("s_mov_b32 m0, %3\n\ts_nop 0\n\tbuffer_load_dwordx4 %0, %1, %2 offen sc1 lds" :: "v"(vo), "s"(rs), "s"(so), "s"(lds) : "memory");
  if constexpr (N > 1) asm volatile("buffer_load_dwordx4 %0, %1, %2 offen offset:1024 sc1 lds" :: "v"(vo), "s"(rs), "s"(so) : "memory");
  if constexpr (N > 2) asm volatile("buffer_load_dwordx4 %0, %1, %2 offen offset:2048 sc1 lds" :: "v"(vo), "s"(rs), "s"(so) : "memory");
  if constexpr (N > 3) asm volatile("buffer_load_dwordx4 %0, %1, %2 offen offset:3072 sc1 lds" :: "v"(vo), "s"(rs), "s"(so) : "memory");
}
template <int IMM> DI void st4(const u32x4& d, unsigned vo, rsrc_t rs, unsigned so) {
  asm volatile("buffer_store_dwordx4 %0, %1, %2, %3 offen offset:%c4 sc1\n\ts_nop 1" :: "v"(d), "v"(vo), "s"(rs), "s"(so), "i"(IMM) : "memory");
}
template <int IMM> DI void st4p(const u32x4& d, unsigned vo, rsrc_t rs, unsigned so) {
  asm volatile("buffer_store_dwordx4 %0, %1, %2, %3 offen offset:%c4\n\ts_nop 1" :: "v"(d), "v"(vo), "s"(rs), "s"(so), "i"(IMM) : "memory");
}
template <int IMM> DI void st2(const u32x2& d, unsigned vo, rsrc_t rs, unsigned so) {
  asm volatile("buffer_store_dwordx2 %0, %1, %2, %3 offen offset:%c4 sc1" :: "v"(d), "v"(vo), "s"(rs), "s"(so), "i"(IMM) : "memory");
}
DI void st1(float d, unsigned vo, rsrc_t rs, unsigned so) {
  asm volatile("buffer_store_dword %0, %1, %2, %3 offen sc1" :: "v"(d), "v"(vo), "s"(rs), "s"(so) : "memory");
}
template <int N> DI void wv() { asm volatile("s_waitcnt vmcnt(%c0)" :: "i"(N) : "memory"); SB(); }
template <int N> DI void wl() { asm volatile("s_waitcnt lgkmcnt(%c0)" :: "i"(N) : "memory"); SB(); }
template <class T> DI void pin(T& x) { asm volatile("" : "+v"(x)); }
DI void bar() { asm volatile("s_waitcnt lgkmcnt(0)\n\ts_barrier" ::: "memory"); }
template <int N> DI void nop() { asm volatile("s_nop %c0" :: "i"(N)); SB(); }

template <int OFF> DI void trd(u32x2& r, unsigned a) { asm volatile("ds_read_b64_tr_b16 %0, %1 offset:%c2" : "=v"(r) : "v"(a), "i"(OFF)); }
template <int OFF> DI void dsr(u32x4& r, unsigned a) { asm volatile("ds_read_b128 %0, %1 offset:%c2" : "=v"(r) : "v"(a), "i"(OFF)); }

DI void m32(f32x16& c, const u32x4& a, const u32x4& b)  { asm volatile("v_mfma_f32_32x32x16_bf16 %0, %1, %2, %0" : "+v"(c) : "v"(a), "v"(b)); }
DI void m32z(f32x16& c, const u32x4& a, const u32x4& b) { asm volatile("v_mfma_f32_32x32x16_bf16 %0, %1, %2, 0" : "=&v"(c) : "v"(a), "v"(b)); }
template <int AR, bool Z> DI void m32a(f32x16& c, const u32x4& b) {
  if constexpr (Z) asm volatile("v_mfma_f32_32x32x16_bf16 %0, a[%c2:%c3], %1, 0" : "=&v"(c) : "v"(b), "i"(AR), "i"(AR + 3));
  else             asm volatile("v_mfma_f32_32x32x16_bf16 %0, a[%c2:%c3], %1, %0" : "+v"(c) : "v"(b), "i"(AR), "i"(AR + 3));
}
template <bool Z> DI void m16(f32x4& c, const u32x4& a, const u32x4& b) {
  if constexpr (Z) asm volatile("v_mfma_f32_16x16x32_bf16 %0, %1, %2, 0" : "=&v"(c) : "v"(a), "v"(b));
  else             asm volatile("v_mfma_f32_16x16x32_bf16 %0, %1, %2, %0" : "+v"(c) : "v"(a), "v"(b));
}

template <bool Z, int AR> DI void m16a(f32x4& c, const u32x4& a) {
  if constexpr (Z) asm volatile("v_mfma_f32_16x16x32_bf16 %0, %1, a[%c2:%c3], 0" : "=&v"(c) : "v"(a), "i"(AR), "i"(AR + 3));
  else             asm volatile("v_mfma_f32_16x16x32_bf16 %0, %1, a[%c2:%c3], %0" : "+v"(c) : "v"(a), "i"(AR), "i"(AR + 3));
}
DI void ldflag(unsigned& v, rsrc_t rs, unsigned so) { asm volatile("buffer_load_dword %0, off, %1, %2 sc1" : "=v"(v) : "s"(rs), "s"(so)); }

struct Ctx {
  rsrc_t rs;
  unsigned tlo, thi;
  unsigned vl16;
  unsigned vg16;
  int w, l, c, m;
};
DI void fresh(Ctx& x) {
  asm volatile("" : "+s"(x.w), "+s"(x.c), "+s"(x.m));
  asm volatile("v_mbcnt_lo_u32_b32 %0, -1, 0\n\tv_mbcnt_hi_u32_b32 %0, -1, %0" : "=v"(x.l));
  x.vl16 = x.l * 16;
  const int g = x.l >> 4;
  x.vg16 = ((g >> 1) * 64 + (g & 1) * 32 + (x.l & 15)) * 16;
}
extern __shared__ __attribute__((aligned(16))) char lds[];
constexpr unsigned SCR = 112 * KB;

DI void pstamp(Ctx&) {}
DI void fstamp(Ctx&) {}
DI void stamp(Ctx&) {}
DI void stamp_flush(Ctx&) {}

DI void get_tag(Ctx& x) {
  unsigned lo, hi;
  asm volatile("v_accvgpr_read_b32 v255, a252\n\ts_nop 1\n\tv_readfirstlane_b32 %0, v255\n\tv_accvgpr_read_b32 v255, a253\n\ts_nop 1\n\tv_readfirstlane_b32 %1, v255\n\ts_nop 4"
               : "=s"(lo), "=s"(hi) :: "v255");
  const unsigned long long e = ((unsigned long long)hi << 32) | lo;
  unsigned long long n = e * 0x9E3779B97F4A7C15ull + 0x632BE59BD9B4E019ull;
  if (((n ^ e) >> 16) == 0) n += 0x10000;
  x.tlo = (unsigned)n; x.thi = (unsigned)(n >> 32);
}
DI bool wave0(const Ctx& x) { int w = x.w; asm volatile("" : "+s"(w)); return w == 0; }
DI void count(Ctx& x, unsigned fl, int n) {
  if (wave0(x)) {
    const unsigned long long mask = (1ull << n) - 1;
    unsigned long long keep;
    asm volatile("s_mov_b64 %0, exec\n\ts_mov_b64 exec, %5\n\tbuffer_atomic_add %1, %2, %3, %4 offen\n\ts_mov_b64 exec, %0"
                 : "=&s"(keep) : "v"(1u), "v"(x.l * 256), "s"(x.rs), "s"(OFF_FLAG + fl), "s"(mask) : "memory");
  }
}
DI void signal(Ctx& x, unsigned fl, int n) { bar(); count(x, fl, n); }
DI void await(Ctx& x, unsigned fl, unsigned target) {
  if (wave0(x)) {
#pragma nounroll
    for (unsigned spin = 0; spin < (1u << 18); ++spin) {
      unsigned v;
      asm volatile("buffer_load_dword %0, off, %1, %2 sc1\n\ts_waitcnt vmcnt(0)" : "=v"(v) : "s"(x.rs), "s"(OFF_FLAG + fl) : "memory");
      if ((unsigned)__builtin_amdgcn_readfirstlane(v) >= target) break;
      __builtin_amdgcn_s_sleep(1);
    }
  }
  bar();
}

DI void recheck(Ctx& x, unsigned v, unsigned fl, unsigned target) {
  pin(v);
#pragma nounroll
  for (unsigned spin = 0; (unsigned)__builtin_amdgcn_readfirstlane(v) < target && spin < (1u << 18); ++spin) {
    __builtin_amdgcn_s_sleep(2);
    asm volatile("buffer_load_dword %0, off, %1, %2 sc1\n\ts_waitcnt vmcnt(0)" : "=v"(v) : "s"(x.rs), "s"(OFF_FLAG + fl) : "memory");
  }
  SB();
}

DI void reduce_put(Ctx& x, const f32x16 (&acc)[4]) {
  sfor<1, 4>([&](auto k) {
    f32x4* p = (f32x4*)(lds + SCR + ((x.w ^ k) * 3 + k - 1) * 4096 + x.vl16);
#pragma unroll
    for (int q = 0; q < 4; ++q) p[q * 64] = f32x4{acc[k][4 * q], acc[k][4 * q + 1], acc[k][4 * q + 2], acc[k][4 * q + 3]};
  });
}
template <int K, int Q> DI void red_put1(Ctx& x, const f32x16& t) {
  asm volatile("ds_write_b128 %0, %1 offset:%c2" :: "v"(SCR + ((x.w ^ K) * 3 + K - 1) * 4096 + x.vl16),
               "v"(__builtin_shufflevector(t, t, 4 * Q, 4 * Q + 1, 4 * Q + 2, 4 * Q + 3)), "i"(Q * 1024) : "memory");
}
DI void reduce_get(Ctx& x, f32x16& own) {
  bar();
  const f32x4* p = (const f32x4*)(lds + SCR + x.w * 3 * 4096 + x.vl16);
#pragma unroll
  for (int k = 0; k < 3; ++k)
#pragma unroll
    for (int q = 0; q < 4; ++q) {
      f32x4 v = p[k * 256 + q * 64];
#pragma unroll
      for (int i = 0; i < 4; ++i) own[4 * q + i] += v[i];
    }
}

constexpr int A_WO = 184, A_WD = 0, A_GX = 224;
constexpr int dn_ks(int o) { return o < 20 ? 2 * ((o >> 1) % 5 + ((o >> 1) % 5 >= 2) + 7 * (o / 10)) + (o & 1) : 2 * (((o - 20) >> 1) * 4 - ((o - 20) >> 2) + 2) + (o & 1); }
static_assert(dn_ks(0) == 0 && dn_ks(4) == 6 && dn_ks(9) == 11 && dn_ks(10) == 14 && dn_ks(19) == 25 && dn_ks(20) == 4 && dn_ks(22) == 12 && dn_ks(24) == 18 && dn_ks(27) == 27, "");
struct Two { unsigned s0, s1; };
constexpr int SL_WQ = 56;
template <int J> DI void pf_wq(Ctx& x) {
  ldl<1>(x.vl16, x.rs, OFF_WQ + ((x.m & 7) * 56 + 14 * x.w + J) * KB, (SL_WQ + 14 * x.w + J) * KB);
}
DI Two base_wo(const Ctx& x) { const int ct = x.m % 7;
  return Two{OFF_WO + ((2 * ct + (x.w & 1)) * 32 + 8 * x.w) * KB, OFF_WO + ((2 * ct + ((x.w & 1) ^ 1)) * 32 + 8 * x.w) * KB}; }
DI void pf_wo(Ctx& x) {
  const Two b = base_wo(x);
  sfor<0, 8>([&](auto i) {
    lda<A_WO + 8 * i, (i & 3) * 1024>(x.vl16, x.rs, b.s0 + (i >> 2) * 4096);
    lda<A_WO + 8 * i + 4, (i & 3) * 1024>(x.vl16, x.rs, b.s1 + (i >> 2) * 4096);
  });
}
DI Two base_wd(const Ctx& x) { const int ct = x.m % 7;
  return Two{OFF_WD + ((2 * ct + (x.w & 1)) * 112 + 28 * x.w) * KB, OFF_WD + ((2 * ct + ((x.w & 1) ^ 1)) * 112 + 28 * x.w) * KB}; }
template <int I0, int I1> DI void pf_wd(Ctx& x, const Two& b) {
  sfor<I0, I1>([&](auto f) {
    constexpr int i = f >> 1;
    lda<A_WD + 4 * f, (i & 3) * 1024>(x.vl16, x.rs, ((f & 1) ? b.s1 : b.s0) + (i >> 2) * 4096);
  });
}
DI void pf_wgu1(Ctx& x) {
  for (int t = 0; t < 14; ++t) ldl<2>(x.vl16, x.rs, OFF_WGU + (x.m * 196 + 8 * t + 2 * x.w) * KB, (8 * t + 2 * x.w) * KB);
}
template <int J> DI void pf_wgu3(Ctx& x) {
  ldl<1>(x.vl16, x.rs, OFF_WGU + (x.m * 196 + 160 + 9 * x.w + J) * KB, (9 * x.w + J) * KB);
}

constexpr unsigned S_QP = SCR, S_KP = SCR + 16384, S_QF = SCR + 24576, S_ST = SCR + 37888, S_CS = SCR + 38912, S_KM = SCR + 36864, S_TR = SCR + 40960;
DI unsigned kv_finish(Ctx& x, const float (&raw)[16], const f32x4 (&cs)[4], float mean, float rstd, bool isv, unsigned so, int slot) {
  const int h = x.l >> 5, r = x.l & 31;
  u32x4 y[2];
#pragma unroll
  for (int d = 0; d < 8; ++d)
    y[d >> 2][d & 3] = cvtpk((raw[2 * d] - mean * cs[d >> 1][2 * (d & 1)]) * rstd, (raw[2 * d + 1] - mean * cs[d >> 1][2 * (d & 1) + 1]) * rstd);
  if (h) { y[1][2] = 0; y[1][3] = 0; }
  unsigned nb = 0;
  if (!isv) {
    u32x4 y1 = y[1];
    if (h) y1[2] = 0x3f80u;
    st4<0>(y[0], x.vl16, x.rs, so); st4<1024>(y1, x.vl16, x.rs, so);
    float n2 = 0.f, n3 = 0.f;
#pragma unroll
    for (int d = 0; d < 4; ++d) { n2 = dot2(y[0][d], y[0][d], n2); n3 = dot2(y[1][d], y[1][d], n3); }
    n2 = sum32(n2 + n3);
    nb = umax64(n2 < 3.0e38f ? __float_as_uint(n2) : 0x7f800000u);
    nb = cvtpk(1.01f * __builtin_amdgcn_sqrtf(__uint_as_float(nb)), 0.f);
  } else {
    if (h) y[1][2] = 0x3f80u;
    char* im = lds + S_TR + slot * 2048;
#pragma unroll
    for (int a = 0; a < 4; ++a) *(u32x2*)(im + r * 64 + 16 * a + 8 * h) = u32x2{y[a >> 1][2 * (a & 1)], y[a >> 1][2 * (a & 1) + 1]};
    asm volatile("s_waitcnt lgkmcnt(0)" ::: "memory");
    const char* tb = im + (4 * h + ((x.l >> 2) & 3)) * 64 + ((x.l >> 4) & 1) * 32 + (x.l & 3) * 8;
    const u32x4 v0 = trfrag(tb, tb + 512), v1 = trfrag(tb + 1024, tb + 1536);
    st4<0>(v0, x.vl16, x.rs, so); st4<1024>(v1, x.vl16, x.rs, so);
  }
  return nb;
}
DI void kv_publish(Ctx& x, int L, int p, unsigned km) {
  get_tag(x);
  st2<0>(u32x2{(x.tlo & 0xffff0000u) | km, x.thi}, x.l < 16 ? x.l * 1024 : ~0u, x.rs, OFF_SLOT + (L * 2048 + p) * 8);
}
DI void q_finish(Ctx& x, const f32x16& qa, const f32x4 (&cq)[4], float mean, float rstd) {
  const int h = x.l >> 5;
  u32x4 qf[2];
  const f32x4* p = (const f32x4*)(lds + S_QP + x.w * 4096 + x.vl16);
  const float sc = rstd * QSCALE;
#pragma unroll
  for (int q = 0; q < 4; ++q) {
    const f32x4 o = p[q * 64];
    float v[4];
#pragma unroll
    for (int i = 0; i < 4; ++i) v[i] = (qa[4 * q + i] + o[i] - mean * cq[q][i]) * sc;
    qf[q >> 1][2 * (q & 1)] = cvtpk(v[0], v[1]); qf[q >> 1][2 * (q & 1) + 1] = cvtpk(v[2], v[3]);
  }
  if (h) { qf[1][2] = 0; qf[1][3] = 0; }
  float qn2 = 0.f, qn3 = 0.f;
#pragma unroll
  for (int d = 0; d < 4; ++d) { qn2 = dot2(qf[0][d], qf[0][d], qn2); qn3 = dot2(qf[1][d], qf[1][d], qn3); }
  qn2 = sum32(qn2 + qn3);
  if (h) qf[1][2] = cvtpk(1.01f * __builtin_amdgcn_sqrtf(qn2), 0.f);
  ((u32x4*)(lds + S_QF + x.w * 2048 + x.vl16))[0] = qf[0];
  ((u32x4*)(lds + S_QF + x.w * 2048 + x.vl16))[64] = qf[1];
}
DI void put_tile(char* p, const f32x16& t) {
#pragma unroll
  for (int q = 0; q < 4; ++q) ((f32x4*)p)[q * 64] = f32x4{t[4 * q], t[4 * q + 1], t[4 * q + 2], t[4 * q + 3]};
}

constexpr unsigned IMG_Q = 0, IMG_T = 56 * KB;
DI rsrc_t mkrs(const char* p, unsigned n) { return __builtin_amdgcn_make_buffer_rsrc((char*)p, 0, n, 0x00020000); }

DI void ask_slots(Ctx& x, int L) {
  if (wave0(x)) asm volatile("buffer_load_dwordx4 a[248:251], %0, %1, %2 offen sc1" :: "v"(x.vl16), "s"(x.rs), "s"(OFF_SLOT + (L * 16 + x.c) * 1024) : ACLOB);
}
DI void kv_wait(Ctx& x, int L) {
  if (wave0(x)) get_tag(x);
  wv<0>();
  if (wave0(x)) {
    unsigned s0, s1, s2, s3;
#pragma nounroll
    for (unsigned spin = 0; spin < (1u << 18); ++spin) {
      asm volatile("v_accvgpr_read_b32 %0, a248\n\tv_accvgpr_read_b32 %1, a249\n\tv_accvgpr_read_b32 %2, a250\n\tv_accvgpr_read_b32 %3, a251" : "=v"(s0), "=v"(s1), "=v"(s2), "=v"(s3));
      if (__all(s1 == x.thi && s3 == x.thi && (((s0 ^ x.tlo) | (s2 ^ x.tlo)) >> 16) == 0)) break;
      __builtin_amdgcn_s_sleep(2);
      ask_slots(x, L); wv<0>();
    }
    const unsigned km = umax64(max(s0 << 16, s2 << 16));
    if (x.l == 0) ((unsigned*)(lds + S_KM))[4] = km;
  }
  bar();
}
DI void ask_kv(Ctx& x, int L, f32x16 (&kf)[3]) {
  const unsigned kvs = OFF_KV + L * SZ_KV + 16 * x.w * 4096;
  asm volatile(".irp i,0,1,2,3\n\tbuffer_load_dwordx4 v[128+4*\\i:131+4*\\i], %3, %4, %5 offen offset:1024*\\i sc1\n\t.endr\n\t"
               ".irp i,0,1,2,3\n\tbuffer_load_dwordx4 v[144+4*\\i:147+4*\\i], %3, %4, %6 offen offset:1024*\\i sc1\n\t.endr\n\t"
               ".irp i,0,1,2,3\n\tbuffer_load_dwordx4 v[160+4*\\i:163+4*\\i], %3, %4, %7 offen offset:1024*\\i sc1\n\t.endr"
               : "={v[128:143]}"(kf[0]), "={v[144:159]}"(kf[1]), "={v[160:175]}"(kf[2]) : "{v204}"(x.vl16), "s"(x.rs), "s"(kvs), "s"(kvs + 4096), "s"(kvs + 8192));
}

DI void phase_pre(Ctx& x, const Args& a, f32x16 (&kf)[3]) {
  const int w = x.w, l = x.l, h = l >> 5, r = l & 31;
  const int th = x.m >> 3, hp = x.m & 7, tl = w & 1, kh = w >> 1, tt = 4 * x.c + 2 * th + tl, wi = 2 * x.c + th;
  const bool kvp = hp < 2;
  u32x4 xf[14];
  { const rsrc_t rx = mkrs(a.x, S * D * 2);
    sfor<0, 16>([&](auto i) {
      const int c = r ^ ((2 * i + h) & 15);
      ldl<1>(c < 28 ? h * 896 + c * 16 : 0x80000000u, rx, (tt * 32 + 2 * i) * 896 + kh * 448, (84 + 16 * w + i) * KB);
    }); }
  if (x.m == 2) asm volatile("buffer_load_dword a116, %0, %1, 0 offen sc1" :: "v"(l < 16 ? OFF_FLAG + (x.c * 16 + l) * 256 : OFF_SLOT + ((l >> 3) & 1) * 16384 + x.c * 1024 + (l & 7) * 128), "s"(x.rs) : ACLOB);
  asm volatile("buffer_load_dwordx2 a[252:253], off, %0, %1 sc1" :: "s"(x.rs), "s"(OFF_TAG) : ACLOB);
  pstamp(x);
  const int task = wi * 6 + hp - 2;
  const rsrc_t rq = mkrs(a.wq, D * D * 2);
  auto ask_q = [&](auto i) {
    ldl<1>((8 * w + (l >> 3)) * 896 + (l & 7) * 16, rq, hp * 112 + i * 32 * 896, IMG_Q + (4 * i + w) * KB);
  };
  auto ask_t = [&](const char* tp, unsigned tn, unsigned ts, unsigned tb) {
    const rsrc_t rt = mkrs(tp, tn);
    const unsigned vo = (8 * w + (l >> 3)) * ts + (l & 7) * 16;
    sfor<0, 7>([&](auto i) { ldl<1>(vo, rt, tb + i * 32 * ts, IMG_T + (4 * i + w) * KB); });
  };
  const int tco = ((l >> 4) & 1) * 32 + (l & 3) * 8;
  const int sro = 4 * h + ((l >> 2) & 3);
  if (kvp) ask_t(hp ? a.wv : a.wk, D * 56, 128, 0);
  else if (task < 112) ask_t(task < 56 ? a.wg : a.wu, D * F * 2, F * 2, (task & 1) * 224 * F * 2 + ((task % 56) >> 1) * 128);
  else if (task < 168) ask_t(a.wd, D * F * 2, 896, ((task - 112) / 7) * 224 * 896 + ((task - 112) % 7) * 128);
  else ask_t(a.wo, task < 182 ? D * D * 2 : 0, 896, ((task - 168) / 7) * 224 * 896 + ((task - 168) % 7) * 128);
  wv<8>();
  sfor<0, 14>([&](auto i) { dsr<0>(xf[i], (84 + w * 16) * KB + r * 512 + (((2 * i + h) ^ (r & 15)) << 4)); });
  wl<0>();
  {
    float s = 0.f, q = 0.f, s1 = 0.f, q1 = 0.f;
    sfor<0, 14>([&](auto i) {
      ask_q(i);
      pin(xf[i]);
      s = dot2(xf[i][0], 0x3f803f80u, s); q = dot2(xf[i][0], xf[i][0], q); s1 = dot2(xf[i][1], 0x3f803f80u, s1); q1 = dot2(xf[i][1], xf[i][1], q1);
      s = dot2(xf[i][2], 0x3f803f80u, s); q = dot2(xf[i][2], xf[i][2], q); s1 = dot2(xf[i][3], 0x3f803f80u, s1); q1 = dot2(xf[i][3], xf[i][3], q1);
      pin(s); pin(q); pin(s1); pin(q1);
    });
    s = sum32(s + s1); q = sum32(q + q1);
    if (h == 0) ((f32x2*)(lds + S_ST))[w * 32 + r] = f32x2{s, q};
  }
  wv<14>();
  bar();
  stamp(x);
  if (!kvp) {
    if (task < 112) {
      const int g = l >> 4, mat = task >= 56, gt = 4 * ((task % 56) >> 1) + w, m = gt / 7, j = gt % 7, lt = (m & 1) ? j : (j == 6 ? 0 : j + 1);
      const char* tb = lds + IMG_T + (16 * (g >> 1) + 4 * (g & 1) + ((l >> 2) & 3)) * 128 + 32 * w + (l & 3) * 8;
      const unsigned st = lt < 5 ? 10 * KB : 4 * KB, so = OFF_WGU + (m * 196 + (lt < 5 ? lt * 2 + mat : 140 + (lt - 5) * 2 + mat)) * KB + (task & 1) * 7 * st;
      u32x4 f[7];
      sfor<0, 7>([&](auto i) { f[i] = trfrag(tb + i * 4096, tb + i * 4096 + 1024); });
      float cs = 0.f;
      sfor<0, 7>([&](auto i) { st4<0>(f[i], x.vl16, x.rs, so + i * st); cs = fragsum(f[i], cs); });
      cs = sum32(sum16(cs));
      st1(cs, l < 16 ? l * 4 : ~0u, x.rs, OFF_CSGU + m * 2048 + (task & 1) * 1024 + (lt * 2 + mat) * 64);
    } else if (task < 168) {
      const int nt = 2 * ((task - 112) % 7) + (w & 1), rb = (task - 112) / 7;
      const char* tb = lds + IMG_T + sro * 128 + 64 * (w & 1) + tco;
      const unsigned so = OFF_WD + (nt * 112 + 28 * (rb >> 1)) * KB, sa = so + (rb & 1) * 10 * KB, sb = so + (20 + (rb & 1) * 4) * KB;
      u32x4 f[7];
      if (w < 2) {
        sfor<0, 7>([&](auto i) { const char* p = tb + (32 * (i >> 1) + 8 * (i & 1)) * 128; f[i] = trfrag(p, p + 2048); });
        sfor<0, 7>([&](auto i) { st4<0>(f[i], x.vl16, x.rs, i < 4 ? sa + i * KB : (i < 6 ? sb + (i - 4) * KB : sa + 4 * KB)); });
      } else {
        sfor<0, 7>([&](auto i) { const char* p = tb + (32 * ((i + 7) >> 1) + 8 * ((i + 7) & 1)) * 128; f[i] = trfrag(p, p + 2048); });
        sfor<0, 7>([&](auto i) { st4<0>(f[i], x.vl16, x.rs, i < 5 ? sa + (5 + i) * KB : sb + (i - 3) * KB); });
      }
      st1(0.f, ~0u, x.rs, 0);
    } else {
      const int nt = 2 * ((task - 168) % 7) + (w & 1);
      const char* tb = lds + IMG_T + (112 * (w >> 1) + sro) * 128 + 64 * (w & 1) + tco;
      const unsigned vo = task < 182 ? x.vl16 : ~0u - 4096;
      u32x4 f[8];
      sfor<0, 8>([&](auto i) { const char* p = tb + (28 * (i >> 1) + 16 * (i & 1)) * 128; f[i] = trfrag(p, p + 1024); if ((i & 1) && h) { f[i][2] = 0; f[i][3] = 0; } });
      sfor<0, 8>([&](auto i) { st4<(i & 3) * 1024>(f[i], vo, x.rs, OFF_WO + (nt * 32 + 16 * ((task - 168) / 7) + 8 * (w >> 1) + (i & ~3)) * KB); });
    }
  }
  pstamp(x);
  if (!kvp) { wv<8>(); bar(); }
  stamp(x);
  float mean, rstd;
  { const f32x2 p0 = ((const f32x2*)(lds + S_ST))[tl * 32 + r], p1 = ((const f32x2*)(lds + S_ST))[(tl + 2) * 32 + r];
    row_stats(p0[0] + p1[0], p0[1] + p1[1], mean, rstd); }
  float* csw = (float*)(lds + S_CS);
  const int tro = 8 * h + ((l >> 2) & 3);
  u32x4 ones = {0x3f803f80u, 0x3f803f80u, 0x3f803f80u, 0x3f803f80u};
  pin(ones);
  if (kvp) {
    f32x16 kv, cs;
    const unsigned tb = IMG_T + (224 * kh + tro) * 56 + tco;
    u32x2 f[14][2];
    auto rd = [&](auto i) { trd<i * 896>(f[i][0], tb); trd<i * 896 + 224>(f[i][1], tb); };
    sfor<0, 7>(rd);
    sfor<0, 14>([&](auto i) {
      if constexpr (i + 7 < 14) { wl<12>(); rd(std::integral_constant<int, i + 7>{}); } else wl<2 * (13 - i)>();
      pin(f[i][0]); pin(f[i][1]);
      u32x4 g = {f[i][0][0], f[i][0][1], f[i][1][0], f[i][1][1]};
      pin(g); nop<0>();
      if constexpr (i == 0) { m32z(kv, g, xf[i]); m32z(cs, g, ones); } else { m32(kv, g, xf[i]); m32(cs, g, ones); }
    });
    nop<11>(); pin(kv); pin(cs);
    if (r == 0) sfor<0, 4>([&](auto q) { *(f32x4*)(csw + (w * 3 + 2) * 32 + 8 * q + 4 * h) = f32x4{cs[4 * q], cs[4 * q + 1], cs[4 * q + 2], cs[4 * q + 3]}; });
    pstamp(x);
    if (kh) put_tile(lds + S_KP + tl * 4096 + x.vl16, kv);
    bar();
    if (!kh) {
      if (x.m == 0 && w == 0) st1(0.f, l < 16 ? l * 256 : ~0u, x.rs, OFF_FLAG + x.c * 4096);
      float raw[16]; f32x4 c4[4];
      sfor<0, 4>([&](auto q) {
        const f32x4 o = ((const f32x4*)(lds + S_KP + tl * 4096 + x.vl16))[q * 64];
        c4[q] = *(const f32x4*)(csw + (w * 3 + 2) * 32 + 8 * q + 4 * h) + *(const f32x4*)(csw + ((w + 2) * 3 + 2) * 32 + 8 * q + 4 * h);
#pragma unroll
        for (int i = 0; i < 4; ++i) raw[4 * q + i] = kv[4 * q + i] + o[i];
      });
      const unsigned km = kv_finish(x, raw, c4, mean, rstd, hp == 1, OFF_KV + (tt * 4 + 2 * hp) * KB, tl);
      pstamp(x);
      wv<0>();
      kv_publish(x, 0, 2 * tt + hp, km);
      if (tt == 0 && r == 0) sfor<0, 4>([&](auto q) { st4<32 * q>(__builtin_bit_cast(u32x4, c4[q]), h * 16, x.rs, OFF_CSQ + (16 + hp) * 128); });
    } else {
      pstamp(x);
      wv<0>();
    }
    bar();
    pstamp(x);
  } else { pstamp(x); pstamp(x); pstamp(x); }
  f32x16 qa;
  {
    f32x16 q1, q2, cs;
    const unsigned t1 = IMG_Q + (224 * kh + tro) * 128 + tco + 56 * tl, t2 = IMG_Q + (224 * kh + tro) * 128 + tco + 56 * (tl ^ 1);
    u32x2 f[14][4];
    auto rd = [&](auto i) { trd<i * 2048>(f[i][0], t1); trd<i * 2048 + 512>(f[i][1], t1); trd<i * 2048>(f[i][2], t2); trd<i * 2048 + 512>(f[i][3], t2); };
    wl<0>();
    sfor<0, 3>(rd);
    sfor<0, 14>([&](auto i) {
      if constexpr (i + 3 < 14) { wl<8>(); rd(std::integral_constant<int, i + 3>{}); } else wl<4 * (13 - i)>();
      sfor<0, 4>([&](auto e) { pin(f[i][e]); });
      u32x4 g1 = {f[i][0][0], f[i][0][1], f[i][1][0], f[i][1][1]}, g2 = {f[i][2][0], f[i][2][1], f[i][3][0], f[i][3][1]};
      pin(g1); pin(g2); nop<0>();
      if constexpr (i == 0) { m32z(q1, g1, xf[i]); m32z(q2, g2, xf[i]); m32z(cs, g1, ones); } else { m32(q1, g1, xf[i]); m32(q2, g2, xf[i]); m32(cs, g1, ones); }
    });
    nop<11>(); pin(q1); pin(q2); pin(cs);
    pstamp(x);
    if (r == 0) sfor<0, 4>([&](auto q) { *(f32x4*)(csw + (w * 3) * 32 + 8 * q + 4 * h) = f32x4{cs[4 * q], cs[4 * q + 1], cs[4 * q + 2], cs[4 * q + 3]}; });
    if (kh == tl) { put_tile(lds + S_QP + (w ^ 2) * 4096 + x.vl16, q2); qa = q1; } else { put_tile(lds + S_QP + (w ^ 2) * 4096 + x.vl16, q1); qa = q2; }
  }
  if (th == 0 && x.c < 14) {
    const int f = 4 * x.c + w, hd = f >= 28, ks = f - 28 * hd;
    const char* tb = lds + IMG_Q + (16 * ks + sro) * 128 + tco + 56 * hd;
    st4<0>(trfrag(tb, tb + 1024), x.vl16, x.rs, OFF_WQ + (hp * 56 + f) * KB);
  }
  if (kvp && th == 1 && x.c < 7) {
    const char* tb = lds + IMG_T + (16 * (4 * x.c + w) + sro) * 56 + tco;
    st4<0>(trfrag(tb, tb + 448), x.vl16, x.rs, OFF_WQ + ((16 + hp) * 28 + 4 * x.c + w) * KB);
  }
  pstamp(x);
  if (!kvp) __builtin_amdgcn_s_sleep(12);
  ask_slots(x, 0);
  kv_wait(x, 0);
  count(x, FL_W(x.c), 1);
  ask_kv(x, 0, kf);
  stamp(x);
  const bool own = x.c == hp && th == 0;
  {
    f32x4 cq[4];
    sfor<0, 4>([&](auto q) {
      cq[q] = *(const f32x4*)(csw + (kh * 3) * 32 + 8 * q + 4 * h) + *(const f32x4*)(csw + ((kh + 2) * 3) * 32 + 8 * q + 4 * h);
      if (own && tl == 0 && r == 0) st4<32 * q>(__builtin_bit_cast(u32x4, cq[q]), h * 16, x.rs, OFF_CSQ + (2 * hp + kh) * 128);
    });
    q_finish(x, qa, cq, mean, rstd);
  }
  pstamp(x);
}

DI void phase_front(Ctx& x, f32x16 (&kf)[3]) {
  const int w = x.w, l = x.l, h = l >> 5, r = l & 31;
  const int th = x.m >> 3, hp = x.m & 7, tl = w & 1, kh = w >> 1, tt = 4 * x.c + 2 * th + tl;
  const unsigned KV = OFF_KV + SZ_KV;
  {
    f32x4 cq[4], ck[4];
    sfor<0, 4>([&](auto q) { ld4f<32 * q>(cq[q], h * 16, x.rs, OFF_CSQ + (2 * hp + kh) * 128); ld4f<32 * q>(ck[q], h * 16, x.rs, OFF_CSQ + (16 + (hp & 1)) * 128); });
    wv<0>();
    await(x, FL_X(x.c, th), 7);
    stamp(x);
    u32x4 xf[14]; f32x2 sp[7];
    {
      const unsigned so = OFF_X + 2 * SZ_X + (tt * 28 + 14 * kh) * KB;
      sfor<0, 7>([&](auto i) { ld4<(i & 3) * 1024>(xf[i], x.vl16, x.rs, so + (i >> 2) * 4096); });
      sfor<0, 7>([&](auto i) { ld2<0>(sp[i], (h * 7 * 2048 + r) * 8, x.rs, OFF_ST + 2 * SZ_ST + (i * 2048 + tt * 32) * 8); });
      sfor<7, 14>([&](auto i) { ld4<(i & 3) * 1024>(xf[i], x.vl16, x.rs, so + (i >> 2) * 4096); });
    }
    float mean, rstd;
    auto stats = [&]() {
      sfor<0, 7>([&](auto i) { pin(sp[i]); });
      sfor<0, 4>([&](auto i) { pin(cq[i]); pin(ck[i]); });
      float s = 0.f, q = 0.f;
#pragma unroll
      for (int i = 0; i < 7; ++i) { s += sp[i][0]; q += sp[i][1]; }
      row_stats(sum32(s), sum32(q), mean, rstd);
      pin(mean); pin(rstd); SB();
    };
    f32x16 qa, qb;
    u32x4 wa[14], wb[14];
    const unsigned pa = x.vl16 + (SL_WQ + 28 * kh + 14 * kh) * KB, pb = x.vl16 + (SL_WQ + 28 * (kh ^ 1) + 14 * kh) * KB;
    if (hp < 2) {
      f32x16 kv;
      sfor<0, 14>([&](auto i) { dsr<i * 1024>(wa[i], x.vl16 + 14 * kh * KB); });
      sfor<0, 14>([&](auto i) {
        wv<(i < 7 ? 20 : 13) - i>(); wl<13 - i>();
        if constexpr (i == 0) m32z(kv, wa[i], xf[i]); else m32(kv, wa[i], xf[i]);
        if constexpr (i == 7) stats();
      });
      nop<11>(); pin(kv);
      if (kh) put_tile(lds + S_KP + tl * 4096 + x.vl16, kv);
      bar();
      unsigned km = 0;
      if (!kh) {
        float raw[16];
        sfor<0, 4>([&](auto q) {
          const f32x4 o = ((const f32x4*)(lds + S_KP + tl * 4096 + x.vl16))[q * 64];
#pragma unroll
          for (int i = 0; i < 4; ++i) raw[4 * q + i] = kv[4 * q + i] + o[i];
        });
        km = kv_finish(x, raw, ck, mean, rstd, hp == 1, KV + (tt * 4 + 2 * hp) * KB, tl);
      }
      wl<0>();
      sfor<0, 7>([&](auto i) { dsr<i * 1024>(wb[i], pb); dsr<i * 1024>(wa[i], pa); });
      sfor<0, 14>([&](auto i) {
        if constexpr (i < 7) wl<12>(); else wl<2 * (13 - i)>();
        if constexpr (i == 0) { m32z(qb, wb[i], xf[i]); m32z(qa, wa[i], xf[i]); } else { m32(qb, wb[i], xf[i]); m32(qa, wa[i], xf[i]); }
        if constexpr (i < 7) { dsr<(i + 7) * 1024>(wb[i + 7], pb); dsr<(i + 7) * 1024>(wa[i + 7], pa); }
        if constexpr (i == 6) { if (!kh) { wv<0>(); kv_publish(x, 1, 2 * tt + hp, km); } SB(); }
      });
    } else {
      sfor<0, 7>([&](auto i) { dsr<i * 1024>(wb[i], pb); dsr<i * 1024>(wa[i], pa); });
      sfor<0, 14>([&](auto i) {
        wv<(i < 7 ? 20 : 13) - i>();
        if constexpr (i < 7) wl<12>(); else wl<2 * (13 - i)>();
        if constexpr (i == 0) { m32z(qb, wb[i], xf[i]); m32z(qa, wa[i], xf[i]); } else { m32(qb, wb[i], xf[i]); m32(qa, wa[i], xf[i]); }
        if constexpr (i < 7) { dsr<(i + 7) * 1024>(wb[i + 7], pb); dsr<(i + 7) * 1024>(wa[i + 7], pa); }
        if constexpr (i == 7) stats();
      });
    }
    nop<11>();
    pin(qa); pin(qb);
    put_tile(lds + S_QP + (w ^ 2) * 4096 + x.vl16, qb);
    bar();
    q_finish(x, qa, cq, mean, rstd);
    stamp(x);
    if (hp >= 2) __builtin_amdgcn_s_sleep(36);
    ask_slots(x, 1);
    kv_wait(x, 1);
    ask_kv(x, 1, kf);
    stamp(x);
  }
}

DI void phase_att(Ctx& x, const int L, f32x16 (&kf)[3], f32x16 (&g2)[3]) {
  const int w = x.w, l = x.l, h = l >> 5, r = l & 31;
  const int th = x.m >> 3, hp = x.m & 7, tl = w & 1, kh = w >> 1, tt = 4 * x.c + 2 * th + tl;
  const unsigned kvs = OFF_KV + L * SZ_KV + 16 * w * 4096;
  stamp(x);
  if (L == 0) bar();
  stamp(x);
  unsigned kma = S_KM + 16; asm volatile("" : "+s"(kma));
  const float kmx = *(const float*)(lds + kma);
  u32x4 q[4][2];
#pragma unroll
  for (int k = 0; k < 4; ++k) {
    q[k][0] = ((const u32x4*)(lds + S_QF + (w ^ k) * 2048 + x.vl16))[0];
    q[k][1] = ((const u32x4*)(lds + S_QF + (w ^ k) * 2048 + x.vl16))[64];
  }
  bool safe = true;
  {
#pragma unroll
    for (int k = 0; k < 4; ++k) {
      const float mrow = bflo(q[k][1][2]) * kmx + 0.01f;
      safe = safe && (!h || mrow <= M_LIMIT);
      if (h) q[k][1][2] = cvtpk(-mrow, 0.f);
    }
  }
  const bool fast = __all(safe);
  stamp(x);
  f32x16 own;
  if (fast) {
    unsigned so_kv = kvs + 3 * 4096, so_w = OFF_WGU + (x.m * 196 + 2 * w) * KB, so_l = 2 * w * KB, so_g = OFF_WGU + (x.m * 196 + 112 + 12 * w) * KB, spin = 1u << 16, fvo = (l & 15) * 4096;
    Two ab = base_wo(x);
    const Two wd = base_wd(x);
    asm volatile(
      ".macro GRP s, pa, pb\n\t"
      "v_exp_f32 v200, v[\\s]\n\tv_exp_f32 v201, v[\\s+1]\n\tv_cvt_pk_bf16_f32 v[\\pa], v202, v203\n\t"
      "v_exp_f32 v202, v[\\s+2]\n\tv_exp_f32 v203, v[\\s+3]\n\tv_cvt_pk_bf16_f32 v[\\pb], v200, v201\n\t"
      ".endm\n\t"
      ".macro MEM kind, i, ld\n\t"
      ".if \\kind == 1\n\t"
      "buffer_load_dwordx4 v[128+16*\\ld+4*\\i:128+16*\\ld+4*\\i+3], %[vo], %[rs], %[skv] offen offset:1024*\\i sc1\n\t"
      ".elseif \\kind == 5\n\t"
      "ds_write_b128 v205, v[4*\\i:4*\\i+3] offset:1024*\\i\n\t"
      ".elseif \\kind == 6\n\t"
      "ds_write_b128 v205, v[4:7] offset:1024\n\t"
      ".elseif \\kind == 7\n\t"
      "ds_write_b128 v205, v[12:15] offset:3072\n\t"
      ".elseif \\kind == 2\n\t"
      "buffer_load_dword v206, v205, %[rs], 0 offen offset:3072 sc1\n\t"
      ".elseif \\kind == 3\n\t"
      ".if att_n < 16 || att_n >= 56\n\t"
      ".if att_n < 102\n\t"
      ".if att_n < 16\n\t.set att_r, 184+4*att_n\n\t.else\n\t.set att_r, 4*(att_n-56)\n\t.endif\n\t"
      ".if att_n & 1\n\t"
      "buffer_load_dwordx4 a[att_r:att_r+3], %[vo], %[rs], %[ab1] offen offset:1024*((att_n>>1)&3) sc1\n\t"
      ".else\n\t"
      "buffer_load_dwordx4 a[att_r:att_r+3], %[vo], %[rs], %[ab0] offen offset:1024*((att_n>>1)&3) sc1\n\t"
      ".endif\n\t"
      ".if att_n == 15\n\t"
      "s_mov_b32 %[ab0], %[wd0]\n\ts_mov_b32 %[ab1], %[wd1]\n\t"
      ".elseif (att_n & 7) == 7\n\t"
      "s_add_u32 %[ab0], %[ab0], 4096\n\ts_add_u32 %[ab1], %[ab1], 4096\n\t"
      ".endif\n\t"
      ".endif\n\t"
      ".elseif att_n < 44\n\t"
      ".if att_n & 1\n\t"
      "buffer_load_dwordx4 %[vo], %[rs], %[sw] offen offset:1024 sc1 lds\n\t"
      "s_add_u32 %[sw], %[sw], 8192\n\ts_add_u32 m0, m0, 8192\n\t"
      ".else\n\t"
      "buffer_load_dwordx4 %[vo], %[rs], %[sw] offen sc1 lds\n\t"
      ".endif\n\t"
      ".else\n\t"
      "buffer_load_dwordx4 v[208+4*(att_n-44):208+4*(att_n-44)+3], %[vo], %[rs], %[sg] offen offset:1024*(att_n&3) sc1\n\t"
      ".if (att_n & 3) == 3\n\ts_add_u32 %[sg], %[sg], 4096\n\t.endif\n\t"
      ".endif\n\t"
      ".set att_n, att_n+1\n\t"
      ".endif\n\t"
      ".endm\n\t"
      ".macro STEP sc, sn, k, nk, pk, tk, nt, pt, ld, m0k, m0i, m1k, m2k, m2i, m3k\n\t"
      "v_mfma_f32_32x32x16_bf16 v[\\sn:\\sn+15], v[128+16*\\nt:128+16*\\nt+3], v[64+8*\\nk:64+8*\\nk+3], 0\n\t"
      "MEM \\m0k, \\m0i, \\ld\n\t"
      "GRP \\sc, 199, 192\n\t"
      "v_mfma_f32_32x32x16_bf16 v[\\sn:\\sn+15], v[132+16*\\nt:132+16*\\nt+3], v[68+8*\\nk:68+8*\\nk+3], v[\\sn:\\sn+15]\n\t"
      "MEM \\m1k, 0, 0\n\t"
      "GRP \\sc+4, 193, 194\n\t"
      "v_mfma_f32_32x32x16_bf16 v[16*\\pk:16*\\pk+15], v[140+16*\\pt:140+16*\\pt+3], v[196:199], v[16*\\pk:16*\\pk+15]\n\t"
      "MEM \\m2k, \\m2i, \\ld\n\t"
      "GRP \\sc+8, 195, 196\n\t"
      "v_mfma_f32_32x32x16_bf16 v[16*\\k:16*\\k+15], v[136+16*\\tk:136+16*\\tk+3], v[192:195], v[16*\\k:16*\\k+15]\n\t"
      "MEM \\m3k, 0, 0\n\t"
      "GRP \\sc+12, 197, 198\n\t"
      ".endm\n\t"
      ".macro TILE tk, nx, pv, on, wait, p, fl=0\n\t"
      "STEP 96, 112, 0, 1, 3, \\tk, \\tk, \\pv, \\pv, \\p, 0, \\p, \\p, 0, \\fl\n\t"
      "STEP 112, 96, 1, 2, 0, \\tk, \\tk, \\tk, \\pv, \\on, 0, \\p, \\on, 1, 0\n\t"
      "STEP 96, 112, 2, 3, 1, \\tk, \\tk, \\tk, \\pv, \\on, 2, \\p, \\on, 3, 0\n\t"
      ".if \\on\n\ts_add_u32 %[skv], %[skv], 4096\n\t.endif\n\t"
      "s_waitcnt vmcnt(\\wait)\n\t"
      "STEP 112, 96, 3, 0, 2, \\tk, \\nx, \\tk, \\pv, \\p, 0, \\p, \\p, 0, 0\n\t"
      ".endm\n\t"
      ".set att_n, 0\n\t"
      "s_mov_b32 m0, %[sl]\n\t"
      "v_mov_b32 v196, 0\n\tv_mov_b32 v197, 0\n\tv_mov_b32 v198, 0\n\tv_mov_b32 v199, 0\n\tv_mov_b32 v202, 0\n\tv_mov_b32 v203, 0\n\t"
      "v_mov_b32 v188, 0\n\tv_mov_b32 v189, 0\n\tv_mov_b32 v190, 0\n\tv_mov_b32 v191, 0\n\t"
      "v_mfma_f32_32x32x16_bf16 v[0:15], v[196:199], v[196:199], 0\n\t"
      "v_mfma_f32_32x32x16_bf16 v[16:31], v[196:199], v[196:199], 0\n\t"
      "v_mfma_f32_32x32x16_bf16 v[32:47], v[196:199], v[196:199], 0\n\t"
      "v_mfma_f32_32x32x16_bf16 v[48:63], v[196:199], v[196:199], 0\n\t"
      "s_waitcnt vmcnt(8)\n\t"
      "v_mfma_f32_32x32x16_bf16 v[96:111], v[128:131], v[64:67], 0\n\t"
      "v_mfma_f32_32x32x16_bf16 v[96:111], v[132:135], v[68:71], v[96:111]\n\t"
      "s_nop 11\n\t"
      "TILE 0, 1, 3, 1, 8, 0\n\t"
      "TILE 1, 2, 0, 1, 8, 0\n\tTILE 2, 3, 1, 1, 9, 0, 2\n\t"
      "s_waitcnt vmcnt(4)\n\t"
      "1:\n\t"
      "v_cmp_gt_u32 vcc, 16, v206\n\t"
      "s_and_b64 vcc, exec, vcc\n\t"
      "s_cbranch_vccz 2f\n\t"
      "s_sleep 24\n\t"
      "buffer_load_dword v206, v205, %[rs], 0 offen offset:3072 sc1\n\t"
      "s_sub_u32 %[spin], %[spin], 1\n\t"
      "s_waitcnt vmcnt(0)\n\t"
      "s_cbranch_scc0 1b\n\t"
      "2:\n\t"
      "s_barrier\n\t"
      "TILE 3, 0, 2, 1, 14, 3\n\t"
      "TILE 0, 1, 3, 1, 21, 3\n\tTILE 1, 2, 0, 1, 24, 3\n\tTILE 2, 3, 1, 1, 24, 3\n\tTILE 3, 0, 2, 1, 24, 3\n\t"
      "TILE 0, 1, 3, 1, 24, 3\n\tTILE 1, 2, 0, 1, 24, 3\n\tTILE 2, 3, 1, 1, 24, 3\n\tTILE 3, 0, 2, 1, 24, 3\n\t"
      "TILE 0, 1, 3, 1, 24, 3\n\tTILE 1, 2, 0, 0, 20, 3\n\tTILE 2, 3, 1, 0, 16, 3\n\t"
      "v_add_u32 v205, %[d1], v204\n\tv_add_u32 v206, %[d2], v204\n\t"
      "STEP 96, 112, 0, 1, 3, 3, 3, 2, 0, 3, 0, 3, 3, 0, 3\n\t"
      "STEP 112, 96, 1, 2, 0, 3, 3, 3, 0, 3, 0, 3, 0, 0, 0\n\t"
      "STEP 96, 112, 2, 3, 1, 3, 3, 3, 0, 5, 0, 6, 5, 2, 7\n\t"
      "ds_write_b128 v206, v[16:19]\n\t"
      "GRP 112, 199, 192\n\t"
      "ds_write_b128 v206, v[20:23] offset:1024\n\t"
      "GRP 116, 193, 194\n\t"
      "v_mfma_f32_32x32x16_bf16 v[32:47], v[188:191], v[196:199], v[32:47]\n\t"
      "ds_write_b128 v206, v[24:27] offset:2048\n\t"
      "GRP 120, 195, 196\n\t"
      "v_mfma_f32_32x32x16_bf16 v[48:63], v[184:187], v[192:195], v[48:63]\n\t"
      "ds_write_b128 v206, v[28:31] offset:3072\n\t"
      "GRP 124, 197, 198\n\t"
      "v_add_u32 v200, %[d3], v204\n\t"
      "v_cvt_pk_bf16_f32 v199, v202, v203\n\t"
      "s_nop 1\n\t"
      "v_mfma_f32_32x32x16_bf16 v[48:63], v[188:191], v[196:199], v[48:63]\n\t"
      "ds_write_b128 v200, v[32:35]\n\tds_write_b128 v200, v[36:39] offset:1024\n\tds_write_b128 v200, v[40:43] offset:2048\n\tds_write_b128 v200, v[44:47] offset:3072\n\t"
      "s_nop 7\n\t"
      ".purgem GRP\n\t.purgem MEM\n\t.purgem STEP\n\t.purgem TILE"
      : "={v[48:63]}"(own), "+{v[128:143]}"(kf[0]), "+{v[144:159]}"(kf[1]), "+{v[160:175]}"(kf[2]),
        "={v[208:223]}"(g2[0]), "={v[224:239]}"(g2[1]), "={v[240:255]}"(g2[2]),
        [skv] "+s"(so_kv), [sw] "+s"(so_w), [ab0] "+s"(ab.s0), [ab1] "+s"(ab.s1), [spin] "+s"(spin), [sg] "+s"(so_g), "+{v205}"(fvo)
      : "{v[64:67]}"(q[1][0]), "{v[68:71]}"(q[1][1]), "{v[72:75]}"(q[2][0]), "{v[76:79]}"(q[2][1]),
        "{v[80:83]}"(q[3][0]), "{v[84:87]}"(q[3][1]), "{v[88:91]}"(q[0][0]), "{v[92:95]}"(q[0][1]),
        [vo] "{v204}"(x.vl16), [d1] "s"(SCR + (w ^ 1) * 3 * 4096), [d2] "s"(SCR + ((w ^ 2) * 3 + 1) * 4096), [d3] "s"(SCR + ((w ^ 3) * 3 + 2) * 4096), [rs] "s"(x.rs), [sl] "s"(so_l), [wd0] "s"(wd.s0), [wd1] "s"(wd.s1)
      : "memory", "scc", "vcc",
        "v0","v1","v2","v3","v4","v5","v6","v7","v8","v9","v10","v11","v12","v13","v14","v15","v16","v17","v18","v19","v20","v21","v22","v23",
        "v24","v25","v26","v27","v28","v29","v30","v31","v32","v33","v34","v35","v36","v37","v38","v39","v40","v41","v42","v43","v44","v45","v46","v47",
        "v96","v97","v98","v99","v100","v101","v102","v103","v104","v105","v106","v107","v108","v109","v110","v111",
        "v112","v113","v114","v115","v116","v117","v118","v119","v120","v121","v122","v123","v124","v125","v126","v127",
        "v176","v177","v178","v179","v180","v181","v182","v183","v184","v185","v186","v187","v188","v189","v190","v191",
        "v192","v193","v194","v195","v196","v197","v198","v199","v200","v201","v202","v203","v206", ACLOB);
  } else {
    f32x16 o[4];
    wv<0>();
    pin(kf[0]); pin(kf[1]); pin(kf[2]);
    float mx[4];
#pragma unroll
    for (int k = 0; k < 4; ++k) {
      mx[k] = -3.0e38f; if (h) q[k][1][2] = 0; pin(q[k][1]);
#pragma unroll
      for (int i = 0; i < 16; ++i) o[k][i] = 0.f;
    }
#pragma nounroll
    for (unsigned spin = 0; spin < (1u << 16); ++spin) {
      float v;
      ld1(v, (l & 15) * 4096, x.rs, OFF_FLAG + FL_W(0)); wv<0>(); pin(v);
      if (__all(__float_as_uint(v) >= 16u)) break;
      __builtin_amdgcn_s_sleep(24);
    }
    pf_wo(x);
    pf_wgu1(x);
    sfor<0, 12>([&](auto j) {
      u32x4 t;
      ld4<0>(t, x.vl16, x.rs, OFF_WGU + (x.m * 196 + 112 + 12 * w + j) * KB); wv<0>(); pin(t);
#pragma unroll
      for (int i = 0; i < 4; ++i) g2[j >> 2][4 * (j & 3) + i] = __uint_as_float(t[i]);
    });
    pf_wd<0, 46>(x, base_wd(x));
    for (int tk = 0; tk < 16; ++tk) {
      u32x4 kf[4];
      sfor<0, 4>([&](auto i) { ld4<i * 1024>(kf[i], x.vl16, x.rs, kvs + tk * 4096); });
      wv<0>();
      sfor<0, 4>([&](auto k) {
        f32x16 s;
        nop<1>();
        m32z(s, kf[0], q[k][0]); m32(s, kf[1], q[k][1]);
        nop<11>(); pin(s);
        float tm = s[0];
#pragma unroll
        for (int i = 1; i < 16; ++i) tm = fmaxf(tm, s[i]);
        tm = max32(tm);
        const float mn = fmaxf(mx[k], tm), al = __builtin_amdgcn_exp2f(mx[k] - mn);
        mx[k] = mn;
#pragma unroll
        for (int i = 0; i < 16; ++i) o[k][i] *= al;
        u32x4 p0, p1;
#pragma unroll
        for (int d = 0; d < 4; ++d) {
          p0[d] = cvtpk(__builtin_amdgcn_exp2f(s[2 * d] - mn), __builtin_amdgcn_exp2f(s[2 * d + 1] - mn));
          p1[d] = cvtpk(__builtin_amdgcn_exp2f(s[8 + 2 * d] - mn), __builtin_amdgcn_exp2f(s[9 + 2 * d] - mn));
        }
        pin(p0); pin(p1); pin(o[k]);
        nop<1>();
        m32(o[k], kf[2], p0); m32(o[k], kf[3], p1);
        nop<11>(); pin(o[k]);
      });
    }
    bar();
    float* mm = (float*)(lds + SCR);
#pragma unroll
    for (int k = 0; k < 4; ++k) if (h == 0) mm[((w ^ k) * 4 + w) * 32 + r] = mx[k];
    bar();
#pragma unroll
    for (int k = 0; k < 4; ++k) {
      const float* p = mm + (w ^ k) * 128 + r;
      const float g = fmaxf(fmaxf(p[0], p[32]), fmaxf(p[64], p[96])), f = __builtin_amdgcn_exp2f(mx[k] - g);
#pragma unroll
      for (int i = 0; i < 16; ++i) o[k][i] *= f;
    }
    bar();
    reduce_put(x, o);
    own = o[0];
  }
  stamp(x);
  reduce_get(x, own);
  pin(own); fstamp(x);
  {
    auto lr = __builtin_amdgcn_permlane32_swap(__float_as_uint(own[12]), __float_as_uint(own[12]), false, false);
    const float inv = __builtin_amdgcn_rcpf(__uint_as_float(lr[1]));
    u32x4 a, b;
#pragma unroll
    for (int d = 0; d < 4; ++d) { a[d] = cvtpk(own[2 * d] * inv, own[2 * d + 1] * inv); b[d] = cvtpk(own[8 + 2 * d] * inv, own[9 + 2 * d] * inv); }
    if (h) { b[2] = 0; b[3] = 0; }
    const unsigned so = OFF_O + L * SZ_O + (tt * 32 + 2 * (2 * hp + kh)) * KB;
    st4<0>(a, x.vl16, x.rs, so); st4<1024>(b, x.vl16, x.rs, so);
  }
  fstamp(x);
  wv<0>();
  fstamp(x);
  signal(x, FL_O(L, x.c, th), 1);
  if (L == 0 && th == 0) {
    if (x.c == hp) count(x, FL_W2, 1);
  }
}

template <bool FINAL> DI void res_epilogue(Ctx& x, const f32x16& acc, const u32x4 (&xr)[2], unsigned Xo, unsigned STo, int tt, int nt, char* out = nullptr) {
  const int h = x.l >> 5, r = x.l & 31;
  u32x4 y[2];
#pragma unroll
  for (int d = 0; d < 8; ++d) y[d >> 2][d & 3] = cvtpk(acc[2 * d] + bflo(xr[d >> 2][d & 3]), acc[2 * d + 1] + bfhi(xr[d >> 2][d & 3]));
  if constexpr (FINAL) {
#pragma unroll
    for (int s = 0; s < 2; ++s) {
      auto a = __builtin_amdgcn_permlane32_swap(y[s][0], y[s][2], false, false);
      auto b = __builtin_amdgcn_permlane32_swap(y[s][1], y[s][3], false, false);
      y[s] = u32x4{a[0], b[0], a[1], b[1]};
    }
    const unsigned vo = r * (D * 2) + h * 16, so = tt * 32 * D * 2 + nt * 64;
    const rsrc_t ro = mkrs(out, S * D * 2);
    st4p<0>(y[0], vo, ro, so); st4p<32>(y[1], vo, ro, so);
  } else {
    const unsigned so = Xo + (tt * 28 + 2 * nt) * KB;
    st4<0>(y[0], x.vl16, x.rs, so); st4<1024>(y[1], x.vl16, x.rs, so);
    float s = 0.f, q = 0.f, s1 = 0.f, q1 = 0.f;
#pragma unroll
    for (int d = 0; d < 4; ++d) {
      s = dot2(y[0][d], 0x3f803f80u, s); q = dot2(y[0][d], y[0][d], q); s1 = dot2(y[1][d], 0x3f803f80u, s1); q1 = dot2(y[1][d], y[1][d], q1);
    }
    s = sum32(s + s1); q = sum32(q + q1);
    if (h == 0) st2<0>(u32x2{__float_as_uint(s), __float_as_uint(q)}, r * 8, x.rs, STo + (nt * 2048 + tt * 32) * 8);
  }
}

DI void phase_oproj(Ctx& x, const int L, const Args& a, const f32x16 (&g2)[3]) {
  const int w = x.w;
  auto put_g2 = [&]() {
#pragma unroll
    for (int j = 0; j < 12; ++j)
      *(f32x4*)(lds + (112 + 12 * w + j) * KB + x.vl16) = f32x4{g2[j >> 2][4 * (j & 3)], g2[j >> 2][4 * (j & 3) + 1], g2[j >> 2][4 * (j & 3) + 2], g2[j >> 2][4 * (j & 3) + 3]};
  };
  if (x.m < 14) {
    const int th = x.m / 7, ct = x.m % 7, tt0 = 4 * x.c + 2 * th, tt = tt0 + (w >> 1), nt = 2 * ct + (w & 1);
    u32x2 x0[4];
    if (L == 0) {
      const rsrc_t rx = mkrs(a.x, S * D * 2);
      sfor<0, 4>([&](auto i) { ld2i<16 * i>(x0[i], (x.l & 31) * 896 + (x.l >> 5) * 8, rx, tt * 32 * 896 + nt * 64); });
    } else {
      sfor<0, 4>([&](auto i) { ld2u<(i >> 1) * 1024 + (i & 1) * 8>(x0[i], x.vl16, x.rs, OFF_X + 2 * SZ_X + (tt * 28 + 2 * nt) * KB); });
    }
    await(x, FL_O(L, x.c, th), 8);
    stamp(x);
    u32x4 b[8][2];
    const unsigned s0 = OFF_O + L * SZ_O + (tt * 32 + 8 * w) * KB, s1 = OFF_O + L * SZ_O + ((tt0 + ((w >> 1) ^ 1)) * 32 + 8 * w) * KB;
    auto ld = [&](auto i) { ld4<(i & 3) * 1024>(b[i][0], x.vl16, x.rs, s0 + (i >> 2) * 4096); ld4<(i & 3) * 1024>(b[i][1], x.vl16, x.rs, s1 + (i >> 2) * 4096); };
    constexpr int WN = 5;
    sfor<0, WN>(ld);
    f32x16 acc[4];
    sfor<0, 8>([&](auto i) {
      constexpr int last = (i + WN - 1 < 7 ? i + WN - 1 : 7);
      wv<2 * (last - i) + 1>();
      m32a<A_WO + 8 * i, i == 0>(acc[0], b[i][0]); m32a<A_WO + 8 * i + 4, i == 0>(acc[1], b[i][0]);
      wv<2 * (last - i)>();
      m32a<A_WO + 8 * i, i == 0>(acc[2], b[i][1]); m32a<A_WO + 8 * i + 4, i == 0>(acc[3], b[i][1]);
      if constexpr (i + WN < 8) ld(std::integral_constant<int, i + WN>{});
    });
    nop<11>();
    sfor<0, 4>([&](auto k) { pin(acc[k]); pin(x0[k]); });
    stamp(x);
    reduce_put(x, acc); reduce_get(x, acc[0]);
    pin(acc[0]); fstamp(x);
    const u32x4 x2[2] = {u32x4{x0[0][0], x0[0][1], x0[1][0], x0[1][1]}, u32x4{x0[2][0], x0[2][1], x0[3][0], x0[3][1]}};
    res_epilogue<false>(x, acc[0], x2, OFF_X + (2 * L + 1) * SZ_X, OFF_ST + (2 * L + 1) * SZ_ST, tt, nt);
    fstamp(x);
    put_g2();
    wv<0>();
    fstamp(x);
    signal(x, FL_X1(L, x.c), 1);
    fstamp(x);
  } else {
    put_g2();
    stamp(x); stamp(x); fstamp(x); fstamp(x); fstamp(x); fstamp(x);
  }
}

constexpr int DWN = 7;
constexpr bool dn_half(int o) { return o == 4 || o == 5 || o == 14 || o == 15; }
struct DnSrc { unsigned s0, s1, l0, l1; };
DI DnSrc dn_src(const Ctx& x, int L) {
  const int tt0 = 4 * x.c + 2 * ((x.m / 7) & 1), tt = tt0 + (x.w >> 1), tto = tt0 + ((x.w >> 1) ^ 1);
  return DnSrc{OFF_H + L * SZ_H + (tt * 112 + 28 * x.w) * KB, OFF_H + L * SZ_H + (tto * 112 + 28 * x.w) * KB,
               OFF_HL + ((L * 64 + tt) * 16 + 4 * x.w) * KB, OFF_HL + ((L * 64 + tto) * 16 + 4 * x.w) * KB};
}
template <int O> DI void dn_ld(Ctx& x, const DnSrc& d, u32x4 (&b)[DWN][2]) {
  constexpr int i = dn_ks(O);
  if constexpr (dn_half(O)) {
    constexpr int imm = (O >= 14 ? 2048 : 0) + (O & 1) * 512;
    u32x2 lo, hi, lo1, hi1;
    ld2u<imm>(lo, x.l * 8, x.rs, d.l0); ld2u<imm + 1024>(hi, x.l * 8, x.rs, d.l0);
    ld2u<imm>(lo1, x.l * 8, x.rs, d.l1); ld2u<imm + 1024>(hi1, x.l * 8, x.rs, d.l1);
    b[O % DWN][0] = u32x4{lo[0], lo[1], hi[0], hi[1]}; b[O % DWN][1] = u32x4{lo1[0], lo1[1], hi1[0], hi1[1]};
  } else {
    ld4<(i & 3) * 1024>(b[O % DWN][0], x.vl16, x.rs, d.s0 + (i >> 2) * 4096); ld4<(i & 3) * 1024>(b[O % DWN][1], x.vl16, x.rs, d.s1 + (i >> 2) * 4096);
  }
}

DI void gu_tile(const f32x4 (&acc)[2][2], const u32x4 (&cs)[2], const float (&mean)[2], const float (&r2)[2], u32x2 (&hp)[2]) {
  const f32x4 cg = __builtin_bit_cast(f32x4, cs[0]), cu = __builtin_bit_cast(f32x4, cs[1]);
#pragma unroll
  for (int u = 0; u < 2; ++u) {
    const f32x2 nm = {-mean[u], -mean[u]}, rr = {r2[u], r2[u]};
#pragma unroll
    for (int e = 0; e < 2; ++e) {
      const f32x2 g = __builtin_elementwise_fma(nm, f32x2{cg[2 * e], cg[2 * e + 1]}, f32x2{acc[0][u][2 * e], acc[0][u][2 * e + 1]});
      const f32x2 v = __builtin_elementwise_fma(nm, f32x2{cu[2 * e], cu[2 * e + 1]}, f32x2{acc[1][u][2 * e], acc[1][u][2 * e + 1]});
      const f32x2 y = g * rr * v;
      hp[u][e] = cvtpk(y[0], y[1]);
    }
  }
}

DI void phase_gu(Ctx& x, const int L, u32x4 (&b)[DWN][2]) {
  const int w = x.w, l = x.l, g = l >> 4, c16 = l & 15, m = x.m, tt = 4 * x.c + w;
  if (L == 0 && wave0(x)) { unsigned v; ldflag(v, x.rs, OFF_FLAG + FL_W2); wv<0>(); recheck(x, v, FL_W2, 8); }
  await(x, FL_X1(L, x.c), 14);
  stamp(x);
  constexpr int NXV = 11;
  u32x4 xf[NXV][2]; f32x2 sp[2][4];
  const unsigned sx = OFF_X + (2 * L + 1) * SZ_X + tt * 28 * KB, st = OFF_ST + (2 * L + 1) * SZ_ST + tt * 32 * 8;
  auto ldx = [&](auto k, auto u) {
    if constexpr (k < NXV) ld4<(k & 1) * 2048 + 256 * u>(xf[k][u], x.vg16, x.rs, sx + (k >> 1) * 4096);
    else if constexpr (k < 14) lda<A_GX + 8 * (k - NXV) + 4 * u, (k & 1) * 2048 + 256 * u>(x.vg16, x.rs, sx + (k >> 1) * 4096);
    else { constexpr int i = k - 14; ld2<128 * u>(sp[u][i], (((i < 3 || g < 2) ? g + 4 * i : g) * 2048 + c16) * 8, x.rs, st); }
  };
  using I0 = std::integral_constant<int, 0>; using I1 = std::integral_constant<int, 1>;
  constexpr int XA = 3;
  sfor<0, XA>([&](auto k) { ldx(k, I0{}); ldx(k, I1{}); });
  const unsigned a0 = x.vl16, a1 = x.vl16 + 65536, a2 = x.vl16 + 131072;
  constexpr int DP = 6;
  u32x4 wf[8];
  auto rd = [&](auto ii) {
    constexpr int i = ii, slot = i >= 160 ? i - 160 : i;
    if constexpr (slot < 64) dsr<slot * 1024>(wf[i & 7], a0); else if constexpr (slot < 128) dsr<(slot - 64) * 1024>(wf[i & 7], a1); else dsr<(slot - 128) * 1024>(wf[i & 7], a2);
  };
  auto mm = [&](f32x4& c, const u32x4& a, auto k, auto u) {
    if constexpr (k < NXV) m16<k == 0>(c, a, xf[k][u]); else m16a<false, A_GX + 8 * (k - NXV) + 4 * u>(c, a);
  };
  const Two wd = base_wd(x);
  f32x4 aa[5][2][2], ch0, ch1;
  float mean[2], r2[2];
  sfor<0, DP>(rd);
  sfor<0, 140>([&](auto i) {
    constexpr int KS = i / 10, s = i % 10, t = s >> 1, mat = i & 1;
    using K = std::integral_constant<int, KS>;
    constexpr auto side = [](int k) { return (k >= 0 && k <= 3) || (k >= 5 && k <= 12); };
    constexpr auto all = [](int k) { return (k + XA < 14 ? 2 : 0) + ((k >= 0 && k <= 3) || (k >= 5 && k <= 9) ? 2 : 0) + (k == 5 ? 1 : 0) + (k >= 10 ? 3 : 0); };
    if constexpr (s == 0) {
      constexpr int cnt = [&] { int n = side(KS - XA) ? 1 : 0; for (int k = KS - XA + 1; k < KS; ++k) n += all(k); return n; }();
      wv<cnt>();
      if constexpr (KS == 5) asm volatile("s_barrier" ::: "memory");
    }
    if constexpr ((i & 1) == 0) { rd(std::integral_constant<int, i + DP>{}); rd(std::integral_constant<int, i + DP + 1>{}); wl<DP>(); }
    mm(aa[t][mat][0], wf[i & 7], K{}, I0{});
    if constexpr (s == 1 && KS + XA < 14) ldx(std::integral_constant<int, KS + XA>{}, I0{});
    if constexpr (s == 6 && KS + XA < 14) ldx(std::integral_constant<int, KS + XA>{}, I1{});
    if constexpr (KS <= 3 && s == 3) ldx(std::integral_constant<int, 14 + KS>{}, I0{});
    if constexpr (KS <= 3 && s == 8) ldx(std::integral_constant<int, 14 + KS>{}, I1{});
    if constexpr (KS == 5 && s == 3) asm volatile("buffer_load_dwordx4 %0, %2, %3, %4 offen sc1\n\tbuffer_load_dwordx4 %1, %2, %3, %4 offen offset:1024 sc1" : "=&v"(ch0), "=&v"(ch1) : "v"(x.vl16), "s"(x.rs), "s"(OFF_CSGU + m * 2048));
    if constexpr (KS == 11 && s == 2) { pin(ch0); pin(ch1); *(f32x4*)(lds + 40 * KB + x.vl16) = ch0 + ch1; }
    if constexpr (KS >= 5 && KS <= 9 && (s == 3 || s == 8) && !(KS == 5 && s == 3)) pf_wgu3<2 * (KS - 5) + (s == 8) - 1>(x);
    if constexpr (KS >= 10 && (s == 3 || s == 5 || s == 8) && 3 * (KS - 10) + (s == 3 ? 0 : (s == 5 ? 1 : 2)) < 10) {
      constexpr int f = 46 + 3 * (KS - 10) + (s == 3 ? 0 : (s == 5 ? 1 : 2));
      pf_wd<f, f + 1>(x, wd);
    }
    if constexpr ((KS == 10 || KS == 12) && s == 2) {
      constexpr int u = KS == 12;
      sfor<0, 4>([&](auto e) { pin(sp[u][e]); });
      float sm = sp[u][0][0] + sp[u][1][0] + sp[u][2][0], q = sp[u][0][1] + sp[u][1][1] + sp[u][2][1];
      if (g < 2) { sm += sp[u][3][0]; q += sp[u][3][1]; }
      sm = sum32(sum16(sm)); q = sum32(sum16(q));
      float rs; row_stats(sm, q, mean[u], rs); r2[u] = rs * rs;
      pin(mean[u]); pin(r2[u]);
    }
    mm(aa[t][mat][1], wf[i & 7], K{}, I1{}); SB();
  });
  fstamp(x);
  u32x4 cs[7][2];
  const unsigned csa = 40 * KB + g * 16;
  sfor<0, 10>([&](auto i) { dsr<64 * i>(cs[i >> 1][i & 1], csa); });
  wv<10>();
  asm volatile("s_barrier" ::: "memory");
  stamp(x);
  const unsigned H = OFF_H + L * SZ_H + tt * 112 * KB;
  const int gp = (m & 1) ? 7 * m + 1 : 7 * m;
  auto pair = [&](const f32x4 (&t0)[2][2], const f32x4 (&t1)[2][2], auto lt) {
    u32x2 h0[2], h1[2];
    gu_tile(t0, cs[lt], mean, r2, h0); gu_tile(t1, cs[lt + 1], mean, r2, h1);
    st4<0>(u32x4{h0[0][0], h0[0][1], h1[0][0], h1[0][1]}, x.vg16, x.rs, H + (gp + lt - 1) * 1024);
    st4<256>(u32x4{h0[1][0], h0[1][1], h1[1][0], h1[1][1]}, x.vg16, x.rs, H + (gp + lt - 1) * 1024);
  };
  {
    wl<0>();
    sfor<0, 20>([&](auto i) { pin(aa[i >> 2][(i >> 1) & 1][i & 1]); });
    sfor<0, 10>([&](auto i) { pin(cs[i >> 1][i & 1]); });
    u32x2 hl[2];
    gu_tile(aa[0], cs[0], mean, r2, hl);
    const unsigned sl = OFF_HL + ((L * 64 + tt) * 16 + m) * KB;
    st2<0>(hl[0], x.vg16 >> 1, x.rs, sl); st2<128>(hl[1], x.vg16 >> 1, x.rs, sl);
    pair(aa[1], aa[2], std::integral_constant<int, 1>{});
    pair(aa[3], aa[4], std::integral_constant<int, 3>{});
  }
  SB();
  sfor<10, 14>([&](auto i) { dsr<64 * i>(cs[i >> 1][i & 1], csa); });
  fstamp(x);
  f32x4 ab[2][2][2];
  unsigned fa;
  sfor<140, 196>([&](auto i) {
    constexpr int j = i - 140, KS = j / 4, t = (j % 4) >> 1, mat = i & 1;
    using K = std::integral_constant<int, KS>;
    if constexpr (j == 18) {
      fstamp(x);
      if (L == 0) wv<6>(); else wv<0>();
      asm volatile("s_barrier" ::: "memory");
      count(x, FL_H(L, x.c, 0), 1);
      SB();
      fstamp(x);
    }
    if constexpr ((i & 1) == 0) { if constexpr (i + DP < 196) { rd(std::integral_constant<int, i + DP>{}); rd(std::integral_constant<int, i + DP + 1>{}); wl<DP>(); } else wl<194 - i>(); }
    mm(ab[t][mat][0], wf[i & 7], K{}, I0{});
    if constexpr (j % 3 == 1 && j < 42) { if (L == 0) pf_wq<j / 3>(x); SB(); }
    if constexpr (j == 44) ldflag(fa, x.rs, OFF_FLAG + FL_H(L, x.c, 0));
    mm(ab[t][mat][1], wf[i & 7], K{}, I1{}); SB();
  });
  wv<0>();
  recheck(x, fa, FL_H(L, x.c, 0), 16);
  { const DnSrc d = dn_src(x, L); sfor<0, 3>([&](auto o) { dn_ld<o>(x, d, b); }); }
  stamp(x);
  sfor<0, 8>([&](auto i) { pin(ab[i >> 2][(i >> 1) & 1][i & 1]); });
  sfor<10, 14>([&](auto i) { pin(cs[i >> 1][i & 1]); });
  pair(ab[0], ab[1], std::integral_constant<int, 5>{});
}

DI void phase_down(Ctx& x, const int L, char* out, u32x4 (&b)[DWN][2]) {
  const int w = x.w;
  if (x.m >= 14) {
    wv<0>();
    sfor<0, 3>([&](auto o) { pin(b[o][0]); pin(b[o][1]); });
    signal(x, FL_H(L, x.c, 1), 1);
    stamp(x); fstamp(x); fstamp(x); stamp(x);
    return;
  }
  const int th = x.m / 7, ct = x.m % 7, tt = 4 * x.c + 2 * th + (w >> 1), nt = 2 * ct + (w & 1);
  stamp(x);
  constexpr int WN = DWN;
  u32x4 xr[2];
  const DnSrc d = dn_src(x, L);
  auto ld = [&](auto o) { dn_ld<o>(x, d, b); };
  sfor<3, WN>(ld);
  f32x16 acc[4];
  unsigned fb;
  sfor<0, 25>([&](auto i) {
    constexpr int last = i + WN - 1 < 27 ? i + WN - 1 : 27, ex = i <= 2 ? 2 : ((i >= 8 && i <= 13) ? 1 : ((i >= 21) ? 2 : 0));
    constexpr int later = [&] { int n = 0; for (int o = i + 1; o <= last; ++o) n += dn_half(o) ? 4 : 2; return n; }();
    wv<later + (dn_half(i) ? 2 : 1) + ex>();
    if constexpr (i == 3) signal(x, FL_H(L, x.c, 1), 1);
    m32a<A_WD + 8 * i, i == 0>(acc[0], b[i % WN][0]); m32a<A_WD + 8 * i + 4, i == 0>(acc[1], b[i % WN][0]);
    wv<later + ex>();
    m32a<A_WD + 8 * i, i == 0>(acc[2], b[i % WN][1]); m32a<A_WD + 8 * i + 4, i == 0>(acc[3], b[i % WN][1]);
    if constexpr (i == 7) ldflag(fb, x.rs, OFF_FLAG + FL_H(L, x.c, 1));
    if constexpr (i + WN == 20) { fstamp(x); wv<12>(); recheck(x, fb, FL_H(L, x.c, 1), 16); fstamp(x); }
    if constexpr (i + WN < 28) ld(std::integral_constant<int, i + WN>{});
    if constexpr (i + WN == 27) {
      const unsigned sr = OFF_X + (2 * L + 1) * SZ_X + (tt * 28 + 2 * nt) * KB;
      ld4<0>(xr[0], x.vl16, x.rs, sr); ld4<1024>(xr[1], x.vl16, x.rs, sr);
    }
  });
  wv<2>();
  sfor<1, 5>([&](auto t) {
    constexpr int k = t & 3;
    m32a<A_WD + 8 * 25 + 4 * (k & 1), false>(acc[k], b[25 % WN][k >> 1]);
    m32a<A_WD + 8 * 26 + 4 * (k & 1), false>(acc[k], b[26 % WN][k >> 1]);
    if constexpr (t > 1) { nop<(t == 2 ? 9 : 7)>(); red_put1<t - 1, 0>(x, acc[t - 1]); red_put1<t - 1, 1>(x, acc[t - 1]); }
    m32a<A_WD + 8 * 27 + 4 * (k & 1), false>(acc[k], b[27 % WN][k >> 1]);
    if constexpr (t > 1) { red_put1<t - 1, 2>(x, acc[t - 1]); red_put1<t - 1, 3>(x, acc[t - 1]); }
  });
  if (L == 0 && (x.m & 7) < 2) {
    ldl<4>(x.vl16, x.rs, OFF_WQ + ((16 + (x.m & 1)) * 28 + 7 * w) * KB, 7 * w * KB);
    ldl<3>(x.vl16, x.rs, OFF_WQ + ((16 + (x.m & 1)) * 28 + 7 * w + 4) * KB, (7 * w + 4) * KB);
  }
  nop<9>();
  pin(acc[0]);
  stamp(x);
  reduce_get(x, acc[0]);
  wv<0>();
  pin(xr[0]); pin(xr[1]);
  if (L > 0) {
    res_epilogue<true>(x, acc[0], xr, 0, 0, tt, nt, out);
  } else {
    res_epilogue<false>(x, acc[0], xr, OFF_X + 2 * SZ_X, OFF_ST + 2 * SZ_ST, tt, nt);
    wv<0>();
    signal(x, FL_X(x.c, th), 1);
  }
}

#define STAMP_SYNC
__global__ __launch_bounds__(256, 1) void k_main(Args) {
  typedef unsigned long long u64x8 __attribute__((ext_vector_type(8)));
  typedef unsigned long long u64x2 __attribute__((ext_vector_type(2)));
  u64x8 pin8; u64x2 pws;
  asm volatile("s_load_dwordx16 s[8:23], %4, 0x0\n\ts_load_dwordx4 s[24:27], %4, 0x40\n\t"
               "s_getpc_b64 s[4:5]\n.Lk_begin:\n\t"
               "s_mov_b32 s6, .Lk_end-.Lk_begin\n\ts_mov_b32 s7, 0x20000\n\ts_and_b32 s5, s5, 0xffff\n\t"
               "v_lshlrev_b32 v1, 4, %2\n\t"
               "s_bfe_u32 s28, %3, 0x20003\n\ts_lshl_b32 s28, s28, 12\n\t"
               "buffer_load_dwordx4 a[112:115], v1, s[4:7], s28 offen\n\t"
               "s_bfe_u32 s28, %3, 0x30004\n\ts_lshl_b32 s28, s28, 13\n\t"
               "buffer_load_dwordx4 a[112:115], v1, s[4:7], s28 offen\n\t"
               "s_add_u32 s28, s28, 4096\n\t"
               "buffer_load_dwordx4 a[112:115], v1, s[4:7], s28 offen\n\t"
               "s_waitcnt lgkmcnt(0)"
               : "={s[8:23]}"(pin8), "={s[24:27]}"(pws) : "v"(threadIdx.x), "{s2}"(blockIdx.x), "{s[0:1]}"(__builtin_amdgcn_kernarg_segment_ptr())
               : "s4", "s5", "s6", "s7", "s28", "v1", "scc", ACLOB);
  const Args a = {(const char*)pin8[0], (const char*)pin8[1], (const char*)pin8[2], (const char*)pin8[3], (const char*)pin8[4], (const char*)pin8[5],
                  (const char*)pin8[6], (const char*)pin8[7], (char*)pws[0], (char*)pws[1]};
  Ctx x;
  x.rs = mkrs(a.ws, WS_USED);
  x.w = __builtin_amdgcn_readfirstlane(threadIdx.x >> 6);
  asm volatile("s_nop 4" : "+s"(x.w));
  { const int b = blockIdx.x; x.c = 2 * (b & 7) + (b >> 7); x.m = (b >> 3) & 15; }
  f32x16 kf[3];
  { Ctx y = x; fresh(y); stamp(y); phase_pre(y, a, kf); STAMP_SYNC }
  for (int L = 0;; ++L) {
    f32x16 g2[3];
    int Lo = L; asm volatile("" : "+s"(Lo));
    { Ctx y = x; fresh(y); phase_att(y, Lo, kf, g2); stamp(y); STAMP_SYNC }
    { Ctx y = x; fresh(y); phase_oproj(y, Lo, a, g2); stamp(y); STAMP_SYNC }
    { Ctx y = x; u32x4 b[DWN][2]; fresh(y); phase_gu(y, Lo, b); stamp(y); fresh(y); phase_down(y, Lo, a.out, b); stamp(y); STAMP_SYNC }
    if (L == 1) break;
    { Ctx y = x; fresh(y); phase_front(y, kf); STAMP_SYNC }
  }
  fresh(x);
  if (blockIdx.x == 0 && wave0(x)) {
    get_tag(x);
    if (x.l == 0) st2<0>(u32x2{x.tlo, x.thi}, 0, x.rs, OFF_TAG);
  }
  stamp_flush(x);
  asm volatile(".Lk_end:");
}

extern "C" void kernel_launch(void* const* d_in, const int* in_sizes, int n_in, void* d_out, int out_size, void* d_ws, size_t ws_size,
                              hipStream_t stream) {
  Args a;
  a.x = (const char*)d_in[0]; a.wq = (const char*)d_in[1]; a.wk = (const char*)d_in[2]; a.wv = (const char*)d_in[3];
  a.wo = (const char*)d_in[4]; a.wg = (const char*)d_in[6]; a.wu = (const char*)d_in[7]; a.wd = (const char*)d_in[8];
  a.ws = (char*)d_ws; a.out = (char*)d_out;
  (void)hipFuncSetAttribute((const void*)k_main, hipFuncAttributeMaxDynamicSharedMemorySize, 160 * 1024);
  k_main<<<256, 256, 160 * 1024, stream>>>(a);
}
```

```cpp
#include <hip/hip_runtime.h>
#include <stdint.h>
#include <type_traits>

typedef float    f32x16 __attribute__((ext_vector_type(16)));
typedef float    f32x4  __attribute__((ext_vector_type(4)));
typedef float    f32x2  __attribute__((ext_vector_type(2)));
typedef unsigned u32x4  __attribute__((ext_vector_type(4)));
typedef unsigned u32x2  __attribute__((ext_vector_type(2)));
typedef __bf16   bf16x2 __attribute__((ext_vector_type(2)));
typedef __amdgpu_buffer_rsrc_t rsrc_t;
#define DI __device__ __forceinline__
#define SB() __builtin_amdgcn_sched_barrier(0)

constexpr int S = 2048, D = 448, F = 1792;
constexpr unsigned KB = 1024;
constexpr unsigned OFF_FLAG = 0;
constexpr unsigned OFF_CSQ  = 64 * KB;
constexpr unsigned OFF_CSGU = OFF_CSQ + 4 * KB;
constexpr unsigned OFF_SLOT = OFF_CSGU + 32 * KB;
constexpr unsigned OFF_TAG  = OFF_SLOT + 32 * KB;
constexpr unsigned OFF_WQ   = 160 * KB;
constexpr unsigned OFF_WO   = OFF_WQ + 18 * 28 * KB;
constexpr unsigned OFF_WGU  = OFF_WO + 14 * 32 * KB;
constexpr unsigned OFF_WD   = OFF_WGU + 16 * 196 * KB;
constexpr unsigned SZ_X = 64 * 28 * KB, SZ_ST = 14 * 2048 * 8, SZ_O = 64 * 32 * KB, SZ_H = 64 * 112 * KB, SZ_KV = 64 * 4 * KB;
constexpr unsigned OFF_X    = OFF_WD + 14 * 112 * KB;
constexpr unsigned OFF_ST   = OFF_X + 4 * SZ_X;
constexpr unsigned OFF_O    = OFF_ST + 4 * SZ_ST;
constexpr unsigned OFF_H    = OFF_O + 2 * SZ_O;
constexpr unsigned OFF_HL   = OFF_H + 2 * SZ_H;
constexpr unsigned OFF_KV   = OFF_HL + 2 * 1024 * KB;
constexpr unsigned OFF_TS   = OFF_KV + 2 * SZ_KV;
constexpr unsigned WS_USED  = OFF_TS + 520 * KB;
DI constexpr unsigned FL_X(int c, int th)         { return (c * 16 + th) * 256u; }
DI constexpr unsigned FL_O(int L, int c, int th)  { return (c * 16 + 2 + L * 2 + th) * 256u; }
DI constexpr unsigned FL_X1(int L, int c)         { return (c * 16 + 6 + L) * 256u; }
DI constexpr unsigned FL_H(int L, int c, int p)   { return (c * 16 + 8 + L * 2 + p) * 256u; }
DI constexpr unsigned FL_W(int c)                 { return (c * 16 + 12) * 256u; }
constexpr unsigned FL_W2 = 13 * 256u;

constexpr float QSCALE = 0.18898223650461362f * 1.4426950408889634f;
constexpr float M_LIMIT = 48.0f;

template <int I, int N, class Fn> DI void sfor(Fn&& f) { if constexpr (I < N) { f(std::integral_constant<int, I>{}); sfor<I + 1, N>(f); } }

DI unsigned cvtpk(float lo, float hi) { f32x2 v = {lo, hi}; bf16x2 b = __builtin_convertvector(v, bf16x2); return __builtin_bit_cast(unsigned, b); }
DI float bflo(unsigned u) { return __uint_as_float(u << 16); }
DI float bfhi(unsigned u) { return __uint_as_float(u & 0xffff0000u); }
DI float dot2(unsigned a, unsigned b, float c) { return __builtin_amdgcn_fdot2_f32_bf16(__builtin_bit_cast(bf16x2, a), __builtin_bit_cast(bf16x2, b), c, false); }
DI float sum32(float v) { auto r = __builtin_amdgcn_permlane32_swap(__float_as_uint(v), __float_as_uint(v), false, false); return __uint_as_float(r[0]) + __uint_as_float(r[1]); }
DI float sum16(float v) { auto r = __builtin_amdgcn_permlane16_swap(__float_as_uint(v), __float_as_uint(v), false, false); return __uint_as_float(r[0]) + __uint_as_float(r[1]); }
DI float max32(float v) { auto r = __builtin_amdgcn_permlane32_swap(__float_as_uint(v), __float_as_uint(v), false, false); return fmaxf(__uint_as_float(r[0]), __uint_as_float(r[1])); }

DI unsigned umax16(unsigned v) {
  v = max(v, (unsigned)__builtin_amdgcn_update_dpp(0, (int)v, 0x128, 0xf, 0xf, false));
  v = max(v, (unsigned)__builtin_amdgcn_update_dpp(0, (int)v, 0x124, 0xf, 0xf, false));
  v = max(v, (unsigned)__builtin_amdgcn_update_dpp(0, (int)v, 0x122, 0xf, 0xf, false));
  v = max(v, (unsigned)__builtin_amdgcn_update_dpp(0, (int)v, 0x121, 0xf, 0xf, false));
  return v;
}
DI unsigned umax64(unsigned v) {
  v = umax16(v);
  auto a = __builtin_amdgcn_permlane16_swap(v, v, false, false); v = max(a[0], a[1]);
  auto b = __builtin_amdgcn_permlane32_swap(v, v, false, false); return max(b[0], b[1]);
}

typedef short s16x4 __attribute__((ext_vector_type(4)));
DI u32x2 trread(const char* p) { return __builtin_bit_cast(u32x2, __builtin_amdgcn_ds_read_tr16_b64_v4i16((__attribute__((address_space(3))) s16x4*)p)); }
DI u32x4 trfrag(const char* lo, const char* hi) { const u32x2 a = trread(lo), b = trread(hi); return u32x4{a[0], a[1], b[0], b[1]}; }
DI void row_stats(float s, float q, float& mean, float& rstd) {
  mean = s * (1.0f / D);
  const float var = fmaxf(q * (1.0f / D) - mean * mean, 0.f);
  rstd = __builtin_amdgcn_rsqf(var + 1e-5f);
}
DI float fragsum(const u32x4& f, float s) {
#pragma unroll
  for (int d = 0; d < 4; ++d) s = dot2(f[d], 0x3f803f80u, s);
  return s;
}
struct Args { const char *x, *wq, *wk, *wv, *wo, *wg, *wu, *wd; char* ws; char* out; };

#define ACLOB "a0","a1","a2","a3","a4","a5","a6","a7","a8","a9","a10","a11","a12","a13","a14","a15","a16","a17","a18","a19","a20","a21","a22","a23","a24","a25","a26","a27","a28","a29","a30","a31","a32","a33","a34","a35","a36","a37","a38","a39","a40","a41","a42","a43","a44","a45","a46","a47","a48","a49","a50","a51","a52","a53","a54","a55","a56","a57","a58","a59","a60","a61","a62","a63","a64","a65","a66","a67","a68","a69","a70","a71","a72","a73","a74","a75","a76","a77","a78","a79","a80","a81","a82","a83","a84","a85","a86","a87","a88","a89","a90","a91","a92","a93","a94","a95","a96","a97","a98","a99","a100","a101","a102","a103","a104","a105","a106","a107","a108","a109","a110","a111","a112","a113","a114","a115","a116","a117","a118","a119","a120","a121","a122","a123","a124","a125","a126","a127","a128","a129","a130","a131","a132","a133","a134","a135","a136","a137","a138","a139","a140","a141","a142","a143","a144","a145","a146","a147","a148","a149","a150","a151","a152","a153","a154","a155","a156","a157","a158","a159","a160","a161","a162","a163","a164","a165","a166","a167","a168","a169","a170","a171","a172","a173","a174","a175","a176","a177","a178","a179","a180","a181","a182","a183","a184","a185","a186","a187","a188","a189","a190","a191","a192","a193","a194","a195","a196","a197","a198","a199","a200","a201","a202","a203","a204","a205","a206","a207","a208","a209","a210","a211","a212","a213","a214","a215","a216","a217","a218","a219","a220","a221","a222","a223","a224","a225","a226","a227","a228","a229","a230","a231","a232","a233","a234","a235","a236","a237","a238","a239","a240","a241","a242","a243","a244","a245","a246","a247","a248","a249","a250","a251","a252","a253","a254","a255"

template <int IMM> DI void ld4(u32x4& d, unsigned vo, rsrc_t rs, unsigned so) {
  asm volatile("buffer_load_dwordx4 %0, %1, %2, %3 offen offset:%c4 sc1" : "=v"(d) : "v"(vo), "s"(rs), "s"(so), "i"(IMM));
}
template <int IMM> DI void ld2(f32x2& d, unsigned vo, rsrc_t rs, unsigned so) {
  asm volatile("buffer_load_dwordx2 %0, %1, %2, %3 offen offset:%c4 sc1" : "=v"(d) : "v"(vo), "s"(rs), "s"(so), "i"(IMM));
}
template <int IMM> DI void ld2i(u32x2& d, unsigned vo, rsrc_t rs, unsigned so) {
  asm volatile("buffer_load_dwordx2 %0, %1, %2, %3 offen offset:%c4" : "=v"(d) : "v"(vo), "s"(rs), "s"(so), "i"(IMM));
}
template <int IMM> DI void ld2u(u32x2& d, unsigned vo, rsrc_t rs, unsigned so) {
  asm volatile("buffer_load_dwordx2 %0, %1, %2, %3 offen offset:%c4 sc1" : "=v"(d) : "v"(vo), "s"(rs), "s"(so), "i"(IMM));
}
template <int IMM> DI void ld4f(f32x4& d, unsigned vo, rsrc_t rs, unsigned so) {
  asm volatile("buffer_load_dwordx4 %0, %1, %2, %3 offen offset:%c4 sc1" : "=v"(d) : "v"(vo), "s"(rs), "s"(so), "i"(IMM));
}
DI void ld1(float& d, unsigned vo, rsrc_t rs, unsigned so) {
  asm volatile("buffer_load_dword %0, %1, %2, %3 offen sc1" : "=v"(d) : "v"(vo), "s"(rs), "s"(so));
}
template <int AR, int IMM> DI void lda(unsigned vo, rsrc_t rs, unsigned so) {
  asm volatile("buffer_load_dwordx4 a[%c3:%c4], %0, %1, %2 offen offset:%c5 sc1" :: "v"(vo), "s"(rs), "s"(so), "i"(AR), "i"(AR + 3), "i"(IMM) : ACLOB);
}
template <int N> DI void ldl(unsigned vo, rsrc_t rs, unsigned so, unsigned lds) {
  asm volatile("s_mov_b32 m0, %3\n\ts_nop 0\n\tbuffer_load_dwordx4 %0, %1, %2 offen sc1 lds" :: "v"(vo), "s"(rs), "s"(so), "s"(lds) : "memory");
  if constexpr (N > 1) asm volatile("buffer_load_dwordx4 %0, %1, %2 offen offset:1024 sc1 lds" :: "v"(vo), "s"(rs), "s"(so) : "memory");
  if constexpr (N > 2) asm volatile("buffer_load_dwordx4 %0, %1, %2 offen offset:2048 sc1 lds" :: "v"(vo), "s"(rs), "s"(so) : "memory");
  if constexpr (N > 3) asm volatile("buffer_load_dwordx4 %0, %1, %2 offen offset:3072 sc1 lds" :: "v"(vo), "s"(rs), "s"(so) : "memory");
}
template <int IMM> DI void st4(const u32x4& d, unsigned vo, rsrc_t rs, unsigned so) {
  asm volatile("buffer_store_dwordx4 %0, %1, %2, %3 offen offset:%c4 sc1\n\ts_nop 1" :: "v"(d), "v"(vo), "s"(rs), "s"(so), "i"(IMM) : "memory");
}
template <int IMM> DI void st4p(const u32x4& d, unsigned vo, rsrc_t rs, unsigned so) {
  asm volatile("buffer_store_dwordx4 %0, %1, %2, %3 offen offset:%c4\n\ts_nop 1" :: "v"(d), "v"(vo), "s"(rs), "s"(so), "i"(IMM) : "memory");
}
template <int IMM> DI void st2(const u32x2& d, unsigned vo, rsrc_t rs, unsigned so) {
  asm volatile("buffer_store_dwordx2 %0, %1, %2, %3 offen offset:%c4 sc1" :: "v"(d), "v"(vo), "s"(rs), "s"(so), "i"(IMM) : "memory");
}
DI void st1(float d, unsigned vo, rsrc_t rs, unsigned so) {
  asm volatile("buffer_store_dword %0, %1, %2, %3 offen sc1" :: "v"(d), "v"(vo), "s"(rs), "s"(so) : "memory");
}
template <int N> DI void wv() { asm volatile("s_waitcnt vmcnt(%c0)" :: "i"(N) : "memory"); SB(); }
template <int N> DI void wl() { asm volatile("s_waitcnt lgkmcnt(%c0)" :: "i"(N) : "memory"); SB(); }
template <class T> DI void pin(T& x) { asm volatile("" : "+v"(x)); }
DI void bar() { asm volatile("s_waitcnt lgkmcnt(0)\n\ts_barrier" ::: "memory"); }
template <int N> DI void nop() { asm volatile("s_nop %c0" :: "i"(N)); SB(); }

template <int OFF> DI void trd(u32x2& r, unsigned a) { asm volatile("ds_read_b64_tr_b16 %0, %1 offset:%c2" : "=v"(r) : "v"(a), "i"(OFF)); }
template <int OFF> DI void dsr(u32x4& r, unsigned a) { asm volatile("ds_read_b128 %0, %1 offset:%c2" : "=v"(r) : "v"(a), "i"(OFF)); }

DI void m32(f32x16& c, const u32x4& a, const u32x4& b)  { asm volatile("v_mfma_f32_32x32x16_bf16 %0, %1, %2, %0" : "+v"(c) : "v"(a), "v"(b)); }
DI void m32z(f32x16& c, const u32x4& a, const u32x4& b) { asm volatile("v_mfma_f32_32x32x16_bf16 %0, %1, %2, 0" : "=&v"(c) : "v"(a), "v"(b)); }
template <int AR, bool Z> DI void m32a(f32x16& c, const u32x4& b) {
  if constexpr (Z) asm volatile("v_mfma_f32_32x32x16_bf16 %0, a[%c2:%c3], %1, 0" : "=&v"(c) : "v"(b), "i"(AR), "i"(AR + 3));
  else             asm volatile("v_mfma_f32_32x32x16_bf16 %0, a[%c2:%c3], %1, %0" : "+v"(c) : "v"(b), "i"(AR), "i"(AR + 3));
}
template <bool Z> DI void m16(f32x4& c, const u32x4& a, const u32x4& b) {
  if constexpr (Z) asm volatile("v_mfma_f32_16x16x32_bf16 %0, %1, %2, 0" : "=&v"(c) : "v"(a), "v"(b));
  else             asm volatile("v_mfma_f32_16x16x32_bf16 %0, %1, %2, %0" : "+v"(c) : "v"(a), "v"(b));
}

template <bool Z, int AR> DI void m16a(f32x4& c, const u32x4& a) {
  if constexpr (Z) asm volatile("v_mfma_f32_16x16x32_bf16 %0, %1, a[%c2:%c3], 0" : "=&v"(c) : "v"(a), "i"(AR), "i"(AR + 3));
  else             asm volatile("v_mfma_f32_16x16x32_bf16 %0, %1, a[%c2:%c3], %0" : "+v"(c) : "v"(a), "i"(AR), "i"(AR + 3));
}
DI void ldflag(unsigned& v, rsrc_t rs, unsigned so) { asm volatile("buffer_load_dword %0, off, %1, %2 sc1" : "=v"(v) : "s"(rs), "s"(so)); }

struct Ctx {
  rsrc_t rs;
  unsigned tlo, thi;
  unsigned vl16;
  unsigned vg16;
  int w, l, c, m;
};
DI void fresh(Ctx& x) {
  asm volatile("" : "+s"(x.w), "+s"(x.c), "+s"(x.m));
  asm volatile("v_mbcnt_lo_u32_b32 %0, -1, 0\n\tv_mbcnt_hi_u32_b32 %0, -1, %0" : "=v"(x.l));
  x.vl16 = x.l * 16;
  const int g = x.l >> 4;
  x.vg16 = ((g >> 1) * 64 + (g & 1) * 32 + (x.l & 15)) * 16;
}
extern __shared__ __attribute__((aligned(16))) char lds[];
constexpr unsigned SCR = 112 * KB;

DI void pstamp(Ctx&) {}
DI void fstamp(Ctx&) {}
DI void stamp(Ctx&) {}
DI void stamp_flush(Ctx&) {}

DI void get_tag(Ctx& x) {
  unsigned lo, hi;
  asm volatile("v_accvgpr_read_b32 v255, a252\n\ts_nop 1\n\tv_readfirstlane_b32 %0, v255\n\tv_accvgpr_read_b32 v255, a253\n\ts_nop 1\n\tv_readfirstlane_b32 %1, v255\n\ts_nop 4"
               : "=s"(lo), "=s"(hi) :: "v255");
  const unsigned long long e = ((unsigned long long)hi << 32) | lo;
  unsigned long long n = e * 0x9E3779B97F4A7C15ull + 0x632BE59BD9B4E019ull;
  if (((n ^ e) >> 16) == 0) n += 0x10000;
  x.tlo = (unsigned)n; x.thi = (unsigned)(n >> 32);
}
DI bool wave0(const Ctx& x) { int w = x.w; asm volatile("" : "+s"(w)); return w == 0; }
DI void count(Ctx& x, unsigned fl, int n) {
  if (wave0(x)) {
    const unsigned long long mask = (1ull << n) - 1;
    unsigned long long keep;
    asm volatile("s_mov_b64 %0, exec\n\ts_mov_b64 exec, %5\n\tbuffer_atomic_add %1, %2, %3, %4 offen\n\ts_mov_b64 exec, %0"
                 : "=&s"(keep) : "v"(1u), "v"(x.l * 256), "s"(x.rs), "s"(OFF_FLAG + fl), "s"(mask) : "memory");
  }
}
DI void signal(Ctx& x, unsigned fl, int n) { bar(); count(x, fl, n); }
DI void await(Ctx& x, unsigned fl, unsigned target) {
  if (wave0(x)) {
#pragma nounroll
    for (unsigned spin = 0; spin < (1u << 18); ++spin) {
      unsigned v;
      asm volatile("buffer_load_dword %0, off, %1, %2 sc1\n\ts_waitcnt vmcnt(0)" : "=v"(v) : "s"(x.rs), "s"(OFF_FLAG + fl) : "memory");
      if ((unsigned)__builtin_amdgcn_readfirstlane(v) >= target) break;
      __builtin_amdgcn_s_sleep(1);
    }
  }
  bar();
}

DI void recheck(Ctx& x, unsigned v, unsigned fl, unsigned target) {
  pin(v);
#pragma nounroll
  for (unsigned spin = 0; (unsigned)__builtin_amdgcn_readfirstlane(v) < target && spin < (1u << 18); ++spin) {
    __builtin_amdgcn_s_sleep(2);
    asm volatile("buffer_load_dword %0, off, %1, %2 sc1\n\ts_waitcnt vmcnt(0)" : "=v"(v) : "s"(x.rs), "s"(OFF_FLAG + fl) : "memory");
  }
  SB();
}

DI void reduce_put(Ctx& x, const f32x16 (&acc)[4]) {
  sfor<1, 4>([&](auto k) {
    f32x4* p = (f32x4*)(lds + SCR + ((x.w ^ k) * 3 + k - 1) * 4096 + x.vl16);
#pragma unroll
    for (int q = 0; q < 4; ++q) p[q * 64] = f32x4{acc[k][4 * q], acc[k][4 * q + 1], acc[k][4 * q + 2], acc[k][4 * q + 3]};
  });
}
template <int K, int Q> DI void red_put1(Ctx& x, const f32x16& t) {
  asm volatile("ds_write_b128 %0, %1 offset:%c2" :: "v"(SCR + ((x.w ^ K) * 3 + K - 1) * 4096 + x.vl16),
               "v"(__builtin_shufflevector(t, t, 4 * Q, 4 * Q + 1, 4 * Q + 2, 4 * Q + 3)), "i"(Q * 1024) : "memory");
}
DI void reduce_get(Ctx& x, f32x16& own) {
  bar();
  const f32x4* p = (const f32x4*)(lds + SCR + x.w * 3 * 4096 + x.vl16);
#pragma unroll
  for (int k = 0; k < 3; ++k)
#pragma unroll
    for (int q = 0; q < 4; ++q) {
      f32x4 v = p[k * 256 + q * 64];
#pragma unroll
      for (int i = 0; i < 4; ++i) own[4 * q + i] += v[i];
    }
}

constexpr int A_WO = 184, A_WD = 0, A_GX = 224;
constexpr int dn_ks(int o) { return o < 20 ? 2 * ((o >> 1) % 5 + ((o >> 1) % 5 >= 2) + 7 * (o / 10)) + (o & 1) : 2 * (((o - 20) >> 1) * 4 - ((o - 20) >> 2) + 2) + (o & 1); }
static_assert(dn_ks(0) == 0 && dn_ks(4) == 6 && dn_ks(9) == 11 && dn_ks(10) == 14 && dn_ks(19) == 25 && dn_ks(20) == 4 && dn_ks(22) == 12 && dn_ks(24) == 18 && dn_ks(27) == 27, "");
struct Two { unsigned s0, s1; };
constexpr int SL_WQ = 56;
template <int J> DI void pf_wq(Ctx& x) {
  ldl<1>(x.vl16, x.rs, OFF_WQ + ((x.m & 7) * 56 + 14 * x.w + J) * KB, (SL_WQ + 14 * x.w + J) * KB);
}
DI Two base_wo(const Ctx& x) { const int ct = x.m % 7;
  return Two{OFF_WO + ((2 * ct + (x.w & 1)) * 32 + 8 * x.w) * KB, OFF_WO + ((2 * ct + ((x.w & 1) ^ 1)) * 32 + 8 * x.w) * KB}; }
DI void pf_wo(Ctx& x) {
  const Two b = base_wo(x);
  sfor<0, 8>([&](auto i) {
    lda<A_WO + 8 * i, (i & 3) * 1024>(x.vl16, x.rs, b.s0 + (i >> 2) * 4096);
    lda<A_WO + 8 * i + 4, (i & 3) * 1024>(x.vl16, x.rs, b.s1 + (i >> 2) * 4096);
  });
}
DI Two base_wd(const Ctx& x) { const int ct = x.m % 7;
  return Two{OFF_WD + ((2 * ct + (x.w & 1)) * 112 + 28 * x.w) * KB, OFF_WD + ((2 * ct + ((x.w & 1) ^ 1)) * 112 + 28 * x.w) * KB}; }
template <int I0, int I1> DI void pf_wd(Ctx& x, const Two& b) {
  sfor<I0, I1>([&](auto f) {
    constexpr int i = f >> 1;
    lda<A_WD + 4 * f, (i & 3) * 1024>(x.vl16, x.rs, ((f & 1) ? b.s1 : b.s0) + (i >> 2) * 4096);
  });
}
DI void pf_wgu1(Ctx& x) {
  for (int t = 0; t < 14; ++t) ldl<2>(x.vl16, x.rs, OFF_WGU + (x.m * 196 + 8 * t + 2 * x.w) * KB, (8 * t + 2 * x.w) * KB);
}
template <int J> DI void pf_wgu3(Ctx& x) {
  ldl<1>(x.vl16, x.rs, OFF_WGU + (x.m * 196 + 160 + 9 * x.w + J) * KB, (9 * x.w + J) * KB);
}

constexpr unsigned S_QP = SCR, S_KP = SCR + 16384, S_QF = SCR + 24576, S_ST = SCR + 37888, S_CS = SCR + 38912, S_KM = SCR + 36864, S_TR = SCR + 40960;
DI unsigned kv_finish(Ctx& x, const float (&raw)[16], const f32x4 (&cs)[4], float mean, float rstd, bool isv, unsigned so, int slot) {
  const int h = x.l >> 5, r = x.l & 31;
  u32x4 y[2];
#pragma unroll
  for (int d = 0; d < 8; ++d)
    y[d >> 2][d & 3] = cvtpk((raw[2 * d] - mean * cs[d >> 1][2 * (d & 1)]) * rstd, (raw[2 * d + 1] - mean * cs[d >> 1][2 * (d & 1) + 1]) * rstd);
  if (h) { y[1][2] = 0; y[1][3] = 0; }
  unsigned nb = 0;
  if (!isv) {
    u32x4 y1 = y[1];
    if (h) y1[2] = 0x3f80u;
    st4<0>(y[0], x.vl16, x.rs, so); st4<1024>(y1, x.vl16, x.rs, so);
    float n2 = 0.f, n3 = 0.f;
#pragma unroll
    for (int d = 0; d < 4; ++d) { n2 = dot2(y[0][d], y[0][d], n2); n3 = dot2(y[1][d], y[1][d], n3); }
    n2 = sum32(n2 + n3);
    nb = umax64(n2 < 3.0e38f ? __float_as_uint(n2) : 0x7f800000u);
    nb = cvtpk(1.01f * __builtin_amdgcn_sqrtf(__uint_as_float(nb)), 0.f);
  } else {
    if (h) y[1][2] = 0x3f80u;
    char* im = lds + S_TR + slot * 2048;
#pragma unroll
    for (int a = 0; a < 4; ++a) *(u32x2*)(im + r * 64 + 16 * a + 8 * h) = u32x2{y[a >> 1][2 * (a & 1)], y[a >> 1][2 * (a & 1) + 1]};
    asm volatile("s_waitcnt lgkmcnt(0)" ::: "memory");
    const char* tb = im + (4 * h + ((x.l >> 2) & 3)) * 64 + ((x.l >> 4) & 1) * 32 + (x.l & 3) * 8;
    const u32x4 v0 = trfrag(tb, tb + 512), v1 = trfrag(tb + 1024, tb + 1536);
    st4<0>(v0, x.vl16, x.rs, so); st4<1024>(v1, x.vl16, x.rs, so);
  }
  return nb;
}
DI void kv_publish(Ctx& x, int L, int p, unsigned km) {
  get_tag(x);
  st2<0>(u32x2{(x.tlo & 0xffff0000u) | km, x.thi}, x.l < 16 ? x.l * 1024 : ~0u, x.rs, OFF_SLOT + (L * 2048 + p) * 8);
}
DI void q_finish(Ctx& x, const f32x16& qa, const f32x4 (&cq)[4], float mean, float rstd) {
  const int h = x.l >> 5;
  u32x4 qf[2];
  const f32x4* p = (const f32x4*)(lds + S_QP + x.w * 4096 + x.vl16);
  const float sc = rstd * QSCALE;
#pragma unroll
  for (int q = 0; q < 4; ++q) {
    const f32x4 o = p[q * 64];
    float v[4];
#pragma unroll
    for (int i = 0; i < 4; ++i) v[i] = (qa[4 * q + i] + o[i] - mean * cq[q][i]) * sc;
    qf[q >> 1][2 * (q & 1)] = cvtpk(v[0], v[1]); qf[q >> 1][2 * (q & 1) + 1] = cvtpk(v[2], v[3]);
  }
  if (h) { qf[1][2] = 0; qf[1][3] = 0; }
  float qn2 = 0.f, qn3 = 0.f;
#pragma unroll
  for (int d = 0; d < 4; ++d) { qn2 = dot2(qf[0][d], qf[0][d], qn2); qn3 = dot2(qf[1][d], qf[1][d], qn3); }
  qn2 = sum32(qn2 + qn3);
  if (h) qf[1][2] = cvtpk(1.01f * __builtin_amdgcn_sqrtf(qn2), 0.f);
  ((u32x4*)(lds + S_QF + x.w * 2048 + x.vl16))[0] = qf[0];
  ((u32x4*)(lds + S_QF + x.w * 2048 + x.vl16))[64] = qf[1];
}
DI void put_tile(char* p, const f32x16& t) {
#pragma unroll
  for (int q = 0; q < 4; ++q) ((f32x4*)p)[q * 64] = f32x4{t[4 * q], t[4 * q + 1], t[4 * q + 2], t[4 * q + 3]};
}

constexpr unsigned IMG_Q = 0, IMG_T = 56 * KB;
DI rsrc_t mkrs(const char* p, unsigned n) { return __builtin_amdgcn_make_buffer_rsrc((char*)p, 0, n, 0x00020000); }

DI void ask_slots(Ctx& x, int L) {
  if (wave0(x)) asm volatile("buffer_load_dwordx4 a[248:251], %0, %1, %2 offen sc1" :: "v"(x.vl16), "s"(x.rs), "s"(OFF_SLOT + (L * 16 + x.c) * 1024) : ACLOB);
}
DI void kv_wait(Ctx& x, int L) {
  if (wave0(x)) get_tag(x);
  wv<0>();
  if (wave0(x)) {
    unsigned s0, s1, s2, s3;
#pragma nounroll
    for (unsigned spin = 0; spin < (1u << 18); ++spin) {
      asm volatile("v_accvgpr_read_b32 %0, a248\n\tv_accvgpr_read_b32 %1, a249\n\tv_accvgpr_read_b32 %2, a250\n\tv_accvgpr_read_b32 %3, a251" : "=v"(s0), "=v"(s1), "=v"(s2), "=v"(s3));
      if (__all(s1 == x.thi && s3 == x.thi && (((s0 ^ x.tlo) | (s2 ^ x.tlo)) >> 16) == 0)) break;
      __builtin_amdgcn_s_sleep(2);
      ask_slots(x, L); wv<0>();
    }
    const unsigned km = umax64(max(s0 << 16, s2 << 16));
    if (x.l == 0) ((unsigned*)(lds + S_KM))[4] = km;
  }
  bar();
}
DI void ask_kv(Ctx& x, int L, f32x16 (&kf)[3]) {
  const unsigned kvs = OFF_KV + L * SZ_KV + 16 * x.w * 4096;
  asm volatile(".irp i,0,1,2,3\n\tbuffer_load_dwordx4 v[128+4*\\i:131+4*\\i], %3, %4, %5 offen offset:1024*\\i sc1\n\t.endr\n\t"
               ".irp i,0,1,2,3\n\tbuffer_load_dwordx4 v[144+4*\\i:147+4*\\i], %3, %4, %6 offen offset:1024*\\i sc1\n\t.endr\n\t"
               ".irp i,0,1,2,3\n\tbuffer_load_dwordx4 v[160+4*\\i:163+4*\\i], %3, %4, %7 offen offset:1024*\\i sc1\n\t.endr"
               : "={v[128:143]}"(kf[0]), "={v[144:159]}"(kf[1]), "={v[160:175]}"(kf[2]) : "{v204}"(x.vl16), "s"(x.rs), "s"(kvs), "s"(kvs + 4096), "s"(kvs + 8192));
}

DI void phase_pre(Ctx& x, const Args& a, f32x16 (&kf)[3]) {
  const int w = x.w, l = x.l, h = l >> 5, r = l & 31;
  const int th = x.m >> 3, hp = x.m & 7, tl = w & 1, kh = w >> 1, tt = 4 * x.c + 2 * th + tl, wi = 2 * x.c + th;
  const bool kvp = hp < 2;
  u32x4 xf[14];
  { const rsrc_t rx = mkrs(a.x, S * D * 2);
    sfor<0, 16>([&](auto i) {
      const int c = r ^ ((2 * i + h) & 15);
      ldl<1>(c < 28 ? h * 896 + c * 16 : 0x80000000u, rx, (tt * 32 + 2 * i) * 896 + kh * 448, (84 + 16 * w + i) * KB);
    }); }
  if (x.m == 2) asm volatile("buffer_load_dword a116, %0, %1, 0 offen sc1" :: "v"(l < 16 ? OFF_FLAG + (x.c * 16 + l) * 256 : OFF_SLOT + ((l >> 3) & 1) * 16384 + x.c * 1024 + (l & 7) * 128), "s"(x.rs) : ACLOB);
  asm volatile("buffer_load_dwordx2 a[252:253], off, %0, %1 sc1" :: "s"(x.rs), "s"(OFF_TAG) : ACLOB);
  pstamp(x);
  const int task = wi * 6 + hp - 2;
  const rsrc_t rq = mkrs(a.wq, D * D * 2);
  auto ask_q = [&](auto i) {
    ldl<1>((8 * w + (l >> 3)) * 896 + (l & 7) * 16, rq, hp * 112 + i * 32 * 896, IMG_Q + (4 * i + w) * KB);
  };
  auto ask_t = [&](const char* tp, unsigned tn, unsigned ts, unsigned tb) {
    const rsrc_t rt = mkrs(tp, tn);
    const unsigned vo = (8 * w + (l >> 3)) * ts + (l & 7) * 16;
    sfor<0, 7>([&](auto i) { ldl<1>(vo, rt, tb + i * 32 * ts, IMG_T + (4 * i + w) * KB); });
  };
  const int tco = ((l >> 4) & 1) * 32 + (l & 3) * 8;
  const int sro = 4 * h + ((l >> 2) & 3);
  if (kvp) ask_t(hp ? a.wv : a.wk, D * 56, 128, 0);
  else if (task < 112) ask_t(task < 56 ? a.wg : a.wu, D * F * 2, F * 2, (task & 1) * 224 * F * 2 + ((task % 56) >> 1) * 128);
  else if (task < 168) ask_t(a.wd, D * F * 2, 896, ((task - 112) / 7) * 224 * 896 + ((task - 112) % 7) * 128);
  else ask_t(a.wo, task < 182 ? D * D * 2 : 0, 896, ((task - 168) / 7) * 224 * 896 + ((task - 168) % 7) * 128);
  wv<8>();
  sfor<0, 14>([&](auto i) { dsr<0>(xf[i], (84 + w * 16) * KB + r * 512 + (((2 * i + h) ^ (r & 15)) << 4)); });
  wl<0>();
  {
    float s = 0.f, q = 0.f, s1 = 0.f, q1 = 0.f;
    sfor<0, 14>([&](auto i) {
      ask_q(i);
      pin(xf[i]);
      s = dot2(xf[i][0], 0x3f803f80u, s); q = dot2(xf[i][0], xf[i][0], q); s1 = dot2(xf[i][1], 0x3f803f80u, s1); q1 = dot2(xf[i][1], xf[i][1], q1);
      s = dot2(xf[i][2], 0x3f803f80u, s); q = dot2(xf[i][2], xf[i][2], q); s1 = dot2(xf[i][3], 0x3f803f80u, s1); q1 = dot2(xf[i][3], xf[i][3], q1);
      pin(s); pin(q); pin(s1); pin(q1);
    });
    s = sum32(s + s1); q = sum32(q + q1);
    if (h == 0) ((f32x2*)(lds + S_ST))[w * 32 + r] = f32x2{s, q};
  }
  wv<14>();
  bar();
  stamp(x);
  if (!kvp) {
    if (task < 112) {
      const int g = l >> 4, mat = task >= 56, gt = 4 * ((task % 56) >> 1) + w, m = gt / 7, j = gt % 7, lt = (m & 1) ? j : (j == 6 ? 0 : j + 1);
      const char* tb = lds + IMG_T + (16 * (g >> 1) + 4 * (g & 1) + ((l >> 2) & 3)) * 128 + 32 * w + (l & 3) * 8;
      const unsigned st = lt < 5 ? 10 * KB : 4 * KB, so = OFF_WGU + (m * 196 + (lt < 5 ? lt * 2 + mat : 140 + (lt - 5) * 2 + mat)) * KB + (task & 1) * 7 * st;
      u32x4 f[7];
      sfor<0, 7>([&](auto i) { f[i] = trfrag(tb + i * 4096, tb + i * 4096 + 1024); });
      float cs = 0.f;
      sfor<0, 7>([&](auto i) { st4<0>(f[i], x.vl16, x.rs, so + i * st); cs = fragsum(f[i], cs); });
      cs = sum32(sum16(cs));
      st1(cs, l < 16 ? l * 4 : ~0u, x.rs, OFF_CSGU + m * 2048 + (task & 1) * 1024 + (lt * 2 + mat) * 64);
    } else if (task < 168) {
      const int nt = 2 * ((task - 112) % 7) + (w & 1), rb = (task - 112) / 7;
      const char* tb = lds + IMG_T + sro * 128 + 64 * (w & 1) + tco;
      const unsigned so = OFF_WD + (nt * 112 + 28 * (rb >> 1)) * KB, sa = so + (rb & 1) * 10 * KB, sb = so + (20 + (rb & 1) * 4) * KB;
      u32x4 f[7];
      if (w < 2) {
        sfor<0, 7>([&](auto i) { const char* p = tb + (32 * (i >> 1) + 8 * (i & 1)) * 128; f[i] = trfrag(p, p + 2048); });
        sfor<0, 7>([&](auto i) { st4<0>(f[i], x.vl16, x.rs, i < 4 ? sa + i * KB : (i < 6 ? sb + (i - 4) * KB : sa + 4 * KB)); });
      } else {
        sfor<0, 7>([&](auto i) { const char* p = tb + (32 * ((i + 7) >> 1) + 8 * ((i + 7) & 1)) * 128; f[i] = trfrag(p, p + 2048); });
        sfor<0, 7>([&](auto i) { st4<0>(f[i], x.vl16, x.rs, i < 5 ? sa + (5 + i) * KB : sb + (i - 3) * KB); });
      }
      st1(0.f, ~0u, x.rs, 0);
    } else {
      const int nt = 2 * ((task - 168) % 7) + (w & 1);
      const char* tb = lds + IMG_T + (112 * (w >> 1) + sro) * 128 + 64 * (w & 1) + tco;
      const unsigned vo = task < 182 ? x.vl16 : ~0u - 4096;
      u32x4 f[8];
      sfor<0, 8>([&](auto i) { const char* p = tb + (28 * (i >> 1) + 16 * (i & 1)) * 128; f[i] = trfrag(p, p + 1024); if ((i & 1) && h) { f[i][2] = 0; f[i][3] = 0; } });
      sfor<0, 8>([&](auto i) { st4<(i & 3) * 1024>(f[i], vo, x.rs, OFF_WO + (nt * 32 + 16 * ((task - 168) / 7) + 8 * (w >> 1) + (i & ~3)) * KB); });
    }
  }
  pstamp(x);
  if (!kvp) { wv<8>(); bar(); }
  stamp(x);
  float mean, rstd;
  { const f32x2 p0 = ((const f32x2*)(lds + S_ST))[tl * 32 + r], p1 = ((const f32x2*)(lds + S_ST))[(tl + 2) * 32 + r];
    row_stats(p0[0] + p1[0], p0[1] + p1[1], mean, rstd); }
  float* csw = (float*)(lds + S_CS);
  const int tro = 8 * h + ((l >> 2) & 3);
  u32x4 ones = {0x3f803f80u, 0x3f803f80u, 0x3f803f80u, 0x3f803f80u};
  pin(ones);
  if (kvp) {
    f32x16 kv, cs;
    const unsigned tb = IMG_T + (224 * kh + tro) * 56 + tco;
    u32x2 f[14][2];
    auto rd = [&](auto i) { trd<i * 896>(f[i][0], tb); trd<i * 896 + 224>(f[i][1], tb); };
    sfor<0, 7>(rd);
    sfor<0, 14>([&](auto i) {
      if constexpr (i + 7 < 14) { wl<12>(); rd(std::integral_constant<int, i + 7>{}); } else wl<2 * (13 - i)>();
      pin(f[i][0]); pin(f[i][1]);
      u32x4 g = {f[i][0][0], f[i][0][1], f[i][1][0], f[i][1][1]};
      pin(g); nop<0>();
      if constexpr (i == 0) { m32z(kv, g, xf[i]); m32z(cs, g, ones); } else { m32(kv, g, xf[i]); m32(cs, g, ones); }
    });
    nop<11>(); pin(kv); pin(cs);
    if (r == 0) sfor<0, 4>([&](auto q) { *(f32x4*)(csw + (w * 3 + 2) * 32 + 8 * q + 4 * h) = f32x4{cs[4 * q], cs[4 * q + 1], cs[4 * q + 2], cs[4 * q + 3]}; });
    pstamp(x);
    if (kh) put_tile(lds + S_KP + tl * 4096 + x.vl16, kv);
    bar();
    if (!kh) {
      if (x.m == 0 && w == 0) st1(0.f, l < 16 ? l * 256 : ~0u, x.rs, OFF_FLAG + x.c * 4096);
      float raw[16]; f32x4 c4[4];
      sfor<0, 4>([&](auto q) {
        const f32x4 o = ((const f32x4*)(lds + S_KP + tl * 4096 + x.vl16))[q * 64];
        c4[q] = *(const f32x4*)(csw + (w * 3 + 2) * 32 + 8 * q + 4 * h) + *(const f32x4*)(csw + ((w + 2) * 3 + 2) * 32 + 8 * q + 4 * h);
#pragma unroll
        for (int i = 0; i < 4; ++i) raw[4 * q + i] = kv[4 * q + i] + o[i];
      });
      const unsigned km = kv_finish(x, raw, c4, mean, rstd, hp == 1, OFF_KV + (tt * 4 + 2 * hp) * KB, tl);
      pstamp(x);
      wv<0>();
      kv_publish(x, 0, 2 * tt + hp, km);
      if (tt == 0 && r == 0) sfor<0, 4>([&](auto q) { st4<32 * q>(__builtin_bit_cast(u32x4, c4[q]), h * 16, x.rs, OFF_CSQ + (16 + hp) * 128); });
    } else {
      pstamp(x);
      wv<0>();
    }
    bar();
    pstamp(x);
  } else { pstamp(x); pstamp(x); pstamp(x); }
  f32x16 qa;
  {
    f32x16 q1, q2, cs;
    const unsigned t1 = IMG_Q + (224 * kh + tro) * 128 + tco + 56 * tl, t2 = IMG_Q + (224 * kh + tro) * 128 + tco + 56 * (tl ^ 1);
    u32x2 f[14][4];
    auto rd = [&](auto i) { trd<i * 2048>(f[i][0], t1); trd<i * 2048 + 512>(f[i][1], t1); trd<i * 2048>(f[i][2], t2); trd<i * 2048 + 512>(f[i][3], t2); };
    wl<0>();
    sfor<0, 3>(rd);
    sfor<0, 14>([&](auto i) {
      if constexpr (i + 3 < 14) { wl<8>(); rd(std::integral_constant<int, i + 3>{}); } else wl<4 * (13 - i)>();
      sfor<0, 4>([&](auto e) { pin(f[i][e]); });
      u32x4 g1 = {f[i][0][0], f[i][0][1], f[i][1][0], f[i][1][1]}, g2 = {f[i][2][0], f[i][2][1], f[i][3][0], f[i][3][1]};
      pin(g1); pin(g2); nop<0>();
      if constexpr (i == 0) { m32z(q1, g1, xf[i]); m32z(q2, g2, xf[i]); m32z(cs, g1, ones); } else { m32(q1, g1, xf[i]); m32(q2, g2, xf[i]); m32(cs, g1, ones); }
    });
    nop<11>(); pin(q1); pin(q2); pin(cs);
    pstamp(x);
    if (r == 0) sfor<0, 4>([&](auto q) { *(f32x4*)(csw + (w * 3) * 32 + 8 * q + 4 * h) = f32x4{cs[4 * q], cs[4 * q + 1], cs[4 * q + 2], cs[4 * q + 3]}; });
    if (kh == tl) { put_tile(lds + S_QP + (w ^ 2) * 4096 + x.vl16, q2); qa = q1; } else { put_tile(lds + S_QP + (w ^ 2) * 4096 + x.vl16, q1); qa = q2; }
  }
  if (th == 0 && x.c < 14) {
    const int f = 4 * x.c + w, hd = f >= 28, ks = f - 28 * hd;
    const char* tb = lds + IMG_Q + (16 * ks + sro) * 128 + tco + 56 * hd;
    st4<0>(trfrag(tb, tb + 1024), x.vl16, x.rs, OFF_WQ + (hp * 56 + f) * KB);
  }
  if (kvp && th == 1 && x.c < 7) {
    const char* tb = lds + IMG_T + (16 * (4 * x.c + w) + sro) * 56 + tco;
    st4<0>(trfrag(tb, tb + 448), x.vl16, x.rs, OFF_WQ + ((16 + hp) * 28 + 4 * x.c + w) * KB);
  }
  pstamp(x);
  if (!kvp) __builtin_amdgcn_s_sleep(12);
  ask_slots(x, 0);
  kv_wait(x, 0);
  count(x, FL_W(x.c), 1);
  ask_kv(x, 0, kf);
  stamp(x);
  const bool own = x.c == hp && th == 0;
  {
    f32x4 cq[4];
    sfor<0, 4>([&](auto q) {
      cq[q] = *(const f32x4*)(csw + (kh * 3) * 32 + 8 * q + 4 * h) + *(const f32x4*)(csw + ((kh + 2) * 3) * 32 + 8 * q + 4 * h);
      if (own && tl == 0 && r == 0) st4<32 * q>(__builtin_bit_cast(u32x4, cq[q]), h * 16, x.rs, OFF_CSQ + (2 * hp + kh) * 128);
    });
    q_finish(x, qa, cq, mean, rstd);
  }
  pstamp(x);
}

DI void phase_front(Ctx& x, f32x16 (&kf)[3]) {
  const int w = x.w, l = x.l, h = l >> 5, r = l & 31;
  const int th = x.m >> 3, hp = x.m & 7, tl = w & 1, kh = w >> 1, tt = 4 * x.c + 2 * th + tl;
  const unsigned KV = OFF_KV + SZ_KV;
  {
    f32x4 cq[4], ck[4];
    sfor<0, 4>([&](auto q) { ld4f<32 * q>(cq[q], h * 16, x.rs, OFF_CSQ + (2 * hp + kh) * 128); ld4f<32 * q>(ck[q], h * 16, x.rs, OFF_CSQ + (16 + (hp & 1)) * 128); });
    wv<0>();
    await(x, FL_X(x.c, th), 7);
    stamp(x);
    u32x4 xf[14]; f32x2 sp[7];
    {
      const unsigned so = OFF_X + 2 * SZ_X + (tt * 28 + 14 * kh) * KB;
      sfor<0, 7>([&](auto i) { ld4<(i & 3) * 1024>(xf[i], x.vl16, x.rs, so + (i >> 2) * 4096); });
      sfor<0, 7>([&](auto i) { ld2<0>(sp[i], (h * 7 * 2048 + r) * 8, x.rs, OFF_ST + 2 * SZ_ST + (i * 2048 + tt * 32) * 8); });
      sfor<7, 14>([&](auto i) { ld4<(i & 3) * 1024>(xf[i], x.vl16, x.rs, so + (i >> 2) * 4096); });
    }
    float mean, rstd;
    auto stats = [&]() {
      sfor<0, 7>([&](auto i) { pin(sp[i]); });
      sfor<0, 4>([&](auto i) { pin(cq[i]); pin(ck[i]); });
      float s = 0.f, q = 0.f;
#pragma unroll
      for (int i = 0; i < 7; ++i) { s += sp[i][0]; q += sp[i][1]; }
      row_stats(sum32(s), sum32(q), mean, rstd);
      pin(mean); pin(rstd); SB();
    };
    f32x16 qa, qb;
    u32x4 wa[14], wb[14];
    const unsigned pa = x.vl16 + (SL_WQ + 28 * kh + 14 * kh) * KB, pb = x.vl16 + (SL_WQ + 28 * (kh ^ 1) + 14 * kh) * KB;
    if (hp < 2) {
      f32x16 kv;
      sfor<0, 14>([&](auto i) { dsr<i * 1024>(wa[i], x.vl16 + 14 * kh * KB); });
      sfor<0, 14>([&](auto i) {
        wv<(i < 7 ? 20 : 13) - i>(); wl<13 - i>();
        if constexpr (i == 0) m32z(kv, wa[i], xf[i]); else m32(kv, wa[i], xf[i]);
        if constexpr (i == 7) stats();
      });
      nop<11>(); pin(kv);
      if (kh) put_tile(lds + S_KP + tl * 4096 + x.vl16, kv);
      bar();
      unsigned km = 0;
      if (!kh) {
        float raw[16];
        sfor<0, 4>([&](auto q) {
          const f32x4 o = ((const f32x4*)(lds + S_KP + tl * 4096 + x.vl16))[q * 64];
#pragma unroll
          for (int i = 0; i < 4; ++i) raw[4 * q + i] = kv[4 * q + i] + o[i];
        });
        km = kv_finish(x, raw, ck, mean, rstd, hp == 1, KV + (tt * 4 + 2 * hp) * KB, tl);
      }
      wl<0>();
      sfor<0, 7>([&](auto i) { dsr<i * 1024>(wb[i], pb); dsr<i * 1024>(wa[i], pa); });
      sfor<0, 14>([&](auto i) {
        if constexpr (i < 7) wl<12>(); else wl<2 * (13 - i)>();
        if constexpr (i == 0) { m32z(qb, wb[i], xf[i]); m32z(qa, wa[i], xf[i]); } else { m32(qb, wb[i], xf[i]); m32(qa, wa[i], xf[i]); }
        if constexpr (i < 7) { dsr<(i + 7) * 1024>(wb[i + 7], pb); dsr<(i + 7) * 1024>(wa[i + 7], pa); }
        if constexpr (i == 6) { if (!kh) { wv<0>(); kv_publish(x, 1, 2 * tt + hp, km); } SB(); }
      });
    } else {
      sfor<0, 7>([&](auto i) { dsr<i * 1024>(wb[i], pb); dsr<i * 1024>(wa[i], pa); });
      sfor<0, 14>([&](auto i) {
        wv<(i < 7 ? 20 : 13) - i>();
        if constexpr (i < 7) wl<12>(); else wl<2 * (13 - i)>();
        if constexpr (i == 0) { m32z(qb, wb[i], xf[i]); m32z(qa, wa[i], xf[i]); } else { m32(qb, wb[i], xf[i]); m32(qa, wa[i], xf[i]); }
        if constexpr (i < 7) { dsr<(i + 7) * 1024>(wb[i + 7], pb); dsr<(i + 7) * 1024>(wa[i + 7], pa); }
        if constexpr (i == 7) stats();
      });
    }
    nop<11>();
    pin(qa); pin(qb);
    put_tile(lds + S_QP + (w ^ 2) * 4096 + x.vl16, qb);
    bar();
    q_finish(x, qa, cq, mean, rstd);
    stamp(x);
    if (hp >= 2) __builtin_amdgcn_s_sleep(36);
    ask_slots(x, 1);
    kv_wait(x, 1);
    ask_kv(x, 1, kf);
    stamp(x);
  }
}

DI void phase_att(Ctx& x, const int L, f32x16 (&kf)[3], f32x16 (&g2)[3]) {
  const int w = x.w, l = x.l, h = l >> 5, r = l & 31;
  const int th = x.m >> 3, hp = x.m & 7, tl = w & 1, kh = w >> 1, tt = 4 * x.c + 2 * th + tl;
  const unsigned kvs = OFF_KV + L * SZ_KV + 16 * w * 4096;
  stamp(x);
  if (L == 0) bar();
  stamp(x);
  unsigned kma = S_KM + 16; asm volatile("" : "+s"(kma));
  const float kmx = *(const float*)(lds + kma);
  u32x4 q[4][2];
#pragma unroll
  for (int k = 0; k < 4; ++k) {
    q[k][0] = ((const u32x4*)(lds + S_QF + (w ^ k) * 2048 + x.vl16))[0];
    q[k][1] = ((const u32x4*)(lds + S_QF + (w ^ k) * 2048 + x.vl16))[64];
  }
  bool safe = true;
  {
#pragma unroll
    for (int k = 0; k < 4; ++k) {
      const float mrow = bflo(q[k][1][2]) * kmx + 0.01f;
      safe = safe && (!h || mrow <= M_LIMIT);
      if (h) q[k][1][2] = cvtpk(-mrow, 0.f);
    }
  }
  const bool fast = __all(safe);
  stamp(x);
  f32x16 own;
  if (fast) {
    unsigned so_kv = kvs + 3 * 4096, so_w = OFF_WGU + (x.m * 196 + 2 * w) * KB, so_l = 2 * w * KB, so_g = OFF_WGU + (x.m * 196 + 112 + 12 * w) * KB, spin = 1u << 16, fvo = (l & 15) * 4096;
    Two ab = base_wo(x);
    const Two wd = base_wd(x);
    asm volatile(
      ".macro GRP s, pa, pb\n\t"
      "v_exp_f32 v200, v[\\s]\n\tv_exp_f32 v201, v[\\s+1]\n\tv_cvt_pk_bf16_f32 v[\\pa], v202, v203\n\t"
      "v_exp_f32 v202, v[\\s+2]\n\tv_exp_f32 v203, v[\\s+3]\n\tv_cvt_pk_bf16_f32 v[\\pb], v200, v201\n\t"
      ".endm\n\t"
      ".macro MEM kind, i, ld\n\t"
      ".if \\kind == 1\n\t"
      "buffer_load_dwordx4 v[128+16*\\ld+4*\\i:128+16*\\ld+4*\\i+3], %[vo], %[rs], %[skv] offen offset:1024*\\i sc1\n\t"
      ".elseif \\kind == 5\n\t"
      "ds_write_b128 v205, v[4*\\i:4*\\i+3] offset:1024*\\i\n\t"
      ".elseif \\kind == 6\n\t"
      "ds_write_b128 v205, v[4:7] offset:1024\n\t"
      ".elseif \\kind == 7\n\t"
      "ds_write_b128 v205, v[12:15] offset:3072\n\t"
      ".elseif \\kind == 2\n\t"
      "buffer_load_dword v206, v205, %[rs], 0 offen offset:3072 sc1\n\t"
      ".elseif \\kind == 3\n\t"
      ".if att_n < 16 || att_n >= 56\n\t"
      ".if att_n < 78\n\t"
      ".if att_n < 16\n\t.set att_r, 184+4*att_n\n\t.else\n\t.set att_r, 4*(att_n-56)\n\t.endif\n\t"
      ".if att_n & 1\n\t"
      "buffer_load_dwordx4 a[att_r:att_r+3], %[vo], %[rs], %[ab1] offen offset:1024*((att_n>>1)&3) sc1\n\t"
      ".else\n\t"
      "buffer_load_dwordx4 a[att_r:att_r+3], %[vo], %[rs], %[ab0] offen offset:1024*((att_n>>1)&3) sc1\n\t"
      ".endif\n\t"
      ".if att_n == 15\n\t"
      "s_mov_b32 %[ab0], %[wd0]\n\ts_mov_b32 %[ab1], %[wd1]\n\t"
      ".elseif (att_n & 7) == 7\n\t"
      "s_add_u32 %[ab0], %[ab0], 4096\n\ts_add_u32 %[ab1], %[ab1], 4096\n\t"
      ".endif\n\t"
      ".endif\n\t"
      ".elseif att_n < 44\n\t"
      ".if att_n & 1\n\t"
      "buffer_load_dwordx4 %[vo], %[rs], %[sw] offen offset:1024 sc1 lds\n\t"
      "s_add_u32 %[sw], %[sw], 8192\n\ts_add_u32 m0, m0, 8192\n\t"
      ".else\n\t"
      "buffer_load_dwordx4 %[vo], %[rs], %[sw] offen sc1 lds\n\t"
      ".endif\n\t"
      ".else\n\t"
      "buffer_load_dwordx4 v[208+4*(att_n-44):208+4*(att_n-44)+3], %[vo], %[rs], %[sg] offen offset:1024*(att_n&3) sc1\n\t"
      ".if (att_n & 3) == 3\n\ts_add_u32 %[sg], %[sg], 4096\n\t.endif\n\t"
      ".endif\n\t"
      ".set att_n, att_n+1\n\t"
      ".endif\n\t"
      ".endm\n\t"
      ".macro STEP sc, sn, k, nk, pk, tk, nt, pt, ld, m0k, m0i, m1k, m2k, m2i, m3k\n\t"
      "v_mfma_f32_32x32x16_bf16 v[\\sn:\\sn+15], v[128+16*\\nt:128+16*\\nt+3], v[64+8*\\nk:64+8*\\nk+3], 0\n\t"
      "MEM \\m0k, \\m0i, \\ld\n\t"
      "GRP \\sc, 199, 192\n\t"
      "v_mfma_f32_32x32x16_bf16 v[\\sn:\\sn+15], v[132+16*\\nt:132+16*\\nt+3], v[68+8*\\nk:68+8*\\nk+3], v[\\sn:\\sn+15]\n\t"
      "MEM \\m1k, 0, 0\n\t"
      "GRP \\sc+4, 193, 194\n\t"
      "v_mfma_f32_32x32x16_bf16 v[16*\\pk:16*\\pk+15], v[140+16*\\pt:140+16*\\pt+3], v[196:199], v[16*\\pk:16*\\pk+15]\n\t"
      "MEM \\m2k, \\m2i, \\ld\n\t"
      "GRP \\sc+8, 195, 196\n\t"
      "v_mfma_f32_32x32x16_bf16 v[16*\\k:16*\\k+15], v[136+16*\\tk:136+16*\\tk+3], v[192:195], v[16*\\k:16*\\k+15]\n\t"
      "MEM \\m3k, 0, 0\n\t"
      "GRP \\sc+12, 197, 198\n\t"
      ".endm\n\t"
      ".macro TILE tk, nx, pv, on, wait, p, fl=0\n\t"
      "STEP 96, 112, 0, 1, 3, \\tk, \\tk, \\pv, \\pv, \\p, 0, 0, \\p, 0, \\fl\n\t"
      "STEP 112, 96, 1, 2, 0, \\tk, \\tk, \\tk, \\pv, \\on, 0, \\p, \\on, 1, 0\n\t"
      "STEP 96, 112, 2, 3, 1, \\tk, \\tk, \\tk, \\pv, \\on, 2, \\p, \\on, 3, 0\n\t"
      ".if \\on\n\ts_add_u32 %[skv], %[skv], 4096\n\t.endif\n\t"
      "s_waitcnt vmcnt(\\wait)\n\t"
      "STEP 112, 96, 3, 0, 2, \\tk, \\nx, \\tk, \\pv, \\p, 0, 0, \\p, 0, 0\n\t"
      ".endm\n\t"
      ".set att_n, 0\n\t"
      "s_mov_b32 m0, %[sl]\n\t"
      "v_mov_b32 v196, 0\n\tv_mov_b32 v197, 0\n\tv_mov_b32 v198, 0\n\tv_mov_b32 v199, 0\n\tv_mov_b32 v202, 0\n\tv_mov_b32 v203, 0\n\t"
      "v_mov_b32 v188, 0\n\tv_mov_b32 v189, 0\n\tv_mov_b32 v190, 0\n\tv_mov_b32 v191, 0\n\t"
      "v_mfma_f32_32x32x16_bf16 v[0:15], v[196:199], v[196:199], 0\n\t"
      "v_mfma_f32_32x32x16_bf16 v[16:31], v[196:199], v[196:199], 0\n\t"
      "v_mfma_f32_32x32x16_bf16 v[32:47], v[196:199], v[196:199], 0\n\t"
      "v_mfma_f32_32x32x16_bf16 v[48:63], v[196:199], v[196:199], 0\n\t"
      "s_waitcnt vmcnt(8)\n\t"
      "v_mfma_f32_32x32x16_bf16 v[96:111], v[128:131], v[64:67], 0\n\t"
      "v_mfma_f32_32x32x16_bf16 v[96:111], v[132:135], v[68:71], v[96:111]\n\t"
      "s_nop 11\n\t"
      "TILE 0, 1, 3, 1, 8, 0\n\t"
      "TILE 1, 2, 0, 1, 8, 0\n\tTILE 2, 3, 1, 1, 9, 0, 2\n\t"
      "s_waitcnt vmcnt(4)\n\t"
      "1:\n\t"
      "v_cmp_gt_u32 vcc, 16, v206\n\t"
      "s_and_b64 vcc, exec, vcc\n\t"
      "s_cbranch_vccz 2f\n\t"
      "s_sleep 24\n\t"
      "buffer_load_dword v206, v205, %[rs], 0 offen offset:3072 sc1\n\t"
      "s_sub_u32 %[spin], %[spin], 1\n\t"
      "s_waitcnt vmcnt(0)\n\t"
      "s_cbranch_scc0 1b\n\t"
      "2:\n\t"
      "s_barrier\n\t"
      "TILE 3, 0, 2, 1, 13, 3\n\t"
      "TILE 0, 1, 3, 1, 18, 3\n\tTILE 1, 2, 0, 1, 20, 3\n\tTILE 2, 3, 1, 1, 20, 3\n\tTILE 3, 0, 2, 1, 20, 3\n\t"
      "TILE 0, 1, 3, 1, 20, 3\n\tTILE 1, 2, 0, 1, 20, 3\n\tTILE 2, 3, 1, 1, 20, 3\n\tTILE 3, 0, 2, 1, 20, 3\n\t"
      "TILE 0, 1, 3, 1, 20, 3\n\tTILE 1, 2, 0, 0, 16, 3\n\tTILE 2, 3, 1, 0, 12, 3\n\t"
      "v_add_u32 v205, %[d1], v204\n\tv_add_u32 v206, %[d2], v204\n\t"
      "STEP 96, 112, 0, 1, 3, 3, 3, 2, 0, 3, 0, 3, 3, 0, 3\n\t"
      "STEP 112, 96, 1, 2, 0, 3, 3, 3, 0, 3, 0, 3, 0, 0, 0\n\t"
      "STEP 96, 112, 2, 3, 1, 3, 3, 3, 0, 5, 0, 6, 5, 2, 7\n\t"
      "ds_write_b128 v206, v[16:19]\n\t"
      "GRP 112, 199, 192\n\t"
      "ds_write_b128 v206, v[20:23] offset:1024\n\t"
      "GRP 116, 193, 194\n\t"
      "v_mfma_f32_32x32x16_bf16 v[32:47], v[188:191], v[196:199], v[32:47]\n\t"
      "ds_write_b128 v206, v[24:27] offset:2048\n\t"
      "GRP 120, 195, 196\n\t"
      "v_mfma_f32_32x32x16_bf16 v[48:63], v[184:187], v[192:195], v[48:63]\n\t"
      "ds_write_b128 v206, v[28:31] offset:3072\n\t"
      "GRP 124, 197, 198\n\t"
      "v_add_u32 v200, %[d3], v204\n\t"
      "v_cvt_pk_bf16_f32 v199, v202, v203\n\t"
      "s_nop 1\n\t"
      "v_mfma_f32_32x32x16_bf16 v[48:63], v[188:191], v[196:199], v[48:63]\n\t"
      "ds_write_b128 v200, v[32:35]\n\tds_write_b128 v200, v[36:39] offset:1024\n\tds_write_b128 v200, v[40:43] offset:2048\n\tds_write_b128 v200, v[44:47] offset:3072\n\t"
      "s_nop 7\n\t"
      ".purgem GRP\n\t.purgem MEM\n\t.purgem STEP\n\t.purgem TILE"
      : "={v[48:63]}"(own), "+{v[128:143]}"(kf[0]), "+{v[144:159]}"(kf[1]), "+{v[160:175]}"(kf[2]),
        "={v[208:223]}"(g2[0]), "={v[224:239]}"(g2[1]), "={v[240:255]}"(g2[2]),
        [skv] "+s"(so_kv), [sw] "+s"(so_w), [ab0] "+s"(ab.s0), [ab1] "+s"(ab.s1), [spin] "+s"(spin), [sg] "+s"(so_g), "+{v205}"(fvo)
      : "{v[64:67]}"(q[1][0]), "{v[68:71]}"(q[1][1]), "{v[72:75]}"(q[2][0]), "{v[76:79]}"(q[2][1]),
        "{v[80:83]}"(q[3][0]), "{v[84:87]}"(q[3][1]), "{v[88:91]}"(q[0][0]), "{v[92:95]}"(q[0][1]),
        [vo] "{v204}"(x.vl16), [d1] "s"(SCR + (w ^ 1) * 3 * 4096), [d2] "s"(SCR + ((w ^ 2) * 3 + 1) * 4096), [d3] "s"(SCR + ((w ^ 3) * 3 + 2) * 4096), [rs] "s"(x.rs), [sl] "s"(so_l), [wd0] "s"(wd.s0), [wd1] "s"(wd.s1)
      : "memory", "scc", "vcc",
        "v0","v1","v2","v3","v4","v5","v6","v7","v8","v9","v10","v11","v12","v13","v14","v15","v16","v17","v18","v19","v20","v21","v22","v23",
        "v24","v25","v26","v27","v28","v29","v30","v31","v32","v33","v34","v35","v36","v37","v38","v39","v40","v41","v42","v43","v44","v45","v46","v47",
        "v96","v97","v98","v99","v100","v101","v102","v103","v104","v105","v106","v107","v108","v109","v110","v111",
        "v112","v113","v114","v115","v116","v117","v118","v119","v120","v121","v122","v123","v124","v125","v126","v127",
        "v176","v177","v178","v179","v180","v181","v182","v183","v184","v185","v186","v187","v188","v189","v190","v191",
        "v192","v193","v194","v195","v196","v197","v198","v199","v200","v201","v202","v203","v206", ACLOB);
  } else {
    f32x16 o[4];
    wv<0>();
    pin(kf[0]); pin(kf[1]); pin(kf[2]);
    float mx[4];
#pragma unroll
    for (int k = 0; k < 4; ++k) {
      mx[k] = -3.0e38f; if (h) q[k][1][2] = 0; pin(q[k][1]);
#pragma unroll
      for (int i = 0; i < 16; ++i) o[k][i] = 0.f;
    }
#pragma nounroll
    for (unsigned spin = 0; spin < (1u << 16); ++spin) {
      float v;
      ld1(v, (l & 15) * 4096, x.rs, OFF_FLAG + FL_W(0)); wv<0>(); pin(v);
      if (__all(__float_as_uint(v) >= 16u)) break;
      __builtin_amdgcn_s_sleep(24);
    }
    pf_wo(x);
    pf_wgu1(x);
    sfor<0, 12>([&](auto j) {
      u32x4 t;
      ld4<0>(t, x.vl16, x.rs, OFF_WGU + (x.m * 196 + 112 + 12 * w + j) * KB); wv<0>(); pin(t);
#pragma unroll
      for (int i = 0; i < 4; ++i) g2[j >> 2][4 * (j & 3) + i] = __uint_as_float(t[i]);
    });
    pf_wd<0, 22>(x, base_wd(x));
    for (int tk = 0; tk < 16; ++tk) {
      u32x4 kf[4];
      sfor<0, 4>([&](auto i) { ld4<i * 1024>(kf[i], x.vl16, x.rs, kvs + tk * 4096); });
      wv<0>();
      sfor<0, 4>([&](auto k) {
        f32x16 s;
        nop<1>();
        m32z(s, kf[0], q[k][0]); m32(s, kf[1], q[k][1]);
        nop<11>(); pin(s);
        float tm = s[0];
#pragma unroll
        for (int i = 1; i < 16; ++i) tm = fmaxf(tm, s[i]);
        tm = max32(tm);
        const float mn = fmaxf(mx[k], tm), al = __builtin_amdgcn_exp2f(mx[k] - mn);
        mx[k] = mn;
#pragma unroll
        for (int i = 0; i < 16; ++i) o[k][i] *= al;
        u32x4 p0, p1;
#pragma unroll
        for (int d = 0; d < 4; ++d) {
          p0[d] = cvtpk(__builtin_amdgcn_exp2f(s[2 * d] - mn), __builtin_amdgcn_exp2f(s[2 * d + 1] - mn));
          p1[d] = cvtpk(__builtin_amdgcn_exp2f(s[8 + 2 * d] - mn), __builtin_amdgcn_exp2f(s[9 + 2 * d] - mn));
        }
        pin(p0); pin(p1); pin(o[k]);
        nop<1>();
        m32(o[k], kf[2], p0); m32(o[k], kf[3], p1);
        nop<11>(); pin(o[k]);
      });
    }
    bar();
    float* mm = (float*)(lds + SCR);
#pragma unroll
    for (int k = 0; k < 4; ++k) if (h == 0) mm[((w ^ k) * 4 + w) * 32 + r] = mx[k];
    bar();
#pragma unroll
    for (int k = 0; k < 4; ++k) {
      const float* p = mm + (w ^ k) * 128 + r;
      const float g = fmaxf(fmaxf(p[0], p[32]), fmaxf(p[64], p[96])), f = __builtin_amdgcn_exp2f(mx[k] - g);
#pragma unroll
      for (int i = 0; i < 16; ++i) o[k][i] *= f;
    }
    bar();
    reduce_put(x, o);
    own = o[0];
  }
  stamp(x);
  reduce_get(x, own);
  pin(own); fstamp(x);
  {
    auto lr = __builtin_amdgcn_permlane32_swap(__float_as_uint(own[12]), __float_as_uint(own[12]), false, false);
    const float inv = __builtin_amdgcn_rcpf(__uint_as_float(lr[1]));
    u32x4 a, b;
#pragma unroll
    for (int d = 0; d < 4; ++d) { a[d] = cvtpk(own[2 * d] * inv, own[2 * d + 1] * inv); b[d] = cvtpk(own[8 + 2 * d] * inv, own[9 + 2 * d] * inv); }
    if (h) { b[2] = 0; b[3] = 0; }
    const unsigned so = OFF_O + L * SZ_O + (tt * 32 + 2 * (2 * hp + kh)) * KB;
    st4<0>(a, x.vl16, x.rs, so); st4<1024>(b, x.vl16, x.rs, so);
  }
  fstamp(x);
  wv<0>();
  fstamp(x);
  signal(x, FL_O(L, x.c, th), 1);
  if (L == 0 && th == 0) {
    if (x.c == hp) count(x, FL_W2, 1);
  }
}

template <bool FINAL> DI void res_epilogue(Ctx& x, const f32x16& acc, const u32x4 (&xr)[2], unsigned Xo, unsigned STo, int tt, int nt, char* out = nullptr) {
  const int h = x.l >> 5, r = x.l & 31;
  u32x4 y[2];
#pragma unroll
  for (int d = 0; d < 8; ++d) y[d >> 2][d & 3] = cvtpk(acc[2 * d] + bflo(xr[d >> 2][d & 3]), acc[2 * d + 1] + bfhi(xr[d >> 2][d & 3]));
  if constexpr (FINAL) {
#pragma unroll
    for (int s = 0; s < 2; ++s) {
      auto a = __builtin_amdgcn_permlane32_swap(y[s][0], y[s][2], false, false);
      auto b = __builtin_amdgcn_permlane32_swap(y[s][1], y[s][3], false, false);
      y[s] = u32x4{a[0], b[0], a[1], b[1]};
    }
    const unsigned vo = r * (D * 2) + h * 16, so = tt * 32 * D * 2 + nt * 64;
    const rsrc_t ro = mkrs(out, S * D * 2);
    st4p<0>(y[0], vo, ro, so); st4p<32>(y[1], vo, ro, so);
  } else {
    const unsigned so = Xo + (tt * 28 + 2 * nt) * KB;
    st4<0>(y[0], x.vl16, x.rs, so); st4<1024>(y[1], x.vl16, x.rs, so);
    float s = 0.f, q = 0.f, s1 = 0.f, q1 = 0.f;
#pragma unroll
    for (int d = 0; d < 4; ++d) {
      s = dot2(y[0][d], 0x3f803f80u, s); q = dot2(y[0][d], y[0][d], q); s1 = dot2(y[1][d], 0x3f803f80u, s1); q1 = dot2(y[1][d], y[1][d], q1);
    }
    s = sum32(s + s1); q = sum32(q + q1);
    if (h == 0) st2<0>(u32x2{__float_as_uint(s), __float_as_uint(q)}, r * 8, x.rs, STo + (nt * 2048 + tt * 32) * 8);
  }
}

DI void phase_oproj(Ctx& x, const int L, const Args& a, const f32x16 (&g2)[3]) {
  const int w = x.w;
  auto put_g2 = [&]() {
#pragma unroll
    for (int j = 0; j < 12; ++j)
      *(f32x4*)(lds + (112 + 12 * w + j) * KB + x.vl16) = f32x4{g2[j >> 2][4 * (j & 3)], g2[j >> 2][4 * (j & 3) + 1], g2[j >> 2][4 * (j & 3) + 2], g2[j >> 2][4 * (j & 3) + 3]};
  };
  if (x.m < 14) {
    const int th = x.m / 7, ct = x.m % 7, tt0 = 4 * x.c + 2 * th, tt = tt0 + (w >> 1), nt = 2 * ct + (w & 1);
    u32x2 x0[4];
    if (L == 0) {
      const rsrc_t rx = mkrs(a.x, S * D * 2);
      sfor<0, 4>([&](auto i) { ld2i<16 * i>(x0[i], (x.l & 31) * 896 + (x.l >> 5) * 8, rx, tt * 32 * 896 + nt * 64); });
    } else {
      sfor<0, 4>([&](auto i) { ld2u<(i >> 1) * 1024 + (i & 1) * 8>(x0[i], x.vl16, x.rs, OFF_X + 2 * SZ_X + (tt * 28 + 2 * nt) * KB); });
    }
    await(x, FL_O(L, x.c, th), 8);
    stamp(x);
    u32x4 b[8][2];
    const unsigned s0 = OFF_O + L * SZ_O + (tt * 32 + 8 * w) * KB, s1 = OFF_O + L * SZ_O + ((tt0 + ((w >> 1) ^ 1)) * 32 + 8 * w) * KB;
    auto ld = [&](auto i) { ld4<(i & 3) * 1024>(b[i][0], x.vl16, x.rs, s0 + (i >> 2) * 4096); ld4<(i & 3) * 1024>(b[i][1], x.vl16, x.rs, s1 + (i >> 2) * 4096); };
    constexpr int WN = 5;
    sfor<0, WN>(ld);
    f32x16 acc[4];
    sfor<0, 8>([&](auto i) {
      constexpr int last = (i + WN - 1 < 7 ? i + WN - 1 : 7);
      wv<2 * (last - i) + 1>();
      m32a<A_WO + 8 * i, i == 0>(acc[0], b[i][0]); m32a<A_WO + 8 * i + 4, i == 0>(acc[1], b[i][0]);
      wv<2 * (last - i)>();
      m32a<A_WO + 8 * i, i == 0>(acc[2], b[i][1]); m32a<A_WO + 8 * i + 4, i == 0>(acc[3], b[i][1]);
      if constexpr (i + WN < 8) ld(std::integral_constant<int, i + WN>{});
    });
    nop<11>();
    sfor<0, 4>([&](auto k) { pin(acc[k]); pin(x0[k]); });
    stamp(x);
    reduce_put(x, acc); reduce_get(x, acc[0]);
    pin(acc[0]); fstamp(x);
    const u32x4 x2[2] = {u32x4{x0[0][0], x0[0][1], x0[1][0], x0[1][1]}, u32x4{x0[2][0], x0[2][1], x0[3][0], x0[3][1]}};
    res_epilogue<false>(x, acc[0], x2, OFF_X + (2 * L + 1) * SZ_X, OFF_ST + (2 * L + 1) * SZ_ST, tt, nt);
    fstamp(x);
    put_g2();
    wv<0>();
    fstamp(x);
    signal(x, FL_X1(L, x.c), 1);
    fstamp(x);
  } else {
    put_g2();
    stamp(x); stamp(x); fstamp(x); fstamp(x); fstamp(x); fstamp(x);
  }
}

constexpr int DWN = 7;
constexpr bool dn_half(int o) { return o == 4 || o == 5 || o == 14 || o == 15; }
struct DnSrc { unsigned s0, s1, l0, l1; };
DI DnSrc dn_src(const Ctx& x, int L) {
  const int tt0 = 4 * x.c + 2 * ((x.m / 7) & 1), tt = tt0 + (x.w >> 1), tto = tt0 + ((x.w >> 1) ^ 1);
  return DnSrc{OFF_H + L * SZ_H + (tt * 112 + 28 * x.w) * KB, OFF_H + L * SZ_H + (tto * 112 + 28 * x.w) * KB,
               OFF_HL + ((L * 64 + tt) * 16 + 4 * x.w) * KB, OFF_HL + ((L * 64 + tto) * 16 + 4 * x.w) * KB};
}
template <int O> DI void dn_ld(Ctx& x, const DnSrc& d, u32x4 (&b)[DWN][2]) {
  constexpr int i = dn_ks(O);
  if constexpr (dn_half(O)) {
    constexpr int imm = (O >= 14 ? 2048 : 0) + (O & 1) * 512;
    u32x2 lo, hi, lo1, hi1;
    ld2u<imm>(lo, x.l * 8, x.rs, d.l0); ld2u<imm + 1024>(hi, x.l * 8, x.rs, d.l0);
    ld2u<imm>(lo1, x.l * 8, x.rs, d.l1); ld2u<imm + 1024>(hi1, x.l * 8, x.rs, d.l1);
    b[O % DWN][0] = u32x4{lo[0], lo[1], hi[0], hi[1]}; b[O % DWN][1] = u32x4{lo1[0], lo1[1], hi1[0], hi1[1]};
  } else {
    ld4<(i & 3) * 1024>(b[O % DWN][0], x.vl16, x.rs, d.s0 + (i >> 2) * 4096); ld4<(i & 3) * 1024>(b[O % DWN][1], x.vl16, x.rs, d.s1 + (i >> 2) * 4096);
  }
}

DI void gu_tile(const f32x4 (&acc)[2][2], const u32x4 (&cs)[2], const float (&mean)[2], const float (&r2)[2], u32x2 (&hp)[2]) {
  const f32x4 cg = __builtin_bit_cast(f32x4, cs[0]), cu = __builtin_bit_cast(f32x4, cs[1]);
#pragma unroll
  for (int u = 0; u < 2; ++u) {
    const f32x2 nm = {-mean[u], -mean[u]}, rr = {r2[u], r2[u]};
#pragma unroll
    for (int e = 0; e < 2; ++e) {
      const f32x2 g = __builtin_elementwise_fma(nm, f32x2{cg[2 * e], cg[2 * e + 1]}, f32x2{acc[0][u][2 * e], acc[0][u][2 * e + 1]});
      const f32x2 v = __builtin_elementwise_fma(nm, f32x2{cu[2 * e], cu[2 * e + 1]}, f32x2{acc[1][u][2 * e], acc[1][u][2 * e + 1]});
      const f32x2 y = g * rr * v;
      hp[u][e] = cvtpk(y[0], y[1]);
    }
  }
}

DI void phase_gu(Ctx& x, const int L, u32x4 (&b)[DWN][2]) {
  const int w = x.w, l = x.l, g = l >> 4, c16 = l & 15, m = x.m, tt = 4 * x.c + w;
  if (L == 0 && wave0(x)) { unsigned v; ldflag(v, x.rs, OFF_FLAG + FL_W2); wv<0>(); recheck(x, v, FL_W2, 8); }
  await(x, FL_X1(L, x.c), 14);
  stamp(x);
  constexpr int NXV = 11;
  u32x4 xf[NXV][2]; f32x2 sp[2][4];
  const unsigned sx = OFF_X + (2 * L + 1) * SZ_X + tt * 28 * KB, st = OFF_ST + (2 * L + 1) * SZ_ST + tt * 32 * 8;
  auto ldx = [&](auto k, auto u) {
    if constexpr (k < NXV) ld4<(k & 1) * 2048 + 256 * u>(xf[k][u], x.vg16, x.rs, sx + (k >> 1) * 4096);
    else if constexpr (k < 14) lda<A_GX + 8 * (k - NXV) + 4 * u, (k & 1) * 2048 + 256 * u>(x.vg16, x.rs, sx + (k >> 1) * 4096);
    else { constexpr int i = k - 14; ld2<128 * u>(sp[u][i], (((i < 3 || g < 2) ? g + 4 * i : g) * 2048 + c16) * 8, x.rs, st); }
  };
  using I0 = std::integral_constant<int, 0>; using I1 = std::integral_constant<int, 1>;
  constexpr int XA = 3;
  sfor<0, XA>([&](auto k) { ldx(k, I0{}); ldx(k, I1{}); });
  const unsigned a0 = x.vl16, a1 = x.vl16 + 65536, a2 = x.vl16 + 131072;
  constexpr int DP = 6;
  u32x4 wf[8];
  auto rd = [&](auto ii) {
    constexpr int i = ii, slot = i >= 160 ? i - 160 : i;
    if constexpr (slot < 64) dsr<slot * 1024>(wf[i & 7], a0); else if constexpr (slot < 128) dsr<(slot - 64) * 1024>(wf[i & 7], a1); else dsr<(slot - 128) * 1024>(wf[i & 7], a2);
  };
  auto mm = [&](f32x4& c, const u32x4& a, auto k, auto u) {
    if constexpr (k < NXV) m16<k == 0>(c, a, xf[k][u]); else m16a<false, A_GX + 8 * (k - NXV) + 4 * u>(c, a);
  };
  const Two wd = base_wd(x);
  f32x4 aa[5][2][2], ch0, ch1;
  float mean[2], r2[2];
  sfor<0, DP>(rd);
  sfor<0, 140>([&](auto i) {
    constexpr int KS = i / 10, s = i % 10, t = s >> 1, mat = i & 1;
    using K = std::integral_constant<int, KS>;
    constexpr auto side = [](int k) { return (k >= 0 && k <= 3) || (k >= 5 && k <= 12); };
    constexpr auto all = [](int k) { return (k + XA < 14 ? 2 : 0) + ((k >= 0 && k <= 3) || (k >= 5 && k <= 9) ? 2 : 0) + (k == 5 ? 1 : 0) + (k >= 5 && k <= 9 ? 2 : 0) + (k >= 10 ? 3 : 0); };
    if constexpr (s == 0) {
      constexpr int cnt = [&] { int n = side(KS - XA) ? 1 : 0; for (int k = KS - XA + 1; k < KS; ++k) n += all(k); return n; }();
      wv<cnt>();
      if constexpr (KS == 5) asm volatile("s_barrier" ::: "memory");
    }
    if constexpr ((i & 1) == 0) { rd(std::integral_constant<int, i + DP>{}); rd(std::integral_constant<int, i + DP + 1>{}); wl<DP>(); }
    mm(aa[t][mat][0], wf[i & 7], K{}, I0{});
    if constexpr (s == 1 && KS + XA < 14) ldx(std::integral_constant<int, KS + XA>{}, I0{});
    if constexpr (s == 6 && KS + XA < 14) ldx(std::integral_constant<int, KS + XA>{}, I1{});
    if constexpr (KS <= 3 && s == 3) ldx(std::integral_constant<int, 14 + KS>{}, I0{});
    if constexpr (KS <= 3 && s == 8) ldx(std::integral_constant<int, 14 + KS>{}, I1{});
    if constexpr (KS == 5 && s == 3) asm volatile("buffer_load_dwordx4 %0, %2, %3, %4 offen sc1\n\tbuffer_load_dwordx4 %1, %2, %3, %4 offen offset:1024 sc1" : "=&v"(ch0), "=&v"(ch1) : "v"(x.vl16), "s"(x.rs), "s"(OFF_CSGU + m * 2048));
    if constexpr (KS == 11 && s == 2) { pin(ch0); pin(ch1); *(f32x4*)(lds + 40 * KB + x.vl16) = ch0 + ch1; }
    if constexpr (KS >= 5 && KS <= 9 && (s == 3 || s == 8) && !(KS == 5 && s == 3)) pf_wgu3<2 * (KS - 5) + (s == 8) - 1>(x);
    if constexpr (KS >= 5 && KS <= 9 && (s == 0 || s == 5)) { constexpr int f = 22 + 2 * (KS - 5) + (s == 5); pf_wd<f, f + 1>(x, wd); }
    if constexpr (KS >= 10 && (s == 3 || s == 5 || s == 8)) {
      constexpr int f = 32 + 3 * (KS - 10) + (s == 3 ? 0 : (s == 5 ? 1 : 2));
      pf_wd<f, f + 1>(x, wd);
    }
    if constexpr ((KS == 10 || KS == 12) && s == 2) {
      constexpr int u = KS == 12;
      sfor<0, 4>([&](auto e) { pin(sp[u][e]); });
      float sm = sp[u][0][0] + sp[u][1][0] + sp[u][2][0], q = sp[u][0][1] + sp[u][1][1] + sp[u][2][1];
      if (g < 2) { sm += sp[u][3][0]; q += sp[u][3][1]; }
      sm = sum32(sum16(sm)); q = sum32(sum16(q));
      float rs; row_stats(sm, q, mean[u], rs); r2[u] = rs * rs;
      pin(mean[u]); pin(r2[u]);
    }
    mm(aa[t][mat][1], wf[i & 7], K{}, I1{}); SB();
  });
  fstamp(x);
  u32x4 cs[7][2];
  const unsigned csa = 40 * KB + g * 16;
  sfor<0, 10>([&](auto i) { dsr<64 * i>(cs[i >> 1][i & 1], csa); });
  wv<12>();
  asm volatile("s_barrier" ::: "memory");
  stamp(x);
  const unsigned H = OFF_H + L * SZ_H + tt * 112 * KB;
  const int gp = (m & 1) ? 7 * m + 1 : 7 * m;
  auto pair = [&](const f32x4 (&t0)[2][2], const f32x4 (&t1)[2][2], auto lt) {
    u32x2 h0[2], h1[2];
    gu_tile(t0, cs[lt], mean, r2, h0); gu_tile(t1, cs[lt + 1], mean, r2, h1);
    st4<0>(u32x4{h0[0][0], h0[0][1], h1[0][0], h1[0][1]}, x.vg16, x.rs, H + (gp + lt - 1) * 1024);
    st4<256>(u32x4{h0[1][0], h0[1][1], h1[1][0], h1[1][1]}, x.vg16, x.rs, H + (gp + lt - 1) * 1024);
  };
  {
    wl<0>();
    sfor<0, 20>([&](auto i) { pin(aa[i >> 2][(i >> 1) & 1][i & 1]); });
    sfor<0, 10>([&](auto i) { pin(cs[i >> 1][i & 1]); });
    u32x2 hl[2];
    gu_tile(aa[0], cs[0], mean, r2, hl);
    const unsigned sl = OFF_HL + ((L * 64 + tt) * 16 + m) * KB;
    st2<0>(hl[0], x.vg16 >> 1, x.rs, sl); st2<128>(hl[1], x.vg16 >> 1, x.rs, sl);
    pair(aa[1], aa[2], std::integral_constant<int, 1>{});
    pair(aa[3], aa[4], std::integral_constant<int, 3>{});
  }
  SB();
  sfor<10, 14>([&](auto i) { dsr<64 * i>(cs[i >> 1][i & 1], csa); });
  fstamp(x);
  f32x4 ab[2][2][2];
  unsigned fa;
  sfor<140, 196>([&](auto i) {
    constexpr int j = i - 140, KS = j / 4, t = (j % 4) >> 1, mat = i & 1;
    using K = std::integral_constant<int, KS>;
    if constexpr (j == 18) {
      fstamp(x);
      if (L == 0) wv<12>(); else wv<6>();
      asm volatile("s_barrier" ::: "memory");
      count(x, FL_H(L, x.c, 0), 1);
      SB();
      fstamp(x);
    }
    if constexpr ((i & 1) == 0) { if constexpr (i + DP < 196) { rd(std::integral_constant<int, i + DP>{}); rd(std::integral_constant<int, i + DP + 1>{}); wl<DP>(); } else wl<194 - i>(); }
    mm(ab[t][mat][0], wf[i & 7], K{}, I0{});
    if constexpr (j % 3 == 0 && j < 36) pf_wd<44 + j / 3, 45 + j / 3>(x, wd);
    if constexpr (j % 3 == 1 && j < 42) { if (L == 0) pf_wq<j / 3>(x); SB(); }
    if constexpr (j == 44) ldflag(fa, x.rs, OFF_FLAG + FL_H(L, x.c, 0));
    mm(ab[t][mat][1], wf[i & 7], K{}, I1{}); SB();
  });
  wv<0>();
  recheck(x, fa, FL_H(L, x.c, 0), 16);
  { const DnSrc d = dn_src(x, L); sfor<0, 3>([&](auto o) { dn_ld<o>(x, d, b); }); }
  stamp(x);
  sfor<0, 8>([&](auto i) { pin(ab[i >> 2][(i >> 1) & 1][i & 1]); });
  sfor<10, 14>([&](auto i) { pin(cs[i >> 1][i & 1]); });
  pair(ab[0], ab[1], std::integral_constant<int, 5>{});
}

DI void phase_down(Ctx& x, const int L, char* out, u32x4 (&b)[DWN][2]) {
  const int w = x.w;
  if (x.m >= 14) {
    wv<0>();
    sfor<0, 3>([&](auto o) { pin(b[o][0]); pin(b[o][1]); });
    signal(x, FL_H(L, x.c, 1), 1);
    stamp(x); fstamp(x); fstamp(x); stamp(x);
    return;
  }
  const int th = x.m / 7, ct = x.m % 7, tt = 4 * x.c + 2 * th + (w >> 1), nt = 2 * ct + (w & 1);
  stamp(x);
  constexpr int WN = DWN;
  u32x4 xr[2];
  const DnSrc d = dn_src(x, L);
  auto ld = [&](auto o) { dn_ld<o>(x, d, b); };
  sfor<3, WN>(ld);
  f32x16 acc[4];
  unsigned fb;
  sfor<0, 25>([&](auto i) {
    constexpr int last = i + WN - 1 < 27 ? i + WN - 1 : 27, ex = i <= 2 ? 2 : ((i >= 8 && i <= 13) ? 1 : ((i >= 21) ? 2 : 0));
    constexpr int later = [&] { int n = 0; for (int o = i + 1; o <= last; ++o) n += dn_half(o) ? 4 : 2; return n; }();
    wv<later + (dn_half(i) ? 2 : 1) + ex>();
    if constexpr (i == 3) signal(x, FL_H(L, x.c, 1), 1);
    m32a<A_WD + 8 * i, i == 0>(acc[0], b[i % WN][0]); m32a<A_WD + 8 * i + 4, i == 0>(acc[1], b[i % WN][0]);
    wv<later + ex>();
    m32a<A_WD + 8 * i, i == 0>(acc[2], b[i % WN][1]); m32a<A_WD + 8 * i + 4, i == 0>(acc[3], b[i % WN][1]);
    if constexpr (i == 7) ldflag(fb, x.rs, OFF_FLAG + FL_H(L, x.c, 1));
    if constexpr (i + WN == 20) { fstamp(x); wv<12>(); recheck(x, fb, FL_H(L, x.c, 1), 16); fstamp(x); }
    if constexpr (i + WN < 28) ld(std::integral_constant<int, i + WN>{});
    if constexpr (i + WN == 27) {
      const unsigned sr = OFF_X + (2 * L + 1) * SZ_X + (tt * 28 + 2 * nt) * KB;
      ld4<0>(xr[0], x.vl16, x.rs, sr); ld4<1024>(xr[1], x.vl16, x.rs, sr);
    }
  });
  wv<2>();
  sfor<1, 5>([&](auto t) {
    constexpr int k = t & 3;
    m32a<A_WD + 8 * 25 + 4 * (k & 1), false>(acc[k], b[25 % WN][k >> 1]);
    m32a<A_WD + 8 * 26 + 4 * (k & 1), false>(acc[k], b[26 % WN][k >> 1]);
    if constexpr (t > 1) { nop<(t == 2 ? 9 : 7)>(); red_put1<t - 1, 0>(x, acc[t - 1]); red_put1<t - 1, 1>(x, acc[t - 1]); }
    m32a<A_WD + 8 * 27 + 4 * (k & 1), false>(acc[k], b[27 % WN][k >> 1]);
    if constexpr (t > 1) { red_put1<t - 1, 2>(x, acc[t - 1]); red_put1<t - 1, 3>(x, acc[t - 1]); }
  });
  if (L == 0 && (x.m & 7) < 2) {
    ldl<4>(x.vl16, x.rs, OFF_WQ + ((16 + (x.m & 1)) * 28 + 7 * w) * KB, 7 * w * KB);
    ldl<3>(x.vl16, x.rs, OFF_WQ + ((16 + (x.m & 1)) * 28 + 7 * w + 4) * KB, (7 * w + 4) * KB);
  }
  nop<9>();
  pin(acc[0]);
  stamp(x);
  reduce_get(x, acc[0]);
  wv<0>();
  pin(xr[0]); pin(xr[1]);
  if (L > 0) {
    res_epilogue<true>(x, acc[0], xr, 0, 0, tt, nt, out);
  } else {
    res_epilogue<false>(x, acc[0], xr, OFF_X + 2 * SZ_X, OFF_ST + 2 * SZ_ST, tt, nt);
    wv<0>();
    signal(x, FL_X(x.c, th), 1);
  }
}

#define STAMP_SYNC
__global__ __launch_bounds__(256, 1) void k_main(Args) {
  typedef unsigned long long u64x8 __attribute__((ext_vector_type(8)));
  typedef unsigned long long u64x2 __attribute__((ext_vector_type(2)));
  u64x8 pin8; u64x2 pws;
  asm volatile("s_load_dwordx16 s[8:23], %4, 0x0\n\ts_load_dwordx4 s[24:27], %4, 0x40\n\t"
               "s_getpc_b64 s[4:5]\n.Lk_begin:\n\t"
               "s_mov_b32 s6, .Lk_end-.Lk_begin\n\ts_mov_b32 s7, 0x20000\n\ts_and_b32 s5, s5, 0xffff\n\t"
               "v_lshlrev_b32 v1, 4, %2\n\t"
               "s_bfe_u32 s28, %3, 0x20003\n\ts_lshl_b32 s28, s28, 12\n\t"
               "buffer_load_dwordx4 a[112:115], v1, s[4:7], s28 offen\n\t"
               "s_bfe_u32 s28, %3, 0x30004\n\ts_lshl_b32 s28, s28, 13\n\t"
               "buffer_load_dwordx4 a[112:115], v1, s[4:7], s28 offen\n\t"
               "s_add_u32 s28, s28, 4096\n\t"
               "buffer_load_dwordx4 a[112:115], v1, s[4:7], s28 offen\n\t"
               "s_waitcnt lgkmcnt(0)"
               : "={s[8:23]}"(pin8), "={s[24:27]}"(pws) : "v"(threadIdx.x), "{s2}"(blockIdx.x), "{s[0:1]}"(__builtin_amdgcn_kernarg_segment_ptr())
               : "s4", "s5", "s6", "s7", "s28", "v1", "scc", ACLOB);
  const Args a = {(const char*)pin8[0], (const char*)pin8[1], (const char*)pin8[2], (const char*)pin8[3], (const char*)pin8[4], (const char*)pin8[5],
                  (const char*)pin8[6], (const char*)pin8[7], (char*)pws[0], (char*)pws[1]};
  Ctx x;
  x.rs = mkrs(a.ws, WS_USED);
  x.w = __builtin_amdgcn_readfirstlane(threadIdx.x >> 6);
  asm volatile("s_nop 4" : "+s"(x.w));
  { const int b = blockIdx.x; x.c = 2 * (b & 7) + (b >> 7); x.m = (b >> 3) & 15; }
  f32x16 kf[3];
  { Ctx y = x; fresh(y); stamp(y); phase_pre(y, a, kf); STAMP_SYNC }
  for (int L = 0;; ++L) {
    f32x16 g2[3];
    int Lo = L; asm volatile("" : "+s"(Lo));
    { Ctx y = x; fresh(y); phase_att(y, Lo, kf, g2); stamp(y); STAMP_SYNC }
    { Ctx y = x; fresh(y); phase_oproj(y, Lo, a, g2); stamp(y); STAMP_SYNC }
    { Ctx y = x; u32x4 b[DWN][2]; fresh(y); phase_gu(y, Lo, b); stamp(y); fresh(y); phase_down(y, Lo, a.out, b); stamp(y); STAMP_SYNC }
    if (L == 1) break;
    { Ctx y = x; fresh(y); phase_front(y, kf); STAMP_SYNC }
  }
  fresh(x);
  if (blockIdx.x == 0 && wave0(x)) {
    get_tag(x);
    if (x.l == 0) st2<0>(u32x2{x.tlo, x.thi}, 0, x.rs, OFF_TAG);
  }
  stamp_flush(x);
  asm volatile(".Lk_end:");
}

extern "C" void kernel_launch(void* const* d_in, const int* in_sizes, int n_in, void* d_out, int out_size, void* d_ws, size_t ws_size,
                              hipStream_t stream) {
  Args a;
  a.x = (const char*)d_in[0]; a.wq = (const char*)d_in[1]; a.wk = (const char*)d_in[2]; a.wv = (const char*)d_in[3];
  a.wo = (const char*)d_in[4]; a.wg = (const char*)d_in[6]; a.wu = (const char*)d_in[7]; a.wd = (const char*)d_in[8];
  a.ws = (char*)d_ws; a.out = (char*)d_out;
  (void)hipFuncSetAttribute((const void*)k_main, hipFuncAttributeMaxDynamicSharedMemorySize, 160 * 1024);
  k_main<<<256, 256, 160 * 1024, stream>>>(a);
}
```

```cpp
#include <hip/hip_runtime.h>
#include <stdint.h>
#include <type_traits>

typedef float    f32x16 __attribute__((ext_vector_type(16)));
typedef float    f32x4  __attribute__((ext_vector_type(4)));
typedef float    f32x2  __attribute__((ext_vector_type(2)));
typedef unsigned u32x4  __attribute__((ext_vector_type(4)));
typedef unsigned u32x2  __attribute__((ext_vector_type(2)));
typedef __bf16   bf16x2 __attribute__((ext_vector_type(2)));
typedef __amdgpu_buffer_rsrc_t rsrc_t;
#define DI __device__ __forceinline__
#define SB() __builtin_amdgcn_sched_barrier(0)

constexpr int S = 2048, D = 448, F = 1792;
constexpr unsigned KB = 1024;
constexpr unsigned OFF_FLAG = 0;
constexpr unsigned OFF_CSQ  = 64 * KB;
constexpr unsigned OFF_CSGU = OFF_CSQ + 4 * KB;
constexpr unsigned OFF_SLOT = OFF_CSGU + 32 * KB;
constexpr unsigned OFF_TAG  = OFF_SLOT + 32 * KB;
constexpr unsigned OFF_WQ   = 160 * KB;
constexpr unsigned OFF_WO   = OFF_WQ + 18 * 28 * KB;
constexpr unsigned OFF_WGU  = OFF_WO + 14 * 32 * KB;
constexpr unsigned OFF_WD   = OFF_WGU + 16 * 196 * KB;
constexpr unsigned SZ_X = 64 * 28 * KB, SZ_ST = 14 * 2048 * 8, SZ_O = 64 * 32 * KB, SZ_H = 64 * 112 * KB, SZ_KV = 64 * 4 * KB;
constexpr unsigned OFF_X    = OFF_WD + 14 * 112 * KB;
constexpr unsigned OFF_ST   = OFF_X + 4 * SZ_X;
constexpr unsigned OFF_O    = OFF_ST + 4 * SZ_ST;
constexpr unsigned OFF_H    = OFF_O + 2 * SZ_O;
constexpr unsigned OFF_HL   = OFF_H + 2 * SZ_H;
constexpr unsigned OFF_KV   = OFF_HL + 2 * 1024 * KB;
constexpr unsigned WS_USED  = OFF_KV + 2 * SZ_KV;
DI constexpr unsigned FL_X(int c, int th)         { return (c * 16 + th) * 256u; }
DI constexpr unsigned FL_O(int L, int c, int th)  { return (c * 16 + 2 + L * 2 + th) * 256u; }
DI constexpr unsigned FL_X1(int L, int c)         { return (c * 16 + 6 + L) * 256u; }
DI constexpr unsigned FL_H(int L, int c, int p)   { return (c * 16 + 8 + L * 2 + p) * 256u; }
DI constexpr unsigned FL_W(int c)                 { return (c * 16 + 12) * 256u; }
constexpr unsigned FL_W2 = 13 * 256u;

constexpr float QSCALE = 0.18898223650461362f * 1.4426950408889634f;
constexpr float M_LIMIT = 48.0f;

template <int I, int N, class Fn> DI void sfor(Fn&& f) { if constexpr (I < N) { f(std::integral_constant<int, I>{}); sfor<I + 1, N>(f); } }

DI unsigned cvtpk(float lo, float hi) { f32x2 v = {lo, hi}; bf16x2 b = __builtin_convertvector(v, bf16x2); return __builtin_bit_cast(unsigned, b); }
DI float bflo(unsigned u) { return __uint_as_float(u << 16); }
DI float bfhi(unsigned u) { return __uint_as_float(u & 0xffff0000u); }
DI float dot2(unsigned a, unsigned b, float c) { return __builtin_amdgcn_fdot2_f32_bf16(__builtin_bit_cast(bf16x2, a), __builtin_bit_cast(bf16x2, b), c, false); }
DI float sum32(float v) { auto r = __builtin_amdgcn_permlane32_swap(__float_as_uint(v), __float_as_uint(v), false, false); return __uint_as_float(r[0]) + __uint_as_float(r[1]); }
DI float sum16(float v) { auto r = __builtin_amdgcn_permlane16_swap(__float_as_uint(v), __float_as_uint(v), false, false); return __uint_as_float(r[0]) + __uint_as_float(r[1]); }
DI float max32(float v) { auto r = __builtin_amdgcn_permlane32_swap(__float_as_uint(v), __float_as_uint(v), false, false); return fmaxf(__uint_as_float(r[0]), __uint_as_float(r[1])); }

DI unsigned umax16(unsigned v) {
  v = max(v, (unsigned)__builtin_amdgcn_update_dpp(0, (int)v, 0x128, 0xf, 0xf, false));
  v = max(v, (unsigned)__builtin_amdgcn_update_dpp(0, (int)v, 0x124, 0xf, 0xf, false));
  v = max(v, (unsigned)__builtin_amdgcn_update_dpp(0, (int)v, 0x122, 0xf, 0xf, false));
  v = max(v, (unsigned)__builtin_amdgcn_update_dpp(0, (int)v, 0x121, 0xf, 0xf, false));
  return v;
}
DI unsigned umax64(unsigned v) {
  v = umax16(v);
  auto a = __builtin_amdgcn_permlane16_swap(v, v, false, false); v = max(a[0], a[1]);
  auto b = __builtin_amdgcn_permlane32_swap(v, v, false, false); return max(b[0], b[1]);
}

typedef short s16x4 __attribute__((ext_vector_type(4)));
DI u32x2 trread(const char* p) { return __builtin_bit_cast(u32x2, __builtin_amdgcn_ds_read_tr16_b64_v4i16((__attribute__((address_space(3))) s16x4*)p)); }
DI u32x4 trfrag(const char* lo, const char* hi) { const u32x2 a = trread(lo), b = trread(hi); return u32x4{a[0], a[1], b[0], b[1]}; }
DI void row_stats(float s, float q, float& mean, float& rstd) {
  mean = s * (1.0f / D);
  const float var = fmaxf(q * (1.0f / D) - mean * mean, 0.f);
  rstd = __builtin_amdgcn_rsqf(var + 1e-5f);
}
DI float fragsum(const u32x4& f, float s) {
#pragma unroll
  for (int d = 0; d < 4; ++d) s = dot2(f[d], 0x3f803f80u, s);
  return s;
}
struct Args { const char *x, *wq, *wk, *wv, *wo, *wg, *wu, *wd; char* ws; char* out; };

#define ACLOB "a0","a1","a2","a3","a4","a5","a6","a7","a8","a9","a10","a11","a12","a13","a14","a15","a16","a17","a18","a19","a20","a21","a22","a23","a24","a25","a26","a27","a28","a29","a30","a31","a32","a33","a34","a35","a36","a37","a38","a39","a40","a41","a42","a43","a44","a45","a46","a47","a48","a49","a50","a51","a52","a53","a54","a55","a56","a57","a58","a59","a60","a61","a62","a63","a64","a65","a66","a67","a68","a69","a70","a71","a72","a73","a74","a75","a76","a77","a78","a79","a80","a81","a82","a83","a84","a85","a86","a87","a88","a89","a90","a91","a92","a93","a94","a95","a96","a97","a98","a99","a100","a101","a102","a103","a104","a105","a106","a107","a108","a109","a110","a111","a112","a113","a114","a115","a116","a117","a118","a119","a120","a121","a122","a123","a124","a125","a126","a127","a128","a129","a130","a131","a132","a133","a134","a135","a136","a137","a138","a139","a140","a141","a142","a143","a144","a145","a146","a147","a148","a149","a150","a151","a152","a153","a154","a155","a156","a157","a158","a159","a160","a161","a162","a163","a164","a165","a166","a167","a168","a169","a170","a171","a172","a173","a174","a175","a176","a177","a178","a179","a180","a181","a182","a183","a184","a185","a186","a187","a188","a189","a190","a191","a192","a193","a194","a195","a196","a197","a198","a199","a200","a201","a202","a203","a204","a205","a206","a207","a208","a209","a210","a211","a212","a213","a214","a215","a216","a217","a218","a219","a220","a221","a222","a223","a224","a225","a226","a227","a228","a229","a230","a231","a232","a233","a234","a235","a236","a237","a238","a239","a240","a241","a242","a243","a244","a245","a246","a247","a248","a249","a250","a251","a252","a253","a254","a255"

template <int IMM> DI void ld4(u32x4& d, unsigned vo, rsrc_t rs, unsigned so) {
  asm volatile("buffer_load_dwordx4 %0, %1, %2, %3 offen offset:%c4 sc1" : "=v"(d) : "v"(vo), "s"(rs), "s"(so), "i"(IMM));
}
template <int IMM> DI void ld2(f32x2& d, unsigned vo, rsrc_t rs, unsigned so) {
  asm volatile("buffer_load_dwordx2 %0, %1, %2, %3 offen offset:%c4 sc1" : "=v"(d) : "v"(vo), "s"(rs), "s"(so), "i"(IMM));
}
template <int IMM> DI void ld2i(u32x2& d, unsigned vo, rsrc_t rs, unsigned so) {
  asm volatile("buffer_load_dwordx2 %0, %1, %2, %3 offen offset:%c4" : "=v"(d) : "v"(vo), "s"(rs), "s"(so), "i"(IMM));
}
template <int IMM> DI void ld2u(u32x2& d, unsigned vo, rsrc_t rs, unsigned so) {
  asm volatile("buffer_load_dwordx2 %0, %1, %2, %3 offen offset:%c4 sc1" : "=v"(d) : "v"(vo), "s"(rs), "s"(so), "i"(IMM));
}
template <int IMM> DI void ld4f(f32x4& d, unsigned vo, rsrc_t rs, unsigned so) {
  asm volatile("buffer_load_dwordx4 %0, %1, %2, %3 offen offset:%c4 sc1" : "=v"(d) : "v"(vo), "s"(rs), "s"(so), "i"(IMM));
}
DI void ld1(float& d, unsigned vo, rsrc_t rs, unsigned so) {
  asm volatile("buffer_load_dword %0, %1, %2, %3 offen sc1" : "=v"(d) : "v"(vo), "s"(rs), "s"(so));
}
template <int AR, int IMM> DI void lda(unsigned vo, rsrc_t rs, unsigned so) {
  asm volatile("buffer_load_dwordx4 a[%c3:%c4], %0, %1, %2 offen offset:%c5 sc1" :: "v"(vo), "s"(rs), "s"(so), "i"(AR), "i"(AR + 3), "i"(IMM) : ACLOB);
}
template <int N> DI void ldl(unsigned vo, rsrc_t rs, unsigned so, unsigned lds) {
  asm volatile("s_mov_b32 m0, %3\n\ts_nop 0\n\tbuffer_load_dwordx4 %0, %1, %2 offen sc1 lds" :: "v"(vo), "s"(rs), "s"(so), "s"(lds) : "memory");
  if constexpr (N > 1) asm volatile("buffer_load_dwordx4 %0, %1, %2 offen offset:1024 sc1 lds" :: "v"(vo), "s"(rs), "s"(so) : "memory");
  if constexpr (N > 2) asm volatile("buffer_load_dwordx4 %0, %1, %2 offen offset:2048 sc1 lds" :: "v"(vo), "s"(rs), "s"(so) : "memory");
  if constexpr (N > 3) asm volatile("buffer_load_dwordx4 %0, %1, %2 offen offset:3072 sc1 lds" :: "v"(vo), "s"(rs), "s"(so) : "memory");
}
template <int IMM> DI void st4(const u32x4& d, unsigned vo, rsrc_t rs, unsigned so) {
  asm volatile("buffer_store_dwordx4 %0, %1, %2, %3 offen offset:%c4 sc1\n\ts_nop 1" :: "v"(d), "v"(vo), "s"(rs), "s"(so), "i"(IMM) : "memory");
}
template <int IMM> DI void st4p(const u32x4& d, unsigned vo, rsrc_t rs, unsigned so) {
  asm volatile("buffer_store_dwordx4 %0, %1, %2, %3 offen offset:%c4\n\ts_nop 1" :: "v"(d), "v"(vo), "s"(rs), "s"(so), "i"(IMM) : "memory");
}
template <int IMM> DI void st2(const u32x2& d, unsigned vo, rsrc_t rs, unsigned so) {
  asm volatile("buffer_store_dwordx2 %0, %1, %2, %3 offen offset:%c4 sc1" :: "v"(d), "v"(vo), "s"(rs), "s"(so), "i"(IMM) : "memory");
}
DI void st1(float d, unsigned vo, rsrc_t rs, unsigned so) {
  asm volatile("buffer_store_dword %0, %1, %2, %3 offen sc1" :: "v"(d), "v"(vo), "s"(rs), "s"(so) : "memory");
}
template <int N> DI void wv() { asm volatile("s_waitcnt vmcnt(%c0)" :: "i"(N) : "memory"); SB(); }
template <int N> DI void wl() { asm volatile("s_waitcnt lgkmcnt(%c0)" :: "i"(N) : "memory"); SB(); }
template <class T> DI void pin(T& x) { asm volatile("" : "+v"(x)); }
DI void bar() { asm volatile("s_waitcnt lgkmcnt(0)\n\ts_barrier" ::: "memory"); }
template <int N> DI void nop() { asm volatile("s_nop %c0" :: "i"(N)); SB(); }

template <int OFF> DI void trd(u32x2& r, unsigned a) { asm volatile("ds_read_b64_tr_b16 %0, %1 offset:%c2" : "=v"(r) : "v"(a), "i"(OFF)); }
template <int OFF> DI void dsr(u32x4& r, unsigned a) { asm volatile("ds_read_b128 %0, %1 offset:%c2" : "=v"(r) : "v"(a), "i"(OFF)); }

DI void m32(f32x16& c, const u32x4& a, const u32x4& b)  { asm volatile("v_mfma_f32_32x32x16_bf16 %0, %1, %2, %0" : "+v"(c) : "v"(a), "v"(b)); }
DI void m32z(f32x16& c, const u32x4& a, const u32x4& b) { asm volatile("v_mfma_f32_32x32x16_bf16 %0, %1, %2, 0" : "=&v"(c) : "v"(a), "v"(b)); }
template <int AR, bool Z> DI void m32a(f32x16& c, const u32x4& b) {
  if constexpr (Z) asm volatile("v_mfma_f32_32x32x16_bf16 %0, a[%c2:%c3], %1, 0" : "=&v"(c) : "v"(b), "i"(AR), "i"(AR + 3));
  else             asm volatile("v_mfma_f32_32x32x16_bf16 %0, a[%c2:%c3], %1, %0" : "+v"(c) : "v"(b), "i"(AR), "i"(AR + 3));
}
template <bool Z> DI void m16(f32x4& c, const u32x4& a, const u32x4& b) {
  if constexpr (Z) asm volatile("v_mfma_f32_16x16x32_bf16 %0, %1, %2, 0" : "=&v"(c) : "v"(a), "v"(b));
  else             asm volatile("v_mfma_f32_16x16x32_bf16 %0, %1, %2, %0" : "+v"(c) : "v"(a), "v"(b));
}

template <bool Z, int AR> DI void m16a(f32x4& c, const u32x4& a) {
  if constexpr (Z) asm volatile("v_mfma_f32_16x16x32_bf16 %0, %1, a[%c2:%c3], 0" : "=&v"(c) : "v"(a), "i"(AR), "i"(AR + 3));
  else             asm volatile("v_mfma_f32_16x16x32_bf16 %0, %1, a[%c2:%c3], %0" : "+v"(c) : "v"(a), "i"(AR), "i"(AR + 3));
}
DI void ldflag(unsigned& v, rsrc_t rs, unsigned so) { asm volatile("buffer_load_dword %0, off, %1, %2 sc1" : "=v"(v) : "s"(rs), "s"(so)); }

struct Ctx {
  rsrc_t rs;
  unsigned tlo, thi;
  unsigned vl16;
  unsigned vg16;
  int w, l, c, m;
};
DI void fresh(Ctx& x) {
  asm volatile("" : "+s"(x.w), "+s"(x.c), "+s"(x.m));
  asm volatile("v_mbcnt_lo_u32_b32 %0, -1, 0\n\tv_mbcnt_hi_u32_b32 %0, -1, %0" : "=v"(x.l));
  x.vl16 = x.l * 16;
  const int g = x.l >> 4;
  x.vg16 = ((g >> 1) * 64 + (g & 1) * 32 + (x.l & 15)) * 16;
}
extern __shared__ __attribute__((aligned(16))) char lds[];
constexpr unsigned SCR = 112 * KB;

DI void get_tag(Ctx& x) {
  unsigned lo, hi;
  asm volatile("v_accvgpr_read_b32 v255, a252\n\ts_nop 1\n\tv_readfirstlane_b32 %0, v255\n\tv_accvgpr_read_b32 v255, a253\n\ts_nop 1\n\tv_readfirstlane_b32 %1, v255\n\ts_nop 4"
               : "=s"(lo), "=s"(hi) :: "v255");
  const unsigned long long e = ((unsigned long long)hi << 32) | lo;
  unsigned long long n = e * 0x9E3779B97F4A7C15ull + 0x632BE59BD9B4E019ull;
  if (((n ^ e) >> 16) == 0) n += 0x10000;
  x.tlo = (unsigned)n; x.thi = (unsigned)(n >> 32);
}
DI bool wave0(const Ctx& x) { int w = x.w; asm volatile("" : "+s"(w)); return w == 0; }
DI void count(Ctx& x, unsigned fl, int n) {
  if (wave0(x)) {
    const unsigned long long mask = (1ull << n) - 1;
    unsigned long long keep;
    asm volatile("s_mov_b64 %0, exec\n\ts_mov_b64 exec, %5\n\tbuffer_atomic_add %1, %2, %3, %4 offen\n\ts_mov_b64 exec, %0"
                 : "=&s"(keep) : "v"(1u), "v"(x.l * 256), "s"(x.rs), "s"(OFF_FLAG + fl), "s"(mask) : "memory");
  }
}
DI void signal(Ctx& x, unsigned fl, int n) { bar(); count(x, fl, n); }
DI void await(Ctx& x, unsigned fl, unsigned target) {
  if (wave0(x)) {
#pragma nounroll
    for (unsigned spin = 0; spin < (1u << 18); ++spin) {
      unsigned v;
      asm volatile("buffer_load_dword %0, off, %1, %2 sc1\n\ts_waitcnt vmcnt(0)" : "=v"(v) : "s"(x.rs), "s"(OFF_FLAG + fl) : "memory");
      if ((unsigned)__builtin_amdgcn_readfirstlane(v) >= target) break;
      __builtin_amdgcn_s_sleep(1);
    }
  }
  bar();
}

DI void recheck(Ctx& x, unsigned v, unsigned fl, unsigned target) {
  pin(v);
#pragma nounroll
  for (unsigned spin = 0; (unsigned)__builtin_amdgcn_readfirstlane(v) < target && spin < (1u << 18); ++spin) {
    __builtin_amdgcn_s_sleep(2);
    asm volatile("buffer_load_dword %0, off, %1, %2 sc1\n\ts_waitcnt vmcnt(0)" : "=v"(v) : "s"(x.rs), "s"(OFF_FLAG + fl) : "memory");
  }
  SB();
}

DI void reduce_put(Ctx& x, const f32x16 (&acc)[4]) {
  sfor<1, 4>([&](auto k) {
    f32x4* p = (f32x4*)(lds + SCR + ((x.w ^ k) * 3 + k - 1) * 4096 + x.vl16);
#pragma unroll
    for (int q = 0; q < 4; ++q) p[q * 64] = f32x4{acc[k][4 * q], acc[k][4 * q + 1], acc[k][4 * q + 2], acc[k][4 * q + 3]};
  });
}
template <int K, int Q> DI void red_put1(Ctx& x, const f32x16& t) {
  asm volatile("ds_write_b128 %0, %1 offset:%c2" :: "v"(SCR + ((x.w ^ K) * 3 + K - 1) * 4096 + x.vl16),
               "v"(__builtin_shufflevector(t, t, 4 * Q, 4 * Q + 1, 4 * Q + 2, 4 * Q + 3)), "i"(Q * 1024) : "memory");
}
DI void reduce_get(Ctx& x, f32x16& own) {
  bar();
  const f32x4* p = (const f32x4*)(lds + SCR + x.w * 3 * 4096 + x.vl16);
#pragma unroll
  for (int k = 0; k < 3; ++k)
#pragma unroll
    for (int q = 0; q < 4; ++q) {
      f32x4 v = p[k * 256 + q * 64];
#pragma unroll
      for (int i = 0; i < 4; ++i) own[4 * q + i] += v[i];
    }
}

constexpr int A_WO = 184, A_WD = 0, A_GX = 224;
constexpr int dn_ks(int o) { return o < 20 ? 2 * ((o >> 1) % 5 + ((o >> 1) % 5 >= 2) + 7 * (o / 10)) + (o & 1) : 2 * (((o - 20) >> 1) * 4 - ((o - 20) >> 2) + 2) + (o & 1); }
static_assert(dn_ks(0) == 0 && dn_ks(4) == 6 && dn_ks(9) == 11 && dn_ks(10) == 14 && dn_ks(19) == 25 && dn_ks(20) == 4 && dn_ks(22) == 12 && dn_ks(24) == 18 && dn_ks(27) == 27, "");
struct Two { unsigned s0, s1; };
constexpr int SL_WQ = 56;
template <int J> DI void pf_wq(Ctx& x) {
  ldl<1>(x.vl16, x.rs, OFF_WQ + ((x.m & 7) * 56 + 14 * x.w + J) * KB, (SL_WQ + 14 * x.w + J) * KB);
}
DI Two base_wo(const Ctx& x) { const int ct = x.m % 7;
  return Two{OFF_WO + ((2 * ct + (x.w & 1)) * 32 + 8 * x.w) * KB, OFF_WO + ((2 * ct + ((x.w & 1) ^ 1)) * 32 + 8 * x.w) * KB}; }
DI void pf_wo(Ctx& x) {
  const Two b = base_wo(x);
  sfor<0, 8>([&](auto i) {
    lda<A_WO + 8 * i, (i & 3) * 1024>(x.vl16, x.rs, b.s0 + (i >> 2) * 4096);
    lda<A_WO + 8 * i + 4, (i & 3) * 1024>(x.vl16, x.rs, b.s1 + (i >> 2) * 4096);
  });
}
DI Two base_wd(const Ctx& x) { const int ct = x.m % 7;
  return Two{OFF_WD + ((2 * ct + (x.w & 1)) * 112 + 28 * x.w) * KB, OFF_WD + ((2 * ct + ((x.w & 1) ^ 1)) * 112 + 28 * x.w) * KB}; }
template <int I0, int I1> DI void pf_wd(Ctx& x, const Two& b) {
  sfor<I0, I1>([&](auto f) {
    constexpr int i = f >> 1;
    lda<A_WD + 4 * f, (i & 3) * 1024>(x.vl16, x.rs, ((f & 1) ? b.s1 : b.s0) + (i >> 2) * 4096);
  });
}
DI void pf_wgu1(Ctx& x) {
  for (int t = 0; t < 14; ++t) ldl<2>(x.vl16, x.rs, OFF_WGU + (x.m * 196 + 8 * t + 2 * x.w) * KB, (8 * t + 2 * x.w) * KB);
}
template <int J> DI void pf_wgu3(Ctx& x) {
  ldl<1>(x.vl16, x.rs, OFF_WGU + (x.m * 196 + 160 + 9 * x.w + J) * KB, (9 * x.w + J) * KB);
}

constexpr unsigned S_QP = SCR, S_KP = SCR + 16384, S_QF = SCR + 24576, S_ST = SCR + 37888, S_CS = SCR + 38912, S_KM = SCR + 36864, S_TR = SCR + 40960;
DI unsigned kv_finish(Ctx& x, const float (&raw)[16], const f32x4 (&cs)[4], float mean, float rstd, bool isv, unsigned so, int slot) {
  const int h = x.l >> 5, r = x.l & 31;
  u32x4 y[2];
#pragma unroll
  for (int d = 0; d < 8; ++d)
    y[d >> 2][d & 3] = cvtpk((raw[2 * d] - mean * cs[d >> 1][2 * (d & 1)]) * rstd, (raw[2 * d + 1] - mean * cs[d >> 1][2 * (d & 1) + 1]) * rstd);
  if (h) { y[1][2] = 0; y[1][3] = 0; }
  unsigned nb = 0;
  if (!isv) {
    u32x4 y1 = y[1];
    if (h) y1[2] = 0x3f80u;
    st4<0>(y[0], x.vl16, x.rs, so); st4<1024>(y1, x.vl16, x.rs, so);
    float n2 = 0.f, n3 = 0.f;
#pragma unroll
    for (int d = 0; d < 4; ++d) { n2 = dot2(y[0][d], y[0][d], n2); n3 = dot2(y[1][d], y[1][d], n3); }
    n2 = sum32(n2 + n3);
    nb = umax64(n2 < 3.0e38f ? __float_as_uint(n2) : 0x7f800000u);
    nb = cvtpk(1.01f * __builtin_amdgcn_sqrtf(__uint_as_float(nb)), 0.f);
  } else {
    if (h) y[1][2] = 0x3f80u;
    char* im = lds + S_TR + slot * 2048;
#pragma unroll
    for (int a = 0; a < 4; ++a) *(u32x2*)(im + r * 64 + 16 * a + 8 * h) = u32x2{y[a >> 1][2 * (a & 1)], y[a >> 1][2 * (a & 1) + 1]};
    asm volatile("s_waitcnt lgkmcnt(0)" ::: "memory");
    const char* tb = im + (4 * h + ((x.l >> 2) & 3)) * 64 + ((x.l >> 4) & 1) * 32 + (x.l & 3) * 8;
    const u32x4 v0 = trfrag(tb, tb + 512), v1 = trfrag(tb + 1024, tb + 1536);
    st4<0>(v0, x.vl16, x.rs, so); st4<1024>(v1, x.vl16, x.rs, so);
  }
  return nb;
}
DI void kv_publish(Ctx& x, int L, int p, unsigned km) {
  get_tag(x);
  st2<0>(u32x2{(x.tlo & 0xffff0000u) | km, x.thi}, x.l < 16 ? x.l * 1024 : ~0u, x.rs, OFF_SLOT + (L * 2048 + p) * 8);
}
DI void q_finish(Ctx& x, const f32x16& qa, const f32x4 (&cq)[4], float mean, float rstd) {
  const int h = x.l >> 5;
  u32x4 qf[2];
  const f32x4* p = (const f32x4*)(lds + S_QP + x.w * 4096 + x.vl16);
  const float sc = rstd * QSCALE;
#pragma unroll
  for (int q = 0; q < 4; ++q) {
    const f32x4 o = p[q * 64];
    float v[4];
#pragma unroll
    for (int i = 0; i < 4; ++i) v[i] = (qa[4 * q + i] + o[i] - mean * cq[q][i]) * sc;
    qf[q >> 1][2 * (q & 1)] = cvtpk(v[0], v[1]); qf[q >> 1][2 * (q & 1) + 1] = cvtpk(v[2], v[3]);
  }
  if (h) { qf[1][2] = 0; qf[1][3] = 0; }
  float qn2 = 0.f, qn3 = 0.f;
#pragma unroll
  for (int d = 0; d < 4; ++d) { qn2 = dot2(qf[0][d], qf[0][d], qn2); qn3 = dot2(qf[1][d], qf[1][d], qn3); }
  qn2 = sum32(qn2 + qn3);
  if (h) qf[1][2] = cvtpk(1.01f * __builtin_amdgcn_sqrtf(qn2), 0.f);
  ((u32x4*)(lds + S_QF + x.w * 2048 + x.vl16))[0] = qf[0];
  ((u32x4*)(lds + S_QF + x.w * 2048 + x.vl16))[64] = qf[1];
}
DI void put_tile(char* p, const f32x16& t) {
#pragma unroll
  for (int q = 0; q < 4; ++q) ((f32x4*)p)[q * 64] = f32x4{t[4 * q], t[4 * q + 1], t[4 * q + 2], t[4 * q + 3]};
}

constexpr unsigned IMG_Q = 0, IMG_T = 56 * KB;
DI rsrc_t mkrs(const char* p, unsigned n) { return __builtin_amdgcn_make_buffer_rsrc((char*)p, 0, n, 0x00020000); }

DI void ask_slots(Ctx& x, int L) {
  if (wave0(x)) asm volatile("buffer_load_dwordx4 a[248:251], %0, %1, %2 offen sc1" :: "v"(x.vl16), "s"(x.rs), "s"(OFF_SLOT + (L * 16 + x.c) * 1024) : ACLOB);
}
DI void kv_wait(Ctx& x, int L) {
  if (wave0(x)) get_tag(x);
  wv<0>();
  if (wave0(x)) {
    unsigned s0, s1, s2, s3;
#pragma nounroll
    for (unsigned spin = 0; spin < (1u << 18); ++spin) {
      asm volatile("v_accvgpr_read_b32 %0, a248\n\tv_accvgpr_read_b32 %1, a249\n\tv_accvgpr_read_b32 %2, a250\n\tv_accvgpr_read_b32 %3, a251" : "=v"(s0), "=v"(s1), "=v"(s2), "=v"(s3));
      if (__all(s1 == x.thi && s3 == x.thi && (((s0 ^ x.tlo) | (s2 ^ x.tlo)) >> 16) == 0)) break;
      __builtin_amdgcn_s_sleep(2);
      ask_slots(x, L); wv<0>();
    }
    const unsigned km = umax64(max(s0 << 16, s2 << 16));
    if (x.l == 0) ((unsigned*)(lds + S_KM))[4] = km;
  }
  bar();
}
DI void ask_kv(Ctx& x, int L, f32x16 (&kf)[3]) {
  const unsigned kvs = OFF_KV + L * SZ_KV + 16 * x.w * 4096;
  asm volatile(".irp i,0,1,2,3\n\tbuffer_load_dwordx4 v[128+4*\\i:131+4*\\i], %3, %4, %5 offen offset:1024*\\i sc1\n\t.endr\n\t"
               ".irp i,0,1,2,3\n\tbuffer_load_dwordx4 v[144+4*\\i:147+4*\\i], %3, %4, %6 offen offset:1024*\\i sc1\n\t.endr\n\t"
               ".irp i,0,1,2,3\n\tbuffer_load_dwordx4 v[160+4*\\i:163+4*\\i], %3, %4, %7 offen offset:1024*\\i sc1\n\t.endr"
               : "={v[128:143]}"(kf[0]), "={v[144:159]}"(kf[1]), "={v[160:175]}"(kf[2]) : "{v204}"(x.vl16), "s"(x.rs), "s"(kvs), "s"(kvs + 4096), "s"(kvs + 8192));
}

DI void phase_pre(Ctx& x, const Args& a, f32x16 (&kf)[3]) {
  const int w = x.w, l = x.l, h = l >> 5, r = l & 31;
  const int th = x.m >> 3, hp = x.m & 7, tl = w & 1, kh = w >> 1, tt = 4 * x.c + 2 * th + tl, wi = 2 * x.c + th;
  const bool kvp = hp < 2;
  u32x4 xf[14];
  { const rsrc_t rx = mkrs(a.x, S * D * 2);
    sfor<0, 16>([&](auto i) {
      const int c = r ^ ((2 * i + h) & 15);
      ldl<1>(c < 28 ? h * 896 + c * 16 : 0x80000000u, rx, (tt * 32 + 2 * i) * 896 + kh * 448, (84 + 16 * w + i) * KB);
    }); }
  if (x.m == 2) asm volatile("buffer_load_dword a116, %0, %1, 0 offen sc1" :: "v"(l < 16 ? OFF_FLAG + (x.c * 16 + l) * 256 : OFF_SLOT + ((l >> 3) & 1) * 16384 + x.c * 1024 + (l & 7) * 128), "s"(x.rs) : ACLOB);
  asm volatile("buffer_load_dwordx2 a[252:253], off, %0, %1 sc1" :: "s"(x.rs), "s"(OFF_TAG) : ACLOB);
  const int task = wi * 6 + hp - 2;
  const rsrc_t rq = mkrs(a.wq, D * D * 2);
  auto ask_q = [&](auto i) {
    ldl<1>((8 * w + (l >> 3)) * 896 + (l & 7) * 16, rq, hp * 112 + i * 32 * 896, IMG_Q + (4 * i + w) * KB);
  };
  auto ask_t = [&](const char* tp, unsigned tn, unsigned ts, unsigned tb) {
    const rsrc_t rt = mkrs(tp, tn);
    const unsigned vo = (8 * w + (l >> 3)) * ts + (l & 7) * 16;
    sfor<0, 7>([&](auto i) { ldl<1>(vo, rt, tb + i * 32 * ts, IMG_T + (4 * i + w) * KB); });
  };
  const int tco = ((l >> 4) & 1) * 32 + (l & 3) * 8;
  const int sro = 4 * h + ((l >> 2) & 3);
  if (kvp) ask_t(hp ? a.wv : a.wk, D * 56, 128, 0);
  else if (task < 112) ask_t(task < 56 ? a.wg : a.wu, D * F * 2, F * 2, (task & 1) * 224 * F * 2 + ((task % 56) >> 1) * 128);
  else if (task < 168) ask_t(a.wd, D * F * 2, 896, ((task - 112) / 7) * 224 * 896 + ((task - 112) % 7) * 128);
  else ask_t(a.wo, task < 182 ? D * D * 2 : 0, 896, ((task - 168) / 7) * 224 * 896 + ((task - 168) % 7) * 128);
  wv<8>();
  sfor<0, 14>([&](auto i) { dsr<0>(xf[i], (84 + w * 16) * KB + r * 512 + (((2 * i + h) ^ (r & 15)) << 4)); });
  wl<0>();
  {
    float s = 0.f, q = 0.f, s1 = 0.f, q1 = 0.f;
    sfor<0, 14>([&](auto i) {
      ask_q(i);
      pin(xf[i]);
      s = dot2(xf[i][0], 0x3f803f80u, s); q = dot2(xf[i][0], xf[i][0], q); s1 = dot2(xf[i][1], 0x3f803f80u, s1); q1 = dot2(xf[i][1], xf[i][1], q1);
      s = dot2(xf[i][2], 0x3f803f80u, s); q = dot2(xf[i][2], xf[i][2], q); s1 = dot2(xf[i][3], 0x3f803f80u, s1); q1 = dot2(xf[i][3], xf[i][3], q1);
      pin(s); pin(q); pin(s1); pin(q1);
    });
    s = sum32(s + s1); q = sum32(q + q1);
    if (h == 0) ((f32x2*)(lds + S_ST))[w * 32 + r] = f32x2{s, q};
  }
  wv<14>();
  bar();
  if (!kvp) {
    if (task < 112) {
      const int g = l >> 4, mat = task >= 56, gt = 4 * ((task % 56) >> 1) + w, m = gt / 7, j = gt % 7, lt = (m & 1) ? j : (j == 6 ? 0 : j + 1);
      const char* tb = lds + IMG_T + (16 * (g >> 1) + 4 * (g & 1) + ((l >> 2) & 3)) * 128 + 32 * w + (l & 3) * 8;
      const unsigned st = lt < 5 ? 10 * KB : 4 * KB, so = OFF_WGU + (m * 196 + (lt < 5 ? lt * 2 + mat : 140 + (lt - 5) * 2 + mat)) * KB + (task & 1) * 7 * st;
      u32x4 f[7];
      sfor<0, 7>([&](auto i) { f[i] = trfrag(tb + i * 4096, tb + i * 4096 + 1024); });
      float cs = 0.f;
      sfor<0, 7>([&](auto i) { st4<0>(f[i], x.vl16, x.rs, so + i * st); cs = fragsum(f[i], cs); });
      cs = sum32(sum16(cs));
      st1(cs, l < 16 ? l * 4 : ~0u, x.rs, OFF_CSGU + m * 2048 + (task & 1) * 1024 + (lt * 2 + mat) * 64);
    } else if (task < 168) {
      const int nt = 2 * ((task - 112) % 7) + (w & 1), rb = (task - 112) / 7;
      const char* tb = lds + IMG_T + sro * 128 + 64 * (w & 1) + tco;
      const unsigned so = OFF_WD + (nt * 112 + 28 * (rb >> 1)) * KB, sa = so + (rb & 1) * 10 * KB, sb = so + (20 + (rb & 1) * 4) * KB;
      u32x4 f[7];
      if (w < 2) {
        sfor<0, 7>([&](auto i) { const char* p = tb + (32 * (i >> 1) + 8 * (i & 1)) * 128; f[i] = trfrag(p, p + 2048); });
        sfor<0, 7>([&](auto i) { st4<0>(f[i], x.vl16, x.rs, i < 4 ? sa + i * KB : (i < 6 ? sb + (i - 4) * KB : sa + 4 * KB)); });
      } else {
        sfor<0, 7>([&](auto i) { const char* p = tb + (32 * ((i + 7) >> 1) + 8 * ((i + 7) & 1)) * 128; f[i] = trfrag(p, p + 2048); });
        sfor<0, 7>([&](auto i) { st4<0>(f[i], x.vl16, x.rs, i < 5 ? sa + (5 + i) * KB : sb + (i - 3) * KB); });
      }
      st1(0.f, ~0u, x.rs, 0);
    } else {
      const int nt = 2 * ((task - 168) % 7) + (w & 1);
      const char* tb = lds + IMG_T + (112 * (w >> 1) + sro) * 128 + 64 * (w & 1) + tco;
      const unsigned vo = task < 182 ? x.vl16 : ~0u - 4096;
      u32x4 f[8];
      sfor<0, 8>([&](auto i) { const char* p = tb + (28 * (i >> 1) + 16 * (i & 1)) * 128; f[i] = trfrag(p, p + 1024); if ((i & 1) && h) { f[i][2] = 0; f[i][3] = 0; } });
      sfor<0, 8>([&](auto i) { st4<(i & 3) * 1024>(f[i], vo, x.rs, OFF_WO + (nt * 32 + 16 * ((task - 168) / 7) + 8 * (w >> 1) + (i & ~3)) * KB); });
    }
  }
  if (!kvp) { wv<8>(); bar(); }
  float mean, rstd;
  { const f32x2 p0 = ((const f32x2*)(lds + S_ST))[tl * 32 + r], p1 = ((const f32x2*)(lds + S_ST))[(tl + 2) * 32 + r];
    row_stats(p0[0] + p1[0], p0[1] + p1[1], mean, rstd); }
  float* csw = (float*)(lds + S_CS);
  const int tro = 8 * h + ((l >> 2) & 3);
  u32x4 ones = {0x3f803f80u, 0x3f803f80u, 0x3f803f80u, 0x3f803f80u};
  pin(ones);
  if (kvp) {
    f32x16 kv, cs;
    const unsigned tb = IMG_T + (224 * kh + tro) * 56 + tco;
    u32x2 f[14][2];
    auto rd = [&](auto i) { trd<i * 896>(f[i][0], tb); trd<i * 896 + 224>(f[i][1], tb); };
    sfor<0, 7>(rd);
    sfor<0, 14>([&](auto i) {
      if constexpr (i + 7 < 14) { wl<12>(); rd(std::integral_constant<int, i + 7>{}); } else wl<2 * (13 - i)>();
      pin(f[i][0]); pin(f[i][1]);
      u32x4 g = {f[i][0][0], f[i][0][1], f[i][1][0], f[i][1][1]};
      pin(g); nop<0>();
      if constexpr (i == 0) { m32z(kv, g, xf[i]); m32z(cs, g, ones); } else { m32(kv, g, xf[i]); m32(cs, g, ones); }
    });
    nop<11>(); pin(kv); pin(cs);
    if (r == 0) sfor<0, 4>([&](auto q) { *(f32x4*)(csw + (w * 3 + 2) * 32 + 8 * q + 4 * h) = f32x4{cs[4 * q], cs[4 * q + 1], cs[4 * q + 2], cs[4 * q + 3]}; });
    if (kh) put_tile(lds + S_KP + tl * 4096 + x.vl16, kv);
    bar();
    if (!kh) {
      if (x.m == 0 && w == 0) st1(0.f, l < 16 ? l * 256 : ~0u, x.rs, OFF_FLAG + x.c * 4096);
      float raw[16]; f32x4 c4[4];
      sfor<0, 4>([&](auto q) {
        const f32x4 o = ((const f32x4*)(lds + S_KP + tl * 4096 + x.vl16))[q * 64];
        c4[q] = *(const f32x4*)(csw + (w * 3 + 2) * 32 + 8 * q + 4 * h) + *(const f32x4*)(csw + ((w + 2) * 3 + 2) * 32 + 8 * q + 4 * h);
#pragma unroll
        for (int i = 0; i < 4; ++i) raw[4 * q + i] = kv[4 * q + i] + o[i];
      });
      const unsigned km = kv_finish(x, raw, c4, mean, rstd, hp == 1, OFF_KV + (tt * 4 + 2 * hp) * KB, tl);
      wv<0>();
      kv_publish(x, 0, 2 * tt + hp, km);
      if (tt == 0 && r == 0) sfor<0, 4>([&](auto q) { st4<32 * q>(__builtin_bit_cast(u32x4, c4[q]), h * 16, x.rs, OFF_CSQ + (16 + hp) * 128); });
    } else {
      wv<0>();
    }
    bar();
  }
  f32x16 qa;
  {
    f32x16 q1, q2, cs;
    const unsigned t1 = IMG_Q + (224 * kh + tro) * 128 + tco + 56 * tl, t2 = IMG_Q + (224 * kh + tro) * 128 + tco + 56 * (tl ^ 1);
    u32x2 f[14][4];
    auto rd = [&](auto i) { trd<i * 2048>(f[i][0], t1); trd<i * 2048 + 512>(f[i][1], t1); trd<i * 2048>(f[i][2], t2); trd<i * 2048 + 512>(f[i][3], t2); };
    wl<0>();
    sfor<0, 3>(rd);
    sfor<0, 14>([&](auto i) {
      if constexpr (i + 3 < 14) { wl<8>(); rd(std::integral_constant<int, i + 3>{}); } else wl<4 * (13 - i)>();
      sfor<0, 4>([&](auto e) { pin(f[i][e]); });
      u32x4 g1 = {f[i][0][0], f[i][0][1], f[i][1][0], f[i][1][1]}, g2 = {f[i][2][0], f[i][2][1], f[i][3][0], f[i][3][1]};
      pin(g1); pin(g2); nop<0>();
      if constexpr (i == 0) { m32z(q1, g1, xf[i]); m32z(q2, g2, xf[i]); m32z(cs, g1, ones); } else { m32(q1, g1, xf[i]); m32(q2, g2, xf[i]); m32(cs, g1, ones); }
    });
    nop<11>(); pin(q1); pin(q2); pin(cs);
    if (r == 0) sfor<0, 4>([&](auto q) { *(f32x4*)(csw + (w * 3) * 32 + 8 * q + 4 * h) = f32x4{cs[4 * q], cs[4 * q + 1], cs[4 * q + 2], cs[4 * q + 3]}; });
    if (kh == tl) { put_tile(lds + S_QP + (w ^ 2) * 4096 + x.vl16, q2); qa = q1; } else { put_tile(lds + S_QP + (w ^ 2) * 4096 + x.vl16, q1); qa = q2; }
  }
  if (th == 0 && x.c < 14) {
    const int f = 4 * x.c + w, hd = f >= 28, ks = f - 28 * hd;
    const char* tb = lds + IMG_Q + (16 * ks + sro) * 128 + tco + 56 * hd;
    st4<0>(trfrag(tb, tb + 1024), x.vl16, x.rs, OFF_WQ + (hp * 56 + f) * KB);
  }
  if (kvp && th == 1 && x.c < 7) {
    const char* tb = lds + IMG_T + (16 * (4 * x.c + w) + sro) * 56 + tco;
    st4<0>(trfrag(tb, tb + 448), x.vl16, x.rs, OFF_WQ + ((16 + hp) * 28 + 4 * x.c + w) * KB);
  }
  if (!kvp) __builtin_amdgcn_s_sleep(12);
  ask_slots(x, 0);
  kv_wait(x, 0);
  count(x, FL_W(x.c), 1);
  ask_kv(x, 0, kf);
  const bool own = x.c == hp && th == 0;
  {
    f32x4 cq[4];
    sfor<0, 4>([&](auto q) {
      cq[q] = *(const f32x4*)(csw + (kh * 3) * 32 + 8 * q + 4 * h) + *(const f32x4*)(csw + ((kh + 2) * 3) * 32 + 8 * q + 4 * h);
      if (own && tl == 0 && r == 0) st4<32 * q>(__builtin_bit_cast(u32x4, cq[q]), h * 16, x.rs, OFF_CSQ + (2 * hp + kh) * 128);
    });
    q_finish(x, qa, cq, mean, rstd);
  }
}

DI void phase_front(Ctx& x, f32x16 (&kf)[3]) {
  const int w = x.w, l = x.l, h = l >> 5, r = l & 31;
  const int th = x.m >> 3, hp = x.m & 7, tl = w & 1, kh = w >> 1, tt = 4 * x.c + 2 * th + tl;
  const unsigned KV = OFF_KV + SZ_KV;
  {
    f32x4 cq[4], ck[4];
    sfor<0, 4>([&](auto q) { ld4f<32 * q>(cq[q], h * 16, x.rs, OFF_CSQ + (2 * hp + kh) * 128); ld4f<32 * q>(ck[q], h * 16, x.rs, OFF_CSQ + (16 + (hp & 1)) * 128); });
    wv<0>();
    await(x, FL_X(x.c, th), 7);
    u32x4 xf[14]; f32x2 sp[7];
    {
      const unsigned so = OFF_X + 2 * SZ_X + (tt * 28 + 14 * kh) * KB;
      sfor<0, 7>([&](auto i) { ld4<(i & 3) * 1024>(xf[i], x.vl16, x.rs, so + (i >> 2) * 4096); });
      sfor<0, 7>([&](auto i) { ld2<0>(sp[i], (h * 7 * 2048 + r) * 8, x.rs, OFF_ST + 2 * SZ_ST + (i * 2048 + tt * 32) * 8); });
      sfor<7, 14>([&](auto i) { ld4<(i & 3) * 1024>(xf[i], x.vl16, x.rs, so + (i >> 2) * 4096); });
    }
    float mean, rstd;
    auto stats = [&]() {
      sfor<0, 7>([&](auto i) { pin(sp[i]); });
      sfor<0, 4>([&](auto i) { pin(cq[i]); pin(ck[i]); });
      float s = 0.f, q = 0.f;
#pragma unroll
      for (int i = 0; i < 7; ++i) { s += sp[i][0]; q += sp[i][1]; }
      row_stats(sum32(s), sum32(q), mean, rstd);
      pin(mean); pin(rstd); SB();
    };
    f32x16 qa, qb;
    u32x4 wa[14], wb[14];
    const unsigned pa = x.vl16 + (SL_WQ + 28 * kh + 14 * kh) * KB, pb = x.vl16 + (SL_WQ + 28 * (kh ^ 1) + 14 * kh) * KB;
    if (hp < 2) {
      f32x16 kv;
      sfor<0, 14>([&](auto i) { dsr<i * 1024>(wa[i], x.vl16 + 14 * kh * KB); });
      sfor<0, 14>([&](auto i) {
        wv<(i < 7 ? 20 : 13) - i>(); wl<13 - i>();
        if constexpr (i == 0) m32z(kv, wa[i], xf[i]); else m32(kv, wa[i], xf[i]);
        if constexpr (i == 7) stats();
      });
      nop<11>(); pin(kv);
      if (kh) put_tile(lds + S_KP + tl * 4096 + x.vl16, kv);
      bar();
      unsigned km = 0;
      if (!kh) {
        float raw[16];
        sfor<0, 4>([&](auto q) {
          const f32x4 o = ((const f32x4*)(lds + S_KP + tl * 4096 + x.vl16))[q * 64];
#pragma unroll
          for (int i = 0; i < 4; ++i) raw[4 * q + i] = kv[4 * q + i] + o[i];
        });
        km = kv_finish(x, raw, ck, mean, rstd, hp == 1, KV + (tt * 4 + 2 * hp) * KB, tl);
      }
      wl<0>();
      sfor<0, 7>([&](auto i) { dsr<i * 1024>(wb[i], pb); dsr<i * 1024>(wa[i], pa); });
      sfor<0, 14>([&](auto i) {
        if constexpr (i < 7) wl<12>(); else wl<2 * (13 - i)>();
        if constexpr (i == 0) { m32z(qb, wb[i], xf[i]); m32z(qa, wa[i], xf[i]); } else { m32(qb, wb[i], xf[i]); m32(qa, wa[i], xf[i]); }
        if constexpr (i < 7) { dsr<(i + 7) * 1024>(wb[i + 7], pb); dsr<(i + 7) * 1024>(wa[i + 7], pa); }
        if constexpr (i == 6) { if (!kh) { wv<0>(); kv_publish(x, 1, 2 * tt + hp, km); } SB(); }
      });
    } else {
      sfor<0, 7>([&](auto i) { dsr<i * 1024>(wb[i], pb); dsr<i * 1024>(wa[i], pa); });
      sfor<0, 14>([&](auto i) {
        wv<(i < 7 ? 20 : 13) - i>();
        if constexpr (i < 7) wl<12>(); else wl<2 * (13 - i)>();
        if constexpr (i == 0) { m32z(qb, wb[i], xf[i]); m32z(qa, wa[i], xf[i]); } else { m32(qb, wb[i], xf[i]); m32(qa, wa[i], xf[i]); }
        if constexpr (i < 7) { dsr<(i + 7) * 1024>(wb[i + 7], pb); dsr<(i + 7) * 1024>(wa[i + 7], pa); }
        if constexpr (i == 7) stats();
      });
    }
    nop<11>();
    pin(qa); pin(qb);
    put_tile(lds + S_QP + (w ^ 2) * 4096 + x.vl16, qb);
    bar();
    q_finish(x, qa, cq, mean, rstd);
    if (hp >= 2) __builtin_amdgcn_s_sleep(36);
    ask_slots(x, 1);
    kv_wait(x, 1);
    ask_kv(x, 1, kf);
  }
}

DI void phase_att(Ctx& x, const int L, f32x16 (&kf)[3], f32x16 (&g2)[3]) {
  const int w = x.w, l = x.l, h = l >> 5, r = l & 31;
  const int th = x.m >> 3, hp = x.m & 7, tl = w & 1, kh = w >> 1, tt = 4 * x.c + 2 * th + tl;
  const unsigned kvs = OFF_KV + L * SZ_KV + 16 * w * 4096;
  if (L == 0) bar();
  unsigned kma = S_KM + 16; asm volatile("" : "+s"(kma));
  const float kmx = *(const float*)(lds + kma);
  u32x4 q[4][2];
#pragma unroll
  for (int k = 0; k < 4; ++k) {
    q[k][0] = ((const u32x4*)(lds + S_QF + (w ^ k) * 2048 + x.vl16))[0];
    q[k][1] = ((const u32x4*)(lds + S_QF + (w ^ k) * 2048 + x.vl16))[64];
  }
  bool safe = true;
  {
#pragma unroll
    for (int k = 0; k < 4; ++k) {
      const float mrow = bflo(q[k][1][2]) * kmx + 0.01f;
      safe = safe && (!h || mrow <= M_LIMIT);
      if (h) q[k][1][2] = cvtpk(-mrow, 0.f);
    }
  }
  const bool fast = __all(safe);
  f32x16 own;
  if (fast) {
    unsigned so_kv = kvs + 3 * 4096, so_w = OFF_WGU + (x.m * 196 + 2 * w) * KB, so_l = 2 * w * KB, so_g = OFF_WGU + (x.m * 196 + 112 + 12 * w) * KB, spin = 1u << 16, fvo = (l & 15) * 4096;
    Two ab = base_wo(x);
    const Two wd = base_wd(x);
    asm volatile(
      ".macro GRP s, pa, pb\n\t"
      "v_exp_f32 v200, v[\\s]\n\tv_exp_f32 v201, v[\\s+1]\n\tv_cvt_pk_bf16_f32 v[\\pa], v202, v203\n\t"
      "v_exp_f32 v202, v[\\s+2]\n\tv_exp_f32 v203, v[\\s+3]\n\tv_cvt_pk_bf16_f32 v[\\pb], v200, v201\n\t"
      ".endm\n\t"
      ".macro MEM kind, i, ld\n\t"
      ".if \\kind == 1\n\t"
      "buffer_load_dwordx4 v[128+16*\\ld+4*\\i:128+16*\\ld+4*\\i+3], %[vo], %[rs], %[skv] offen offset:1024*\\i sc1\n\t"
      ".elseif \\kind == 5\n\t"
      "ds_write_b128 v205, v[4*\\i:4*\\i+3] offset:1024*\\i\n\t"
      ".elseif \\kind == 6\n\t"
      "ds_write_b128 v205, v[4:7] offset:1024\n\t"
      ".elseif \\kind == 7\n\t"
      "ds_write_b128 v205, v[12:15] offset:3072\n\t"
      ".elseif \\kind == 2\n\t"
      "buffer_load_dword v206, v205, %[rs], 0 offen offset:3072 sc1\n\t"
      ".elseif \\kind == 3\n\t"
      ".if att_n < 16 || att_n >= 56\n\t"
      ".if att_n < 78\n\t"
      ".if att_n < 16\n\t.set att_r, 184+4*att_n\n\t.else\n\t.set att_r, 4*(att_n-56)\n\t.endif\n\t"
      ".if att_n & 1\n\t"
      "buffer_load_dwordx4 a[att_r:att_r+3], %[vo], %[rs], %[ab1] offen offset:1024*((att_n>>1)&3) sc1\n\t"
      ".else\n\t"
      "buffer_load_dwordx4 a[att_r:att_r+3], %[vo], %[rs], %[ab0] offen offset:1024*((att_n>>1)&3) sc1\n\t"
      ".endif\n\t"
      ".if att_n == 15\n\t"
      "s_mov_b32 %[ab0], %[wd0]\n\ts_mov_b32 %[ab1], %[wd1]\n\t"
      ".elseif (att_n & 7) == 7\n\t"
      "s_add_u32 %[ab0], %[ab0], 4096\n\ts_add_u32 %[ab1], %[ab1], 4096\n\t"
      ".endif\n\t"
      ".endif\n\t"
      ".elseif att_n < 44\n\t"
      ".if att_n & 1\n\t"
      "buffer_load_dwordx4 %[vo], %[rs], %[sw] offen offset:1024 sc1 lds\n\t"
      "s_add_u32 %[sw], %[sw], 8192\n\ts_add_u32 m0, m0, 8192\n\t"
      ".else\n\t"
      "buffer_load_dwordx4 %[vo], %[rs], %[sw] offen sc1 lds\n\t"
      ".endif\n\t"
      ".else\n\t"
      "buffer_load_dwordx4 v[208+4*(att_n-44):208+4*(att_n-44)+3], %[vo], %[rs], %[sg] offen offset:1024*(att_n&3) sc1\n\t"
      ".if (att_n & 3) == 3\n\ts_add_u32 %[sg], %[sg], 4096\n\t.endif\n\t"
      ".endif\n\t"
      ".set att_n, att_n+1\n\t"
      ".endif\n\t"
      ".endm\n\t"
      ".macro STEP sc, sn, k, nk, pk, tk, nt, pt, ld, m0k, m0i, m1k, m2k, m2i, m3k\n\t"
      "v_mfma_f32_32x32x16_bf16 v[\\sn:\\sn+15], v[128+16*\\nt:128+16*\\nt+3], v[64+8*\\nk:64+8*\\nk+3], 0\n\t"
      "MEM \\m0k, \\m0i, \\ld\n\t"
      "GRP \\sc, 199, 192\n\t"
      "v_mfma_f32_32x32x16_bf16 v[\\sn:\\sn+15], v[132+16*\\nt:132+16*\\nt+3], v[68+8*\\nk:68+8*\\nk+3], v[\\sn:\\sn+15]\n\t"
      "MEM \\m1k, 0, 0\n\t"
      "GRP \\sc+4, 193, 194\n\t"
      "v_mfma_f32_32x32x16_bf16 v[16*\\pk:16*\\pk+15], v[140+16*\\pt:140+16*\\pt+3], v[196:199], v[16*\\pk:16*\\pk+15]\n\t"
      "MEM \\m2k, \\m2i, \\ld\n\t"
      "GRP \\sc+8, 195, 196\n\t"
      "v_mfma_f32_32x32x16_bf16 v[16*\\k:16*\\k+15], v[136+16*\\tk:136+16*\\tk+3], v[192:195], v[16*\\k:16*\\k+15]\n\t"
      "MEM \\m3k, 0, 0\n\t"
      "GRP \\sc+12, 197, 198\n\t"
      ".endm\n\t"
      ".macro TILE tk, nx, pv, on, wait, p, fl=0\n\t"
      "STEP 96, 112, 0, 1, 3, \\tk, \\tk, \\pv, \\pv, \\p, 0, 0, \\p, 0, \\fl\n\t"
      "STEP 112, 96, 1, 2, 0, \\tk, \\tk, \\tk, \\pv, \\on, 0, \\p, \\on, 1, 0\n\t"
      "STEP 96, 112, 2, 3, 1, \\tk, \\tk, \\tk, \\pv, \\on, 2, \\p, \\on, 3, 0\n\t"
      ".if \\on\n\ts_add_u32 %[skv], %[skv], 4096\n\t.endif\n\t"
      "s_waitcnt vmcnt(\\wait)\n\t"
      "STEP 112, 96, 3, 0, 2, \\tk, \\nx, \\tk, \\pv, \\p, 0, 0, \\p, 0, 0\n\t"
      ".endm\n\t"
      ".set att_n, 0\n\t"
      "s_mov_b32 m0, %[sl]\n\t"
      "v_mov_b32 v196, 0\n\tv_mov_b32 v197, 0\n\tv_mov_b32 v198, 0\n\tv_mov_b32 v199, 0\n\tv_mov_b32 v202, 0\n\tv_mov_b32 v203, 0\n\t"
      "v_mov_b32 v188, 0\n\tv_mov_b32 v189, 0\n\tv_mov_b32 v190, 0\n\tv_mov_b32 v191, 0\n\t"
      "v_mfma_f32_32x32x16_bf16 v[0:15], v[196:199], v[196:199], 0\n\t"
      "v_mfma_f32_32x32x16_bf16 v[16:31], v[196:199], v[196:199], 0\n\t"
      "v_mfma_f32_32x32x16_bf16 v[32:47], v[196:199], v[196:199], 0\n\t"
      "v_mfma_f32_32x32x16_bf16 v[48:63], v[196:199], v[196:199], 0\n\t"
      "s_waitcnt vmcnt(8)\n\t"
      "v_mfma_f32_32x32x16_bf16 v[96:111], v[128:131], v[64:67], 0\n\t"
      "v_mfma_f32_32x32x16_bf16 v[96:111], v[132:135], v[68:71], v[96:111]\n\t"
      "s_nop 11\n\t"
      "TILE 0, 1, 3, 1, 8, 0\n\t"
      "TILE 1, 2, 0, 1, 8, 0\n\tTILE 2, 3, 1, 1, 9, 0, 2\n\t"
      "s_waitcnt vmcnt(4)\n\t"
      "1:\n\t"
      "v_cmp_gt_u32 vcc, 16, v206\n\t"
      "s_and_b64 vcc, exec, vcc\n\t"
      "s_cbranch_vccz 2f\n\t"
      "s_sleep 24\n\t"
      "buffer_load_dword v206, v205, %[rs], 0 offen offset:3072 sc1\n\t"
      "s_sub_u32 %[spin], %[spin], 1\n\t"
      "s_waitcnt vmcnt(0)\n\t"
      "s_cbranch_scc0 1b\n\t"
      "2:\n\t"
      "s_barrier\n\t"
      "TILE 3, 0, 2, 1, 13, 3\n\t"
      "TILE 0, 1, 3, 1, 18, 3\n\tTILE 1, 2, 0, 1, 20, 3\n\tTILE 2, 3, 1, 1, 20, 3\n\tTILE 3, 0, 2, 1, 20, 3\n\t"
      "TILE 0, 1, 3, 1, 20, 3\n\tTILE 1, 2, 0, 1, 20, 3\n\tTILE 2, 3, 1, 1, 20, 3\n\tTILE 3, 0, 2, 1, 20, 3\n\t"
      "TILE 0, 1, 3, 1, 20, 3\n\tTILE 1, 2, 0, 0, 16, 3\n\tTILE 2, 3, 1, 0, 12, 3\n\t"
      "v_add_u32 v205, %[d1], v204\n\tv_add_u32 v206, %[d2], v204\n\t"
      "STEP 96, 112, 0, 1, 3, 3, 3, 2, 0, 3, 0, 3, 3, 0, 3\n\t"
      "STEP 112, 96, 1, 2, 0, 3, 3, 3, 0, 3, 0, 3, 0, 0, 0\n\t"
      "STEP 96, 112, 2, 3, 1, 3, 3, 3, 0, 5, 0, 6, 5, 2, 7\n\t"
      "ds_write_b128 v206, v[16:19]\n\t"
      "GRP 112, 199, 192\n\t"
      "ds_write_b128 v206, v[20:23] offset:1024\n\t"
      "GRP 116, 193, 194\n\t"
      "v_mfma_f32_32x32x16_bf16 v[32:47], v[188:191], v[196:199], v[32:47]\n\t"
      "ds_write_b128 v206, v[24:27] offset:2048\n\t"
      "GRP 120, 195, 196\n\t"
      "v_mfma_f32_32x32x16_bf16 v[48:63], v[184:187], v[192:195], v[48:63]\n\t"
      "ds_write_b128 v206, v[28:31] offset:3072\n\t"
      "GRP 124, 197, 198\n\t"
      "v_add_u32 v200, %[d3], v204\n\t"
      "v_cvt_pk_bf16_f32 v199, v202, v203\n\t"
      "s_nop 1\n\t"
      "v_mfma_f32_32x32x16_bf16 v[48:63], v[188:191], v[196:199], v[48:63]\n\t"
      "ds_write_b128 v200, v[32:35]\n\tds_write_b128 v200, v[36:39] offset:1024\n\tds_write_b128 v200, v[40:43] offset:2048\n\tds_write_b128 v200, v[44:47] offset:3072\n\t"
      "s_nop 7\n\t"
      ".purgem GRP\n\t.purgem MEM\n\t.purgem STEP\n\t.purgem TILE"
      : "={v[48:63]}"(own), "+{v[128:143]}"(kf[0]), "+{v[144:159]}"(kf[1]), "+{v[160:175]}"(kf[2]),
        "={v[208:223]}"(g2[0]), "={v[224:239]}"(g2[1]), "={v[240:255]}"(g2[2]),
        [skv] "+s"(so_kv), [sw] "+s"(so_w), [ab0] "+s"(ab.s0), [ab1] "+s"(ab.s1), [spin] "+s"(spin), [sg] "+s"(so_g), "+{v205}"(fvo)
      : "{v[64:67]}"(q[1][0]), "{v[68:71]}"(q[1][1]), "{v[72:75]}"(q[2][0]), "{v[76:79]}"(q[2][1]),
        "{v[80:83]}"(q[3][0]), "{v[84:87]}"(q[3][1]), "{v[88:91]}"(q[0][0]), "{v[92:95]}"(q[0][1]),
        [vo] "{v204}"(x.vl16), [d1] "s"(SCR + (w ^ 1) * 3 * 4096), [d2] "s"(SCR + ((w ^ 2) * 3 + 1) * 4096), [d3] "s"(SCR + ((w ^ 3) * 3 + 2) * 4096), [rs] "s"(x.rs), [sl] "s"(so_l), [wd0] "s"(wd.s0), [wd1] "s"(wd.s1)
      : "memory", "scc", "vcc",
        "v0","v1","v2","v3","v4","v5","v6","v7","v8","v9","v10","v11","v12","v13","v14","v15","v16","v17","v18","v19","v20","v21","v22","v23",
        "v24","v25","v26","v27","v28","v29","v30","v31","v32","v33","v34","v35","v36","v37","v38","v39","v40","v41","v42","v43","v44","v45","v46","v47",
        "v96","v97","v98","v99","v100","v101","v102","v103","v104","v105","v106","v107","v108","v109","v110","v111",
        "v112","v113","v114","v115","v116","v117","v118","v119","v120","v121","v122","v123","v124","v125","v126","v127",
        "v176","v177","v178","v179","v180","v181","v182","v183","v184","v185","v186","v187","v188","v189","v190","v191",
        "v192","v193","v194","v195","v196","v197","v198","v199","v200","v201","v202","v203","v206", ACLOB);
  } else {
    f32x16 o[4];
    wv<0>();
    pin(kf[0]); pin(kf[1]); pin(kf[2]);
    float mx[4];
#pragma unroll
    for (int k = 0; k < 4; ++k) {
      mx[k] = -3.0e38f; if (h) q[k][1][2] = 0; pin(q[k][1]);
#pragma unroll
      for (int i = 0; i < 16; ++i) o[k][i] = 0.f;
    }
#pragma nounroll
    for (unsigned spin = 0; spin < (1u << 16); ++spin) {
      float v;
      ld1(v, (l & 15) * 4096, x.rs, OFF_FLAG + FL_W(0)); wv<0>(); pin(v);
      if (__all(__float_as_uint(v) >= 16u)) break;
      __builtin_amdgcn_s_sleep(24);
    }
    pf_wo(x);
    pf_wgu1(x);
    sfor<0, 12>([&](auto j) {
      u32x4 t;
      ld4<0>(t, x.vl16, x.rs, OFF_WGU + (x.m * 196 + 112 + 12 * w + j) * KB); wv<0>(); pin(t);
#pragma unroll
      for (int i = 0; i < 4; ++i) g2[j >> 2][4 * (j & 3) + i] = __uint_as_float(t[i]);
    });
    pf_wd<0, 22>(x, base_wd(x));
    for (int tk = 0; tk < 16; ++tk) {
      u32x4 kf[4];
      sfor<0, 4>([&](auto i) { ld4<i * 1024>(kf[i], x.vl16, x.rs, kvs + tk * 4096); });
      wv<0>();
      sfor<0, 4>([&](auto k) {
        f32x16 s;
        nop<1>();
        m32z(s, kf[0], q[k][0]); m32(s, kf[1], q[k][1]);
        nop<11>(); pin(s);
        float tm = s[0];
#pragma unroll
        for (int i = 1; i < 16; ++i) tm = fmaxf(tm, s[i]);
        tm = max32(tm);
        const float mn = fmaxf(mx[k], tm), al = __builtin_amdgcn_exp2f(mx[k] - mn);
        mx[k] = mn;
#pragma unroll
        for (int i = 0; i < 16; ++i) o[k][i] *= al;
        u32x4 p0, p1;
#pragma unroll
        for (int d = 0; d < 4; ++d) {
          p0[d] = cvtpk(__builtin_amdgcn_exp2f(s[2 * d] - mn), __builtin_amdgcn_exp2f(s[2 * d + 1] - mn));
          p1[d] = cvtpk(__builtin_amdgcn_exp2f(s[8 + 2 * d] - mn), __builtin_amdgcn_exp2f(s[9 + 2 * d] - mn));
        }
        pin(p0); pin(p1); pin(o[k]);
        nop<1>();
        m32(o[k], kf[2], p0); m32(o[k], kf[3], p1);
        nop<11>(); pin(o[k]);
      });
    }
    bar();
    float* mm = (float*)(lds + SCR);
#pragma unroll
    for (int k = 0; k < 4; ++k) if (h == 0) mm[((w ^ k) * 4 + w) * 32 + r] = mx[k];
    bar();
#pragma unroll
    for (int k = 0; k < 4; ++k) {
      const float* p = mm + (w ^ k) * 128 + r;
      const float g = fmaxf(fmaxf(p[0], p[32]), fmaxf(p[64], p[96])), f = __builtin_amdgcn_exp2f(mx[k] - g);
#pragma unroll
      for (int i = 0; i < 16; ++i) o[k][i] *= f;
    }
    bar();
    reduce_put(x, o);
    own = o[0];
  }
  reduce_get(x, own);
  pin(own);
  {
    auto lr = __builtin_amdgcn_permlane32_swap(__float_as_uint(own[12]), __float_as_uint(own[12]), false, false);
    const float inv = __builtin_amdgcn_rcpf(__uint_as_float(lr[1]));
    u32x4 a, b;
#pragma unroll
    for (int d = 0; d < 4; ++d) { a[d] = cvtpk(own[2 * d] * inv, own[2 * d + 1] * inv); b[d] = cvtpk(own[8 + 2 * d] * inv, own[9 + 2 * d] * inv); }
    if (h) { b[2] = 0; b[3] = 0; }
    const unsigned so = OFF_O + L * SZ_O + (tt * 32 + 2 * (2 * hp + kh)) * KB;
    st4<0>(a, x.vl16, x.rs, so); st4<1024>(b, x.vl16, x.rs, so);
  }
  wv<0>();
  signal(x, FL_O(L, x.c, th), 1);
  if (L == 0 && th == 0) {
    if (x.c == hp) count(x, FL_W2, 1);
  }
}

template <bool FINAL> DI void res_epilogue(Ctx& x, const f32x16& acc, const u32x4 (&xr)[2], unsigned Xo, unsigned STo, int tt, int nt, char* out = nullptr) {
  const int h = x.l >> 5, r = x.l & 31;
  u32x4 y[2];
#pragma unroll
  for (int d = 0; d < 8; ++d) y[d >> 2][d & 3] = cvtpk(acc[2 * d] + bflo(xr[d >> 2][d & 3]), acc[2 * d + 1] + bfhi(xr[d >> 2][d & 3]));
  if constexpr (FINAL) {
#pragma unroll
    for (int s = 0; s < 2; ++s) {
      auto a = __builtin_amdgcn_permlane32_swap(y[s][0], y[s][2], false, false);
      auto b = __builtin_amdgcn_permlane32_swap(y[s][1], y[s][3], false, false);
      y[s] = u32x4{a[0], b[0], a[1], b[1]};
    }
    const unsigned vo = r * (D * 2) + h * 16, so = tt * 32 * D * 2 + nt * 64;
    const rsrc_t ro = mkrs(out, S * D * 2);
    st4p<0>(y[0], vo, ro, so); st4p<32>(y[1], vo, ro, so);
  } else {
    const unsigned so = Xo + (tt * 28 + 2 * nt) * KB;
    st4<0>(y[0], x.vl16, x.rs, so); st4<1024>(y[1], x.vl16, x.rs, so);
    float s = 0.f, q = 0.f, s1 = 0.f, q1 = 0.f;
#pragma unroll
    for (int d = 0; d < 4; ++d) {
      s = dot2(y[0][d], 0x3f803f80u, s); q = dot2(y[0][d], y[0][d], q); s1 = dot2(y[1][d], 0x3f803f80u, s1); q1 = dot2(y[1][d], y[1][d], q1);
    }
    s = sum32(s + s1); q = sum32(q + q1);
    if (h == 0) st2<0>(u32x2{__float_as_uint(s), __float_as_uint(q)}, r * 8, x.rs, STo + (nt * 2048 + tt * 32) * 8);
  }
}

DI void phase_oproj(Ctx& x, const int L, const Args& a, const f32x16 (&g2)[3]) {
  const int w = x.w;
  auto put_g2 = [&]() {
#pragma unroll
    for (int j = 0; j < 12; ++j)
      *(f32x4*)(lds + (112 + 12 * w + j) * KB + x.vl16) = f32x4{g2[j >> 2][4 * (j & 3)], g2[j >> 2][4 * (j & 3) + 1], g2[j >> 2][4 * (j & 3) + 2], g2[j >> 2][4 * (j & 3) + 3]};
  };
  if (x.m < 14) {
    const int th = x.m / 7, ct = x.m % 7, tt0 = 4 * x.c + 2 * th, tt = tt0 + (w >> 1), nt = 2 * ct + (w & 1);
    u32x2 x0[4];
    if (L == 0) {
      const rsrc_t rx = mkrs(a.x, S * D * 2);
      sfor<0, 4>([&](auto i) { ld2i<16 * i>(x0[i], (x.l & 31) * 896 + (x.l >> 5) * 8, rx, tt * 32 * 896 + nt * 64); });
    } else {
      sfor<0, 4>([&](auto i) { ld2u<(i >> 1) * 1024 + (i & 1) * 8>(x0[i], x.vl16, x.rs, OFF_X + 2 * SZ_X + (tt * 28 + 2 * nt) * KB); });
    }
    await(x, FL_O(L, x.c, th), 8);
    u32x4 b[8][2];
    const unsigned s0 = OFF_O + L * SZ_O + (tt * 32 + 8 * w) * KB, s1 = OFF_O + L * SZ_O + ((tt0 + ((w >> 1) ^ 1)) * 32 + 8 * w) * KB;
    auto ld = [&](auto i) { ld4<(i & 3) * 1024>(b[i][0], x.vl16, x.rs, s0 + (i >> 2) * 4096); ld4<(i & 3) * 1024>(b[i][1], x.vl16, x.rs, s1 + (i >> 2) * 4096); };
    constexpr int WN = 5;
    sfor<0, WN>(ld);
    f32x16 acc[4];
    sfor<0, 8>([&](auto i) {
      constexpr int last = (i + WN - 1 < 7 ? i + WN - 1 : 7);
      wv<2 * (last - i) + 1>();
      m32a<A_WO + 8 * i, i == 0>(acc[0], b[i][0]); m32a<A_WO + 8 * i + 4, i == 0>(acc[1], b[i][0]);
      wv<2 * (last - i)>();
      m32a<A_WO + 8 * i, i == 0>(acc[2], b[i][1]); m32a<A_WO + 8 * i + 4, i == 0>(acc[3], b[i][1]);
      if constexpr (i + WN < 8) ld(std::integral_constant<int, i + WN>{});
    });
    nop<11>();
    sfor<0, 4>([&](auto k) { pin(acc[k]); pin(x0[k]); });
    reduce_put(x, acc); reduce_get(x, acc[0]);
    pin(acc[0]);
    const u32x4 x2[2] = {u32x4{x0[0][0], x0[0][1], x0[1][0], x0[1][1]}, u32x4{x0[2][0], x0[2][1], x0[3][0], x0[3][1]}};
    res_epilogue<false>(x, acc[0], x2, OFF_X + (2 * L + 1) * SZ_X, OFF_ST + (2 * L + 1) * SZ_ST, tt, nt);
    put_g2();
    wv<0>();
    signal(x, FL_X1(L, x.c), 1);
  } else {
    put_g2();
  }
}

constexpr int DWN = 7;
constexpr bool dn_half(int o) { return o == 4 || o == 5 || o == 14 || o == 15; }
struct DnSrc { unsigned s0, s1, l0, l1; };
DI DnSrc dn_src(const Ctx& x, int L) {
  const int tt0 = 4 * x.c + 2 * ((x.m / 7) & 1), tt = tt0 + (x.w >> 1), tto = tt0 + ((x.w >> 1) ^ 1);
  return DnSrc{OFF_H + L * SZ_H + (tt * 112 + 28 * x.w) * KB, OFF_H + L * SZ_H + (tto * 112 + 28 * x.w) * KB,
               OFF_HL + ((L * 64 + tt) * 16 + 4 * x.w) * KB, OFF_HL + ((L * 64 + tto) * 16 + 4 * x.w) * KB};
}
template <int O> DI void dn_ld(Ctx& x, const DnSrc& d, u32x4 (&b)[DWN][2]) {
  constexpr int i = dn_ks(O);
  if constexpr (dn_half(O)) {
    constexpr int imm = (O >= 14 ? 2048 : 0) + (O & 1) * 512;
    u32x2 lo, hi, lo1, hi1;
    ld2u<imm>(lo, x.l * 8, x.rs, d.l0); ld2u<imm + 1024>(hi, x.l * 8, x.rs, d.l0);
    ld2u<imm>(lo1, x.l * 8, x.rs, d.l1); ld2u<imm + 1024>(hi1, x.l * 8, x.rs, d.l1);
    b[O % DWN][0] = u32x4{lo[0], lo[1], hi[0], hi[1]}; b[O % DWN][1] = u32x4{lo1[0], lo1[1], hi1[0], hi1[1]};
  } else {
    ld4<(i & 3) * 1024>(b[O % DWN][0], x.vl16, x.rs, d.s0 + (i >> 2) * 4096); ld4<(i & 3) * 1024>(b[O % DWN][1], x.vl16, x.rs, d.s1 + (i >> 2) * 4096);
  }
}

DI void gu_tile(const f32x4 (&acc)[2][2], const u32x4 (&cs)[2], const float (&mean)[2], const float (&r2)[2], u32x2 (&hp)[2]) {
  const f32x4 cg = __builtin_bit_cast(f32x4, cs[0]), cu = __builtin_bit_cast(f32x4, cs[1]);
#pragma unroll
  for (int u = 0; u < 2; ++u) {
    const f32x2 nm = {-mean[u], -mean[u]}, rr = {r2[u], r2[u]};
#pragma unroll
    for (int e = 0; e < 2; ++e) {
      const f32x2 g = __builtin_elementwise_fma(nm, f32x2{cg[2 * e], cg[2 * e + 1]}, f32x2{acc[0][u][2 * e], acc[0][u][2 * e + 1]});
      const f32x2 v = __builtin_elementwise_fma(nm, f32x2{cu[2 * e], cu[2 * e + 1]}, f32x2{acc[1][u][2 * e], acc[1][u][2 * e + 1]});
      const f32x2 y = g * rr * v;
      hp[u][e] = cvtpk(y[0], y[1]);
    }
  }
}

DI void phase_gu(Ctx& x, const int L, u32x4 (&b)[DWN][2]) {
  const int w = x.w, l = x.l, g = l >> 4, c16 = l & 15, m = x.m, tt = 4 * x.c + w;
  if (L == 0 && wave0(x)) { unsigned v; ldflag(v, x.rs, OFF_FLAG + FL_W2); wv<0>(); recheck(x, v, FL_W2, 8); }
  await(x, FL_X1(L, x.c), 14);
  constexpr int NXV = 11;
  u32x4 xf[NXV][2]; f32x2 sp[2][4];
  const unsigned sx = OFF_X + (2 * L + 1) * SZ_X + tt * 28 * KB, st = OFF_ST + (2 * L + 1) * SZ_ST + tt * 32 * 8;
  auto ldx = [&](auto k, auto u) {
    if constexpr (k < NXV) ld4<(k & 1) * 2048 + 256 * u>(xf[k][u], x.vg16, x.rs, sx + (k >> 1) * 4096);
    else if constexpr (k < 14) lda<A_GX + 8 * (k - NXV) + 4 * u, (k & 1) * 2048 + 256 * u>(x.vg16, x.rs, sx + (k >> 1) * 4096);
    else { constexpr int i = k - 14; ld2<128 * u>(sp[u][i], (((i < 3 || g < 2) ? g + 4 * i : g) * 2048 + c16) * 8, x.rs, st); }
  };
  using I0 = std::integral_constant<int, 0>; using I1 = std::integral_constant<int, 1>;
  constexpr int XA = 3;
  sfor<0, XA>([&](auto k) { ldx(k, I0{}); ldx(k, I1{}); });
  const unsigned a0 = x.vl16, a1 = x.vl16 + 65536, a2 = x.vl16 + 131072;
  constexpr int DP = 6;
  u32x4 wf[8];
  auto rd = [&](auto ii) {
    constexpr int i = ii, slot = i >= 160 ? i - 160 : i;
    if constexpr (slot < 64) dsr<slot * 1024>(wf[i & 7], a0); else if constexpr (slot < 128) dsr<(slot - 64) * 1024>(wf[i & 7], a1); else dsr<(slot - 128) * 1024>(wf[i & 7], a2);
  };
  auto mm = [&](f32x4& c, const u32x4& a, auto k, auto u) {
    if constexpr (k < NXV) m16<k == 0>(c, a, xf[k][u]); else m16a<false, A_GX + 8 * (k - NXV) + 4 * u>(c, a);
  };
  const Two wd = base_wd(x);
  f32x4 aa[5][2][2], ch0, ch1;
  float mean[2], r2[2];
  sfor<0, DP>(rd);
  sfor<0, 140>([&](auto i) {
    constexpr int KS = i / 10, s = i % 10, t = s >> 1, mat = i & 1;
    using K = std::integral_constant<int, KS>;
    constexpr auto side = [](int k) { return (k >= 0 && k <= 3) || (k >= 5 && k <= 12); };
    constexpr auto all = [](int k) { return (k + XA < 14 ? 2 : 0) + ((k >= 0 && k <= 3) || (k >= 5 && k <= 9) ? 2 : 0) + (k == 5 ? 1 : 0) + (k >= 5 && k <= 9 ? 2 : 0) + (k >= 10 ? 3 : 0); };
    if constexpr (s == 0) {
      constexpr int cnt = [&] { int n = side(KS - XA) ? 1 : 0; for (int k = KS - XA + 1; k < KS; ++k) n += all(k); return n; }();
      wv<cnt>();
      if constexpr (KS == 5) asm volatile("s_barrier" ::: "memory");
    }
    if constexpr ((i & 1) == 0) { rd(std::integral_constant<int, i + DP>{}); rd(std::integral_constant<int, i + DP + 1>{}); wl<DP>(); }
    mm(aa[t][mat][0], wf[i & 7], K{}, I0{});
    if constexpr (s == 1 && KS + XA < 14) ldx(std::integral_constant<int, KS + XA>{}, I0{});
    if constexpr (s == 6 && KS + XA < 14) ldx(std::integral_constant<int, KS + XA>{}, I1{});
    if constexpr (KS <= 3 && s == 3) ldx(std::integral_constant<int, 14 + KS>{}, I0{});
    if constexpr (KS <= 3 && s == 8) ldx(std::integral_constant<int, 14 + KS>{}, I1{});
    if constexpr (KS == 5 && s == 3) asm volatile("buffer_load_dwordx4 %0, %2, %3, %4 offen sc1\n\tbuffer_load_dwordx4 %1, %2, %3, %4 offen offset:1024 sc1" : "=&v"(ch0), "=&v"(ch1) : "v"(x.vl16), "s"(x.rs), "s"(OFF_CSGU + m * 2048));
    if constexpr (KS == 11 && s == 2) { pin(ch0); pin(ch1); *(f32x4*)(lds + 40 * KB + x.vl16) = ch0 + ch1; }
    if constexpr (KS >= 5 && KS <= 9 && (s == 3 || s == 8) && !(KS == 5 && s == 3)) pf_wgu3<2 * (KS - 5) + (s == 8) - 1>(x);
    if constexpr (KS >= 5 && KS <= 9 && (s == 0 || s == 5)) { constexpr int f = 22 + 2 * (KS - 5) + (s == 5); pf_wd<f, f + 1>(x, wd); }
    if constexpr (KS >= 10 && (s == 3 || s == 5 || s == 8)) {
      constexpr int f = 32 + 3 * (KS - 10) + (s == 3 ? 0 : (s == 5 ? 1 : 2));
      pf_wd<f, f + 1>(x, wd);
    }
    if constexpr ((KS == 10 || KS == 12) && s == 2) {
      constexpr int u = KS == 12;
      sfor<0, 4>([&](auto e) { pin(sp[u][e]); });
      float sm = sp[u][0][0] + sp[u][1][0] + sp[u][2][0], q = sp[u][0][1] + sp[u][1][1] + sp[u][2][1];
      if (g < 2) { sm += sp[u][3][0]; q += sp[u][3][1]; }
      sm = sum32(sum16(sm)); q = sum32(sum16(q));
      float rs; row_stats(sm, q, mean[u], rs); r2[u] = rs * rs;
      pin(mean[u]); pin(r2[u]);
    }
    mm(aa[t][mat][1], wf[i & 7], K{}, I1{}); SB();
  });
  u32x4 cs[7][2];
  const unsigned csa = 40 * KB + g * 16;
  sfor<0, 10>([&](auto i) { dsr<64 * i>(cs[i >> 1][i & 1], csa); });
  wv<12>();
  asm volatile("s_barrier" ::: "memory");
  const unsigned H = OFF_H + L * SZ_H + tt * 112 * KB;
  const int gp = (m & 1) ? 7 * m + 1 : 7 * m;
  auto pair = [&](const f32x4 (&t0)[2][2], const f32x4 (&t1)[2][2], auto lt) {
    u32x2 h0[2], h1[2];
    gu_tile(t0, cs[lt], mean, r2, h0); gu_tile(t1, cs[lt + 1], mean, r2, h1);
    st4<0>(u32x4{h0[0][0], h0[0][1], h1[0][0], h1[0][1]}, x.vg16, x.rs, H + (gp + lt - 1) * 1024);
    st4<256>(u32x4{h0[1][0], h0[1][1], h1[1][0], h1[1][1]}, x.vg16, x.rs, H + (gp + lt - 1) * 1024);
  };
  {
    wl<0>();
    sfor<0, 20>([&](auto i) { pin(aa[i >> 2][(i >> 1) & 1][i & 1]); });
    sfor<0, 10>([&](auto i) { pin(cs[i >> 1][i & 1]); });
    u32x2 hl[2];
    gu_tile(aa[0], cs[0], mean, r2, hl);
    const unsigned sl = OFF_HL + ((L * 64 + tt) * 16 + m) * KB;
    st2<0>(hl[0], x.vg16 >> 1, x.rs, sl); st2<128>(hl[1], x.vg16 >> 1, x.rs, sl);
    pair(aa[1], aa[2], std::integral_constant<int, 1>{});
    pair(aa[3], aa[4], std::integral_constant<int, 3>{});
  }
  SB();
  sfor<10, 14>([&](auto i) { dsr<64 * i>(cs[i >> 1][i & 1], csa); });
  f32x4 ab[2][2][2];
  unsigned fa;
  sfor<140, 196>([&](auto i) {
    constexpr int j = i - 140, KS = j / 4, t = (j % 4) >> 1, mat = i & 1;
    using K = std::integral_constant<int, KS>;
    if constexpr (j == 18) {
      if (L == 0) wv<12>(); else wv<6>();
      asm volatile("s_barrier" ::: "memory");
      count(x, FL_H(L, x.c, 0), 1);
      SB();
    }
    if constexpr ((i & 1) == 0) { if constexpr (i + DP < 196) { rd(std::integral_constant<int, i + DP>{}); rd(std::integral_constant<int, i + DP + 1>{}); wl<DP>(); } else wl<194 - i>(); }
    mm(ab[t][mat][0], wf[i & 7], K{}, I0{});
    if constexpr (j % 3 == 0 && j < 36) pf_wd<44 + j / 3, 45 + j / 3>(x, wd);
    if constexpr (j % 3 == 1 && j < 42) { if (L == 0) pf_wq<j / 3>(x); SB(); }
    if constexpr (j == 44) ldflag(fa, x.rs, OFF_FLAG + FL_H(L, x.c, 0));
    mm(ab[t][mat][1], wf[i & 7], K{}, I1{}); SB();
  });
  wv<0>();
  recheck(x, fa, FL_H(L, x.c, 0), 16);
  { const DnSrc d = dn_src(x, L); sfor<0, 3>([&](auto o) { dn_ld<o>(x, d, b); }); }
  sfor<0, 8>([&](auto i) { pin(ab[i >> 2][(i >> 1) & 1][i & 1]); });
  sfor<10, 14>([&](auto i) { pin(cs[i >> 1][i & 1]); });
  pair(ab[0], ab[1], std::integral_constant<int, 5>{});
}

DI void phase_down(Ctx& x, const int L, char* out, u32x4 (&b)[DWN][2]) {
  const int w = x.w;
  if (x.m >= 14) {
    wv<0>();
    sfor<0, 3>([&](auto o) { pin(b[o][0]); pin(b[o][1]); });
    signal(x, FL_H(L, x.c, 1), 1);
    return;
  }
  const int th = x.m / 7, ct = x.m % 7, tt = 4 * x.c + 2 * th + (w >> 1), nt = 2 * ct + (w & 1);
  constexpr int WN = DWN;
  u32x4 xr[2];
  const DnSrc d = dn_src(x, L);
  auto ld = [&](auto o) { dn_ld<o>(x, d, b); };
  sfor<3, WN>(ld);
  f32x16 acc[4];
  unsigned fb;
  sfor<0, 25>([&](auto i) {
    constexpr int last = i + WN - 1 < 27 ? i + WN - 1 : 27, ex = i <= 2 ? 2 : ((i >= 8 && i <= 13) ? 1 : ((i >= 21) ? 2 : 0));
    constexpr int later = [&] { int n = 0; for (int o = i + 1; o <= last; ++o) n += dn_half(o) ? 4 : 2; return n; }();
    wv<later + (dn_half(i) ? 2 : 1) + ex>();
    if constexpr (i == 3) signal(x, FL_H(L, x.c, 1), 1);
    m32a<A_WD + 8 * i, i == 0>(acc[0], b[i % WN][0]); m32a<A_WD + 8 * i + 4, i == 0>(acc[1], b[i % WN][0]);
    wv<later + ex>();
    m32a<A_WD + 8 * i, i == 0>(acc[2], b[i % WN][1]); m32a<A_WD + 8 * i + 4, i == 0>(acc[3], b[i % WN][1]);
    if constexpr (i == 7) ldflag(fb, x.rs, OFF_FLAG + FL_H(L, x.c, 1));
    if constexpr (i + WN == 20) { wv<12>(); recheck(x, fb, FL_H(L, x.c, 1), 16); }
    if constexpr (i + WN < 28) ld(std::integral_constant<int, i + WN>{});
    if constexpr (i + WN == 27) {
      const unsigned sr = OFF_X + (2 * L + 1) * SZ_X + (tt * 28 + 2 * nt) * KB;
      ld4<0>(xr[0], x.vl16, x.rs, sr); ld4<1024>(xr[1], x.vl16, x.rs, sr);
    }
  });
  wv<2>();
  sfor<1, 5>([&](auto t) {
    constexpr int k = t & 3;
    m32a<A_WD + 8 * 25 + 4 * (k & 1), false>(acc[k], b[25 % WN][k >> 1]);
    m32a<A_WD + 8 * 26 + 4 * (k & 1), false>(acc[k], b[26 % WN][k >> 1]);
    if constexpr (t > 1) { nop<(t == 2 ? 9 : 7)>(); red_put1<t - 1, 0>(x, acc[t - 1]); red_put1<t - 1, 1>(x, acc[t - 1]); }
    m32a<A_WD + 8 * 27 + 4 * (k & 1), false>(acc[k], b[27 % WN][k >> 1]);
    if constexpr (t > 1) { red_put1<t - 1, 2>(x, acc[t - 1]); red_put1<t - 1, 3>(x, acc[t - 1]); }
  });
  if (L == 0 && (x.m & 7) < 2) {
    ldl<4>(x.vl16, x.rs, OFF_WQ + ((16 + (x.m & 1)) * 28 + 7 * w) * KB, 7 * w * KB);
    ldl<3>(x.vl16, x.rs, OFF_WQ + ((16 + (x.m & 1)) * 28 + 7 * w + 4) * KB, (7 * w + 4) * KB);
  }
  nop<9>();
  pin(acc[0]);
  reduce_get(x, acc[0]);
  wv<0>();
  pin(xr[0]); pin(xr[1]);
  if (L > 0) {
    res_epilogue<true>(x, acc[0], xr, 0, 0, tt, nt, out);
  } else {
    res_epilogue<false>(x, acc[0], xr, OFF_X + 2 * SZ_X, OFF_ST + 2 * SZ_ST, tt, nt);
    wv<0>();
    signal(x, FL_X(x.c, th), 1);
  }
}

__global__ __launch_bounds__(256, 1) void k_main(Args) {
  typedef unsigned long long u64x8 __attribute__((ext_vector_type(8)));
  typedef unsigned long long u64x2 __attribute__((ext_vector_type(2)));
  u64x8 pin8; u64x2 pws;
  asm volatile("s_load_dwordx16 s[8:23], %4, 0x0\n\ts_load_dwordx4 s[24:27], %4, 0x40\n\t"
               "s_getpc_b64 s[4:5]\n.Lk_begin:\n\t"
               "s_mov_b32 s6, .Lk_end-.Lk_begin\n\ts_mov_b32 s7, 0x20000\n\ts_and_b32 s5, s5, 0xffff\n\t"
               "v_lshlrev_b32 v1, 4, %2\n\t"
               "s_bfe_u32 s28, %3, 0x20003\n\ts_lshl_b32 s28, s28, 12\n\t"
               "buffer_load_dwordx4 a[112:115], v1, s[4:7], s28 offen\n\t"
               "s_bfe_u32 s28, %3, 0x30004\n\ts_lshl_b32 s28, s28, 13\n\t"
               "buffer_load_dwordx4 a[112:115], v1, s[4:7], s28 offen\n\t"
               "s_add_u32 s28, s28, 4096\n\t"
               "buffer_load_dwordx4 a[112:115], v1, s[4:7], s28 offen\n\t"
               "s_waitcnt lgkmcnt(0)"
               : "={s[8:23]}"(pin8), "={s[24:27]}"(pws) : "v"(threadIdx.x), "{s2}"(blockIdx.x), "{s[0:1]}"(__builtin_amdgcn_kernarg_segment_ptr())
               : "s4", "s5", "s6", "s7", "s28", "v1", "scc", ACLOB);
  const Args a = {(const char*)pin8[0], (const char*)pin8[1], (const char*)pin8[2], (const char*)pin8[3], (const char*)pin8[4], (const char*)pin8[5],
                  (const char*)pin8[6], (const char*)pin8[7], (char*)pws[0], (char*)pws[1]};
  Ctx x;
  x.rs = mkrs(a.ws, WS_USED);
  x.w = __builtin_amdgcn_readfirstlane(threadIdx.x >> 6);
  asm volatile("s_nop 4" : "+s"(x.w));
  { const int b = blockIdx.x; x.c = 2 * (b & 7) + (b >> 7); x.m = (b >> 3) & 15; }
  f32x16 kf[3];
  { Ctx y = x; fresh(y); phase_pre(y, a, kf); }
  for (int L = 0;; ++L) {
    f32x16 g2[3];
    int Lo = L; asm volatile("" : "+s"(Lo));
    { Ctx y = x; fresh(y); phase_att(y, Lo, kf, g2); }
    { Ctx y = x; fresh(y); phase_oproj(y, Lo, a, g2); }
    { Ctx y = x; u32x4 b[DWN][2]; fresh(y); phase_gu(y, Lo, b); fresh(y); phase_down(y, Lo, a.out, b); }
    if (L == 1) break;
    { Ctx y = x; fresh(y); phase_front(y, kf); }
  }
  fresh(x);
  if (blockIdx.x == 0 && wave0(x)) {
    get_tag(x);
    if (x.l == 0) st2<0>(u32x2{x.tlo, x.thi}, 0, x.rs, OFF_TAG);
  }
  asm volatile(".Lk_end:");
}

extern "C" void kernel_launch(void* const* d_in, const int* in_sizes, int n_in, void* d_out, int out_size, void* d_ws, size_t ws_size,
                              hipStream_t stream) {
  Args a;
  a.x = (const char*)d_in[0]; a.wq = (const char*)d_in[1]; a.wk = (const char*)d_in[2]; a.wv = (const char*)d_in[3];
  a.wo = (const char*)d_in[4]; a.wg = (const char*)d_in[6]; a.wu = (const char*)d_in[7]; a.wd = (const char*)d_in[8];
  a.ws = (char*)d_ws; a.out = (char*)d_out;
  (void)hipFuncSetAttribute((const void*)k_main, hipFuncAttributeMaxDynamicSharedMemorySize, 160 * 1024);
  k_main<<<256, 256, 160 * 1024, stream>>>(a);
}
```

```cpp
#include <hip/hip_runtime.h>
#include <stdint.h>
#include <type_traits>

typedef float    f32x16 __attribute__((ext_vector_type(16)));
typedef float    f32x4  __attribute__((ext_vector_type(4)));
typedef float    f32x2  __attribute__((ext_vector_type(2)));
typedef unsigned u32x4  __attribute__((ext_vector_type(4)));
typedef unsigned u32x2  __attribute__((ext_vector_type(2)));
typedef __bf16   bf16x2 __attribute__((ext_vector_type(2)));
typedef __amdgpu_buffer_rsrc_t rsrc_t;
#define DI __device__ __forceinline__
#define SB() __builtin_amdgcn_sched_barrier(0)

constexpr int S = 2048, D = 448, F = 1792;
constexpr unsigned KB = 1024;
constexpr unsigned OFF_FLAG = 0;
constexpr unsigned OFF_CSQ  = 64 * KB;
constexpr unsigned OFF_CSGU = OFF_CSQ + 4 * KB;
constexpr unsigned OFF_SLOT = OFF_CSGU + 32 * KB;
constexpr unsigned OFF_TAG  = OFF_SLOT + 32 * KB;
constexpr unsigned OFF_WQ   = 160 * KB;
constexpr unsigned OFF_WO   = OFF_WQ + 18 * 28 * KB;
constexpr unsigned OFF_WGU  = OFF_WO + 14 * 32 * KB;
constexpr unsigned OFF_WD   = OFF_WGU + 16 * 196 * KB;
constexpr unsigned SZ_X = 64 * 28 * KB, SZ_ST = 14 * 2048 * 8, SZ_O = 64 * 32 * KB, SZ_H = 64 * 112 * KB, SZ_KV = 64 * 4 * KB;
constexpr unsigned OFF_X    = OFF_WD + 14 * 112 * KB;
constexpr unsigned OFF_ST   = OFF_X + 4 * SZ_X;
constexpr unsigned OFF_O    = OFF_ST + 4 * SZ_ST;
constexpr unsigned OFF_H    = OFF_O + 2 * SZ_O;
constexpr unsigned OFF_HL   = OFF_H + 2 * SZ_H;
constexpr unsigned OFF_KV   = OFF_HL + 2 * 1024 * KB;
constexpr unsigned WS_USED  = OFF_KV + 2 * SZ_KV;
DI constexpr unsigned FL_X(int c, int th)         { return (c * 16 + th) * 256u; }
DI constexpr unsigned FL_O(int L, int c, int th)  { return (c * 16 + 2 + L * 2 + th) * 256u; }
DI constexpr unsigned FL_X1(int L, int c)         { return (c * 16 + 6 + L) * 256u; }
DI constexpr unsigned FL_H(int L, int c, int p)   { return (c * 16 + 8 + L * 2 + p) * 256u; }
DI constexpr unsigned FL_W(int c)                 { return (c * 16 + 12) * 256u; }
constexpr unsigned FL_W2 = 13 * 256u;

constexpr int NWL = 18, NWB = 12;
constexpr float QSCALE = 0.18898223650461362f * 1.4426950408889634f;
constexpr float M_LIMIT = 48.0f;

template <int I, int N, class Fn> DI void sfor(Fn&& f) { if constexpr (I < N) { f(std::integral_constant<int, I>{}); sfor<I + 1, N>(f); } }

DI unsigned cvtpk(float lo, float hi) { f32x2 v = {lo, hi}; bf16x2 b = __builtin_convertvector(v, bf16x2); return __builtin_bit_cast(unsigned, b); }
DI float bflo(unsigned u) { return __uint_as_float(u << 16); }
DI float bfhi(unsigned u) { return __uint_as_float(u & 0xffff0000u); }
DI float dot2(unsigned a, unsigned b, float c) { return __builtin_amdgcn_fdot2_f32_bf16(__builtin_bit_cast(bf16x2, a), __builtin_bit_cast(bf16x2, b), c, false); }
DI float sum32(float v) { auto r = __builtin_amdgcn_permlane32_swap(__float_as_uint(v), __float_as_uint(v), false, false); return __uint_as_float(r[0]) + __uint_as_float(r[1]); }
DI float sum16(float v) { auto r = __builtin_amdgcn_permlane16_swap(__float_as_uint(v), __float_as_uint(v), false, false); return __uint_as_float(r[0]) + __uint_as_float(r[1]); }
DI float max32(float v) { auto r = __builtin_amdgcn_permlane32_swap(__float_as_uint(v), __float_as_uint(v), false, false); return fmaxf(__uint_as_float(r[0]), __uint_as_float(r[1])); }

DI unsigned umax16(unsigned v) {
  v = max(v, (unsigned)__builtin_amdgcn_update_dpp(0, (int)v, 0x128, 0xf, 0xf, false));
  v = max(v, (unsigned)__builtin_amdgcn_update_dpp(0, (int)v, 0x124, 0xf, 0xf, false));
  v = max(v, (unsigned)__builtin_amdgcn_update_dpp(0, (int)v, 0x122, 0xf, 0xf, false));
  v = max(v, (unsigned)__builtin_amdgcn_update_dpp(0, (int)v, 0x121, 0xf, 0xf, false));
  return v;
}
DI unsigned umax64(unsigned v) {
  v = umax16(v);
  auto a = __builtin_amdgcn_permlane16_swap(v, v, false, false); v = max(a[0], a[1]);
  auto b = __builtin_amdgcn_permlane32_swap(v, v, false, false); return max(b[0], b[1]);
}

typedef short s16x4 __attribute__((ext_vector_type(4)));
DI u32x2 trread(const char* p) { return __builtin_bit_cast(u32x2, __builtin_amdgcn_ds_read_tr16_b64_v4i16((__attribute__((address_space(3))) s16x4*)p)); }
DI u32x4 trfrag(const char* lo, const char* hi) { const u32x2 a = trread(lo), b = trread(hi); return u32x4{a[0], a[1], b[0], b[1]}; }
DI void row_stats(float s, float q, float& mean, float& rstd) {
  mean = s * (1.0f / D);
  const float var = fmaxf(q * (1.0f / D) - mean * mean, 0.f);
  rstd = __builtin_amdgcn_rsqf(var + 1e-5f);
}
DI float fragsum(const u32x4& f, float s) {
#pragma unroll
  for (int d = 0; d < 4; ++d) s = dot2(f[d], 0x3f803f80u, s);
  return s;
}
struct Args { const char *x, *wq, *wk, *wv, *wo, *wg, *wu, *wd; char* ws; char* out; };

#define ACLOB "a0","a1","a2","a3","a4","a5","a6","a7","a8","a9","a10","a11","a12","a13","a14","a15","a16","a17","a18","a19","a20","a21","a22","a23","a24","a25","a26","a27","a28","a29","a30","a31","a32","a33","a34","a35","a36","a37","a38","a39","a40","a41","a42","a43","a44","a45","a46","a47","a48","a49","a50","a51","a52","a53","a54","a55","a56","a57","a58","a59","a60","a61","a62","a63","a64","a65","a66","a67","a68","a69","a70","a71","a72","a73","a74","a75","a76","a77","a78","a79","a80","a81","a82","a83","a84","a85","a86","a87","a88","a89","a90","a91","a92","a93","a94","a95","a96","a97","a98","a99","a100","a101","a102","a103","a104","a105","a106","a107","a108","a109","a110","a111","a112","a113","a114","a115","a116","a117","a118","a119","a120","a121","a122","a123","a124","a125","a126","a127","a128","a129","a130","a131","a132","a133","a134","a135","a136","a137","a138","a139","a140","a141","a142","a143","a144","a145","a146","a147","a148","a149","a150","a151","a152","a153","a154","a155","a156","a157","a158","a159","a160","a161","a162","a163","a164","a165","a166","a167","a168","a169","a170","a171","a172","a173","a174","a175","a176","a177","a178","a179","a180","a181","a182","a183","a184","a185","a186","a187","a188","a189","a190","a191","a192","a193","a194","a195","a196","a197","a198","a199","a200","a201","a202","a203","a204","a205","a206","a207","a208","a209","a210","a211","a212","a213","a214","a215","a216","a217","a218","a219","a220","a221","a222","a223","a224","a225","a226","a227","a228","a229","a230","a231","a232","a233","a234","a235","a236","a237","a238","a239","a240","a241","a242","a243","a244","a245","a246","a247","a248","a249","a250","a251","a252","a253","a254","a255"

template <int IMM> DI void ld4(u32x4& d, unsigned vo, rsrc_t rs, unsigned so) {
  asm volatile("buffer_load_dwordx4 %0, %1, %2, %3 offen offset:%c4 sc1" : "=v"(d) : "v"(vo), "s"(rs), "s"(so), "i"(IMM));
}
template <int IMM> DI void ld2(f32x2& d, unsigned vo, rsrc_t rs, unsigned so) {
  asm volatile("buffer_load_dwordx2 %0, %1, %2, %3 offen offset:%c4 sc1" : "=v"(d) : "v"(vo), "s"(rs), "s"(so), "i"(IMM));
}
template <int IMM> DI void ld2i(u32x2& d, unsigned vo, rsrc_t rs, unsigned so) {
  asm volatile("buffer_load_dwordx2 %0, %1, %2, %3 offen offset:%c4" : "=v"(d) : "v"(vo), "s"(rs), "s"(so), "i"(IMM));
}
template <int IMM> DI void ld2u(u32x2& d, unsigned vo, rsrc_t rs, unsigned so) {
  asm volatile("buffer_load_dwordx2 %0, %1, %2, %3 offen offset:%c4 sc1" : "=v"(d) : "v"(vo), "s"(rs), "s"(so), "i"(IMM));
}
template <int IMM> DI void ld4f(f32x4& d, unsigned vo, rsrc_t rs, unsigned so) {
  asm volatile("buffer_load_dwordx4 %0, %1, %2, %3 offen offset:%c4 sc1" : "=v"(d) : "v"(vo), "s"(rs), "s"(so), "i"(IMM));
}
DI void ld1(float& d, unsigned vo, rsrc_t rs, unsigned so) {
  asm volatile("buffer_load_dword %0, %1, %2, %3 offen sc1" : "=v"(d) : "v"(vo), "s"(rs), "s"(so));
}
template <int AR, int IMM> DI void lda(unsigned vo, rsrc_t rs, unsigned so) {
  asm volatile("buffer_load_dwordx4 a[%c3:%c4], %0, %1, %2 offen offset:%c5 sc1" :: "v"(vo), "s"(rs), "s"(so), "i"(AR), "i"(AR + 3), "i"(IMM) : ACLOB);
}
template <int N> DI void ldl(unsigned vo, rsrc_t rs, unsigned so, unsigned lds) {
  asm volatile("s_mov_b32 m0, %3\n\ts_nop 0\n\tbuffer_load_dwordx4 %0, %1, %2 offen sc1 lds" :: "v"(vo), "s"(rs), "s"(so), "s"(lds) : "memory");
  if constexpr (N > 1) asm volatile("buffer_load_dwordx4 %0, %1, %2 offen offset:1024 sc1 lds" :: "v"(vo), "s"(rs), "s"(so) : "memory");
  if constexpr (N > 2) asm volatile("buffer_load_dwordx4 %0, %1, %2 offen offset:2048 sc1 lds" :: "v"(vo), "s"(rs), "s"(so) : "memory");
  if constexpr (N > 3) asm volatile("buffer_load_dwordx4 %0, %1, %2 offen offset:3072 sc1 lds" :: "v"(vo), "s"(rs), "s"(so) : "memory");
}
template <int IMM> DI void st4(const u32x4& d, unsigned vo, rsrc_t rs, unsigned so) {
  asm volatile("buffer_store_dwordx4 %0, %1, %2, %3 offen offset:%c4 sc1\n\ts_nop 1" :: "v"(d), "v"(vo), "s"(rs), "s"(so), "i"(IMM) : "memory");
}
template <int IMM> DI void st4p(const u32x4& d, unsigned vo, rsrc_t rs, unsigned so) {
  asm volatile("buffer_store_dwordx4 %0, %1, %2, %3 offen offset:%c4\n\ts_nop 1" :: "v"(d), "v"(vo), "s"(rs), "s"(so), "i"(IMM) : "memory");
}
template <int IMM> DI void st2(const u32x2& d, unsigned vo, rsrc_t rs, unsigned so) {
  asm volatile("buffer_store_dwordx2 %0, %1, %2, %3 offen offset:%c4 sc1" :: "v"(d), "v"(vo), "s"(rs), "s"(so), "i"(IMM) : "memory");
}
DI void st1(float d, unsigned vo, rsrc_t rs, unsigned so) {
  asm volatile("buffer_store_dword %0, %1, %2, %3 offen sc1" :: "v"(d), "v"(vo), "s"(rs), "s"(so) : "memory");
}
template <int N> DI void wv() { asm volatile("s_waitcnt vmcnt(%c0)" :: "i"(N) : "memory"); SB(); }
template <int N> DI void wl() { asm volatile("s_waitcnt lgkmcnt(%c0)" :: "i"(N) : "memory"); SB(); }
template <class T> DI void pin(T& x) { asm volatile("" : "+v"(x)); }
DI void bar() { asm volatile("s_waitcnt lgkmcnt(0)\n\ts_barrier" ::: "memory"); }
template <int N> DI void nop() { asm volatile("s_nop %c0" :: "i"(N)); SB(); }

template <int OFF> DI void trd(u32x2& r, unsigned a) { asm volatile("ds_read_b64_tr_b16 %0, %1 offset:%c2" : "=v"(r) : "v"(a), "i"(OFF)); }
template <int OFF> DI void dsr(u32x4& r, unsigned a) { asm volatile("ds_read_b128 %0, %1 offset:%c2" : "=v"(r) : "v"(a), "i"(OFF)); }

DI void m32(f32x16& c, const u32x4& a, const u32x4& b)  { asm volatile("v_mfma_f32_32x32x16_bf16 %0, %1, %2, %0" : "+v"(c) : "v"(a), "v"(b)); }
DI void m32z(f32x16& c, const u32x4& a, const u32x4& b) { asm volatile("v_mfma_f32_32x32x16_bf16 %0, %1, %2, 0" : "=&v"(c) : "v"(a), "v"(b)); }
template <int AR, bool Z> DI void m32a(f32x16& c, const u32x4& b) {
  if constexpr (Z) asm volatile("v_mfma_f32_32x32x16_bf16 %0, a[%c2:%c3], %1, 0" : "=&v"(c) : "v"(b), "i"(AR), "i"(AR + 3));
  else             asm volatile("v_mfma_f32_32x32x16_bf16 %0, a[%c2:%c3], %1, %0" : "+v"(c) : "v"(b), "i"(AR), "i"(AR + 3));
}
template <bool Z> DI void m16(f32x4& c, const u32x4& a, const u32x4& b) {
  if constexpr (Z) asm volatile("v_mfma_f32_16x16x32_bf16 %0, %1, %2, 0" : "=&v"(c) : "v"(a), "v"(b));
  else             asm volatile("v_mfma_f32_16x16x32_bf16 %0, %1, %2, %0" : "+v"(c) : "v"(a), "v"(b));
}

template <bool Z, int AR> DI void m16a(f32x4& c, const u32x4& a) {
  if constexpr (Z) asm volatile("v_mfma_f32_16x16x32_bf16 %0, %1, a[%c2:%c3], 0" : "=&v"(c) : "v"(a), "i"(AR), "i"(AR + 3));
  else             asm volatile("v_mfma_f32_16x16x32_bf16 %0, %1, a[%c2:%c3], %0" : "+v"(c) : "v"(a), "i"(AR), "i"(AR + 3));
}
DI void ldflag(unsigned& v, rsrc_t rs, unsigned so) { asm volatile("buffer_load_dword %0, off, %1, %2 sc1" : "=v"(v) : "s"(rs), "s"(so)); }

struct Ctx {
  rsrc_t rs;
  unsigned tlo, thi;
  unsigned vl16;
  unsigned vg16;
  int w, l, c, m;
};
DI void fresh(Ctx& x) {
  asm volatile("" : "+s"(x.w), "+s"(x.c), "+s"(x.m));
  asm volatile("v_mbcnt_lo_u32_b32 %0, -1, 0\n\tv_mbcnt_hi_u32_b32 %0, -1, %0" : "=v"(x.l));
  x.vl16 = x.l * 16;
  const int g = x.l >> 4;
  x.vg16 = ((g >> 1) * 64 + (g & 1) * 32 + (x.l & 15)) * 16;
}
extern __shared__ __attribute__((aligned(16))) char lds[];
constexpr unsigned SCR = 112 * KB;

DI void get_tag(Ctx& x) {
  unsigned lo, hi;
  asm volatile("v_accvgpr_read_b32 v255, a252\n\ts_nop 1\n\tv_readfirstlane_b32 %0, v255\n\tv_accvgpr_read_b32 v255, a253\n\ts_nop 1\n\tv_readfirstlane_b32 %1, v255\n\ts_nop 4"
               : "=s"(lo), "=s"(hi) :: "v255");
  const unsigned long long e = ((unsigned long long)hi << 32) | lo;
  unsigned long long n = e * 0x9E3779B97F4A7C15ull + 0x632BE59BD9B4E019ull;
  if (((n ^ e) >> 16) == 0) n += 0x10000;
  x.tlo = (unsigned)n; x.thi = (unsigned)(n >> 32);
}
DI bool wave0(const Ctx& x) { int w = x.w; asm volatile("" : "+s"(w)); return w == 0; }
DI void count(Ctx& x, unsigned fl, int n) {
  if (wave0(x)) {
    const unsigned long long mask = (1ull << n) - 1;
    unsigned long long keep;
    asm volatile("s_mov_b64 %0, exec\n\ts_mov_b64 exec, %5\n\tbuffer_atomic_add %1, %2, %3, %4 offen\n\ts_mov_b64 exec, %0"
                 : "=&s"(keep) : "v"(1u), "v"(x.l * 256), "s"(x.rs), "s"(OFF_FLAG + fl), "s"(mask) : "memory");
  }
}
DI void signal(Ctx& x, unsigned fl, int n) { bar(); count(x, fl, n); }
DI void await(Ctx& x, unsigned fl, unsigned target) {
  if (wave0(x)) {
#pragma nounroll
    for (unsigned spin = 0; spin < (1u << 18); ++spin) {
      unsigned v;
      asm volatile("buffer_load_dword %0, off, %1, %2 sc1\n\ts_waitcnt vmcnt(0)" : "=v"(v) : "s"(x.rs), "s"(OFF_FLAG + fl) : "memory");
      if ((unsigned)__builtin_amdgcn_readfirstlane(v) >= target) break;
      __builtin_amdgcn_s_sleep(1);
    }
  }
  bar();
}

DI void recheck(Ctx& x, unsigned v, unsigned fl, unsigned target) {
  pin(v);
#pragma nounroll
  for (unsigned spin = 0; (unsigned)__builtin_amdgcn_readfirstlane(v) < target && spin < (1u << 18); ++spin) {
    __builtin_amdgcn_s_sleep(2);
    asm volatile("buffer_load_dword %0, off, %1, %2 sc1\n\ts_waitcnt vmcnt(0)" : "=v"(v) : "s"(x.rs), "s"(OFF_FLAG + fl) : "memory");
  }
  SB();
}

DI void reduce_put(Ctx& x, const f32x16 (&acc)[4]) {
  sfor<1, 4>([&](auto k) {
    f32x4* p = (f32x4*)(lds + SCR + ((x.w ^ k) * 3 + k - 1) * 4096 + x.vl16);
#pragma unroll
    for (int q = 0; q < 4; ++q) p[q * 64] = f32x4{acc[k][4 * q], acc[k][4 * q + 1], acc[k][4 * q + 2], acc[k][4 * q + 3]};
  });
}
template <int K, int Q> DI void red_put1(Ctx& x, const f32x16& t) {
  asm volatile("ds_write_b128 %0, %1 offset:%c2" :: "v"(SCR + ((x.w ^ K) * 3 + K - 1) * 4096 + x.vl16),
               "v"(__builtin_shufflevector(t, t, 4 * Q, 4 * Q + 1, 4 * Q + 2, 4 * Q + 3)), "i"(Q * 1024) : "memory");
}
DI void reduce_get(Ctx& x, f32x16& own) {
  bar();
  const f32x4* p = (const f32x4*)(lds + SCR + x.w * 3 * 4096 + x.vl16);
#pragma unroll
  for (int k = 0; k < 3; ++k)
#pragma unroll
    for (int q = 0; q < 4; ++q) {
      f32x4 v = p[k * 256 + q * 64];
#pragma unroll
      for (int i = 0; i < 4; ++i) own[4 * q + i] += v[i];
    }
}

constexpr int A_WO = 96, A_WD = 0, A_GX = 224;
constexpr int dn_ks(int o) { return o < 20 ? 2 * ((o >> 1) % 5 + ((o >> 1) % 5 >= 2) + 7 * (o / 10)) + (o & 1) : 2 * (((o - 20) >> 1) * 4 - ((o - 20) >> 2) + 2) + (o & 1); }
static_assert(dn_ks(0) == 0 && dn_ks(4) == 6 && dn_ks(9) == 11 && dn_ks(10) == 14 && dn_ks(19) == 25 && dn_ks(20) == 4 && dn_ks(22) == 12 && dn_ks(24) == 18 && dn_ks(27) == 27, "");
struct Two { unsigned s0, s1; };
constexpr int SL_WQ = 56;
template <int J> DI void pf_wq(Ctx& x) {
  ldl<1>(x.vl16, x.rs, OFF_WQ + ((x.m & 7) * 56 + 14 * x.w + J) * KB, (SL_WQ + 14 * x.w + J) * KB);
}
DI Two base_wo(const Ctx& x) { const int ct = x.m % 7;
  const int kh = x.w >> 1;
  return Two{OFF_WO + ((2 * ct + kh) * 32 + 16 * kh) * KB, OFF_WO + ((2 * ct + (kh ^ 1)) * 32 + 16 * kh) * KB}; }
DI void pf_wo(Ctx& x) {
  const Two b = base_wo(x);
  sfor<0, 16>([&](auto i) {
    lda<A_WO + 8 * i, (i & 3) * 1024>(x.vl16, x.rs, b.s0 + (i >> 2) * 4096);
    lda<A_WO + 8 * i + 4, (i & 3) * 1024>(x.vl16, x.rs, b.s1 + (i >> 2) * 4096);
  });
}
DI Two base_wd(const Ctx& x) { const int ct = x.m % 7;
  return Two{OFF_WD + ((2 * ct + (x.w & 1)) * 112 + 28 * x.w) * KB, OFF_WD + ((2 * ct + ((x.w & 1) ^ 1)) * 112 + 28 * x.w) * KB}; }
template <int I0, int I1> DI void pf_wd(Ctx& x, const Two& b) {
  sfor<I0, I1>([&](auto f) {
    constexpr int i = f >> 1;
    lda<A_WD + 4 * f, (i & 3) * 1024>(x.vl16, x.rs, ((f & 1) ? b.s1 : b.s0) + (i >> 2) * 4096);
  });
}
DI void pf_wgu1(Ctx& x) {
  for (int t = 0; t < 14; ++t) ldl<2>(x.vl16, x.rs, OFF_WGU + (x.m * 196 + 8 * t + 2 * x.w) * KB, (8 * t + 2 * x.w) * KB);
}
template <int J> DI void pf_wgu3(Ctx& x) {
  ldl<1>(x.vl16, x.rs, OFF_WGU + (x.m * 196 + 160 + 9 * x.w + J) * KB, (9 * x.w + J) * KB);
}

constexpr unsigned S_QP = SCR, S_KP = SCR + 16384, S_QF = SCR + 24576, S_ST = SCR + 37888, S_CS = SCR + 38912, S_KM = SCR + 36864, S_TR = SCR + 40960;
DI unsigned kv_finish(Ctx& x, const float (&raw)[16], const f32x4 (&cs)[4], float mean, float rstd, bool isv, unsigned so, int slot) {
  const int h = x.l >> 5, r = x.l & 31;
  u32x4 y[2];
#pragma unroll
  for (int d = 0; d < 8; ++d)
    y[d >> 2][d & 3] = cvtpk((raw[2 * d] - mean * cs[d >> 1][2 * (d & 1)]) * rstd, (raw[2 * d + 1] - mean * cs[d >> 1][2 * (d & 1) + 1]) * rstd);
  if (h) { y[1][2] = 0; y[1][3] = 0; }
  unsigned nb = 0;
  if (!isv) {
    u32x4 y1 = y[1];
    if (h) y1[2] = 0x3f80u;
    st4<0>(y[0], x.vl16, x.rs, so); st4<1024>(y1, x.vl16, x.rs, so);
    float n2 = 0.f, n3 = 0.f;
#pragma unroll
    for (int d = 0; d < 4; ++d) { n2 = dot2(y[0][d], y[0][d], n2); n3 = dot2(y[1][d], y[1][d], n3); }
    n2 = sum32(n2 + n3);
    nb = umax64(n2 < 3.0e38f ? __float_as_uint(n2) : 0x7f800000u);
    nb = cvtpk(1.01f * __builtin_amdgcn_sqrtf(__uint_as_float(nb)), 0.f);
  } else {
    if (h) y[1][2] = 0x3f80u;
    char* im = lds + S_TR + slot * 2048;
#pragma unroll
    for (int a = 0; a < 4; ++a) *(u32x2*)(im + r * 64 + 16 * a + 8 * h) = u32x2{y[a >> 1][2 * (a & 1)], y[a >> 1][2 * (a & 1) + 1]};
    asm volatile("s_waitcnt lgkmcnt(0)" ::: "memory");
    const char* tb = im + (4 * h + ((x.l >> 2) & 3)) * 64 + ((x.l >> 4) & 1) * 32 + (x.l & 3) * 8;
    const u32x4 v0 = trfrag(tb, tb + 512), v1 = trfrag(tb + 1024, tb + 1536);
    st4<0>(v0, x.vl16, x.rs, so); st4<1024>(v1, x.vl16, x.rs, so);
  }
  return nb;
}
DI void kv_publish(Ctx& x, int L, int p, unsigned km) {
  get_tag(x);
  st2<0>(u32x2{(x.tlo & 0xffff0000u) | km, x.thi}, x.l < 16 ? x.l * 1024 : ~0u, x.rs, OFF_SLOT + (L * 2048 + p) * 8);
}
DI void q_finish(Ctx& x, const f32x16& qa, const f32x4 (&cq)[4], float mean, float rstd) {
  const int h = x.l >> 5;
  u32x4 qf[2];
  const f32x4* p = (const f32x4*)(lds + S_QP + x.w * 4096 + x.vl16);
  const float sc = rstd * QSCALE;
#pragma unroll
  for (int q = 0; q < 4; ++q) {
    const f32x4 o = p[q * 64];
    float v[4];
#pragma unroll
    for (int i = 0; i < 4; ++i) v[i] = (qa[4 * q + i] + o[i] - mean * cq[q][i]) * sc;
    qf[q >> 1][2 * (q & 1)] = cvtpk(v[0], v[1]); qf[q >> 1][2 * (q & 1) + 1] = cvtpk(v[2], v[3]);
  }
  if (h) { qf[1][2] = 0; qf[1][3] = 0; }
  float qn2 = 0.f, qn3 = 0.f;
#pragma unroll
  for (int d = 0; d < 4; ++d) { qn2 = dot2(qf[0][d], qf[0][d], qn2); qn3 = dot2(qf[1][d], qf[1][d], qn3); }
  qn2 = sum32(qn2 + qn3);
  if (h) qf[1][2] = cvtpk(1.01f * __builtin_amdgcn_sqrtf(qn2), 0.f);
  ((u32x4*)(lds + S_QF + x.w * 2048 + x.vl16))[0] = qf[0];
  ((u32x4*)(lds + S_QF + x.w * 2048 + x.vl16))[64] = qf[1];
}
DI void put_tile(char* p, const f32x16& t) {
#pragma unroll
  for (int q = 0; q < 4; ++q) ((f32x4*)p)[q * 64] = f32x4{t[4 * q], t[4 * q + 1], t[4 * q + 2], t[4 * q + 3]};
}

constexpr unsigned IMG_Q = 0, IMG_T = 56 * KB;
DI rsrc_t mkrs(const char* p, unsigned n) { return __builtin_amdgcn_make_buffer_rsrc((char*)p, 0, n, 0x00020000); }

DI void ask_slots(Ctx& x, int L) {
  if (wave0(x)) asm volatile("buffer_load_dwordx4 a[248:251], %0, %1, %2 offen sc1" :: "v"(x.vl16), "s"(x.rs), "s"(OFF_SLOT + (L * 16 + x.c) * 1024) : ACLOB);
}
DI void kv_wait(Ctx& x, int L) {
  if (wave0(x)) get_tag(x);
  wv<0>();
  if (wave0(x)) {
    unsigned s0, s1, s2, s3;
#pragma nounroll
    for (unsigned spin = 0; spin < (1u << 18); ++spin) {
      asm volatile("v_accvgpr_read_b32 %0, a248\n\tv_accvgpr_read_b32 %1, a249\n\tv_accvgpr_read_b32 %2, a250\n\tv_accvgpr_read_b32 %3, a251" : "=v"(s0), "=v"(s1), "=v"(s2), "=v"(s3));
      if (__all(s1 == x.thi && s3 == x.thi && (((s0 ^ x.tlo) | (s2 ^ x.tlo)) >> 16) == 0)) break;
      __builtin_amdgcn_s_sleep(2);
      ask_slots(x, L); wv<0>();
    }
    const unsigned km = umax64(max(s0 << 16, s2 << 16));
    if (x.l == 0) ((unsigned*)(lds + S_KM))[4] = km;
  }
  bar();
}
DI void ask_kv(Ctx& x, int L, f32x16 (&kf)[3]) {
  const unsigned kvs = OFF_KV + L * SZ_KV + 16 * x.w * 4096;
  asm volatile(".irp i,0,1,2,3\n\tbuffer_load_dwordx4 v[128+4*\\i:131+4*\\i], %3, %4, %5 offen offset:1024*\\i sc1\n\t.endr\n\t"
               ".irp i,0,1,2,3\n\tbuffer_load_dwordx4 v[144+4*\\i:147+4*\\i], %3, %4, %6 offen offset:1024*\\i sc1\n\t.endr\n\t"
               ".irp i,0,1,2,3\n\tbuffer_load_dwordx4 v[160+4*\\i:163+4*\\i], %3, %4, %7 offen offset:1024*\\i sc1\n\t.endr"
               : "={v[128:143]}"(kf[0]), "={v[144:159]}"(kf[1]), "={v[160:175]}"(kf[2]) : "{v204}"(x.vl16), "s"(x.rs), "s"(kvs), "s"(kvs + 4096), "s"(kvs + 8192));
}

DI void phase_pre(Ctx& x, const Args& a, f32x16 (&kf)[3]) {
  const int w = x.w, l = x.l, h = l >> 5, r = l & 31;
  const int th = x.m >> 3, hp = x.m & 7, tl = w & 1, kh = w >> 1, tt = 4 * x.c + 2 * th + tl, wi = 2 * x.c + th;
  const bool kvp = hp < 2;
  u32x4 xf[14];
  { const rsrc_t rx = mkrs(a.x, S * D * 2);
    sfor<0, 16>([&](auto i) {
      const int c = r ^ ((2 * i + h) & 15);
      ldl<1>(c < 28 ? h * 896 + c * 16 : 0x80000000u, rx, (tt * 32 + 2 * i) * 896 + kh * 448, (84 + 16 * w + i) * KB);
    }); }
  if (x.m == 2) asm volatile("buffer_load_dword a116, %0, %1, 0 offen sc1" :: "v"(l < 16 ? OFF_FLAG + (x.c * 16 + l) * 256 : OFF_SLOT + ((l >> 3) & 1) * 16384 + x.c * 1024 + (l & 7) * 128), "s"(x.rs) : ACLOB);
  asm volatile("buffer_load_dwordx2 a[252:253], off, %0, %1 sc1" :: "s"(x.rs), "s"(OFF_TAG) : ACLOB);
  const int task = wi * 6 + hp - 2;
  const rsrc_t rq = mkrs(a.wq, D * D * 2);
  auto ask_q = [&](auto i) {
    ldl<1>((8 * w + (l >> 3)) * 896 + (l & 7) * 16, rq, hp * 112 + i * 32 * 896, IMG_Q + (4 * i + w) * KB);
  };
  auto ask_t = [&](const char* tp, unsigned tn, unsigned ts, unsigned tb) {
    const rsrc_t rt = mkrs(tp, tn);
    const unsigned vo = (8 * w + (l >> 3)) * ts + (l & 7) * 16;
    sfor<0, 7>([&](auto i) { ldl<1>(vo, rt, tb + i * 32 * ts, IMG_T + (4 * i + w) * KB); });
  };
  const int tco = ((l >> 4) & 1) * 32 + (l & 3) * 8;
  const int sro = 4 * h + ((l >> 2) & 3);
  if (kvp) ask_t(hp ? a.wv : a.wk, D * 56, 128, 0);
  else if (task < 112) ask_t(task < 56 ? a.wg : a.wu, D * F * 2, F * 2, (task & 1) * 224 * F * 2 + ((task % 56) >> 1) * 128);
  else if (task < 168) ask_t(a.wd, D * F * 2, 896, ((task - 112) / 7) * 224 * 896 + ((task - 112) % 7) * 128);
  else ask_t(a.wo, task < 182 ? D * D * 2 : 0, 896, ((task - 168) / 7) * 224 * 896 + ((task - 168) % 7) * 128);
  wv<8>();
  sfor<0, 14>([&](auto i) { dsr<0>(xf[i], (84 + w * 16) * KB + r * 512 + (((2 * i + h) ^ (r & 15)) << 4)); });
  wl<0>();
  {
    float s = 0.f, q = 0.f, s1 = 0.f, q1 = 0.f;
    sfor<0, 14>([&](auto i) {
      ask_q(i);
      pin(xf[i]);
      s = dot2(xf[i][0], 0x3f803f80u, s); q = dot2(xf[i][0], xf[i][0], q); s1 = dot2(xf[i][1], 0x3f803f80u, s1); q1 = dot2(xf[i][1], xf[i][1], q1);
      s = dot2(xf[i][2], 0x3f803f80u, s); q = dot2(xf[i][2], xf[i][2], q); s1 = dot2(xf[i][3], 0x3f803f80u, s1); q1 = dot2(xf[i][3], xf[i][3], q1);
      pin(s); pin(q); pin(s1); pin(q1);
    });
    s = sum32(s + s1); q = sum32(q + q1);
    if (h == 0) ((f32x2*)(lds + S_ST))[w * 32 + r] = f32x2{s, q};
  }
  wv<14>();
  bar();
  if (!kvp) {
    if (task < 112) {
      const int g = l >> 4, mat = task >= 56, gt = 4 * ((task % 56) >> 1) + w, m = gt / 7, j = gt % 7, lt = (m & 1) ? j : (j == 6 ? 0 : j + 1);
      const char* tb = lds + IMG_T + (16 * (g >> 1) + 4 * (g & 1) + ((l >> 2) & 3)) * 128 + 32 * w + (l & 3) * 8;
      const unsigned st = lt < 5 ? 10 * KB : 4 * KB, so = OFF_WGU + (m * 196 + (lt < 5 ? lt * 2 + mat : 140 + (lt - 5) * 2 + mat)) * KB + (task & 1) * 7 * st;
      u32x4 f[7];
      sfor<0, 7>([&](auto i) { f[i] = trfrag(tb + i * 4096, tb + i * 4096 + 1024); });
      float cs = 0.f;
      sfor<0, 7>([&](auto i) { st4<0>(f[i], x.vl16, x.rs, so + i * st); cs = fragsum(f[i], cs); });
      cs = sum32(sum16(cs));
      st1(cs, l < 16 ? l * 4 : ~0u, x.rs, OFF_CSGU + m * 2048 + (task & 1) * 1024 + (lt * 2 + mat) * 64);
    } else if (task < 168) {
      const int nt = 2 * ((task - 112) % 7) + (w & 1), rb = (task - 112) / 7;
      const char* tb = lds + IMG_T + sro * 128 + 64 * (w & 1) + tco;
      const unsigned so = OFF_WD + (nt * 112 + 28 * (rb >> 1)) * KB, sa = so + (rb & 1) * 10 * KB, sb = so + (20 + (rb & 1) * 4) * KB;
      u32x4 f[7];
      if (w < 2) {
        sfor<0, 7>([&](auto i) { const char* p = tb + (32 * (i >> 1) + 8 * (i & 1)) * 128; f[i] = trfrag(p, p + 2048); });
        sfor<0, 7>([&](auto i) { st4<0>(f[i], x.vl16, x.rs, i < 4 ? sa + i * KB : (i < 6 ? sb + (i - 4) * KB : sa + 4 * KB)); });
      } else {
        sfor<0, 7>([&](auto i) { const char* p = tb + (32 * ((i + 7) >> 1) + 8 * ((i + 7) & 1)) * 128; f[i] = trfrag(p, p + 2048); });
        sfor<0, 7>([&](auto i) { st4<0>(f[i], x.vl16, x.rs, i < 5 ? sa + (5 + i) * KB : sb + (i - 3) * KB); });
      }
      st1(0.f, ~0u, x.rs, 0);
    } else {
      const int nt = 2 * ((task - 168) % 7) + (w & 1);
      const char* tb = lds + IMG_T + (112 * (w >> 1) + sro) * 128 + 64 * (w & 1) + tco;
      const unsigned vo = task < 182 ? x.vl16 : ~0u - 4096;
      u32x4 f[8];
      sfor<0, 8>([&](auto i) { const char* p = tb + (28 * (i >> 1) + 16 * (i & 1)) * 128; f[i] = trfrag(p, p + 1024); if ((i & 1) && h) { f[i][2] = 0; f[i][3] = 0; } });
      sfor<0, 8>([&](auto i) { st4<(i & 3) * 1024>(f[i], vo, x.rs, OFF_WO + (nt * 32 + 16 * ((task - 168) / 7) + 8 * (w >> 1) + (i & ~3)) * KB); });
    }
  }
  if (!kvp) { wv<8>(); bar(); }
  float mean, rstd;
  { const f32x2 p0 = ((const f32x2*)(lds + S_ST))[tl * 32 + r], p1 = ((const f32x2*)(lds + S_ST))[(tl + 2) * 32 + r];
    row_stats(p0[0] + p1[0], p0[1] + p1[1], mean, rstd); }
  float* csw = (float*)(lds + S_CS);
  const int tro = 8 * h + ((l >> 2) & 3);
  u32x4 ones = {0x3f803f80u, 0x3f803f80u, 0x3f803f80u, 0x3f803f80u};
  pin(ones);
  if (kvp) {
    f32x16 kv, cs;
    const unsigned tb = IMG_T + (224 * kh + tro) * 56 + tco;
    u32x2 f[14][2];
    auto rd = [&](auto i) { trd<i * 896>(f[i][0], tb); trd<i * 896 + 224>(f[i][1], tb); };
    sfor<0, 7>(rd);
    sfor<0, 14>([&](auto i) {
      if constexpr (i + 7 < 14) { wl<12>(); rd(std::integral_constant<int, i + 7>{}); } else wl<2 * (13 - i)>();
      pin(f[i][0]); pin(f[i][1]);
      u32x4 g = {f[i][0][0], f[i][0][1], f[i][1][0], f[i][1][1]};
      pin(g); nop<0>();
      if constexpr (i == 0) { m32z(kv, g, xf[i]); m32z(cs, g, ones); } else { m32(kv, g, xf[i]); m32(cs, g, ones); }
    });
    nop<11>(); pin(kv); pin(cs);
    if (r == 0) sfor<0, 4>([&](auto q) { *(f32x4*)(csw + (w * 3 + 2) * 32 + 8 * q + 4 * h) = f32x4{cs[4 * q], cs[4 * q + 1], cs[4 * q + 2], cs[4 * q + 3]}; });
    if (kh) put_tile(lds + S_KP + tl * 4096 + x.vl16, kv);
    bar();
    if (!kh) {
      if (x.m == 0 && w == 0) st1(0.f, l < 16 ? l * 256 : ~0u, x.rs, OFF_FLAG + x.c * 4096);
      float raw[16]; f32x4 c4[4];
      sfor<0, 4>([&](auto q) {
        const f32x4 o = ((const f32x4*)(lds + S_KP + tl * 4096 + x.vl16))[q * 64];
        c4[q] = *(const f32x4*)(csw + (w * 3 + 2) * 32 + 8 * q + 4 * h) + *(const f32x4*)(csw + ((w + 2) * 3 + 2) * 32 + 8 * q + 4 * h);
#pragma unroll
        for (int i = 0; i < 4; ++i) raw[4 * q + i] = kv[4 * q + i] + o[i];
      });
      const unsigned km = kv_finish(x, raw, c4, mean, rstd, hp == 1, OFF_KV + (tt * 4 + 2 * hp) * KB, tl);
      wv<0>();
      kv_publish(x, 0, 2 * tt + hp, km);
      if (tt == 0 && r == 0) sfor<0, 4>([&](auto q) { st4<32 * q>(__builtin_bit_cast(u32x4, c4[q]), h * 16, x.rs, OFF_CSQ + (16 + hp) * 128); });
    } else {
      wv<0>();
    }
    bar();
  }
  f32x16 qa;
  {
    f32x16 q1, q2, cs;
    const unsigned t1 = IMG_Q + (224 * kh + tro) * 128 + tco + 56 * tl, t2 = IMG_Q + (224 * kh + tro) * 128 + tco + 56 * (tl ^ 1);
    u32x2 f[14][4];
    auto rd = [&](auto i) { trd<i * 2048>(f[i][0], t1); trd<i * 2048 + 512>(f[i][1], t1); trd<i * 2048>(f[i][2], t2); trd<i * 2048 + 512>(f[i][3], t2); };
    wl<0>();
    sfor<0, 3>(rd);
    sfor<0, 14>([&](auto i) {
      if constexpr (i + 3 < 14) { wl<8>(); rd(std::integral_constant<int, i + 3>{}); } else wl<4 * (13 - i)>();
      sfor<0, 4>([&](auto e) { pin(f[i][e]); });
      u32x4 g1 = {f[i][0][0], f[i][0][1], f[i][1][0], f[i][1][1]}, g2 = {f[i][2][0], f[i][2][1], f[i][3][0], f[i][3][1]};
      pin(g1); pin(g2); nop<0>();
      if constexpr (i == 0) { m32z(q1, g1, xf[i]); m32z(q2, g2, xf[i]); m32z(cs, g1, ones); } else { m32(q1, g1, xf[i]); m32(q2, g2, xf[i]); m32(cs, g1, ones); }
    });
    nop<11>(); pin(q1); pin(q2); pin(cs);
    if (r == 0) sfor<0, 4>([&](auto q) { *(f32x4*)(csw + (w * 3) * 32 + 8 * q + 4 * h) = f32x4{cs[4 * q], cs[4 * q + 1], cs[4 * q + 2], cs[4 * q + 3]}; });
    if (kh == tl) { put_tile(lds + S_QP + (w ^ 2) * 4096 + x.vl16, q2); qa = q1; } else { put_tile(lds + S_QP + (w ^ 2) * 4096 + x.vl16, q1); qa = q2; }
  }
  if (th == 0 && x.c < 14) {
    const int f = 4 * x.c + w, hd = f >= 28, ks = f - 28 * hd;
    const char* tb = lds + IMG_Q + (16 * ks + sro) * 128 + tco + 56 * hd;
    st4<0>(trfrag(tb, tb + 1024), x.vl16, x.rs, OFF_WQ + (hp * 56 + f) * KB);
  }
  if (kvp && th == 1 && x.c < 7) {
    const char* tb = lds + IMG_T + (16 * (4 * x.c + w) + sro) * 56 + tco;
    st4<0>(trfrag(tb, tb + 448), x.vl16, x.rs, OFF_WQ + ((16 + hp) * 28 + 4 * x.c + w) * KB);
  }
  if (!kvp) __builtin_amdgcn_s_sleep(12);
  ask_slots(x, 0);
  kv_wait(x, 0);
  count(x, FL_W(x.c), 1);
  ask_kv(x, 0, kf);
  const bool own = x.c == hp && th == 0;
  {
    f32x4 cq[4];
    sfor<0, 4>([&](auto q) {
      cq[q] = *(const f32x4*)(csw + (kh * 3) * 32 + 8 * q + 4 * h) + *(const f32x4*)(csw + ((kh + 2) * 3) * 32 + 8 * q + 4 * h);
      if (own && tl == 0 && r == 0) st4<32 * q>(__builtin_bit_cast(u32x4, cq[q]), h * 16, x.rs, OFF_CSQ + (2 * hp + kh) * 128);
    });
    q_finish(x, qa, cq, mean, rstd);
  }
}

DI void phase_front(Ctx& x, f32x16 (&kf)[3]) {
  const int w = x.w, l = x.l, h = l >> 5, r = l & 31;
  const int th = x.m >> 3, hp = x.m & 7, tl = w & 1, kh = w >> 1, tt = 4 * x.c + 2 * th + tl;
  const unsigned KV = OFF_KV + SZ_KV;
  {
    f32x4 cq[4], ck[4];
    sfor<0, 4>([&](auto q) { ld4f<32 * q>(cq[q], h * 16, x.rs, OFF_CSQ + (2 * hp + kh) * 128); ld4f<32 * q>(ck[q], h * 16, x.rs, OFF_CSQ + (16 + (hp & 1)) * 128); });
    wv<0>();
    await(x, FL_X(x.c, th), 7);
    u32x4 xf[14]; f32x2 sp[7];
    {
      const unsigned so = OFF_X + 2 * SZ_X + (tt * 28 + 14 * kh) * KB;
      sfor<0, 7>([&](auto i) { ld4<(i & 3) * 1024>(xf[i], x.vl16, x.rs, so + (i >> 2) * 4096); });
      sfor<0, 7>([&](auto i) { ld2<0>(sp[i], (h * 7 * 2048 + r) * 8, x.rs, OFF_ST + 2 * SZ_ST + (i * 2048 + tt * 32) * 8); });
      sfor<7, 14>([&](auto i) { ld4<(i & 3) * 1024>(xf[i], x.vl16, x.rs, so + (i >> 2) * 4096); });
    }
    float mean, rstd;
    auto stats = [&]() {
      sfor<0, 7>([&](auto i) { pin(sp[i]); });
      sfor<0, 4>([&](auto i) { pin(cq[i]); pin(ck[i]); });
      float s = 0.f, q = 0.f;
#pragma unroll
      for (int i = 0; i < 7; ++i) { s += sp[i][0]; q += sp[i][1]; }
      row_stats(sum32(s), sum32(q), mean, rstd);
      pin(mean); pin(rstd); SB();
    };
    f32x16 qa, qb;
    u32x4 wa[14], wb[14];
    const unsigned pa = x.vl16 + (SL_WQ + 28 * kh + 14 * kh) * KB, pb = x.vl16 + (SL_WQ + 28 * (kh ^ 1) + 14 * kh) * KB;
    if (hp < 2) {
      f32x16 kv;
      sfor<0, 14>([&](auto i) { dsr<i * 1024>(wa[i], x.vl16 + 14 * kh * KB); });
      sfor<0, 14>([&](auto i) {
        wv<(i < 7 ? 20 : 13) - i>(); wl<13 - i>();
        if constexpr (i == 0) m32z(kv, wa[i], xf[i]); else m32(kv, wa[i], xf[i]);
        if constexpr (i == 7) stats();
      });
      nop<11>(); pin(kv);
      if (kh) put_tile(lds + S_KP + tl * 4096 + x.vl16, kv);
      bar();
      unsigned km = 0;
      if (!kh) {
        float raw[16];
        sfor<0, 4>([&](auto q) {
          const f32x4 o = ((const f32x4*)(lds + S_KP + tl * 4096 + x.vl16))[q * 64];
#pragma unroll
          for (int i = 0; i < 4; ++i) raw[4 * q + i] = kv[4 * q + i] + o[i];
        });
        km = kv_finish(x, raw, ck, mean, rstd, hp == 1, KV + (tt * 4 + 2 * hp) * KB, tl);
      }
      wl<0>();
      sfor<0, 7>([&](auto i) { dsr<i * 1024>(wb[i], pb); dsr<i * 1024>(wa[i], pa); });
      sfor<0, 14>([&](auto i) {
        if constexpr (i < 7) wl<12>(); else wl<2 * (13 - i)>();
        if constexpr (i == 0) { m32z(qb, wb[i], xf[i]); m32z(qa, wa[i], xf[i]); } else { m32(qb, wb[i], xf[i]); m32(qa, wa[i], xf[i]); }
        if constexpr (i < 7) { dsr<(i + 7) * 1024>(wb[i + 7], pb); dsr<(i + 7) * 1024>(wa[i + 7], pa); }
        if constexpr (i == 6) { if (!kh) { wv<0>(); kv_publish(x, 1, 2 * tt + hp, km); } SB(); }
      });
    } else {
      sfor<0, 7>([&](auto i) { dsr<i * 1024>(wb[i], pb); dsr<i * 1024>(wa[i], pa); });
      sfor<0, 14>([&](auto i) {
        wv<(i < 7 ? 20 : 13) - i>();
        if constexpr (i < 7) wl<12>(); else wl<2 * (13 - i)>();
        if constexpr (i == 0) { m32z(qb, wb[i], xf[i]); m32z(qa, wa[i], xf[i]); } else { m32(qb, wb[i], xf[i]); m32(qa, wa[i], xf[i]); }
        if constexpr (i < 7) { dsr<(i + 7) * 1024>(wb[i + 7], pb); dsr<(i + 7) * 1024>(wa[i + 7], pa); }
        if constexpr (i == 7) stats();
      });
    }
    nop<11>();
    pin(qa); pin(qb);
    put_tile(lds + S_QP + (w ^ 2) * 4096 + x.vl16, qb);
    bar();
    q_finish(x, qa, cq, mean, rstd);
    if (hp >= 2) __builtin_amdgcn_s_sleep(36);
    ask_slots(x, 1);
    kv_wait(x, 1);
    ask_kv(x, 1, kf);
  }
}

DI void phase_att(Ctx& x, const int L, f32x16 (&kf)[3], f32x16 (&g2)[3]) {
  const int w = x.w, l = x.l, h = l >> 5, r = l & 31;
  const int th = x.m >> 3, hp = x.m & 7, tl = w & 1, kh = w >> 1, tt = 4 * x.c + 2 * th + tl;
  const unsigned kvs = OFF_KV + L * SZ_KV + 16 * w * 4096;
  if (L == 0) bar();
  unsigned kma = S_KM + 16; asm volatile("" : "+s"(kma));
  const float kmx = *(const float*)(lds + kma);
  u32x4 q[4][2];
#pragma unroll
  for (int k = 0; k < 4; ++k) {
    q[k][0] = ((const u32x4*)(lds + S_QF + (w ^ k) * 2048 + x.vl16))[0];
    q[k][1] = ((const u32x4*)(lds + S_QF + (w ^ k) * 2048 + x.vl16))[64];
  }
  bool safe = true;
  {
#pragma unroll
    for (int k = 0; k < 4; ++k) {
      const float mrow = bflo(q[k][1][2]) * kmx + 0.01f;
      safe = safe && (!h || mrow <= M_LIMIT);
      if (h) q[k][1][2] = cvtpk(-mrow, 0.f);
    }
  }
  const bool fast = __all(safe);
  f32x16 own;
  if (fast) {
    unsigned so_kv = kvs + 3 * 4096, so_w = OFF_WGU + (x.m * 196 + 2 * w) * KB, so_l = 2 * w * KB, so_g = OFF_WGU + (x.m * 196 + 112 + 12 * w) * KB, spin = 1u << 16, fvo = (l & 15) * 4096;
    Two ab = base_wo(x);
    const Two wd = base_wd(x);
    asm volatile(
      ".macro GRP s, pa, pb\n\t"
      "v_exp_f32 v200, v[\\s]\n\tv_exp_f32 v201, v[\\s+1]\n\tv_cvt_pk_bf16_f32 v[\\pa], v202, v203\n\t"
      "v_exp_f32 v202, v[\\s+2]\n\tv_exp_f32 v203, v[\\s+3]\n\tv_cvt_pk_bf16_f32 v[\\pb], v200, v201\n\t"
      ".endm\n\t"
      ".macro MEM kind, i, ld\n\t"
      ".if \\kind == 1\n\t"
      "buffer_load_dwordx4 v[128+16*\\ld+4*\\i:128+16*\\ld+4*\\i+3], %[vo], %[rs], %[skv] offen offset:1024*\\i sc1\n\t"
      ".elseif \\kind == 5\n\t"
      "ds_write_b128 v205, v[4*\\i:4*\\i+3] offset:1024*\\i\n\t"
      ".elseif \\kind == 6\n\t"
      "ds_write_b128 v205, v[4:7] offset:1024\n\t"
      ".elseif \\kind == 7\n\t"
      "ds_write_b128 v205, v[12:15] offset:3072\n\t"
      ".elseif \\kind == 2\n\t"
      "buffer_load_dword v206, v205, %[rs], 0 offen offset:3072 sc1\n\t"
      ".elseif \\kind == 3\n\t"
      ".if att_n < 32 || att_n >= 72\n\t"
      ".if att_n < 90\n\t"
      ".if att_n < 32\n\t.set att_r, 96+4*att_n\n\t.else\n\t.set att_r, 4*(att_n-72)\n\t.endif\n\t"
      ".if att_n & 1\n\t"
      "buffer_load_dwordx4 a[att_r:att_r+3], %[vo], %[rs], %[ab1] offen offset:1024*((att_n>>1)&3) sc1\n\t"
      ".else\n\t"
      "buffer_load_dwordx4 a[att_r:att_r+3], %[vo], %[rs], %[ab0] offen offset:1024*((att_n>>1)&3) sc1\n\t"
      ".endif\n\t"
      ".if att_n == 31\n\t"
      "s_mov_b32 %[ab0], %[wd0]\n\ts_mov_b32 %[ab1], %[wd1]\n\t"
      ".elseif (att_n & 7) == 7\n\t"
      "s_add_u32 %[ab0], %[ab0], 4096\n\ts_add_u32 %[ab1], %[ab1], 4096\n\t"
      ".endif\n\t"
      ".endif\n\t"
      ".elseif att_n < 60\n\t"
      ".if att_n & 1\n\t"
      "buffer_load_dwordx4 %[vo], %[rs], %[sw] offen offset:1024 sc1 lds\n\t"
      "s_add_u32 %[sw], %[sw], 8192\n\ts_add_u32 m0, m0, 8192\n\t"
      ".else\n\t"
      "buffer_load_dwordx4 %[vo], %[rs], %[sw] offen sc1 lds\n\t"
      ".endif\n\t"
      ".else\n\t"
      "buffer_load_dwordx4 v[208+4*(att_n-60):208+4*(att_n-60)+3], %[vo], %[rs], %[sg] offen offset:1024*(att_n&3) sc1\n\t"
      ".if (att_n & 3) == 3\n\ts_add_u32 %[sg], %[sg], 4096\n\t.endif\n\t"
      ".endif\n\t"
      ".set att_n, att_n+1\n\t"
      ".endif\n\t"
      ".endm\n\t"
      ".macro STEP sc, sn, k, nk, pk, tk, nt, pt, ld, m0k, m0i, m1k, m2k, m2i, m3k\n\t"
      "v_mfma_f32_32x32x16_bf16 v[\\sn:\\sn+15], v[128+16*\\nt:128+16*\\nt+3], v[64+8*\\nk:64+8*\\nk+3], 0\n\t"
      "MEM \\m0k, \\m0i, \\ld\n\t"
      "GRP \\sc, 199, 192\n\t"
      "v_mfma_f32_32x32x16_bf16 v[\\sn:\\sn+15], v[132+16*\\nt:132+16*\\nt+3], v[68+8*\\nk:68+8*\\nk+3], v[\\sn:\\sn+15]\n\t"
      "MEM \\m1k, 0, 0\n\t"
      "GRP \\sc+4, 193, 194\n\t"
      "v_mfma_f32_32x32x16_bf16 v[16*\\pk:16*\\pk+15], v[140+16*\\pt:140+16*\\pt+3], v[196:199], v[16*\\pk:16*\\pk+15]\n\t"
      "MEM \\m2k, \\m2i, \\ld\n\t"
      "GRP \\sc+8, 195, 196\n\t"
      "v_mfma_f32_32x32x16_bf16 v[16*\\k:16*\\k+15], v[136+16*\\tk:136+16*\\tk+3], v[192:195], v[16*\\k:16*\\k+15]\n\t"
      "MEM \\m3k, 0, 0\n\t"
      "GRP \\sc+12, 197, 198\n\t"
      ".endm\n\t"
      ".macro TILE tk, nx, pv, on, wait, p, fl=0\n\t"
      "STEP 96, 112, 0, 1, 3, \\tk, \\tk, \\pv, \\pv, \\p, 0, 0, \\p, 0, \\fl\n\t"
      "STEP 112, 96, 1, 2, 0, \\tk, \\tk, \\tk, \\pv, \\on, 0, \\p, \\on, 1, 0\n\t"
      "STEP 96, 112, 2, 3, 1, \\tk, \\tk, \\tk, \\pv, \\on, 2, \\p, \\on, 3, 0\n\t"
      ".if \\on\n\ts_add_u32 %[skv], %[skv], 4096\n\t.endif\n\t"
      "s_waitcnt vmcnt(\\wait)\n\t"
      "STEP 112, 96, 3, 0, 2, \\tk, \\nx, \\tk, \\pv, \\p, 0, \\p, \\p, 0, 0\n\t"
      ".endm\n\t"
      ".set att_n, 0\n\t"
      "s_mov_b32 m0, %[sl]\n\t"
      "v_mov_b32 v196, 0\n\tv_mov_b32 v197, 0\n\tv_mov_b32 v198, 0\n\tv_mov_b32 v199, 0\n\tv_mov_b32 v202, 0\n\tv_mov_b32 v203, 0\n\t"
      "v_mov_b32 v188, 0\n\tv_mov_b32 v189, 0\n\tv_mov_b32 v190, 0\n\tv_mov_b32 v191, 0\n\t"
      "v_mfma_f32_32x32x16_bf16 v[0:15], v[196:199], v[196:199], 0\n\t"
      "v_mfma_f32_32x32x16_bf16 v[16:31], v[196:199], v[196:199], 0\n\t"
      "v_mfma_f32_32x32x16_bf16 v[32:47], v[196:199], v[196:199], 0\n\t"
      "v_mfma_f32_32x32x16_bf16 v[48:63], v[196:199], v[196:199], 0\n\t"
      "s_waitcnt vmcnt(8)\n\t"
      "v_mfma_f32_32x32x16_bf16 v[96:111], v[128:131], v[64:67], 0\n\t"
      "v_mfma_f32_32x32x16_bf16 v[96:111], v[132:135], v[68:71], v[96:111]\n\t"
      "s_nop 11\n\t"
      "TILE 0, 1, 3, 1, 8, 0\n\t"
      "TILE 1, 2, 0, 1, 8, 0\n\tTILE 2, 3, 1, 1, 9, 0, 2\n\t"
      "s_waitcnt vmcnt(4)\n\t"
      "1:\n\t"
      "v_cmp_gt_u32 vcc, 16, v206\n\t"
      "s_and_b64 vcc, exec, vcc\n\t"
      "s_cbranch_vccz 2f\n\t"
      "s_sleep 24\n\t"
      "buffer_load_dword v206, v205, %[rs], 0 offen offset:3072 sc1\n\t"
      "s_sub_u32 %[spin], %[spin], 1\n\t"
      "s_waitcnt vmcnt(0)\n\t"
      "s_cbranch_scc0 1b\n\t"
      "2:\n\t"
      "s_barrier\n\t"
      "TILE 3, 0, 2, 1, 13, 3\n\t"
      "TILE 0, 1, 3, 1, 19, 3\n\tTILE 1, 2, 0, 1, 22, 3\n\tTILE 2, 3, 1, 1, 22, 3\n\tTILE 3, 0, 2, 1, 22, 3\n\t"
      "TILE 0, 1, 3, 1, 22, 3\n\tTILE 1, 2, 0, 1, 22, 3\n\tTILE 2, 3, 1, 1, 22, 3\n\tTILE 3, 0, 2, 1, 22, 3\n\t"
      "TILE 0, 1, 3, 1, 22, 3\n\tTILE 1, 2, 0, 0, 18, 3\n\tTILE 2, 3, 1, 0, 14, 3\n\t"
      "v_add_u32 v205, %[d1], v204\n\tv_add_u32 v206, %[d2], v204\n\t"
      "STEP 96, 112, 0, 1, 3, 3, 3, 2, 0, 3, 0, 3, 3, 0, 3\n\t"
      "STEP 112, 96, 1, 2, 0, 3, 3, 3, 0, 3, 0, 3, 0, 0, 0\n\t"
      "STEP 96, 112, 2, 3, 1, 3, 3, 3, 0, 5, 0, 6, 5, 2, 7\n\t"
      "ds_write_b128 v206, v[16:19]\n\t"
      "GRP 112, 199, 192\n\t"
      "ds_write_b128 v206, v[20:23] offset:1024\n\t"
      "GRP 116, 193, 194\n\t"
      "v_mfma_f32_32x32x16_bf16 v[32:47], v[188:191], v[196:199], v[32:47]\n\t"
      "ds_write_b128 v206, v[24:27] offset:2048\n\t"
      "GRP 120, 195, 196\n\t"
      "v_mfma_f32_32x32x16_bf16 v[48:63], v[184:187], v[192:195], v[48:63]\n\t"
      "ds_write_b128 v206, v[28:31] offset:3072\n\t"
      "GRP 124, 197, 198\n\t"
      "v_add_u32 v200, %[d3], v204\n\t"
      "v_cvt_pk_bf16_f32 v199, v202, v203\n\t"
      "s_nop 1\n\t"
      "v_mfma_f32_32x32x16_bf16 v[48:63], v[188:191], v[196:199], v[48:63]\n\t"
      "ds_write_b128 v200, v[32:35]\n\tds_write_b128 v200, v[36:39] offset:1024\n\tds_write_b128 v200, v[40:43] offset:2048\n\tds_write_b128 v200, v[44:47] offset:3072\n\t"
      "s_nop 7\n\t"
      ".purgem GRP\n\t.purgem MEM\n\t.purgem STEP\n\t.purgem TILE"
      : "={v[48:63]}"(own), "+{v[128:143]}"(kf[0]), "+{v[144:159]}"(kf[1]), "+{v[160:175]}"(kf[2]),
        "={v[208:223]}"(g2[0]), "={v[224:239]}"(g2[1]), "={v[240:255]}"(g2[2]),
        [skv] "+s"(so_kv), [sw] "+s"(so_w), [ab0] "+s"(ab.s0), [ab1] "+s"(ab.s1), [spin] "+s"(spin), [sg] "+s"(so_g), "+{v205}"(fvo)
      : "{v[64:67]}"(q[1][0]), "{v[68:71]}"(q[1][1]), "{v[72:75]}"(q[2][0]), "{v[76:79]}"(q[2][1]),
        "{v[80:83]}"(q[3][0]), "{v[84:87]}"(q[3][1]), "{v[88:91]}"(q[0][0]), "{v[92:95]}"(q[0][1]),
        [vo] "{v204}"(x.vl16), [d1] "s"(SCR + (w ^ 1) * 3 * 4096), [d2] "s"(SCR + ((w ^ 2) * 3 + 1) * 4096), [d3] "s"(SCR + ((w ^ 3) * 3 + 2) * 4096), [rs] "s"(x.rs), [sl] "s"(so_l), [wd0] "s"(wd.s0), [wd1] "s"(wd.s1)
      : "memory", "scc", "vcc",
        "v0","v1","v2","v3","v4","v5","v6","v7","v8","v9","v10","v11","v12","v13","v14","v15","v16","v17","v18","v19","v20","v21","v22","v23",
        "v24","v25","v26","v27","v28","v29","v30","v31","v32","v33","v34","v35","v36","v37","v38","v39","v40","v41","v42","v43","v44","v45","v46","v47",
        "v96","v97","v98","v99","v100","v101","v102","v103","v104","v105","v106","v107","v108","v109","v110","v111",
        "v112","v113","v114","v115","v116","v117","v118","v119","v120","v121","v122","v123","v124","v125","v126","v127",
        "v176","v177","v178","v179","v180","v181","v182","v183","v184","v185","v186","v187","v188","v189","v190","v191",
        "v192","v193","v194","v195","v196","v197","v198","v199","v200","v201","v202","v203","v206", ACLOB);
  } else {
    f32x16 o[4];
    wv<0>();
    pin(kf[0]); pin(kf[1]); pin(kf[2]);
    float mx[4];
#pragma unroll
    for (int k = 0; k < 4; ++k) {
      mx[k] = -3.0e38f; if (h) q[k][1][2] = 0; pin(q[k][1]);
#pragma unroll
      for (int i = 0; i < 16; ++i) o[k][i] = 0.f;
    }
#pragma nounroll
    for (unsigned spin = 0; spin < (1u << 16); ++spin) {
      float v;
      ld1(v, (l & 15) * 4096, x.rs, OFF_FLAG + FL_W(0)); wv<0>(); pin(v);
      if (__all(__float_as_uint(v) >= 16u)) break;
      __builtin_amdgcn_s_sleep(24);
    }
    pf_wo(x);
    pf_wgu1(x);
    sfor<0, 12>([&](auto j) {
      u32x4 t;
      ld4<0>(t, x.vl16, x.rs, OFF_WGU + (x.m * 196 + 112 + 12 * w + j) * KB); wv<0>(); pin(t);
#pragma unroll
      for (int i = 0; i < 4; ++i) g2[j >> 2][4 * (j & 3) + i] = __uint_as_float(t[i]);
    });
    pf_wd<0, NWL>(x, base_wd(x));
    for (int tk = 0; tk < 16; ++tk) {
      u32x4 kf[4];
      sfor<0, 4>([&](auto i) { ld4<i * 1024>(kf[i], x.vl16, x.rs, kvs + tk * 4096); });
      wv<0>();
      sfor<0, 4>([&](auto k) {
        f32x16 s;
        nop<1>();
        m32z(s, kf[0], q[k][0]); m32(s, kf[1], q[k][1]);
        nop<11>(); pin(s);
        float tm = s[0];
#pragma unroll
        for (int i = 1; i < 16; ++i) tm = fmaxf(tm, s[i]);
        tm = max32(tm);
        const float mn = fmaxf(mx[k], tm), al = __builtin_amdgcn_exp2f(mx[k] - mn);
        mx[k] = mn;
#pragma unroll
        for (int i = 0; i < 16; ++i) o[k][i] *= al;
        u32x4 p0, p1;
#pragma unroll
        for (int d = 0; d < 4; ++d) {
          p0[d] = cvtpk(__builtin_amdgcn_exp2f(s[2 * d] - mn), __builtin_amdgcn_exp2f(s[2 * d + 1] - mn));
          p1[d] = cvtpk(__builtin_amdgcn_exp2f(s[8 + 2 * d] - mn), __builtin_amdgcn_exp2f(s[9 + 2 * d] - mn));
        }
        pin(p0); pin(p1); pin(o[k]);
        nop<1>();
        m32(o[k], kf[2], p0); m32(o[k], kf[3], p1);
        nop<11>(); pin(o[k]);
      });
    }
    bar();
    float* mm = (float*)(lds + SCR);
#pragma unroll
    for (int k = 0; k < 4; ++k) if (h == 0) mm[((w ^ k) * 4 + w) * 32 + r] = mx[k];
    bar();
#pragma unroll
    for (int k = 0; k < 4; ++k) {
      const float* p = mm + (w ^ k) * 128 + r;
      const float g = fmaxf(fmaxf(p[0], p[32]), fmaxf(p[64], p[96])), f = __builtin_amdgcn_exp2f(mx[k] - g);
#pragma unroll
      for (int i = 0; i < 16; ++i) o[k][i] *= f;
    }
    bar();
    reduce_put(x, o);
    own = o[0];
  }
  reduce_get(x, own);
  pin(own);
  {
    auto lr = __builtin_amdgcn_permlane32_swap(__float_as_uint(own[12]), __float_as_uint(own[12]), false, false);
    const float inv = __builtin_amdgcn_rcpf(__uint_as_float(lr[1]));
    u32x4 a, b;
#pragma unroll
    for (int d = 0; d < 4; ++d) { a[d] = cvtpk(own[2 * d] * inv, own[2 * d + 1] * inv); b[d] = cvtpk(own[8 + 2 * d] * inv, own[9 + 2 * d] * inv); }
    if (h) { b[2] = 0; b[3] = 0; }
    const unsigned so = OFF_O + L * SZ_O + (tt * 32 + 2 * (2 * hp + kh)) * KB;
    st4<0>(a, x.vl16, x.rs, so); st4<1024>(b, x.vl16, x.rs, so);
  }
  wv<0>();
  signal(x, FL_O(L, x.c, th), 1);
  if (L == 0 && th == 0) {
    if (x.c == hp) count(x, FL_W2, 1);
  }
}

template <bool FINAL> DI void res_epilogue(Ctx& x, const f32x16& acc, const u32x4 (&xr)[2], unsigned Xo, unsigned STo, int tt, int nt, char* out = nullptr) {
  const int h = x.l >> 5, r = x.l & 31;
  u32x4 y[2];
#pragma unroll
  for (int d = 0; d < 8; ++d) y[d >> 2][d & 3] = cvtpk(acc[2 * d] + bflo(xr[d >> 2][d & 3]), acc[2 * d + 1] + bfhi(xr[d >> 2][d & 3]));
  if constexpr (FINAL) {
#pragma unroll
    for (int s = 0; s < 2; ++s) {
      auto a = __builtin_amdgcn_permlane32_swap(y[s][0], y[s][2], false, false);
      auto b = __builtin_amdgcn_permlane32_swap(y[s][1], y[s][3], false, false);
      y[s] = u32x4{a[0], b[0], a[1], b[1]};
    }
    const unsigned vo = r * (D * 2) + h * 16, so = tt * 32 * D * 2 + nt * 64;
    const rsrc_t ro = mkrs(out, S * D * 2);
    st4p<0>(y[0], vo, ro, so); st4p<32>(y[1], vo, ro, so);
  } else {
    const unsigned so = Xo + (tt * 28 + 2 * nt) * KB;
    st4<0>(y[0], x.vl16, x.rs, so); st4<1024>(y[1], x.vl16, x.rs, so);
    float s = 0.f, q = 0.f, s1 = 0.f, q1 = 0.f;
#pragma unroll
    for (int d = 0; d < 4; ++d) {
      s = dot2(y[0][d], 0x3f803f80u, s); q = dot2(y[0][d], y[0][d], q); s1 = dot2(y[1][d], 0x3f803f80u, s1); q1 = dot2(y[1][d], y[1][d], q1);
    }
    s = sum32(s + s1); q = sum32(q + q1);
    if (h == 0) st2<0>(u32x2{__float_as_uint(s), __float_as_uint(q)}, r * 8, x.rs, STo + (nt * 2048 + tt * 32) * 8);
  }
}

DI void phase_oproj(Ctx& x, const int L, const Args& a, const f32x16 (&g2)[3]) {
  const int w = x.w;
  auto put_g2 = [&]() {
#pragma unroll
    for (int j = 0; j < 12; ++j)
      *(f32x4*)(lds + (112 + 12 * w + j) * KB + x.vl16) = f32x4{g2[j >> 2][4 * (j & 3)], g2[j >> 2][4 * (j & 3) + 1], g2[j >> 2][4 * (j & 3) + 2], g2[j >> 2][4 * (j & 3) + 3]};
  };
  if (x.m < 14) {
    const int th = x.m / 7, tt = 4 * x.c + 2 * th + (w & 1), nt = 2 * (x.m % 7) + (w >> 1);
    u32x2 x0[4];
    if (L == 0) {
      const rsrc_t rx = mkrs(a.x, S * D * 2);
      sfor<0, 4>([&](auto i) { ld2i<16 * i>(x0[i], (x.l & 31) * 896 + (x.l >> 5) * 8, rx, tt * 32 * 896 + nt * 64); });
    } else {
      sfor<0, 4>([&](auto i) { ld2u<(i >> 1) * 1024 + (i & 1) * 8>(x0[i], x.vl16, x.rs, OFF_X + 2 * SZ_X + (tt * 28 + 2 * nt) * KB); });
    }
    await(x, FL_O(L, x.c, th), 8);
    u32x4 b[16];
    const unsigned s0 = OFF_O + L * SZ_O + (tt * 32 + 16 * (w >> 1)) * KB;
    auto ld = [&](auto i) { ld4<(i & 3) * 1024>(b[i], x.vl16, x.rs, s0 + (i >> 2) * 4096); };
    constexpr int WN = 10;
    sfor<0, WN>(ld);
    f32x16 acc[2];
    sfor<0, 16>([&](auto i) {
      wv<(i + WN - 1 < 15 ? WN - 1 : 15 - i)>();
      m32a<A_WO + 8 * i + 4, i == 0>(acc[1], b[i]); m32a<A_WO + 8 * i, i == 0>(acc[0], b[i]);
      if constexpr (i + WN < 16) ld(std::integral_constant<int, i + WN>{});
    });
    nop<11>();
    pin(acc[0]); pin(acc[1]);
    sfor<0, 4>([&](auto k) { pin(x0[k]); });
    put_tile(lds + SCR + (w ^ 2) * 12 * KB + x.vl16, acc[1]);
    bar();
#pragma unroll
    for (int q = 0; q < 4; ++q) {
      const f32x4 v = ((const f32x4*)(lds + SCR + w * 12 * KB + x.vl16))[q * 64];
#pragma unroll
      for (int i = 0; i < 4; ++i) acc[0][4 * q + i] += v[i];
    }
    pin(acc[0]);
    const u32x4 x2[2] = {u32x4{x0[0][0], x0[0][1], x0[1][0], x0[1][1]}, u32x4{x0[2][0], x0[2][1], x0[3][0], x0[3][1]}};
    res_epilogue<false>(x, acc[0], x2, OFF_X + (2 * L + 1) * SZ_X, OFF_ST + (2 * L + 1) * SZ_ST, tt, nt);
    put_g2();
    wv<0>();
    signal(x, FL_X1(L, x.c), 1);
  } else {
    put_g2();
  }
}

constexpr int DWN = 7;
constexpr bool dn_half(int o) { return o == 4 || o == 5 || o == 14 || o == 15; }
struct DnSrc { unsigned s0, s1, l0, l1; };
DI DnSrc dn_src(const Ctx& x, int L) {
  const int tt0 = 4 * x.c + 2 * ((x.m / 7) & 1), tt = tt0 + (x.w >> 1), tto = tt0 + ((x.w >> 1) ^ 1);
  return DnSrc{OFF_H + L * SZ_H + (tt * 112 + 28 * x.w) * KB, OFF_H + L * SZ_H + (tto * 112 + 28 * x.w) * KB,
               OFF_HL + ((L * 64 + tt) * 16 + 4 * x.w) * KB, OFF_HL + ((L * 64 + tto) * 16 + 4 * x.w) * KB};
}
template <int O> DI void dn_ld(Ctx& x, const DnSrc& d, u32x4 (&b)[DWN][2]) {
  constexpr int i = dn_ks(O);
  if constexpr (dn_half(O)) {
    constexpr int imm = (O >= 14 ? 2048 : 0) + (O & 1) * 512;
    u32x2 lo, hi, lo1, hi1;
    ld2u<imm>(lo, x.l * 8, x.rs, d.l0); ld2u<imm + 1024>(hi, x.l * 8, x.rs, d.l0);
    ld2u<imm>(lo1, x.l * 8, x.rs, d.l1); ld2u<imm + 1024>(hi1, x.l * 8, x.rs, d.l1);
    b[O % DWN][0] = u32x4{lo[0], lo[1], hi[0], hi[1]}; b[O % DWN][1] = u32x4{lo1[0], lo1[1], hi1[0], hi1[1]};
  } else {
    ld4<(i & 3) * 1024>(b[O % DWN][0], x.vl16, x.rs, d.s0 + (i >> 2) * 4096); ld4<(i & 3) * 1024>(b[O % DWN][1], x.vl16, x.rs, d.s1 + (i >> 2) * 4096);
  }
}

DI void gu_tile(const f32x4 (&acc)[2][2], const u32x4 (&cs)[2], const float (&mean)[2], const float (&r2)[2], u32x2 (&hp)[2]) {
  const f32x4 cg = __builtin_bit_cast(f32x4, cs[0]), cu = __builtin_bit_cast(f32x4, cs[1]);
#pragma unroll
  for (int u = 0; u < 2; ++u) {
    const f32x2 nm = {-mean[u], -mean[u]}, rr = {r2[u], r2[u]};
#pragma unroll
    for (int e = 0; e < 2; ++e) {
      const f32x2 g = __builtin_elementwise_fma(nm, f32x2{cg[2 * e], cg[2 * e + 1]}, f32x2{acc[0][u][2 * e], acc[0][u][2 * e + 1]});
      const f32x2 v = __builtin_elementwise_fma(nm, f32x2{cu[2 * e], cu[2 * e + 1]}, f32x2{acc[1][u][2 * e], acc[1][u][2 * e + 1]});
      const f32x2 y = g * rr * v;
      hp[u][e] = cvtpk(y[0], y[1]);
    }
  }
}

constexpr bool gu_wd_slot(int i) {
  if (i >= 140) return (i - 140) % 3 == 0 && i - 140 < 3 * NWB;
  const int k = i / 10, s = i % 10;
  return k <= 4 ? (s == 4 || s == 9) : (k <= 9 ? (s == 0 || s == 5) : (s == 3 || s == 5 || s == 8));
}
constexpr int gu_wd(int i) {
  if (!gu_wd_slot(i)) return -1;
  int later = 0;
  for (int q = i + 1; q < 196; ++q) later += gu_wd_slot(q);
  return 55 - later >= NWL ? 55 - later : -1;
}
constexpr int gu_reqs(int i, int xa) {
  if (i < 0) return (i + 1000) % 10 == 1 || (i + 1000) % 10 == 6;
  const int k = i / 10, s = i % 10;
  return ((s == 1 || s == 6) && k + xa < 14) + ((s == 3 || s == 8) && (k <= 3 || (k >= 5 && k <= 9))) + (k == 5 && s == 3) + (gu_wd(i) >= 0);
}
DI void phase_gu(Ctx& x, const int L, u32x4 (&b)[DWN][2]) {
  const int w = x.w, l = x.l, g = l >> 4, c16 = l & 15, m = x.m, tt = 4 * x.c + w;
  if (L == 0 && wave0(x)) { unsigned v; ldflag(v, x.rs, OFF_FLAG + FL_W2); wv<0>(); recheck(x, v, FL_W2, 8); }
  await(x, FL_X1(L, x.c), 14);
  constexpr int NXV = 11;
  u32x4 xf[NXV][2]; f32x2 sp[2][4];
  const unsigned sx = OFF_X + (2 * L + 1) * SZ_X + tt * 28 * KB, st = OFF_ST + (2 * L + 1) * SZ_ST + tt * 32 * 8;
  auto ldx = [&](auto k, auto u) {
    if constexpr (k < NXV) ld4<(k & 1) * 2048 + 256 * u>(xf[k][u], x.vg16, x.rs, sx + (k >> 1) * 4096);
    else if constexpr (k < 14) lda<A_GX + 8 * (k - NXV) + 4 * u, (k & 1) * 2048 + 256 * u>(x.vg16, x.rs, sx + (k >> 1) * 4096);
    else { constexpr int i = k - 14; ld2<128 * u>(sp[u][i], (((i < 3 || g < 2) ? g + 4 * i : g) * 2048 + c16) * 8, x.rs, st); }
  };
  using I0 = std::integral_constant<int, 0>; using I1 = std::integral_constant<int, 1>;
  constexpr int XA = 3;
  sfor<0, XA>([&](auto k) { ldx(k, I0{}); ldx(k, I1{}); });
  const unsigned a0 = x.vl16, a1 = x.vl16 + 65536, a2 = x.vl16 + 131072;
  constexpr int DP = 6;
  u32x4 wf[8];
  auto rd = [&](auto ii) {
    constexpr int i = ii, slot = i >= 160 ? i - 160 : i;
    if constexpr (slot < 64) dsr<slot * 1024>(wf[i & 7], a0); else if constexpr (slot < 128) dsr<(slot - 64) * 1024>(wf[i & 7], a1); else dsr<(slot - 128) * 1024>(wf[i & 7], a2);
  };
  auto mm = [&](f32x4& c, const u32x4& a, auto k, auto u) {
    if constexpr (k < NXV) m16<k == 0>(c, a, xf[k][u]); else m16a<false, A_GX + 8 * (k - NXV) + 4 * u>(c, a);
  };
  const Two wd = base_wd(x);
  f32x4 aa[5][2][2], ch0, ch1;
  float mean[2], r2[2];
  sfor<0, DP>(rd);
  sfor<0, 140>([&](auto i) {
    constexpr int KS = i / 10, s = i % 10, t = s >> 1, mat = i & 1;
    using K = std::integral_constant<int, KS>;
    if constexpr (s == 0) {
      constexpr int cnt = [] { int n = 0; for (int q = (KS - XA) * 10 + 7; q < KS * 10; ++q) n += gu_reqs(q, XA); return n; }();
      wv<cnt>();
      if constexpr (KS == 5) asm volatile("s_barrier" ::: "memory");
    }
    if constexpr ((i & 1) == 0) { rd(std::integral_constant<int, i + DP>{}); rd(std::integral_constant<int, i + DP + 1>{}); wl<DP>(); }
    mm(aa[t][mat][0], wf[i & 7], K{}, I0{});
    if constexpr (s == 1 && KS + XA < 14) ldx(std::integral_constant<int, KS + XA>{}, I0{});
    if constexpr (s == 6 && KS + XA < 14) ldx(std::integral_constant<int, KS + XA>{}, I1{});
    if constexpr (KS <= 3 && s == 3) ldx(std::integral_constant<int, 14 + KS>{}, I0{});
    if constexpr (KS <= 3 && s == 8) ldx(std::integral_constant<int, 14 + KS>{}, I1{});
    if constexpr (KS == 5 && s == 3) asm volatile("buffer_load_dwordx4 %0, %2, %3, %4 offen sc1\n\tbuffer_load_dwordx4 %1, %2, %3, %4 offen offset:1024 sc1" : "=&v"(ch0), "=&v"(ch1) : "v"(x.vl16), "s"(x.rs), "s"(OFF_CSGU + m * 2048));
    if constexpr (KS == 11 && s == 2) { pin(ch0); pin(ch1); *(f32x4*)(lds + 40 * KB + x.vl16) = ch0 + ch1; }
    if constexpr (KS >= 5 && KS <= 9 && (s == 3 || s == 8) && !(KS == 5 && s == 3)) pf_wgu3<2 * (KS - 5) + (s == 8) - 1>(x);
    if constexpr (gu_wd(i) >= 0) pf_wd<gu_wd(i), gu_wd(i) + 1>(x, wd);
    if constexpr ((KS == 10 || KS == 12) && s == 2) {
      constexpr int u = KS == 12;
      sfor<0, 4>([&](auto e) { pin(sp[u][e]); });
      float sm = sp[u][0][0] + sp[u][1][0] + sp[u][2][0], q = sp[u][0][1] + sp[u][1][1] + sp[u][2][1];
      if (g < 2) { sm += sp[u][3][0]; q += sp[u][3][1]; }
      sm = sum32(sum16(sm)); q = sum32(sum16(q));
      float rs; row_stats(sm, q, mean[u], rs); r2[u] = rs * rs;
      pin(mean[u]); pin(r2[u]);
    }
    mm(aa[t][mat][1], wf[i & 7], K{}, I1{}); SB();
  });
  u32x4 cs[7][2];
  const unsigned csa = 40 * KB + g * 16;
  sfor<0, 10>([&](auto i) { dsr<64 * i>(cs[i >> 1][i & 1], csa); });
  { constexpr int cnt = [] { int n = 0; for (int q = 99; q < 140; ++q) n += gu_reqs(q, XA); return n; }(); wv<cnt>(); }
  asm volatile("s_barrier" ::: "memory");
  const unsigned H = OFF_H + L * SZ_H + tt * 112 * KB;
  const int gp = (m & 1) ? 7 * m + 1 : 7 * m;
  auto pair = [&](const f32x4 (&t0)[2][2], const f32x4 (&t1)[2][2], auto lt) {
    u32x2 h0[2], h1[2];
    gu_tile(t0, cs[lt], mean, r2, h0); gu_tile(t1, cs[lt + 1], mean, r2, h1);
    st4<0>(u32x4{h0[0][0], h0[0][1], h1[0][0], h1[0][1]}, x.vg16, x.rs, H + (gp + lt - 1) * 1024);
    st4<256>(u32x4{h0[1][0], h0[1][1], h1[1][0], h1[1][1]}, x.vg16, x.rs, H + (gp + lt - 1) * 1024);
  };
  {
    wl<0>();
    sfor<0, 20>([&](auto i) { pin(aa[i >> 2][(i >> 1) & 1][i & 1]); });
    sfor<0, 10>([&](auto i) { pin(cs[i >> 1][i & 1]); });
    u32x2 hl[2];
    gu_tile(aa[0], cs[0], mean, r2, hl);
    const unsigned sl = OFF_HL + ((L * 64 + tt) * 16 + m) * KB;
    st2<0>(hl[0], x.vg16 >> 1, x.rs, sl); st2<128>(hl[1], x.vg16 >> 1, x.rs, sl);
    pair(aa[1], aa[2], std::integral_constant<int, 1>{});
    pair(aa[3], aa[4], std::integral_constant<int, 3>{});
  }
  SB();
  sfor<10, 14>([&](auto i) { dsr<64 * i>(cs[i >> 1][i & 1], csa); });
  f32x4 ab[2][2][2];
  unsigned fa;
  sfor<140, 196>([&](auto i) {
    constexpr int j = i - 140, KS = j / 4, t = (j % 4) >> 1, mat = i & 1;
    using K = std::integral_constant<int, KS>;
    if constexpr (j == 18) {
      { constexpr int cnt = [] { int n = 0; for (int q = 140; q < 158; ++q) n += gu_wd(q) >= 0; return n; }(); if (L == 0) wv<cnt + 6>(); else wv<cnt>(); }
      asm volatile("s_barrier" ::: "memory");
      count(x, FL_H(L, x.c, 0), 1);
      SB();
    }
    if constexpr ((i & 1) == 0) { if constexpr (i + DP < 196) { rd(std::integral_constant<int, i + DP>{}); rd(std::integral_constant<int, i + DP + 1>{}); wl<DP>(); } else wl<194 - i>(); }
    mm(ab[t][mat][0], wf[i & 7], K{}, I0{});
    if constexpr (gu_wd(i) >= 0) pf_wd<gu_wd(i), gu_wd(i) + 1>(x, wd);
    if constexpr (j % 3 == 1 && j < 42) { if (L == 0) pf_wq<j / 3>(x); SB(); }
    if constexpr (j == 44) ldflag(fa, x.rs, OFF_FLAG + FL_H(L, x.c, 0));
    mm(ab[t][mat][1], wf[i & 7], K{}, I1{}); SB();
  });
  wv<0>();
  recheck(x, fa, FL_H(L, x.c, 0), 16);
  { const DnSrc d = dn_src(x, L); sfor<0, 3>([&](auto o) { dn_ld<o>(x, d, b); }); }
  sfor<0, 8>([&](auto i) { pin(ab[i >> 2][(i >> 1) & 1][i & 1]); });
  sfor<10, 14>([&](auto i) { pin(cs[i >> 1][i & 1]); });
  pair(ab[0], ab[1], std::integral_constant<int, 5>{});
}

DI void phase_down(Ctx& x, const int L, char* out, u32x4 (&b)[DWN][2]) {
  const int w = x.w;
  if (x.m >= 14) {
    wv<0>();
    sfor<0, 3>([&](auto o) { pin(b[o][0]); pin(b[o][1]); });
    signal(x, FL_H(L, x.c, 1), 1);
    return;
  }
  const int th = x.m / 7, ct = x.m % 7, tt = 4 * x.c + 2 * th + (w >> 1), nt = 2 * ct + (w & 1);
  constexpr int WN = DWN;
  u32x4 xr[2];
  const DnSrc d = dn_src(x, L);
  auto ld = [&](auto o) { dn_ld<o>(x, d, b); };
  sfor<3, WN>(ld);
  f32x16 acc[4];
  unsigned fb;
  sfor<0, 25>([&](auto i) {
    constexpr int last = i + WN - 1 < 27 ? i + WN - 1 : 27, ex = i <= 2 ? 2 : ((i >= 8 && i <= 13) ? 1 : ((i >= 21) ? 2 : 0));
    constexpr int later = [&] { int n = 0; for (int o = i + 1; o <= last; ++o) n += dn_half(o) ? 4 : 2; return n; }();
    wv<later + (dn_half(i) ? 2 : 1) + ex>();
    if constexpr (i == 3) signal(x, FL_H(L, x.c, 1), 1);
    m32a<A_WD + 8 * i, i == 0>(acc[0], b[i % WN][0]); m32a<A_WD + 8 * i + 4, i == 0>(acc[1], b[i % WN][0]);
    wv<later + ex>();
    m32a<A_WD + 8 * i, i == 0>(acc[2], b[i % WN][1]); m32a<A_WD + 8 * i + 4, i == 0>(acc[3], b[i % WN][1]);
    if constexpr (i == 7) ldflag(fb, x.rs, OFF_FLAG + FL_H(L, x.c, 1));
    if constexpr (i + WN == 20) { wv<12>(); recheck(x, fb, FL_H(L, x.c, 1), 16); }
    if constexpr (i + WN < 28) ld(std::integral_constant<int, i + WN>{});
    if constexpr (i + WN == 27) {
      const unsigned sr = OFF_X + (2 * L + 1) * SZ_X + (tt * 28 + 2 * nt) * KB;
      ld4<0>(xr[0], x.vl16, x.rs, sr); ld4<1024>(xr[1], x.vl16, x.rs, sr);
    }
  });
  wv<2>();
  sfor<1, 5>([&](auto t) {
    constexpr int k = t & 3;
    m32a<A_WD + 8 * 25 + 4 * (k & 1), false>(acc[k], b[25 % WN][k >> 1]);
    m32a<A_WD + 8 * 26 + 4 * (k & 1), false>(acc[k], b[26 % WN][k >> 1]);
    if constexpr (t > 1) { nop<(t == 2 ? 9 : 7)>(); red_put1<t - 1, 0>(x, acc[t - 1]); red_put1<t - 1, 1>(x, acc[t - 1]); }
    m32a<A_WD + 8 * 27 + 4 * (k & 1), false>(acc[k], b[27 % WN][k >> 1]);
    if constexpr (t > 1) { red_put1<t - 1, 2>(x, acc[t - 1]); red_put1<t - 1, 3>(x, acc[t - 1]); }
  });
  if (L == 0 && (x.m & 7) < 2) {
    ldl<4>(x.vl16, x.rs, OFF_WQ + ((16 + (x.m & 1)) * 28 + 7 * w) * KB, 7 * w * KB);
    ldl<3>(x.vl16, x.rs, OFF_WQ + ((16 + (x.m & 1)) * 28 + 7 * w + 4) * KB, (7 * w + 4) * KB);
  }
  nop<9>();
  pin(acc[0]);
  reduce_get(x, acc[0]);
  wv<0>();
  pin(xr[0]); pin(xr[1]);
  if (L > 0) {
    res_epilogue<true>(x, acc[0], xr, 0, 0, tt, nt, out);
  } else {
    res_epilogue<false>(x, acc[0], xr, OFF_X + 2 * SZ_X, OFF_ST + 2 * SZ_ST, tt, nt);
    wv<0>();
    signal(x, FL_X(x.c, th), 1);
  }
}

__global__ __launch_bounds__(256, 1) void k_main(Args) {
  typedef unsigned long long u64x8 __attribute__((ext_vector_type(8)));
  typedef unsigned long long u64x2 __attribute__((ext_vector_type(2)));
  u64x8 pin8; u64x2 pws;
  asm volatile("s_load_dwordx16 s[8:23], %4, 0x0\n\ts_load_dwordx4 s[24:27], %4, 0x40\n\t"
               "s_getpc_b64 s[4:5]\n.Lk_begin:\n\t"
               "s_mov_b32 s6, .Lk_end-.Lk_begin\n\ts_mov_b32 s7, 0x20000\n\ts_and_b32 s5, s5, 0xffff\n\t"
               "v_lshlrev_b32 v1, 4, %2\n\t"
               "s_bfe_u32 s28, %3, 0x20003\n\ts_lshl_b32 s28, s28, 12\n\t"
               "buffer_load_dwordx4 a[112:115], v1, s[4:7], s28 offen\n\t"
               "s_bfe_u32 s28, %3, 0x30004\n\ts_lshl_b32 s28, s28, 13\n\t"
               "buffer_load_dwordx4 a[112:115], v1, s[4:7], s28 offen\n\t"
               "s_add_u32 s28, s28, 4096\n\t"
               "buffer_load_dwordx4 a[112:115], v1, s[4:7], s28 offen\n\t"
               "s_waitcnt lgkmcnt(0)"
               : "={s[8:23]}"(pin8), "={s[24:27]}"(pws) : "v"(threadIdx.x), "{s2}"(blockIdx.x), "{s[0:1]}"(__builtin_amdgcn_kernarg_segment_ptr())
               : "s4", "s5", "s6", "s7", "s28", "v1", "scc", ACLOB);
  const Args a = {(const char*)pin8[0], (const char*)pin8[1], (const char*)pin8[2], (const char*)pin8[3], (const char*)pin8[4], (const char*)pin8[5],
                  (const char*)pin8[6], (const char*)pin8[7], (char*)pws[0], (char*)pws[1]};
  Ctx x;
  x.rs = mkrs(a.ws, WS_USED);
  x.w = __builtin_amdgcn_readfirstlane(threadIdx.x >> 6);
  asm volatile("s_nop 4" : "+s"(x.w));
  { const int b = blockIdx.x; x.c = 2 * (b & 7) + (b >> 7); x.m = (b >> 3) & 15; }
  f32x16 kf[3];
  { Ctx y = x; fresh(y); phase_pre(y, a, kf); }
  for (int L = 0;; ++L) {
    f32x16 g2[3];
    int Lo = L; asm volatile("" : "+s"(Lo));
    { Ctx y = x; fresh(y); phase_att(y, Lo, kf, g2); }
    { Ctx y = x; fresh(y); phase_oproj(y, Lo, a, g2); }
    { Ctx y = x; u32x4 b[DWN][2]; fresh(y); phase_gu(y, Lo, b); fresh(y); phase_down(y, Lo, a.out, b); }
    if (L == 1) break;
    { Ctx y = x; fresh(y); phase_front(y, kf); }
  }
  fresh(x);
  if (blockIdx.x == 0 && wave0(x)) {
    get_tag(x);
    if (x.l == 0) st2<0>(u32x2{x.tlo, x.thi}, 0, x.rs, OFF_TAG);
  }
  asm volatile(".Lk_end:");
}

extern "C" void kernel_launch(void* const* d_in, const int* in_sizes, int n_in, void* d_out, int out_size, void* d_ws, size_t ws_size,
                              hipStream_t stream) {
  Args a;
  a.x = (const char*)d_in[0]; a.wq = (const char*)d_in[1]; a.wk = (const char*)d_in[2]; a.wv = (const char*)d_in[3];
  a.wo = (const char*)d_in[4]; a.wg = (const char*)d_in[6]; a.wu = (const char*)d_in[7]; a.wd = (const char*)d_in[8];
  a.ws = (char*)d_ws; a.out = (char*)d_out;
  (void)hipFuncSetAttribute((const void*)k_main, hipFuncAttributeMaxDynamicSharedMemorySize, 160 * 1024);
  k_main<<<256, 256, 160 * 1024, stream>>>(a);
}
```
